```python
import torch

def _softmax(x, axis=-1):
    return torch.softmax(x, dim=axis)
def _gelu(x):
    return torch.nn.functional.gelu(x, approximate="tanh")
def _silu(x):
    return torch.nn.functional.silu(x)
def _sigmoid(x):
    return torch.sigmoid(x)
def _layernorm(x, axis=-1, eps=1e-5):
    m = x.mean(dim=axis, keepdim=True)
    v = x.var(dim=axis, keepdim=True, unbiased=False)
    return (x - m) / torch.sqrt(v + eps)
def _scatter_add(tgt, idx, src, axis=0):
    return tgt.clone().index_add_(axis, idx, src)
def _segment_sum(src, seg, n):
    out = torch.zeros((n,) + src.shape[1:], dtype=src.dtype)
    return out.index_add_(0, seg, src)
def _pad(x, pw):
    flat = tuple(v for pair in reversed(pw) for v in pair)
    return torch.nn.functional.pad(x, flat)

kernel_name = 'standalone__mha_block_cached__near_ridge'

def setup_inputs():

    _w = (lambda a: (lambda t: t.to(torch.bfloat16) if t.is_floating_point() else t)(torch.as_tensor(a)))
    B, Sq, D = 16, 208, 1024
    H, Sk, Dh = 32, 1024, 128
    return {
        "x": _w((lambda *s: torch.randn(*s, dtype=torch.bfloat16) * 0.8)(B, Sq, D)),
        "k_cache": _w((lambda *s: torch.randn(*s, dtype=torch.bfloat16) * 0.8)(B, H, Sk, Dh)),
        "v_cache": _w((lambda *s: torch.randn(*s, dtype=torch.bfloat16) * 0.05)(B, H, Sk, Dh)),
        "wq": _w((lambda *s: torch.randn(*s, dtype=torch.bfloat16) * 0.8)(D, H * Dh) * (D ** -0.5)),
        "wo": _w((lambda *s: torch.randn(*s, dtype=torch.bfloat16) * 0.05)(H * Dh, D) * ((H * Dh) ** -0.5)),
    }

def reference(x, k_cache, v_cache, wq, wo):
    B, Sq, D = x.shape
    H, Dh = 32, 128
    q = (lambda x, *d: x.permute(*d))((x @ wq).reshape(B, Sq, H, Dh), 0, 2, 1, 3)
    scores = torch.einsum("bhqd,bhkd->bhqk", q, k_cache) * (Dh ** -0.5)
    p = _softmax(scores, axis=-1)
    o = torch.einsum("bhqk,bhkd->bhqd", p, v_cache)
    return (lambda x, *d: x.permute(*d))(o, 0, 2, 1, 3).reshape(B, Sq, H * Dh) @ wo
```

```cpp
#include <hip/hip_runtime.h>
#include <hip/hip_bf16.h>
#define CLOBBERS "v1","v2","v3","v4","v5","v6","v7","v8","v9","v10","v11","v12","v13","v14","v15","v16","v17","v18","v19","v20","v21","v22","v23","v24","v25","v26","v27","v28","v29","v30","v31","v32","v33","v34","v35","v36","v37","v38","v39","v40","v41","v42","v43","v44","v45","v46","v47","v48","v49","v50","v51","v52","v53","v54","v55","v56","v57","v58","v59","v60","v61","v62","v63","v64","v65","v66","v67","v68","v69","v70","v71","v72","v73","v74","v75","v76","v77","v78","v79","v80","v81","v82","v83","v84","v85","v86","v87","v88","v89","v90","v91","v92","v93","v94","v95","v96","v97","v98","v99","v100","v101","v102","v103","v104","v105","v106","v107","v108","v109","v110","v111","v112","v113","v114","v115","v116","v117","v118","v119","v120","v121","v122","v123","v124","v125","v126","v127","v128","v129","v130","v131","v132","v133","v134","v135","v136","v137","v138","v139","v140","v141","v142","v143","v144","v145","v146","v147","v148","v149","v150","v151","v152","v153","v154","v155","v156","v157","v158","v159","v160","v161","v162","v163","v164","v165","v166","v167","v168","v169","v170","v171","v172","v173","v174","v175","v176","v177","v178","v179","v180","v181","v182","v183","v184","v185","v186","v187","v188","v189","v190","v191","v192","v193","v194","v195","v196","v197","v198","v199","v200","v201","v202","v203","v204","v205","v206","v207","v208","v209","v210","v211","v212","v213","v214","v215","v216","v217","v218","v219","v220","v221","v222","v223","v224","v225","v226","v227","v228","v229","v230","v231","v232","v233","v234","v235","v236","v237","v238","v239","v240","v241","v242","v243","v244","v245","v246","v247","v248","v249","v250","v251","v252","v253","v254","v255","a0","a1","a2","a3","a4","a5","a6","a7","a8","a9","a10","a11","a12","a13","a14","a15","a16","a17","a18","a19","a20","a21","a22","a23","a24","a25","a26","a27","a28","a29","a30","a31","a32","a33","a34","a35","a36","a37","a38","a39","a40","a41","a42","a43","a44","a45","a46","a47","a48","a49","a50","a51","a52","a53","a54","a55","a56","a57","a58","a59","a60","a61","a62","a63","a64","a65","a66","a67","a68","a69","a70","a71","a72","a73","a74","a75","a76","a77","a78","a79","a80","a81","a82","a83","a84","a85","a86","a87","a88","a89","a90","a91","a92","a93","a94","a95","a96","a97","a98","a99","a100","a101","a102","a103","a104","a105","a106","a107","a108","a109","a110","a111","a112","a113","a114","a115","a116","a117","a118","a119","a120","a121","a122","a123","a124","a125","a126","a127","a128","a129","a130","a131","a132","a133","a134","a135","a136","a137","a138","a139","a140","a141","a142","a143","a144","a145","a146","a147","a148","a149","a150","a151","a152","a153","a154","a155","a156","a157","a158","a159","a160","a161","a162","a163","a164","a165","a166","a167","a168","a169","a170","a171","a172","a173","a174","a175","a176","a177","a178","a179","a180","a181","a182","a183","a184","a185","a186","a187","a188","a189","a190","a191","a192","a193","a194","a195","a196","a197","a198","a199","a200","a201","a202","a203","a204","a205","a206","a207","a208","a209","a210","a211","a212","a213","a214","a215","a216","a217","a218","a219","a220","a221","a222","a223","a224","a225","a226","a227","a228","a229","a230","a231","a232","a233","a234","a235","a236","a237","a238","a239","a240","a241","a242","a243","a244","a245","a246","a247","a248","a249","a250","a251","a252","a253","a254","a255","s8","s9","s10","s11","s12","s13","s14","s15","s16","s17","s18","s19","s20","s21","s22","s23","s24","s25","s26","s27","s28","s29","s30","s31","s32","s33","s34","s35","s36","s37","s38","s39","s40","s41","s42","s43","s44","s45","s46","s47","s48","s49","s50","s51","s52","s53","s54","s55","s56","s57","s58","s59","s60","s61","s62","s63","s64","s65","s66","s67","s68","s69","s70","s71","s72","s73","s74","s75","s76","s77","s78","s79","s80","s81","s82","s83","s84","s85","s86","s87","s88","s89","s90","s91","s92","s93","s94","s95","s96","s97","s98","s99","s100","s101","vcc","scc","memory"
__global__ __launch_bounds__(256, 1) void mha_block(const void* x, const void* k, const void* v, const void* wq, const void* wo, void* ws, void* out) {
  __shared__ __attribute__((aligned(16))) char lds[163840];
  unsigned ldsa = (unsigned)(size_t)(__attribute__((address_space(3))) char*)lds;
  asm volatile(
"L1_start:\n"
"s_load_dwordx8 s[8:15], %0, 0x0\n"
"s_load_dwordx4 s[16:19], %0, 0x20\n"
"s_mov_b32 s20, %2\n"
"s_load_dwordx2 s[68:69], %0, 0x30\n"
"v_lshrrev_b32 v10, 6, v0\n"
"v_and_b32 v4, 63, v0\n"
"v_readfirstlane_b32 s21, v10\n"
"v_lshrrev_b32 v5, 4, v4\n"
"v_and_b32 v6, 15, v4\n"
"v_lshrrev_b32 v7, 2, v6\n"
"v_and_b32 v8, 3, v4\n"
"v_and_b32 v9, 1, v5\n"
"s_getpc_b64 s[100:101]\n"
"s_lshr_b32 s25, s20, 3\n"
"s_lshl_b32 s25, s25, 2\n"
"s_add_u32 s25, s25, s21\n"
"s_lshl_b32 s25, s25, 10\n"
"v_and_b32 v10, 7, v4\n"
"v_lshl_add_u32 v255, v10, 7, s25\n"
"s_and_b32 s100, s100, 4294967040\n"
"v_min_u32 v255, L_end-L1_start-128, v255\n"
"s_nop 2\n"
"global_load_dword v223, v255, s[100:101]\n"
"s_bfe_u32 s23, s20, 131073\n"
"s_lshl_b32 s23, s23, 2\n"
"s_bfe_u32 s25, s20, 131075\n"
"s_add_u32 s23, s23, s25\n"
"s_and_b32 s24, s20, 1\n"
"s_lshl_b32 s24, s24, 3\n"
"s_lshr_b32 s25, s20, 5\n"
"s_add_u32 s24, s24, s25\n"
"s_lshr_b32 s22, s21, 1\n"
"s_lshl_b32 s59, s24, 1\n"
"s_add_u32 s59, s59, s22\n"
"s_mul_i32 s48, s22, 14144\n"
"s_mul_i32 s49, s22, 8704\n"
"s_mul_i32 s50, s22, 17152\n"
"s_mov_b32 s51, 0\n"
"s_mov_b32 s52, 0\n"
"s_mov_b32 s53, 0\n"
"s_mov_b32 s54, 4096\n"
"s_mov_b32 s58, 0\n"
"v_and_b32 v10, 7, v4\n"
"v_xor_b32 v10, v10, v5\n"
"v_lshlrev_b32 v10, 4, v10\n"
"v_lshrrev_b32 v11, 3, v4\n"
"v_lshl_add_u32 v10, v11, 11, v10\n"
"s_mul_i32 s25, s22, 212992\n"
"v_add_u32 v216, s25, v10\n"
"v_xor_b32 v10, v6, v9\n"
"v_lshlrev_b32 v10, 4, v10\n"
"v_lshl_add_u32 v212, v5, 16, v10\n"
"s_waitcnt lgkmcnt(0)\n"
"s_mul_i32 s25, s23, 425984\n"
"s_add_u32 s28, s8, s25\n"
"s_addc_u32 s29, s9, 0\n"
"s_and_b32 s29, s29, 65535\n"
"s_mov_b32 s30, 425984\n"
"s_mov_b32 s31, 131072\n"
"s_lshl_b32 s25, s59, 8\n"
"s_sub_u32 s26, 8388608, s25\n"
"s_add_u32 s32, s14, s25\n"
"s_addc_u32 s33, s15, 0\n"
"s_and_b32 s33, s33, 65535\n"
"s_mov_b32 s34, s26\n"
"s_mov_b32 s35, 131072\n"
"s_bitcmp1_b32 s21, 0\n"
"s_cbranch_scc1 L1_h1\n"
"v_add_u32 v217, 16384, v216\n"
"v_add_u32 v218, 32768, v216\n"
"v_add_u32 v219, 49152, v216\n"
"v_add_u32 v220, 65536, v216\n"
"v_add_u32 v221, 81920, v216\n"
"v_add_u32 v222, 98304, v216\n"
"v_add_u32 v213, 8192, v212\n"
"v_add_u32 v214, 16384, v212\n"
"v_add_u32 v215, 24576, v212\n"
"s_add_u32 m0, s48, 126208\n"
"s_nop 0\n"
"buffer_load_dwordx4 v216, s[28:31], s51 offen lds\n"
"s_add_u32 m0, s48, 127296\n"
"s_nop 0\n"
"buffer_load_dwordx4 v217, s[28:31], s51 offen lds\n"
"s_add_u32 m0, s48, 128384\n"
"s_nop 0\n"
"buffer_load_dwordx4 v218, s[28:31], s51 offen lds\n"
"s_add_u32 m0, s48, 129472\n"
"s_nop 0\n"
"buffer_load_dwordx4 v219, s[28:31], s51 offen lds\n"
"s_add_u32 m0, s48, 130560\n"
"s_nop 0\n"
"buffer_load_dwordx4 v220, s[28:31], s51 offen lds\n"
"s_add_u32 m0, s48, 131648\n"
"s_nop 0\n"
"buffer_load_dwordx4 v221, s[28:31], s51 offen lds\n"
"s_add_u32 m0, s48, 132736\n"
"s_nop 0\n"
"buffer_load_dwordx4 v222, s[28:31], s51 offen lds\n"
"s_add_u32 s51, s51, 128\n"
"s_add_u32 m0, s49, 56576\n"
"s_nop 0\n"
"buffer_load_dwordx4 v212, s[32:35], s52 offen lds\n"
"s_add_u32 m0, s49, 57664\n"
"s_nop 0\n"
"buffer_load_dwordx4 v213, s[32:35], s52 offen lds\n"
"s_add_u32 m0, s49, 58752\n"
"s_nop 0\n"
"buffer_load_dwordx4 v214, s[32:35], s52 offen lds\n"
"s_add_u32 m0, s49, 59840\n"
"s_nop 0\n"
"buffer_load_dwordx4 v215, s[32:35], s52 offen lds\n"
"s_add_u32 s52, s52, 262144\n"
"s_add_u32 m0, s49, 73984\n"
"s_nop 0\n"
"buffer_load_dwordx4 v212, s[32:35], s52 offen lds\n"
"s_add_u32 m0, s49, 75072\n"
"s_nop 0\n"
"buffer_load_dwordx4 v213, s[32:35], s52 offen lds\n"
"s_add_u32 m0, s49, 76160\n"
"s_nop 0\n"
"buffer_load_dwordx4 v214, s[32:35], s52 offen lds\n"
"s_add_u32 m0, s49, 77248\n"
"s_nop 0\n"
"buffer_load_dwordx4 v215, s[32:35], s52 offen lds\n"
"s_add_u32 s52, s52, 262144\n"
"s_add_u32 m0, s48, 0\n"
"s_nop 0\n"
"buffer_load_dwordx4 v216, s[28:31], s51 offen lds\n"
"s_add_u32 m0, s48, 1088\n"
"s_nop 0\n"
"buffer_load_dwordx4 v217, s[28:31], s51 offen lds\n"
"s_add_u32 m0, s48, 2176\n"
"s_nop 0\n"
"buffer_load_dwordx4 v218, s[28:31], s51 offen lds\n"
"s_add_u32 m0, s48, 3264\n"
"s_nop 0\n"
"buffer_load_dwordx4 v219, s[28:31], s51 offen lds\n"
"s_add_u32 m0, s48, 4352\n"
"s_nop 0\n"
"buffer_load_dwordx4 v220, s[28:31], s51 offen lds\n"
"s_add_u32 m0, s48, 5440\n"
"s_nop 0\n"
"buffer_load_dwordx4 v221, s[28:31], s51 offen lds\n"
"s_add_u32 m0, s48, 6528\n"
"s_nop 0\n"
"buffer_load_dwordx4 v222, s[28:31], s51 offen lds\n"
"s_add_u32 s51, s51, 128\n"
"s_add_u32 m0, s49, 91392\n"
"s_nop 0\n"
"buffer_load_dwordx4 v212, s[32:35], s52 offen lds\n"
"s_add_u32 m0, s49, 92480\n"
"s_nop 0\n"
"buffer_load_dwordx4 v213, s[32:35], s52 offen lds\n"
"s_add_u32 m0, s49, 93568\n"
"s_nop 0\n"
"buffer_load_dwordx4 v214, s[32:35], s52 offen lds\n"
"s_add_u32 m0, s49, 94656\n"
"s_nop 0\n"
"buffer_load_dwordx4 v215, s[32:35], s52 offen lds\n"
"s_add_u32 s52, s52, 262144\n"
"s_add_u32 m0, s49, 108800\n"
"s_nop 0\n"
"buffer_load_dwordx4 v212, s[32:35], s52 offen lds\n"
"s_add_u32 m0, s49, 109888\n"
"s_nop 0\n"
"buffer_load_dwordx4 v213, s[32:35], s52 offen lds\n"
"s_add_u32 m0, s49, 110976\n"
"s_nop 0\n"
"buffer_load_dwordx4 v214, s[32:35], s52 offen lds\n"
"s_add_u32 m0, s49, 112064\n"
"s_nop 0\n"
"buffer_load_dwordx4 v215, s[32:35], s52 offen lds\n"
"s_add_u32 s52, s52, 262144\n"
"s_add_u32 m0, s48, 28288\n"
"s_nop 0\n"
"buffer_load_dwordx4 v216, s[28:31], s51 offen lds\n"
"s_add_u32 m0, s48, 29376\n"
"s_nop 0\n"
"buffer_load_dwordx4 v217, s[28:31], s51 offen lds\n"
"s_add_u32 m0, s48, 30464\n"
"s_nop 0\n"
"buffer_load_dwordx4 v218, s[28:31], s51 offen lds\n"
"s_add_u32 m0, s48, 31552\n"
"s_nop 0\n"
"buffer_load_dwordx4 v219, s[28:31], s51 offen lds\n"
"v_lshrrev_b32 v10, 1, v6\n"
"v_and_b32 v10, 3, v10\n"
"v_xor_b32 v10, v10, v5\n"
"v_lshlrev_b32 v10, 4, v10\n"
"v_and_b32 v11, 7, v6\n"
"v_lshl_add_u32 v10, v11, 7, v10\n"
"v_lshrrev_b32 v11, 3, v6\n"
"v_mul_u32_u24 v11, 1088, v11\n"
"v_add_u32 v208, v10, v11\n"
"v_add_u32 v209, 126208, v208\n"
"v_xor_b32 v10, v8, v9\n"
"v_lshlrev_b32 v10, 4, v10\n"
"v_lshl_add_u32 v10, v5, 8, v10\n"
"v_mul_u32_u24 v11, 1088, v7\n"
"v_add3_u32 v10, v10, v11, s49\n"
"v_add_u32 v10, 56576, v10\n"
"v_and_b32 v11, 1, v8\n"
"v_lshl_add_u32 v210, v11, 3, v10\n"
"v_xor_b32 v11, 1, v11\n"
"v_lshl_add_u32 v211, v11, 3, v10\n"
"v_xor_b32 v10, v5, v8\n"
"v_lshlrev_b32 v10, 4, v10\n"
"v_lshl_add_u32 v10, v8, 8, v10\n"
"v_mul_u32_u24 v11, 1088, v7\n"
"v_add3_u32 v244, v10, v11, s50\n"
"v_add_u32 v245, 68608, v244\n"
"v_lshlrev_b32 v10, 3, v8\n"
"v_lshl_add_u32 v10, v5, 8, v10\n"
"v_mul_u32_u24 v11, 1056, v7\n"
"v_add3_u32 v10, v10, v11, s50\n"
"v_add_u32 v10, 8704, v10\n"
"v_lshl_add_u32 v246, v9, 7, v10\n"
"v_xor_b32 v11, 1, v9\n"
"v_lshl_add_u32 v248, v11, 7, v10\n"
"v_add_u32 v247, 68608, v246\n"
"v_add_u32 v249, 68608, v248\n"
"v_lshlrev_b32 v251, 4, v4\n"
"v_xor_b32 v10, v6, v5\n"
"v_lshlrev_b32 v10, 4, v10\n"
"v_lshl_add_u32 v250, v5, 8, v10\n"
"s_lshl_b32 s25, s23, 5\n"
"s_add_u32 s25, s25, s59\n"
"s_lshl_b32 s26, s25, 18\n"
"s_add_u32 s36, s10, s26\n"
"s_addc_u32 s37, s11, 0\n"
"s_and_b32 s37, s37, 65535\n"
"s_mov_b32 s38, 262144\n"
"s_mov_b32 s39, 131072\n"
"s_add_u32 s44, s10, s26\n"
"s_addc_u32 s45, s11, 0\n"
"s_and_b32 s45, s45, 65535\n"
"s_mov_b32 s46, 262144\n"
"s_mov_b32 s47, 131072\n"
"s_lshl_b32 s67, s20, 1\n"
"s_add_u32 s67, s67, s22\n"
"s_and_b32 s67, s67, 31\n"
"s_and_b32 s53, s67, 0\n"
"s_lshl_b32 s53, s53, 12\n"
"s_lshr_b32 s67, s67, 0\n"
"s_lshl_b32 s67, s67, 13\n"
"s_or_b32 s67, s67, s53\n"
"s_mov_b32 s53, s67\n"
"s_add_u32 s54, s67, 4096\n"
"s_mov_b32 s38, -1\n"
"s_mul_i32 s26, s25, 53248\n"
"s_add_u32 s40, s18, s26\n"
"s_addc_u32 s41, s19, 0\n"
"s_and_b32 s41, s41, 65535\n"
"s_mov_b32 s42, 53248\n"
"s_mov_b32 s43, 131072\n"
"s_lshl_b32 s25, s20, 2\n"
"s_add_u32 s25, s25, s21\n"
"s_lshl_b32 s25, s25, 13\n"
"s_add_u32 s60, s16, s25\n"
"s_addc_u32 s61, s17, 0\n"
"s_and_b32 s61, s61, 65535\n"
"s_mov_b32 s62, 8192\n"
"s_mov_b32 s63, 131072\n"
"s_mov_b32 s56, 1040356334\n"
"s_mov_b32 s57, s56\n"
"v_mov_b32 v10, 1065369472\n"
"v_mov_b32 v11, 0\n"
"v_mov_b32 v12, 163832\n"
"v_accvgpr_write_b32 a236, v10\n"
"v_accvgpr_write_b32 a237, v10\n"
"v_accvgpr_write_b32 a238, v10\n"
"v_accvgpr_write_b32 a239, v10\n"
"ds_write_b32 v12, v11\n"
"s_add_u32 s76, s18, 33554432\n"
"s_addc_u32 s77, s19, 0\n"
"s_and_b32 s77, s77, 65535\n"
"s_mov_b32 s78, 131072\n"
"s_mov_b32 s79, 131072\n"
"s_lshl_b32 s25, s23, 8\n"
"s_add_u32 s25, s25, 65536\n"
"buffer_load_dwordx2 a[232:233], off, s[76:79], s25 sc1\n"
"v_add_u32 v254, 4096, v251\n"
"s_bfe_u32 s66, s20, 3\n"
"s_lshl_b32 s66, s66, 13\n"
"s_add_u32 s66, s66, s67\n"
"s_add_u32 s25, s50, 0\n"
"v_add_u32 v252, s25, v251\n"
"v_add_u32 v253, 68608, v252\n"
"s_waitcnt vmcnt(20)\n"
"s_waitcnt lgkmcnt(0)\n"
"s_barrier\n"
"ds_read_b64_tr_b16 a[144:145], v210 offset:128\n"
"ds_read_b64_tr_b16 a[146:147], v210 offset:4480\n"
"ds_read_b128 a[176:179], v209\n"
"ds_read_b64_tr_b16 a[148:149], v210 offset:17536\n"
"ds_read_b64_tr_b16 a[150:151], v210 offset:21888\n"
"ds_read_b128 a[180:183], v209 offset:64\n"
"ds_read_b64_tr_b16 a[152:153], v211 offset:128\n"
"ds_read_b64_tr_b16 a[154:155], v211 offset:4480\n"
"ds_read_b64_tr_b16 a[156:157], v211 offset:17536\n"
"ds_read_b64_tr_b16 a[158:159], v211 offset:21888\n"
"ds_read_b64_tr_b16 a[160:161], v210 offset:192\n"
"ds_read_b64_tr_b16 a[162:163], v210 offset:4544\n"
"ds_read_b64_tr_b16 a[164:165], v210 offset:17600\n"
"ds_read_b64_tr_b16 a[166:167], v210 offset:21952\n"
"ds_read_b64_tr_b16 a[168:169], v211 offset:192\n"
"ds_read_b64_tr_b16 a[170:171], v211 offset:4544\n"
"ds_read_b64_tr_b16 a[172:173], v211 offset:17600\n"
"ds_read_b64_tr_b16 a[174:175], v211 offset:21952\n"
"ds_read_b128 a[184:187], v209 offset:2176\n"
"ds_read_b128 a[188:191], v209 offset:2240\n"
"ds_read_b128 a[192:195], v209 offset:4352\n"
"ds_read_b128 a[196:199], v209 offset:4416\n"
"ds_read_b128 a[200:203], v209 offset:6528\n"
"ds_read_b128 a[204:207], v209 offset:6592\n"
"ds_read_b128 a[208:211], v209 offset:8704\n"
"ds_read_b128 a[212:215], v209 offset:8768\n"
"ds_read_b128 a[224:227], v209 offset:26112\n"
"ds_read_b128 a[228:231], v209 offset:26176\n"
"s_waitcnt lgkmcnt(15)\n"
"v_mfma_f32_16x16x32_bf16 v[16:19], a[144:147], a[176:179], 0\n"
"s_waitcnt lgkmcnt(15)\n"
"ds_read_b128 a[216:219], v209 offset:10880\n"
"v_mfma_f32_16x16x32_bf16 v[16:19], a[148:151], a[180:183], v[16:19]\n"
"s_waitcnt lgkmcnt(15)\n"
"ds_read_b128 a[220:223], v209 offset:10944\n"
"v_mfma_f32_16x16x32_bf16 v[20:23], a[152:155], a[176:179], 0\n"
"s_waitcnt lgkmcnt(15)\n"
"ds_read_b64_tr_b16 a[112:113], v210\n"
"v_mfma_f32_16x16x32_bf16 v[20:23], a[156:159], a[180:183], v[20:23]\n"
"s_waitcnt lgkmcnt(15)\n"
"ds_read_b64_tr_b16 a[114:115], v210 offset:4352\n"
"v_mfma_f32_16x16x32_bf16 v[24:27], a[160:163], a[176:179], 0\n"
"s_waitcnt lgkmcnt(15)\n"
"ds_read_b64_tr_b16 a[116:117], v210 offset:17408\n"
"v_mfma_f32_16x16x32_bf16 v[24:27], a[164:167], a[180:183], v[24:27]\n"
"s_waitcnt lgkmcnt(15)\n"
"ds_read_b64_tr_b16 a[118:119], v210 offset:21760\n"
"v_mfma_f32_16x16x32_bf16 v[28:31], a[168:171], a[176:179], 0\n"
"s_waitcnt lgkmcnt(15)\n"
"ds_read_b64_tr_b16 a[120:121], v211\n"
"v_mfma_f32_16x16x32_bf16 v[28:31], a[172:175], a[180:183], v[28:31]\n"
"s_waitcnt lgkmcnt(15)\n"
"ds_read_b64_tr_b16 a[122:123], v211 offset:4352\n"
"v_mfma_f32_16x16x32_bf16 v[48:51], a[144:147], a[184:187], 0\n"
"s_waitcnt lgkmcnt(15)\n"
"ds_read_b64_tr_b16 a[124:125], v211 offset:17408\n"
"v_mfma_f32_16x16x32_bf16 v[48:51], a[148:151], a[188:191], v[48:51]\n"
"ds_read_b64_tr_b16 a[126:127], v211 offset:21760\n"
"v_mfma_f32_16x16x32_bf16 v[52:55], a[152:155], a[184:187], 0\n"
"ds_read_b64_tr_b16 a[128:129], v210 offset:64\n"
"v_mfma_f32_16x16x32_bf16 v[52:55], a[156:159], a[188:191], v[52:55]\n"
"ds_read_b64_tr_b16 a[130:131], v210 offset:4416\n"
"v_mfma_f32_16x16x32_bf16 v[56:59], a[160:163], a[184:187], 0\n"
"ds_read_b64_tr_b16 a[132:133], v210 offset:17472\n"
"v_mfma_f32_16x16x32_bf16 v[56:59], a[164:167], a[188:191], v[56:59]\n"
"ds_read_b64_tr_b16 a[134:135], v210 offset:21824\n"
"v_mfma_f32_16x16x32_bf16 v[60:63], a[168:171], a[184:187], 0\n"
"ds_read_b64_tr_b16 a[136:137], v211 offset:64\n"
"v_mfma_f32_16x16x32_bf16 v[60:63], a[172:175], a[188:191], v[60:63]\n"
"s_waitcnt lgkmcnt(15)\n"
"ds_read_b64_tr_b16 a[138:139], v211 offset:4416\n"
"v_mfma_f32_16x16x32_bf16 v[80:83], a[144:147], a[192:195], 0\n"
"s_waitcnt lgkmcnt(15)\n"
"ds_read_b64_tr_b16 a[140:141], v211 offset:17472\n"
"v_mfma_f32_16x16x32_bf16 v[80:83], a[148:151], a[196:199], v[80:83]\n"
"ds_read_b64_tr_b16 a[142:143], v211 offset:21824\n"
"s_add_u32 m0, s48, 32640\n"
"v_mfma_f32_16x16x32_bf16 v[84:87], a[152:155], a[192:195], 0\n"
"buffer_load_dwordx4 v220, s[28:31], s51 offen lds\n"
"v_mfma_f32_16x16x32_bf16 v[84:87], a[156:159], a[196:199], v[84:87]\n"
"v_mfma_f32_16x16x32_bf16 v[88:91], a[160:163], a[192:195], 0\n"
"s_add_u32 m0, s48, 33728\n"
"v_mfma_f32_16x16x32_bf16 v[88:91], a[164:167], a[196:199], v[88:91]\n"
"buffer_load_dwordx4 v221, s[28:31], s51 offen lds\n"
"v_mfma_f32_16x16x32_bf16 v[92:95], a[168:171], a[192:195], 0\n"
"v_mfma_f32_16x16x32_bf16 v[92:95], a[172:175], a[196:199], v[92:95]\n"
"s_waitcnt lgkmcnt(15)\n"
"s_add_u32 m0, s48, 34816\n"
"v_mfma_f32_16x16x32_bf16 v[112:115], a[144:147], a[200:203], 0\n"
"s_waitcnt lgkmcnt(15)\n"
"buffer_load_dwordx4 v222, s[28:31], s51 offen lds\n"
"s_add_u32 s51, s51, 128\n"
"v_mfma_f32_16x16x32_bf16 v[112:115], a[148:151], a[204:207], v[112:115]\n"
"v_mfma_f32_16x16x32_bf16 v[116:119], a[152:155], a[200:203], 0\n"
"v_mfma_f32_16x16x32_bf16 v[116:119], a[156:159], a[204:207], v[116:119]\n"
"v_mfma_f32_16x16x32_bf16 v[120:123], a[160:163], a[200:203], 0\n"
"v_mfma_f32_16x16x32_bf16 v[120:123], a[164:167], a[204:207], v[120:123]\n"
"v_mfma_f32_16x16x32_bf16 v[124:127], a[168:171], a[200:203], 0\n"
"v_mfma_f32_16x16x32_bf16 v[124:127], a[172:175], a[204:207], v[124:127]\n"
"s_waitcnt lgkmcnt(15)\n"
"v_mfma_f32_16x16x32_bf16 v[144:147], a[144:147], a[208:211], 0\n"
"s_waitcnt lgkmcnt(15)\n"
"v_mfma_f32_16x16x32_bf16 v[144:147], a[148:151], a[212:215], v[144:147]\n"
"v_mfma_f32_16x16x32_bf16 v[148:151], a[152:155], a[208:211], 0\n"
"v_mfma_f32_16x16x32_bf16 v[148:151], a[156:159], a[212:215], v[148:151]\n"
"v_mfma_f32_16x16x32_bf16 v[152:155], a[160:163], a[208:211], 0\n"
"v_mfma_f32_16x16x32_bf16 v[152:155], a[164:167], a[212:215], v[152:155]\n"
"v_mfma_f32_16x16x32_bf16 v[156:159], a[168:171], a[208:211], 0\n"
"v_mfma_f32_16x16x32_bf16 v[156:159], a[172:175], a[212:215], v[156:159]\n"
"s_waitcnt lgkmcnt(15)\n"
"v_mfma_f32_16x16x32_bf16 v[176:179], a[144:147], a[216:219], 0\n"
"s_waitcnt lgkmcnt(0)\n"
"s_waitcnt vmcnt(16)\n"
"s_barrier\n"
"v_mfma_f32_16x16x32_bf16 v[176:179], a[148:151], a[220:223], v[176:179]\n"
"s_add_u32 m0, s49, 56576\n"
"v_mfma_f32_16x16x32_bf16 v[180:183], a[152:155], a[216:219], 0\n"
"buffer_load_dwordx4 v212, s[32:35], s52 offen lds\n"
"v_mfma_f32_16x16x32_bf16 v[180:183], a[156:159], a[220:223], v[180:183]\n"
"v_mfma_f32_16x16x32_bf16 v[184:187], a[160:163], a[216:219], 0\n"
"v_mfma_f32_16x16x32_bf16 v[184:187], a[164:167], a[220:223], v[184:187]\n"
"v_mfma_f32_16x16x32_bf16 v[188:191], a[168:171], a[216:219], 0\n"
"s_add_u32 m0, s49, 57664\n"
"v_mfma_f32_16x16x32_bf16 v[188:191], a[172:175], a[220:223], v[188:191]\n"
"buffer_load_dwordx4 v213, s[32:35], s52 offen lds\n"
"v_mfma_f32_16x16x32_bf16 v[192:195], a[112:115], a[224:227], 0\n"
"v_mfma_f32_16x16x32_bf16 v[192:195], a[116:119], a[228:231], v[192:195]\n"
"v_mfma_f32_16x16x32_bf16 v[196:199], a[120:123], a[224:227], 0\n"
"v_mfma_f32_16x16x32_bf16 v[196:199], a[124:127], a[228:231], v[196:199]\n"
"s_add_u32 m0, s49, 58752\n"
"v_mfma_f32_16x16x32_bf16 v[200:203], a[128:131], a[224:227], 0\n"
"buffer_load_dwordx4 v214, s[32:35], s52 offen lds\n"
"v_mfma_f32_16x16x32_bf16 v[200:203], a[132:135], a[228:231], v[200:203]\n"
"v_mfma_f32_16x16x32_bf16 v[204:207], a[136:139], a[224:227], 0\n"
"v_mfma_f32_16x16x32_bf16 v[204:207], a[140:143], a[228:231], v[204:207]\n"
"v_mfma_f32_16x16x32_bf16 v[0:3], a[112:115], a[176:179], 0\n"
"ds_read_b128 a[224:227], v208 offset:26112\n"
"s_add_u32 m0, s49, 59840\n"
"v_mfma_f32_16x16x32_bf16 v[0:3], a[116:119], a[180:183], v[0:3]\n"
"ds_read_b128 a[228:231], v208 offset:26176\n"
"buffer_load_dwordx4 v215, s[32:35], s52 offen lds\n"
"s_add_u32 s52, s52, 262144\n"
"v_mfma_f32_16x16x32_bf16 v[4:7], a[120:123], a[176:179], 0\n"
"v_mfma_f32_16x16x32_bf16 v[4:7], a[124:127], a[180:183], v[4:7]\n"
"v_mfma_f32_16x16x32_bf16 v[8:11], a[128:131], a[176:179], 0\n"
"v_mfma_f32_16x16x32_bf16 v[8:11], a[132:135], a[180:183], v[8:11]\n"
"s_add_u32 m0, s49, 73984\n"
"v_mfma_f32_16x16x32_bf16 v[12:15], a[136:139], a[176:179], 0\n"
"buffer_load_dwordx4 v212, s[32:35], s52 offen lds\n"
"v_mfma_f32_16x16x32_bf16 v[12:15], a[140:143], a[180:183], v[12:15]\n"
"v_mfma_f32_16x16x32_bf16 v[32:35], a[112:115], a[184:187], 0\n"
"ds_read_b128 a[176:179], v208\n"
"v_mfma_f32_16x16x32_bf16 v[32:35], a[116:119], a[188:191], v[32:35]\n"
"ds_read_b128 a[180:183], v208 offset:64\n"
"v_mfma_f32_16x16x32_bf16 v[36:39], a[120:123], a[184:187], 0\n"
"s_add_u32 m0, s49, 75072\n"
"v_mfma_f32_16x16x32_bf16 v[36:39], a[124:127], a[188:191], v[36:39]\n"
"buffer_load_dwordx4 v213, s[32:35], s52 offen lds\n"
"v_mfma_f32_16x16x32_bf16 v[40:43], a[128:131], a[184:187], 0\n"
"v_mfma_f32_16x16x32_bf16 v[40:43], a[132:135], a[188:191], v[40:43]\n"
"v_mfma_f32_16x16x32_bf16 v[44:47], a[136:139], a[184:187], 0\n"
"v_mfma_f32_16x16x32_bf16 v[44:47], a[140:143], a[188:191], v[44:47]\n"
"s_add_u32 m0, s49, 76160\n"
"v_mfma_f32_16x16x32_bf16 v[64:67], a[112:115], a[192:195], 0\n"
"ds_read_b128 a[184:187], v208 offset:2176\n"
"buffer_load_dwordx4 v214, s[32:35], s52 offen lds\n"
"v_mfma_f32_16x16x32_bf16 v[64:67], a[116:119], a[196:199], v[64:67]\n"
"ds_read_b128 a[188:191], v208 offset:2240\n"
"v_mfma_f32_16x16x32_bf16 v[68:71], a[120:123], a[192:195], 0\n"
"v_mfma_f32_16x16x32_bf16 v[68:71], a[124:127], a[196:199], v[68:71]\n"
"v_mfma_f32_16x16x32_bf16 v[72:75], a[128:131], a[192:195], 0\n"
"v_mfma_f32_16x16x32_bf16 v[72:75], a[132:135], a[196:199], v[72:75]\n"
"s_waitcnt vmcnt(15)\n"
"s_barrier\n"
"v_mfma_f32_16x16x32_bf16 v[76:79], a[136:139], a[192:195], 0\n"
"s_add_u32 m0, s49, 77248\n"
"ds_read_b64_tr_b16 a[144:145], v210 offset:34944\n"
"v_mfma_f32_16x16x32_bf16 v[76:79], a[140:143], a[196:199], v[76:79]\n"
"buffer_load_dwordx4 v215, s[32:35], s52 offen lds\n"
"s_add_u32 s52, s52, 262144\n"
"ds_read_b64_tr_b16 a[146:147], v210 offset:39296\n"
"v_mfma_f32_16x16x32_bf16 v[96:99], a[112:115], a[200:203], 0\n"
"ds_read_b128 a[192:195], v208 offset:4352\n"
"ds_read_b64_tr_b16 a[148:149], v210 offset:52352\n"
"v_mfma_f32_16x16x32_bf16 v[96:99], a[116:119], a[204:207], v[96:99]\n"
"ds_read_b128 a[196:199], v208 offset:4416\n"
"s_add_u32 m0, s48, 126208\n"
"ds_read_b64_tr_b16 a[150:151], v210 offset:56704\n"
"v_mfma_f32_16x16x32_bf16 v[100:103], a[120:123], a[200:203], 0\n"
"buffer_load_dwordx4 v216, s[28:31], s51 offen lds\n"
"ds_read_b64_tr_b16 a[152:153], v211 offset:34944\n"
"v_mfma_f32_16x16x32_bf16 v[100:103], a[124:127], a[204:207], v[100:103]\n"
"ds_read_b64_tr_b16 a[154:155], v211 offset:39296\n"
"v_mfma_f32_16x16x32_bf16 v[104:107], a[128:131], a[200:203], 0\n"
"ds_read_b64_tr_b16 a[156:157], v211 offset:52352\n"
"v_mfma_f32_16x16x32_bf16 v[104:107], a[132:135], a[204:207], v[104:107]\n"
"ds_read_b64_tr_b16 a[158:159], v211 offset:56704\n"
"v_mfma_f32_16x16x32_bf16 v[108:111], a[136:139], a[200:203], 0\n"
"s_add_u32 m0, s48, 127296\n"
"ds_read_b64_tr_b16 a[160:161], v210 offset:35008\n"
"v_mfma_f32_16x16x32_bf16 v[108:111], a[140:143], a[204:207], v[108:111]\n"
"buffer_load_dwordx4 v217, s[28:31], s51 offen lds\n"
"ds_read_b64_tr_b16 a[162:163], v210 offset:39360\n"
"v_mfma_f32_16x16x32_bf16 v[128:131], a[112:115], a[208:211], 0\n"
"ds_read_b128 a[200:203], v208 offset:6528\n"
"ds_read_b64_tr_b16 a[164:165], v210 offset:52416\n"
"v_mfma_f32_16x16x32_bf16 v[128:131], a[116:119], a[212:215], v[128:131]\n"
"ds_read_b128 a[204:207], v208 offset:6592\n"
"ds_read_b64_tr_b16 a[166:167], v210 offset:56768\n"
"v_mfma_f32_16x16x32_bf16 v[132:135], a[120:123], a[208:211], 0\n"
"ds_read_b64_tr_b16 a[168:169], v211 offset:35008\n"
"v_mfma_f32_16x16x32_bf16 v[132:135], a[124:127], a[212:215], v[132:135]\n"
"s_add_u32 m0, s48, 128384\n"
"ds_read_b64_tr_b16 a[170:171], v211 offset:39360\n"
"v_mfma_f32_16x16x32_bf16 v[136:139], a[128:131], a[208:211], 0\n"
"buffer_load_dwordx4 v218, s[28:31], s51 offen lds\n"
"ds_read_b64_tr_b16 a[172:173], v211 offset:52416\n"
"v_mfma_f32_16x16x32_bf16 v[136:139], a[132:135], a[212:215], v[136:139]\n"
"ds_read_b64_tr_b16 a[174:175], v211 offset:56768\n"
"v_mfma_f32_16x16x32_bf16 v[140:143], a[136:139], a[208:211], 0\n"
"v_mfma_f32_16x16x32_bf16 v[140:143], a[140:143], a[212:215], v[140:143]\n"
"v_mfma_f32_16x16x32_bf16 v[160:163], a[112:115], a[216:219], 0\n"
"ds_read_b128 a[208:211], v208 offset:8704\n"
"s_add_u32 m0, s48, 129472\n"
"v_mfma_f32_16x16x32_bf16 v[160:163], a[116:119], a[220:223], v[160:163]\n"
"ds_read_b128 a[212:215], v208 offset:8768\n"
"buffer_load_dwordx4 v219, s[28:31], s51 offen lds\n"
"v_mfma_f32_16x16x32_bf16 v[164:167], a[120:123], a[216:219], 0\n"
"v_mfma_f32_16x16x32_bf16 v[164:167], a[124:127], a[220:223], v[164:167]\n"
"v_mfma_f32_16x16x32_bf16 v[168:171], a[128:131], a[216:219], 0\n"
"v_mfma_f32_16x16x32_bf16 v[168:171], a[132:135], a[220:223], v[168:171]\n"
"s_add_u32 m0, s48, 130560\n"
"v_mfma_f32_16x16x32_bf16 v[172:175], a[136:139], a[216:219], 0\n"
"buffer_load_dwordx4 v220, s[28:31], s51 offen lds\n"
"s_waitcnt lgkmcnt(2)\n"
"v_mfma_f32_16x16x32_bf16 v[172:175], a[140:143], a[220:223], v[172:175]\n"
"v_mfma_f32_16x16x32_bf16 v[16:19], a[144:147], a[176:179], v[16:19]\n"
"ds_read_b128 a[216:219], v208 offset:10880\n"
"v_mfma_f32_16x16x32_bf16 v[16:19], a[148:151], a[180:183], v[16:19]\n"
"ds_read_b128 a[220:223], v208 offset:10944\n"
"v_mfma_f32_16x16x32_bf16 v[20:23], a[152:155], a[176:179], v[20:23]\n"
"ds_read_b64_tr_b16 a[112:113], v210 offset:34816\n"
"s_add_u32 m0, s48, 131648\n"
"v_mfma_f32_16x16x32_bf16 v[20:23], a[156:159], a[180:183], v[20:23]\n"
"ds_read_b64_tr_b16 a[114:115], v210 offset:39168\n"
"buffer_load_dwordx4 v221, s[28:31], s51 offen lds\n"
"v_mfma_f32_16x16x32_bf16 v[24:27], a[160:163], a[176:179], v[24:27]\n"
"ds_read_b64_tr_b16 a[116:117], v210 offset:52224\n"
"v_mfma_f32_16x16x32_bf16 v[24:27], a[164:167], a[180:183], v[24:27]\n"
"ds_read_b64_tr_b16 a[118:119], v210 offset:56576\n"
"v_mfma_f32_16x16x32_bf16 v[28:31], a[168:171], a[176:179], v[28:31]\n"
"ds_read_b64_tr_b16 a[120:121], v211 offset:34816\n"
"v_mfma_f32_16x16x32_bf16 v[28:31], a[172:175], a[180:183], v[28:31]\n"
"ds_read_b64_tr_b16 a[122:123], v211 offset:39168\n"
"s_add_u32 m0, s48, 132736\n"
"v_mfma_f32_16x16x32_bf16 v[48:51], a[144:147], a[184:187], v[48:51]\n"
"ds_read_b64_tr_b16 a[124:125], v211 offset:52224\n"
"buffer_load_dwordx4 v222, s[28:31], s51 offen lds\n"
"s_add_u32 s51, s51, 128\n"
"v_mfma_f32_16x16x32_bf16 v[48:51], a[148:151], a[188:191], v[48:51]\n"
"ds_read_b64_tr_b16 a[126:127], v211 offset:56576\n"
"v_mfma_f32_16x16x32_bf16 v[52:55], a[152:155], a[184:187], v[52:55]\n"
"ds_read_b64_tr_b16 a[128:129], v210 offset:34880\n"
"v_mfma_f32_16x16x32_bf16 v[52:55], a[156:159], a[188:191], v[52:55]\n"
"ds_read_b64_tr_b16 a[130:131], v210 offset:39232\n"
"v_mfma_f32_16x16x32_bf16 v[56:59], a[160:163], a[184:187], v[56:59]\n"
"ds_read_b64_tr_b16 a[132:133], v210 offset:52288\n"
"v_mfma_f32_16x16x32_bf16 v[56:59], a[164:167], a[188:191], v[56:59]\n"
"ds_read_b64_tr_b16 a[134:135], v210 offset:56640\n"
"buffer_load_dwordx4 a[0:3], v250, s[36:39], s53 offen nt\n"
"v_mfma_f32_16x16x32_bf16 v[60:63], a[168:171], a[184:187], v[60:63]\n"
"ds_read_b64_tr_b16 a[136:137], v211 offset:34880\n"
"v_mfma_f32_16x16x32_bf16 v[60:63], a[172:175], a[188:191], v[60:63]\n"
"ds_read_b64_tr_b16 a[138:139], v211 offset:39232\n"
"v_mfma_f32_16x16x32_bf16 v[80:83], a[144:147], a[192:195], v[80:83]\n"
"ds_read_b64_tr_b16 a[140:141], v211 offset:52288\n"
"v_mfma_f32_16x16x32_bf16 v[80:83], a[148:151], a[196:199], v[80:83]\n"
"ds_read_b64_tr_b16 a[142:143], v211 offset:56640\n"
"v_mfma_f32_16x16x32_bf16 v[84:87], a[152:155], a[192:195], v[84:87]\n"
"buffer_load_dwordx4 a[4:7], v250, s[36:39], s53 offen offset:1024 nt\n"
"v_mfma_f32_16x16x32_bf16 v[84:87], a[156:159], a[196:199], v[84:87]\n"
"v_mfma_f32_16x16x32_bf16 v[88:91], a[160:163], a[192:195], v[88:91]\n"
"v_mfma_f32_16x16x32_bf16 v[88:91], a[164:167], a[196:199], v[88:91]\n"
"v_mfma_f32_16x16x32_bf16 v[92:95], a[168:171], a[192:195], v[92:95]\n"
"v_mfma_f32_16x16x32_bf16 v[92:95], a[172:175], a[196:199], v[92:95]\n"
"v_mfma_f32_16x16x32_bf16 v[112:115], a[144:147], a[200:203], v[112:115]\n"
"s_waitcnt lgkmcnt(0)\n"
"s_waitcnt vmcnt(17)\n"
"s_barrier\n"
"v_mfma_f32_16x16x32_bf16 v[112:115], a[148:151], a[204:207], v[112:115]\n"
"s_add_u32 m0, s49, 91392\n"
"v_mfma_f32_16x16x32_bf16 v[116:119], a[152:155], a[200:203], v[116:119]\n"
"buffer_load_dwordx4 v212, s[32:35], s52 offen lds\n"
"v_mfma_f32_16x16x32_bf16 v[116:119], a[156:159], a[204:207], v[116:119]\n"
"v_mfma_f32_16x16x32_bf16 v[120:123], a[160:163], a[200:203], v[120:123]\n"
"v_mfma_f32_16x16x32_bf16 v[120:123], a[164:167], a[204:207], v[120:123]\n"
"v_mfma_f32_16x16x32_bf16 v[124:127], a[168:171], a[200:203], v[124:127]\n"
"v_mfma_f32_16x16x32_bf16 v[124:127], a[172:175], a[204:207], v[124:127]\n"
"s_add_u32 m0, s49, 92480\n"
"v_mfma_f32_16x16x32_bf16 v[144:147], a[144:147], a[208:211], v[144:147]\n"
"buffer_load_dwordx4 v213, s[32:35], s52 offen lds\n"
"v_mfma_f32_16x16x32_bf16 v[144:147], a[148:151], a[212:215], v[144:147]\n"
"v_mfma_f32_16x16x32_bf16 v[148:151], a[152:155], a[208:211], v[148:151]\n"
"v_mfma_f32_16x16x32_bf16 v[148:151], a[156:159], a[212:215], v[148:151]\n"
"v_mfma_f32_16x16x32_bf16 v[152:155], a[160:163], a[208:211], v[152:155]\n"
"v_mfma_f32_16x16x32_bf16 v[152:155], a[164:167], a[212:215], v[152:155]\n"
"s_add_u32 m0, s49, 93568\n"
"v_mfma_f32_16x16x32_bf16 v[156:159], a[168:171], a[208:211], v[156:159]\n"
"buffer_load_dwordx4 v214, s[32:35], s52 offen lds\n"
"v_mfma_f32_16x16x32_bf16 v[156:159], a[172:175], a[212:215], v[156:159]\n"
"v_mfma_f32_16x16x32_bf16 v[176:179], a[144:147], a[216:219], v[176:179]\n"
"v_mfma_f32_16x16x32_bf16 v[176:179], a[148:151], a[220:223], v[176:179]\n"
"v_mfma_f32_16x16x32_bf16 v[180:183], a[152:155], a[216:219], v[180:183]\n"
"v_mfma_f32_16x16x32_bf16 v[180:183], a[156:159], a[220:223], v[180:183]\n"
"s_add_u32 m0, s49, 94656\n"
"v_mfma_f32_16x16x32_bf16 v[184:187], a[160:163], a[216:219], v[184:187]\n"
"buffer_load_dwordx4 v215, s[32:35], s52 offen lds\n"
"s_add_u32 s52, s52, 262144\n"
"v_mfma_f32_16x16x32_bf16 v[184:187], a[164:167], a[220:223], v[184:187]\n"
"v_mfma_f32_16x16x32_bf16 v[188:191], a[168:171], a[216:219], v[188:191]\n"
"v_mfma_f32_16x16x32_bf16 v[188:191], a[172:175], a[220:223], v[188:191]\n"
"v_mfma_f32_16x16x32_bf16 v[192:195], a[112:115], a[224:227], v[192:195]\n"
"v_mfma_f32_16x16x32_bf16 v[192:195], a[116:119], a[228:231], v[192:195]\n"
"s_add_u32 m0, s49, 108800\n"
"v_mfma_f32_16x16x32_bf16 v[196:199], a[120:123], a[224:227], v[196:199]\n"
"buffer_load_dwordx4 v212, s[32:35], s52 offen lds\n"
"v_mfma_f32_16x16x32_bf16 v[196:199], a[124:127], a[228:231], v[196:199]\n"
"v_mfma_f32_16x16x32_bf16 v[200:203], a[128:131], a[224:227], v[200:203]\n"
"v_mfma_f32_16x16x32_bf16 v[200:203], a[132:135], a[228:231], v[200:203]\n"
"v_mfma_f32_16x16x32_bf16 v[204:207], a[136:139], a[224:227], v[204:207]\n"
"v_mfma_f32_16x16x32_bf16 v[204:207], a[140:143], a[228:231], v[204:207]\n"
"s_add_u32 m0, s49, 109888\n"
"v_mfma_f32_16x16x32_bf16 v[0:3], a[112:115], a[176:179], v[0:3]\n"
"ds_read_b128 a[224:227], v208 offset:54400\n"
"buffer_load_dwordx4 v213, s[32:35], s52 offen lds\n"
"v_mfma_f32_16x16x32_bf16 v[0:3], a[116:119], a[180:183], v[0:3]\n"
"ds_read_b128 a[228:231], v208 offset:54464\n"
"v_mfma_f32_16x16x32_bf16 v[4:7], a[120:123], a[176:179], v[4:7]\n"
"v_mfma_f32_16x16x32_bf16 v[4:7], a[124:127], a[180:183], v[4:7]\n"
"v_mfma_f32_16x16x32_bf16 v[8:11], a[128:131], a[176:179], v[8:11]\n"
"v_mfma_f32_16x16x32_bf16 v[8:11], a[132:135], a[180:183], v[8:11]\n"
"s_add_u32 m0, s49, 110976\n"
"v_mfma_f32_16x16x32_bf16 v[12:15], a[136:139], a[176:179], v[12:15]\n"
"buffer_load_dwordx4 v214, s[32:35], s52 offen lds\n"
"v_mfma_f32_16x16x32_bf16 v[12:15], a[140:143], a[180:183], v[12:15]\n"
"v_mfma_f32_16x16x32_bf16 v[32:35], a[112:115], a[184:187], v[32:35]\n"
"ds_read_b128 a[176:179], v208 offset:28288\n"
"v_mfma_f32_16x16x32_bf16 v[32:35], a[116:119], a[188:191], v[32:35]\n"
"ds_read_b128 a[180:183], v208 offset:28352\n"
"v_mfma_f32_16x16x32_bf16 v[36:39], a[120:123], a[184:187], v[36:39]\n"
"v_mfma_f32_16x16x32_bf16 v[36:39], a[124:127], a[188:191], v[36:39]\n"
"s_add_u32 m0, s49, 112064\n"
"v_mfma_f32_16x16x32_bf16 v[40:43], a[128:131], a[184:187], v[40:43]\n"
"buffer_load_dwordx4 v215, s[32:35], s52 offen lds\n"
"s_add_u32 s52, s52, 262144\n"
"v_mfma_f32_16x16x32_bf16 v[40:43], a[132:135], a[188:191], v[40:43]\n"
"v_mfma_f32_16x16x32_bf16 v[44:47], a[136:139], a[184:187], v[44:47]\n"
"v_mfma_f32_16x16x32_bf16 v[44:47], a[140:143], a[188:191], v[44:47]\n"
"v_mfma_f32_16x16x32_bf16 v[64:67], a[112:115], a[192:195], v[64:67]\n"
"ds_read_b128 a[184:187], v208 offset:30464\n"
"v_mfma_f32_16x16x32_bf16 v[64:67], a[116:119], a[196:199], v[64:67]\n"
"ds_read_b128 a[188:191], v208 offset:30528\n"
"s_add_u32 m0, s48, 0\n"
"v_mfma_f32_16x16x32_bf16 v[68:71], a[120:123], a[192:195], v[68:71]\n"
"buffer_load_dwordx4 v216, s[28:31], s51 offen lds\n"
"v_mfma_f32_16x16x32_bf16 v[68:71], a[124:127], a[196:199], v[68:71]\n"
"v_mfma_f32_16x16x32_bf16 v[72:75], a[128:131], a[192:195], v[72:75]\n"
"v_mfma_f32_16x16x32_bf16 v[72:75], a[132:135], a[196:199], v[72:75]\n"
"s_waitcnt vmcnt(18)\n"
"s_barrier\n"
"v_mfma_f32_16x16x32_bf16 v[76:79], a[136:139], a[192:195], v[76:79]\n"
"ds_read_b64_tr_b16 a[144:145], v210 offset:128\n"
"v_mfma_f32_16x16x32_bf16 v[76:79], a[140:143], a[196:199], v[76:79]\n"
"s_add_u32 m0, s48, 1088\n"
"ds_read_b64_tr_b16 a[146:147], v210 offset:4480\n"
"v_mfma_f32_16x16x32_bf16 v[96:99], a[112:115], a[200:203], v[96:99]\n"
"ds_read_b128 a[192:195], v208 offset:32640\n"
"buffer_load_dwordx4 v217, s[28:31], s51 offen lds\n"
"ds_read_b64_tr_b16 a[148:149], v210 offset:17536\n"
"v_mfma_f32_16x16x32_bf16 v[96:99], a[116:119], a[204:207], v[96:99]\n"
"ds_read_b128 a[196:199], v208 offset:32704\n"
"ds_read_b64_tr_b16 a[150:151], v210 offset:21888\n"
"v_mfma_f32_16x16x32_bf16 v[100:103], a[120:123], a[200:203], v[100:103]\n"
"ds_read_b64_tr_b16 a[152:153], v211 offset:128\n"
"v_mfma_f32_16x16x32_bf16 v[100:103], a[124:127], a[204:207], v[100:103]\n"
"ds_read_b64_tr_b16 a[154:155], v211 offset:4480\n"
"v_mfma_f32_16x16x32_bf16 v[104:107], a[128:131], a[200:203], v[104:107]\n"
"ds_read_b64_tr_b16 a[156:157], v211 offset:17536\n"
"v_mfma_f32_16x16x32_bf16 v[104:107], a[132:135], a[204:207], v[104:107]\n"
"s_add_u32 m0, s48, 2176\n"
"ds_read_b64_tr_b16 a[158:159], v211 offset:21888\n"
"v_mfma_f32_16x16x32_bf16 v[108:111], a[136:139], a[200:203], v[108:111]\n"
"buffer_load_dwordx4 v218, s[28:31], s51 offen lds\n"
"ds_read_b64_tr_b16 a[160:161], v210 offset:192\n"
"v_mfma_f32_16x16x32_bf16 v[108:111], a[140:143], a[204:207], v[108:111]\n"
"ds_read_b64_tr_b16 a[162:163], v210 offset:4544\n"
"v_mfma_f32_16x16x32_bf16 v[128:131], a[112:115], a[208:211], v[128:131]\n"
"ds_read_b128 a[200:203], v208 offset:34816\n"
"ds_read_b64_tr_b16 a[164:165], v210 offset:17600\n"
"v_mfma_f32_16x16x32_bf16 v[128:131], a[116:119], a[212:215], v[128:131]\n"
"ds_read_b128 a[204:207], v208 offset:34880\n"
"ds_read_b64_tr_b16 a[166:167], v210 offset:21952\n"
"v_mfma_f32_16x16x32_bf16 v[132:135], a[120:123], a[208:211], v[132:135]\n"
"ds_read_b64_tr_b16 a[168:169], v211 offset:192\n"
"v_mfma_f32_16x16x32_bf16 v[132:135], a[124:127], a[212:215], v[132:135]\n"
"s_add_u32 m0, s48, 3264\n"
"ds_read_b64_tr_b16 a[170:171], v211 offset:4544\n"
"v_mfma_f32_16x16x32_bf16 v[136:139], a[128:131], a[208:211], v[136:139]\n"
"buffer_load_dwordx4 v219, s[28:31], s51 offen lds\n"
"ds_read_b64_tr_b16 a[172:173], v211 offset:17600\n"
"v_mfma_f32_16x16x32_bf16 v[136:139], a[132:135], a[212:215], v[136:139]\n"
"ds_read_b64_tr_b16 a[174:175], v211 offset:21952\n"
"v_mfma_f32_16x16x32_bf16 v[140:143], a[136:139], a[208:211], v[140:143]\n"
"v_mfma_f32_16x16x32_bf16 v[140:143], a[140:143], a[212:215], v[140:143]\n"
"v_mfma_f32_16x16x32_bf16 v[160:163], a[112:115], a[216:219], v[160:163]\n"
"ds_read_b128 a[208:211], v208 offset:36992\n"
"v_mfma_f32_16x16x32_bf16 v[160:163], a[116:119], a[220:223], v[160:163]\n"
"ds_read_b128 a[212:215], v208 offset:37056\n"
"s_add_u32 m0, s48, 4352\n"
"v_mfma_f32_16x16x32_bf16 v[164:167], a[120:123], a[216:219], v[164:167]\n"
"buffer_load_dwordx4 v220, s[28:31], s51 offen lds\n"
"v_mfma_f32_16x16x32_bf16 v[164:167], a[124:127], a[220:223], v[164:167]\n"
"v_mfma_f32_16x16x32_bf16 v[168:171], a[128:131], a[216:219], v[168:171]\n"
"v_mfma_f32_16x16x32_bf16 v[168:171], a[132:135], a[220:223], v[168:171]\n"
"v_mfma_f32_16x16x32_bf16 v[172:175], a[136:139], a[216:219], v[172:175]\n"
"s_waitcnt lgkmcnt(2)\n"
"v_mfma_f32_16x16x32_bf16 v[172:175], a[140:143], a[220:223], v[172:175]\n"
"v_mfma_f32_16x16x32_bf16 v[16:19], a[144:147], a[176:179], v[16:19]\n"
"ds_read_b128 a[216:219], v208 offset:39168\n"
"s_add_u32 m0, s48, 5440\n"
"v_mfma_f32_16x16x32_bf16 v[16:19], a[148:151], a[180:183], v[16:19]\n"
"ds_read_b128 a[220:223], v208 offset:39232\n"
"buffer_load_dwordx4 v221, s[28:31], s51 offen lds\n"
"v_mfma_f32_16x16x32_bf16 v[20:23], a[152:155], a[176:179], v[20:23]\n"
"ds_read_b64_tr_b16 a[112:113], v210\n"
"v_mfma_f32_16x16x32_bf16 v[20:23], a[156:159], a[180:183], v[20:23]\n"
"ds_read_b64_tr_b16 a[114:115], v210 offset:4352\n"
"v_mfma_f32_16x16x32_bf16 v[24:27], a[160:163], a[176:179], v[24:27]\n"
"ds_read_b64_tr_b16 a[116:117], v210 offset:17408\n"
"v_mfma_f32_16x16x32_bf16 v[24:27], a[164:167], a[180:183], v[24:27]\n"
"ds_read_b64_tr_b16 a[118:119], v210 offset:21760\n"
"s_add_u32 m0, s48, 6528\n"
"v_mfma_f32_16x16x32_bf16 v[28:31], a[168:171], a[176:179], v[28:31]\n"
"ds_read_b64_tr_b16 a[120:121], v211\n"
"buffer_load_dwordx4 v222, s[28:31], s51 offen lds\n"
"s_add_u32 s51, s51, 128\n"
"v_mfma_f32_16x16x32_bf16 v[28:31], a[172:175], a[180:183], v[28:31]\n"
"ds_read_b64_tr_b16 a[122:123], v211 offset:4352\n"
"v_mfma_f32_16x16x32_bf16 v[48:51], a[144:147], a[184:187], v[48:51]\n"
"ds_read_b64_tr_b16 a[124:125], v211 offset:17408\n"
"v_mfma_f32_16x16x32_bf16 v[48:51], a[148:151], a[188:191], v[48:51]\n"
"ds_read_b64_tr_b16 a[126:127], v211 offset:21760\n"
"v_mfma_f32_16x16x32_bf16 v[52:55], a[152:155], a[184:187], v[52:55]\n"
"ds_read_b64_tr_b16 a[128:129], v210 offset:64\n"
"v_mfma_f32_16x16x32_bf16 v[52:55], a[156:159], a[188:191], v[52:55]\n"
"ds_read_b64_tr_b16 a[130:131], v210 offset:4416\n"
"v_mfma_f32_16x16x32_bf16 v[56:59], a[160:163], a[184:187], v[56:59]\n"
"ds_read_b64_tr_b16 a[132:133], v210 offset:17472\n"
"buffer_load_dwordx4 a[8:11], v250, s[36:39], s53 offen offset:2048 nt\n"
"v_mfma_f32_16x16x32_bf16 v[56:59], a[164:167], a[188:191], v[56:59]\n"
"ds_read_b64_tr_b16 a[134:135], v210 offset:21824\n"
"v_mfma_f32_16x16x32_bf16 v[60:63], a[168:171], a[184:187], v[60:63]\n"
"ds_read_b64_tr_b16 a[136:137], v211 offset:64\n"
"v_mfma_f32_16x16x32_bf16 v[60:63], a[172:175], a[188:191], v[60:63]\n"
"ds_read_b64_tr_b16 a[138:139], v211 offset:4416\n"
"v_mfma_f32_16x16x32_bf16 v[80:83], a[144:147], a[192:195], v[80:83]\n"
"ds_read_b64_tr_b16 a[140:141], v211 offset:17472\n"
"v_mfma_f32_16x16x32_bf16 v[80:83], a[148:151], a[196:199], v[80:83]\n"
"ds_read_b64_tr_b16 a[142:143], v211 offset:21824\n"
"v_mfma_f32_16x16x32_bf16 v[84:87], a[152:155], a[192:195], v[84:87]\n"
"buffer_load_dwordx4 a[12:15], v250, s[36:39], s53 offen offset:3072 nt\n"
"v_mfma_f32_16x16x32_bf16 v[84:87], a[156:159], a[196:199], v[84:87]\n"
"v_mfma_f32_16x16x32_bf16 v[88:91], a[160:163], a[192:195], v[88:91]\n"
"v_mfma_f32_16x16x32_bf16 v[88:91], a[164:167], a[196:199], v[88:91]\n"
"v_mfma_f32_16x16x32_bf16 v[92:95], a[168:171], a[192:195], v[92:95]\n"
"v_mfma_f32_16x16x32_bf16 v[92:95], a[172:175], a[196:199], v[92:95]\n"
"v_mfma_f32_16x16x32_bf16 v[112:115], a[144:147], a[200:203], v[112:115]\n"
"s_waitcnt lgkmcnt(0)\n"
"s_waitcnt vmcnt(19)\n"
"s_barrier\n"
"v_mfma_f32_16x16x32_bf16 v[112:115], a[148:151], a[204:207], v[112:115]\n"
"s_add_u32 m0, s49, 56576\n"
"v_mfma_f32_16x16x32_bf16 v[116:119], a[152:155], a[200:203], v[116:119]\n"
"buffer_load_dwordx4 v212, s[32:35], s52 offen lds\n"
"v_mfma_f32_16x16x32_bf16 v[116:119], a[156:159], a[204:207], v[116:119]\n"
"v_mfma_f32_16x16x32_bf16 v[120:123], a[160:163], a[200:203], v[120:123]\n"
"v_mfma_f32_16x16x32_bf16 v[120:123], a[164:167], a[204:207], v[120:123]\n"
"v_mfma_f32_16x16x32_bf16 v[124:127], a[168:171], a[200:203], v[124:127]\n"
"v_mfma_f32_16x16x32_bf16 v[124:127], a[172:175], a[204:207], v[124:127]\n"
"s_add_u32 m0, s49, 57664\n"
"v_mfma_f32_16x16x32_bf16 v[144:147], a[144:147], a[208:211], v[144:147]\n"
"buffer_load_dwordx4 v213, s[32:35], s52 offen lds\n"
"v_mfma_f32_16x16x32_bf16 v[144:147], a[148:151], a[212:215], v[144:147]\n"
"v_mfma_f32_16x16x32_bf16 v[148:151], a[152:155], a[208:211], v[148:151]\n"
"v_mfma_f32_16x16x32_bf16 v[148:151], a[156:159], a[212:215], v[148:151]\n"
"v_mfma_f32_16x16x32_bf16 v[152:155], a[160:163], a[208:211], v[152:155]\n"
"v_mfma_f32_16x16x32_bf16 v[152:155], a[164:167], a[212:215], v[152:155]\n"
"s_add_u32 m0, s49, 58752\n"
"v_mfma_f32_16x16x32_bf16 v[156:159], a[168:171], a[208:211], v[156:159]\n"
"buffer_load_dwordx4 v214, s[32:35], s52 offen lds\n"
"v_mfma_f32_16x16x32_bf16 v[156:159], a[172:175], a[212:215], v[156:159]\n"
"v_mfma_f32_16x16x32_bf16 v[176:179], a[144:147], a[216:219], v[176:179]\n"
"v_mfma_f32_16x16x32_bf16 v[176:179], a[148:151], a[220:223], v[176:179]\n"
"v_mfma_f32_16x16x32_bf16 v[180:183], a[152:155], a[216:219], v[180:183]\n"
"v_mfma_f32_16x16x32_bf16 v[180:183], a[156:159], a[220:223], v[180:183]\n"
"s_add_u32 m0, s49, 59840\n"
"v_mfma_f32_16x16x32_bf16 v[184:187], a[160:163], a[216:219], v[184:187]\n"
"buffer_load_dwordx4 v215, s[32:35], s52 offen lds\n"
"s_add_u32 s52, s52, 262144\n"
"v_mfma_f32_16x16x32_bf16 v[184:187], a[164:167], a[220:223], v[184:187]\n"
"v_mfma_f32_16x16x32_bf16 v[188:191], a[168:171], a[216:219], v[188:191]\n"
"v_mfma_f32_16x16x32_bf16 v[188:191], a[172:175], a[220:223], v[188:191]\n"
"v_mfma_f32_16x16x32_bf16 v[192:195], a[112:115], a[224:227], v[192:195]\n"
"v_mfma_f32_16x16x32_bf16 v[192:195], a[116:119], a[228:231], v[192:195]\n"
"s_add_u32 m0, s49, 73984\n"
"v_mfma_f32_16x16x32_bf16 v[196:199], a[120:123], a[224:227], v[196:199]\n"
"buffer_load_dwordx4 v212, s[32:35], s52 offen lds\n"
"v_mfma_f32_16x16x32_bf16 v[196:199], a[124:127], a[228:231], v[196:199]\n"
"v_mfma_f32_16x16x32_bf16 v[200:203], a[128:131], a[224:227], v[200:203]\n"
"v_mfma_f32_16x16x32_bf16 v[200:203], a[132:135], a[228:231], v[200:203]\n"
"v_mfma_f32_16x16x32_bf16 v[204:207], a[136:139], a[224:227], v[204:207]\n"
"v_mfma_f32_16x16x32_bf16 v[204:207], a[140:143], a[228:231], v[204:207]\n"
"s_add_u32 m0, s49, 75072\n"
"v_mfma_f32_16x16x32_bf16 v[0:3], a[112:115], a[176:179], v[0:3]\n"
"ds_read_b128 a[224:227], v209 offset:26112\n"
"buffer_load_dwordx4 v213, s[32:35], s52 offen lds\n"
"v_mfma_f32_16x16x32_bf16 v[0:3], a[116:119], a[180:183], v[0:3]\n"
"ds_read_b128 a[228:231], v209 offset:26176\n"
"v_mfma_f32_16x16x32_bf16 v[4:7], a[120:123], a[176:179], v[4:7]\n"
"v_mfma_f32_16x16x32_bf16 v[4:7], a[124:127], a[180:183], v[4:7]\n"
"v_mfma_f32_16x16x32_bf16 v[8:11], a[128:131], a[176:179], v[8:11]\n"
"v_mfma_f32_16x16x32_bf16 v[8:11], a[132:135], a[180:183], v[8:11]\n"
"s_add_u32 m0, s49, 76160\n"
"v_mfma_f32_16x16x32_bf16 v[12:15], a[136:139], a[176:179], v[12:15]\n"
"buffer_load_dwordx4 v214, s[32:35], s52 offen lds\n"
"v_mfma_f32_16x16x32_bf16 v[12:15], a[140:143], a[180:183], v[12:15]\n"
"v_mfma_f32_16x16x32_bf16 v[32:35], a[112:115], a[184:187], v[32:35]\n"
"ds_read_b128 a[176:179], v209\n"
"v_mfma_f32_16x16x32_bf16 v[32:35], a[116:119], a[188:191], v[32:35]\n"
"ds_read_b128 a[180:183], v209 offset:64\n"
"v_mfma_f32_16x16x32_bf16 v[36:39], a[120:123], a[184:187], v[36:39]\n"
"v_mfma_f32_16x16x32_bf16 v[36:39], a[124:127], a[188:191], v[36:39]\n"
"s_add_u32 m0, s49, 77248\n"
"v_mfma_f32_16x16x32_bf16 v[40:43], a[128:131], a[184:187], v[40:43]\n"
"buffer_load_dwordx4 v215, s[32:35], s52 offen lds\n"
"s_add_u32 s52, s52, 262144\n"
"v_mfma_f32_16x16x32_bf16 v[40:43], a[132:135], a[188:191], v[40:43]\n"
"v_mfma_f32_16x16x32_bf16 v[44:47], a[136:139], a[184:187], v[44:47]\n"
"v_mfma_f32_16x16x32_bf16 v[44:47], a[140:143], a[188:191], v[44:47]\n"
"v_mfma_f32_16x16x32_bf16 v[64:67], a[112:115], a[192:195], v[64:67]\n"
"ds_read_b128 a[184:187], v209 offset:2176\n"
"v_mfma_f32_16x16x32_bf16 v[64:67], a[116:119], a[196:199], v[64:67]\n"
"ds_read_b128 a[188:191], v209 offset:2240\n"
"s_add_u32 m0, s48, 28288\n"
"v_mfma_f32_16x16x32_bf16 v[68:71], a[120:123], a[192:195], v[68:71]\n"
"buffer_load_dwordx4 v216, s[28:31], s51 offen lds\n"
"v_mfma_f32_16x16x32_bf16 v[68:71], a[124:127], a[196:199], v[68:71]\n"
"v_mfma_f32_16x16x32_bf16 v[72:75], a[128:131], a[192:195], v[72:75]\n"
"v_mfma_f32_16x16x32_bf16 v[72:75], a[132:135], a[196:199], v[72:75]\n"
"s_waitcnt vmcnt(18)\n"
"s_barrier\n"
"v_mfma_f32_16x16x32_bf16 v[76:79], a[136:139], a[192:195], v[76:79]\n"
"ds_read_b64_tr_b16 a[144:145], v210 offset:34944\n"
"v_mfma_f32_16x16x32_bf16 v[76:79], a[140:143], a[196:199], v[76:79]\n"
"s_add_u32 m0, s48, 29376\n"
"ds_read_b64_tr_b16 a[146:147], v210 offset:39296\n"
"v_mfma_f32_16x16x32_bf16 v[96:99], a[112:115], a[200:203], v[96:99]\n"
"ds_read_b128 a[192:195], v209 offset:4352\n"
"buffer_load_dwordx4 v217, s[28:31], s51 offen lds\n"
"ds_read_b64_tr_b16 a[148:149], v210 offset:52352\n"
"v_mfma_f32_16x16x32_bf16 v[96:99], a[116:119], a[204:207], v[96:99]\n"
"ds_read_b128 a[196:199], v209 offset:4416\n"
"ds_read_b64_tr_b16 a[150:151], v210 offset:56704\n"
"v_mfma_f32_16x16x32_bf16 v[100:103], a[120:123], a[200:203], v[100:103]\n"
"ds_read_b64_tr_b16 a[152:153], v211 offset:34944\n"
"v_mfma_f32_16x16x32_bf16 v[100:103], a[124:127], a[204:207], v[100:103]\n"
"ds_read_b64_tr_b16 a[154:155], v211 offset:39296\n"
"v_mfma_f32_16x16x32_bf16 v[104:107], a[128:131], a[200:203], v[104:107]\n"
"ds_read_b64_tr_b16 a[156:157], v211 offset:52352\n"
"v_mfma_f32_16x16x32_bf16 v[104:107], a[132:135], a[204:207], v[104:107]\n"
"s_add_u32 m0, s48, 30464\n"
"ds_read_b64_tr_b16 a[158:159], v211 offset:56704\n"
"v_mfma_f32_16x16x32_bf16 v[108:111], a[136:139], a[200:203], v[108:111]\n"
"buffer_load_dwordx4 v218, s[28:31], s51 offen lds\n"
"ds_read_b64_tr_b16 a[160:161], v210 offset:35008\n"
"v_mfma_f32_16x16x32_bf16 v[108:111], a[140:143], a[204:207], v[108:111]\n"
"ds_read_b64_tr_b16 a[162:163], v210 offset:39360\n"
"v_mfma_f32_16x16x32_bf16 v[128:131], a[112:115], a[208:211], v[128:131]\n"
"ds_read_b128 a[200:203], v209 offset:6528\n"
"ds_read_b64_tr_b16 a[164:165], v210 offset:52416\n"
"v_mfma_f32_16x16x32_bf16 v[128:131], a[116:119], a[212:215], v[128:131]\n"
"ds_read_b128 a[204:207], v209 offset:6592\n"
"ds_read_b64_tr_b16 a[166:167], v210 offset:56768\n"
"v_mfma_f32_16x16x32_bf16 v[132:135], a[120:123], a[208:211], v[132:135]\n"
"ds_read_b64_tr_b16 a[168:169], v211 offset:35008\n"
"v_mfma_f32_16x16x32_bf16 v[132:135], a[124:127], a[212:215], v[132:135]\n"
"s_add_u32 m0, s48, 31552\n"
"ds_read_b64_tr_b16 a[170:171], v211 offset:39360\n"
"v_mfma_f32_16x16x32_bf16 v[136:139], a[128:131], a[208:211], v[136:139]\n"
"buffer_load_dwordx4 v219, s[28:31], s51 offen lds\n"
"ds_read_b64_tr_b16 a[172:173], v211 offset:52416\n"
"v_mfma_f32_16x16x32_bf16 v[136:139], a[132:135], a[212:215], v[136:139]\n"
"ds_read_b64_tr_b16 a[174:175], v211 offset:56768\n"
"v_mfma_f32_16x16x32_bf16 v[140:143], a[136:139], a[208:211], v[140:143]\n"
"v_mfma_f32_16x16x32_bf16 v[140:143], a[140:143], a[212:215], v[140:143]\n"
"v_mfma_f32_16x16x32_bf16 v[160:163], a[112:115], a[216:219], v[160:163]\n"
"ds_read_b128 a[208:211], v209 offset:8704\n"
"v_mfma_f32_16x16x32_bf16 v[160:163], a[116:119], a[220:223], v[160:163]\n"
"ds_read_b128 a[212:215], v209 offset:8768\n"
"s_add_u32 m0, s48, 32640\n"
"v_mfma_f32_16x16x32_bf16 v[164:167], a[120:123], a[216:219], v[164:167]\n"
"buffer_load_dwordx4 v220, s[28:31], s51 offen lds\n"
"v_mfma_f32_16x16x32_bf16 v[164:167], a[124:127], a[220:223], v[164:167]\n"
"v_mfma_f32_16x16x32_bf16 v[168:171], a[128:131], a[216:219], v[168:171]\n"
"v_mfma_f32_16x16x32_bf16 v[168:171], a[132:135], a[220:223], v[168:171]\n"
"v_mfma_f32_16x16x32_bf16 v[172:175], a[136:139], a[216:219], v[172:175]\n"
"s_waitcnt lgkmcnt(2)\n"
"v_mfma_f32_16x16x32_bf16 v[172:175], a[140:143], a[220:223], v[172:175]\n"
"v_mfma_f32_16x16x32_bf16 v[16:19], a[144:147], a[176:179], v[16:19]\n"
"ds_read_b128 a[216:219], v209 offset:10880\n"
"s_add_u32 m0, s48, 33728\n"
"v_mfma_f32_16x16x32_bf16 v[16:19], a[148:151], a[180:183], v[16:19]\n"
"ds_read_b128 a[220:223], v209 offset:10944\n"
"buffer_load_dwordx4 v221, s[28:31], s51 offen lds\n"
"v_mfma_f32_16x16x32_bf16 v[20:23], a[152:155], a[176:179], v[20:23]\n"
"ds_read_b64_tr_b16 a[112:113], v210 offset:34816\n"
"v_mfma_f32_16x16x32_bf16 v[20:23], a[156:159], a[180:183], v[20:23]\n"
"ds_read_b64_tr_b16 a[114:115], v210 offset:39168\n"
"v_mfma_f32_16x16x32_bf16 v[24:27], a[160:163], a[176:179], v[24:27]\n"
"ds_read_b64_tr_b16 a[116:117], v210 offset:52224\n"
"v_mfma_f32_16x16x32_bf16 v[24:27], a[164:167], a[180:183], v[24:27]\n"
"ds_read_b64_tr_b16 a[118:119], v210 offset:56576\n"
"s_add_u32 m0, s48, 34816\n"
"v_mfma_f32_16x16x32_bf16 v[28:31], a[168:171], a[176:179], v[28:31]\n"
"ds_read_b64_tr_b16 a[120:121], v211 offset:34816\n"
"buffer_load_dwordx4 v222, s[28:31], s51 offen lds\n"
"s_add_u32 s51, s51, 128\n"
"v_mfma_f32_16x16x32_bf16 v[28:31], a[172:175], a[180:183], v[28:31]\n"
"ds_read_b64_tr_b16 a[122:123], v211 offset:39168\n"
"v_mfma_f32_16x16x32_bf16 v[48:51], a[144:147], a[184:187], v[48:51]\n"
"ds_read_b64_tr_b16 a[124:125], v211 offset:52224\n"
"v_mfma_f32_16x16x32_bf16 v[48:51], a[148:151], a[188:191], v[48:51]\n"
"ds_read_b64_tr_b16 a[126:127], v211 offset:56576\n"
"v_mfma_f32_16x16x32_bf16 v[52:55], a[152:155], a[184:187], v[52:55]\n"
"ds_read_b64_tr_b16 a[128:129], v210 offset:34880\n"
"v_mfma_f32_16x16x32_bf16 v[52:55], a[156:159], a[188:191], v[52:55]\n"
"ds_read_b64_tr_b16 a[130:131], v210 offset:39232\n"
"v_mfma_f32_16x16x32_bf16 v[56:59], a[160:163], a[184:187], v[56:59]\n"
"ds_read_b64_tr_b16 a[132:133], v210 offset:52288\n"
"buffer_load_dwordx4 a[16:19], v250, s[36:39], s54 offen nt\n"
"v_mfma_f32_16x16x32_bf16 v[56:59], a[164:167], a[188:191], v[56:59]\n"
"ds_read_b64_tr_b16 a[134:135], v210 offset:56640\n"
"v_mfma_f32_16x16x32_bf16 v[60:63], a[168:171], a[184:187], v[60:63]\n"
"ds_read_b64_tr_b16 a[136:137], v211 offset:34880\n"
"v_mfma_f32_16x16x32_bf16 v[60:63], a[172:175], a[188:191], v[60:63]\n"
"ds_read_b64_tr_b16 a[138:139], v211 offset:39232\n"
"v_mfma_f32_16x16x32_bf16 v[80:83], a[144:147], a[192:195], v[80:83]\n"
"ds_read_b64_tr_b16 a[140:141], v211 offset:52288\n"
"v_mfma_f32_16x16x32_bf16 v[80:83], a[148:151], a[196:199], v[80:83]\n"
"ds_read_b64_tr_b16 a[142:143], v211 offset:56640\n"
"v_mfma_f32_16x16x32_bf16 v[84:87], a[152:155], a[192:195], v[84:87]\n"
"buffer_load_dwordx4 a[20:23], v250, s[36:39], s54 offen offset:1024 nt\n"
"v_mfma_f32_16x16x32_bf16 v[84:87], a[156:159], a[196:199], v[84:87]\n"
"v_mfma_f32_16x16x32_bf16 v[88:91], a[160:163], a[192:195], v[88:91]\n"
"v_mfma_f32_16x16x32_bf16 v[88:91], a[164:167], a[196:199], v[88:91]\n"
"v_mfma_f32_16x16x32_bf16 v[92:95], a[168:171], a[192:195], v[92:95]\n"
"v_mfma_f32_16x16x32_bf16 v[92:95], a[172:175], a[196:199], v[92:95]\n"
"v_mfma_f32_16x16x32_bf16 v[112:115], a[144:147], a[200:203], v[112:115]\n"
"s_waitcnt lgkmcnt(0)\n"
"s_waitcnt vmcnt(19)\n"
"s_barrier\n"
"v_mfma_f32_16x16x32_bf16 v[112:115], a[148:151], a[204:207], v[112:115]\n"
"s_add_u32 m0, s49, 91392\n"
"v_mfma_f32_16x16x32_bf16 v[116:119], a[152:155], a[200:203], v[116:119]\n"
"buffer_load_dwordx4 v212, s[32:35], s52 offen lds\n"
"v_mfma_f32_16x16x32_bf16 v[116:119], a[156:159], a[204:207], v[116:119]\n"
"v_mfma_f32_16x16x32_bf16 v[120:123], a[160:163], a[200:203], v[120:123]\n"
"v_mfma_f32_16x16x32_bf16 v[120:123], a[164:167], a[204:207], v[120:123]\n"
"v_mfma_f32_16x16x32_bf16 v[124:127], a[168:171], a[200:203], v[124:127]\n"
"v_mfma_f32_16x16x32_bf16 v[124:127], a[172:175], a[204:207], v[124:127]\n"
"s_add_u32 m0, s49, 92480\n"
"v_mfma_f32_16x16x32_bf16 v[144:147], a[144:147], a[208:211], v[144:147]\n"
"buffer_load_dwordx4 v213, s[32:35], s52 offen lds\n"
"v_mfma_f32_16x16x32_bf16 v[144:147], a[148:151], a[212:215], v[144:147]\n"
"v_mfma_f32_16x16x32_bf16 v[148:151], a[152:155], a[208:211], v[148:151]\n"
"v_mfma_f32_16x16x32_bf16 v[148:151], a[156:159], a[212:215], v[148:151]\n"
"v_mfma_f32_16x16x32_bf16 v[152:155], a[160:163], a[208:211], v[152:155]\n"
"v_mfma_f32_16x16x32_bf16 v[152:155], a[164:167], a[212:215], v[152:155]\n"
"s_add_u32 m0, s49, 93568\n"
"v_mfma_f32_16x16x32_bf16 v[156:159], a[168:171], a[208:211], v[156:159]\n"
"buffer_load_dwordx4 v214, s[32:35], s52 offen lds\n"
"v_mfma_f32_16x16x32_bf16 v[156:159], a[172:175], a[212:215], v[156:159]\n"
"v_mfma_f32_16x16x32_bf16 v[176:179], a[144:147], a[216:219], v[176:179]\n"
"v_mfma_f32_16x16x32_bf16 v[176:179], a[148:151], a[220:223], v[176:179]\n"
"v_mfma_f32_16x16x32_bf16 v[180:183], a[152:155], a[216:219], v[180:183]\n"
"v_mfma_f32_16x16x32_bf16 v[180:183], a[156:159], a[220:223], v[180:183]\n"
"s_add_u32 m0, s49, 94656\n"
"v_mfma_f32_16x16x32_bf16 v[184:187], a[160:163], a[216:219], v[184:187]\n"
"buffer_load_dwordx4 v215, s[32:35], s52 offen lds\n"
"s_add_u32 s52, s52, 262144\n"
"v_mfma_f32_16x16x32_bf16 v[184:187], a[164:167], a[220:223], v[184:187]\n"
"v_mfma_f32_16x16x32_bf16 v[188:191], a[168:171], a[216:219], v[188:191]\n"
"v_mfma_f32_16x16x32_bf16 v[188:191], a[172:175], a[220:223], v[188:191]\n"
"v_mfma_f32_16x16x32_bf16 v[192:195], a[112:115], a[224:227], v[192:195]\n"
"v_mfma_f32_16x16x32_bf16 v[192:195], a[116:119], a[228:231], v[192:195]\n"
"s_add_u32 m0, s49, 108800\n"
"v_mfma_f32_16x16x32_bf16 v[196:199], a[120:123], a[224:227], v[196:199]\n"
"buffer_load_dwordx4 v212, s[32:35], s52 offen lds\n"
"v_mfma_f32_16x16x32_bf16 v[196:199], a[124:127], a[228:231], v[196:199]\n"
"v_mfma_f32_16x16x32_bf16 v[200:203], a[128:131], a[224:227], v[200:203]\n"
"v_mfma_f32_16x16x32_bf16 v[200:203], a[132:135], a[228:231], v[200:203]\n"
"v_mfma_f32_16x16x32_bf16 v[204:207], a[136:139], a[224:227], v[204:207]\n"
"v_mfma_f32_16x16x32_bf16 v[204:207], a[140:143], a[228:231], v[204:207]\n"
"s_add_u32 m0, s49, 109888\n"
"v_mfma_f32_16x16x32_bf16 v[0:3], a[112:115], a[176:179], v[0:3]\n"
"ds_read_b128 a[224:227], v208 offset:26112\n"
"buffer_load_dwordx4 v213, s[32:35], s52 offen lds\n"
"v_mfma_f32_16x16x32_bf16 v[0:3], a[116:119], a[180:183], v[0:3]\n"
"ds_read_b128 a[228:231], v208 offset:26176\n"
"v_mfma_f32_16x16x32_bf16 v[4:7], a[120:123], a[176:179], v[4:7]\n"
"v_mfma_f32_16x16x32_bf16 v[4:7], a[124:127], a[180:183], v[4:7]\n"
"v_mfma_f32_16x16x32_bf16 v[8:11], a[128:131], a[176:179], v[8:11]\n"
"v_mfma_f32_16x16x32_bf16 v[8:11], a[132:135], a[180:183], v[8:11]\n"
"s_add_u32 m0, s49, 110976\n"
"v_mfma_f32_16x16x32_bf16 v[12:15], a[136:139], a[176:179], v[12:15]\n"
"buffer_load_dwordx4 v214, s[32:35], s52 offen lds\n"
"v_mfma_f32_16x16x32_bf16 v[12:15], a[140:143], a[180:183], v[12:15]\n"
"v_mfma_f32_16x16x32_bf16 v[32:35], a[112:115], a[184:187], v[32:35]\n"
"ds_read_b128 a[176:179], v208\n"
"v_mfma_f32_16x16x32_bf16 v[32:35], a[116:119], a[188:191], v[32:35]\n"
"ds_read_b128 a[180:183], v208 offset:64\n"
"v_mfma_f32_16x16x32_bf16 v[36:39], a[120:123], a[184:187], v[36:39]\n"
"v_mfma_f32_16x16x32_bf16 v[36:39], a[124:127], a[188:191], v[36:39]\n"
"s_add_u32 m0, s49, 112064\n"
"v_mfma_f32_16x16x32_bf16 v[40:43], a[128:131], a[184:187], v[40:43]\n"
"buffer_load_dwordx4 v215, s[32:35], s52 offen lds\n"
"s_add_u32 s52, s52, 262144\n"
"v_mfma_f32_16x16x32_bf16 v[40:43], a[132:135], a[188:191], v[40:43]\n"
"v_mfma_f32_16x16x32_bf16 v[44:47], a[136:139], a[184:187], v[44:47]\n"
"v_mfma_f32_16x16x32_bf16 v[44:47], a[140:143], a[188:191], v[44:47]\n"
"v_mfma_f32_16x16x32_bf16 v[64:67], a[112:115], a[192:195], v[64:67]\n"
"ds_read_b128 a[184:187], v208 offset:2176\n"
"v_mfma_f32_16x16x32_bf16 v[64:67], a[116:119], a[196:199], v[64:67]\n"
"ds_read_b128 a[188:191], v208 offset:2240\n"
"s_add_u32 m0, s48, 126208\n"
"v_mfma_f32_16x16x32_bf16 v[68:71], a[120:123], a[192:195], v[68:71]\n"
"buffer_load_dwordx4 v216, s[28:31], s51 offen lds\n"
"v_mfma_f32_16x16x32_bf16 v[68:71], a[124:127], a[196:199], v[68:71]\n"
"v_mfma_f32_16x16x32_bf16 v[72:75], a[128:131], a[192:195], v[72:75]\n"
"v_mfma_f32_16x16x32_bf16 v[72:75], a[132:135], a[196:199], v[72:75]\n"
"s_waitcnt vmcnt(18)\n"
"s_barrier\n"
"v_mfma_f32_16x16x32_bf16 v[76:79], a[136:139], a[192:195], v[76:79]\n"
"ds_read_b64_tr_b16 a[144:145], v210 offset:128\n"
"v_mfma_f32_16x16x32_bf16 v[76:79], a[140:143], a[196:199], v[76:79]\n"
"s_add_u32 m0, s48, 127296\n"
"ds_read_b64_tr_b16 a[146:147], v210 offset:4480\n"
"v_mfma_f32_16x16x32_bf16 v[96:99], a[112:115], a[200:203], v[96:99]\n"
"ds_read_b128 a[192:195], v208 offset:4352\n"
"buffer_load_dwordx4 v217, s[28:31], s51 offen lds\n"
"ds_read_b64_tr_b16 a[148:149], v210 offset:17536\n"
"v_mfma_f32_16x16x32_bf16 v[96:99], a[116:119], a[204:207], v[96:99]\n"
"ds_read_b128 a[196:199], v208 offset:4416\n"
"ds_read_b64_tr_b16 a[150:151], v210 offset:21888\n"
"v_mfma_f32_16x16x32_bf16 v[100:103], a[120:123], a[200:203], v[100:103]\n"
"ds_read_b64_tr_b16 a[152:153], v211 offset:128\n"
"v_mfma_f32_16x16x32_bf16 v[100:103], a[124:127], a[204:207], v[100:103]\n"
"ds_read_b64_tr_b16 a[154:155], v211 offset:4480\n"
"v_mfma_f32_16x16x32_bf16 v[104:107], a[128:131], a[200:203], v[104:107]\n"
"ds_read_b64_tr_b16 a[156:157], v211 offset:17536\n"
"v_mfma_f32_16x16x32_bf16 v[104:107], a[132:135], a[204:207], v[104:107]\n"
"s_add_u32 m0, s48, 128384\n"
"ds_read_b64_tr_b16 a[158:159], v211 offset:21888\n"
"v_mfma_f32_16x16x32_bf16 v[108:111], a[136:139], a[200:203], v[108:111]\n"
"buffer_load_dwordx4 v218, s[28:31], s51 offen lds\n"
"ds_read_b64_tr_b16 a[160:161], v210 offset:192\n"
"v_mfma_f32_16x16x32_bf16 v[108:111], a[140:143], a[204:207], v[108:111]\n"
"ds_read_b64_tr_b16 a[162:163], v210 offset:4544\n"
"v_mfma_f32_16x16x32_bf16 v[128:131], a[112:115], a[208:211], v[128:131]\n"
"ds_read_b128 a[200:203], v208 offset:6528\n"
"ds_read_b64_tr_b16 a[164:165], v210 offset:17600\n"
"v_mfma_f32_16x16x32_bf16 v[128:131], a[116:119], a[212:215], v[128:131]\n"
"ds_read_b128 a[204:207], v208 offset:6592\n"
"ds_read_b64_tr_b16 a[166:167], v210 offset:21952\n"
"v_mfma_f32_16x16x32_bf16 v[132:135], a[120:123], a[208:211], v[132:135]\n"
"ds_read_b64_tr_b16 a[168:169], v211 offset:192\n"
"v_mfma_f32_16x16x32_bf16 v[132:135], a[124:127], a[212:215], v[132:135]\n"
"s_add_u32 m0, s48, 129472\n"
"ds_read_b64_tr_b16 a[170:171], v211 offset:4544\n"
"v_mfma_f32_16x16x32_bf16 v[136:139], a[128:131], a[208:211], v[136:139]\n"
"buffer_load_dwordx4 v219, s[28:31], s51 offen lds\n"
"ds_read_b64_tr_b16 a[172:173], v211 offset:17600\n"
"v_mfma_f32_16x16x32_bf16 v[136:139], a[132:135], a[212:215], v[136:139]\n"
"ds_read_b64_tr_b16 a[174:175], v211 offset:21952\n"
"v_mfma_f32_16x16x32_bf16 v[140:143], a[136:139], a[208:211], v[140:143]\n"
"v_mfma_f32_16x16x32_bf16 v[140:143], a[140:143], a[212:215], v[140:143]\n"
"v_mfma_f32_16x16x32_bf16 v[160:163], a[112:115], a[216:219], v[160:163]\n"
"ds_read_b128 a[208:211], v208 offset:8704\n"
"v_mfma_f32_16x16x32_bf16 v[160:163], a[116:119], a[220:223], v[160:163]\n"
"ds_read_b128 a[212:215], v208 offset:8768\n"
"s_add_u32 m0, s48, 130560\n"
"v_mfma_f32_16x16x32_bf16 v[164:167], a[120:123], a[216:219], v[164:167]\n"
"buffer_load_dwordx4 v220, s[28:31], s51 offen lds\n"
"v_mfma_f32_16x16x32_bf16 v[164:167], a[124:127], a[220:223], v[164:167]\n"
"v_mfma_f32_16x16x32_bf16 v[168:171], a[128:131], a[216:219], v[168:171]\n"
"v_mfma_f32_16x16x32_bf16 v[168:171], a[132:135], a[220:223], v[168:171]\n"
"v_mfma_f32_16x16x32_bf16 v[172:175], a[136:139], a[216:219], v[172:175]\n"
"s_waitcnt lgkmcnt(2)\n"
"v_mfma_f32_16x16x32_bf16 v[172:175], a[140:143], a[220:223], v[172:175]\n"
"v_mfma_f32_16x16x32_bf16 v[16:19], a[144:147], a[176:179], v[16:19]\n"
"ds_read_b128 a[216:219], v208 offset:10880\n"
"s_add_u32 m0, s48, 131648\n"
"v_mfma_f32_16x16x32_bf16 v[16:19], a[148:151], a[180:183], v[16:19]\n"
"ds_read_b128 a[220:223], v208 offset:10944\n"
"buffer_load_dwordx4 v221, s[28:31], s51 offen lds\n"
"v_mfma_f32_16x16x32_bf16 v[20:23], a[152:155], a[176:179], v[20:23]\n"
"ds_read_b64_tr_b16 a[112:113], v210\n"
"v_mfma_f32_16x16x32_bf16 v[20:23], a[156:159], a[180:183], v[20:23]\n"
"ds_read_b64_tr_b16 a[114:115], v210 offset:4352\n"
"v_mfma_f32_16x16x32_bf16 v[24:27], a[160:163], a[176:179], v[24:27]\n"
"ds_read_b64_tr_b16 a[116:117], v210 offset:17408\n"
"v_mfma_f32_16x16x32_bf16 v[24:27], a[164:167], a[180:183], v[24:27]\n"
"ds_read_b64_tr_b16 a[118:119], v210 offset:21760\n"
"s_add_u32 m0, s48, 132736\n"
"v_mfma_f32_16x16x32_bf16 v[28:31], a[168:171], a[176:179], v[28:31]\n"
"ds_read_b64_tr_b16 a[120:121], v211\n"
"buffer_load_dwordx4 v222, s[28:31], s51 offen lds\n"
"s_add_u32 s51, s51, 128\n"
"v_mfma_f32_16x16x32_bf16 v[28:31], a[172:175], a[180:183], v[28:31]\n"
"ds_read_b64_tr_b16 a[122:123], v211 offset:4352\n"
"v_mfma_f32_16x16x32_bf16 v[48:51], a[144:147], a[184:187], v[48:51]\n"
"ds_read_b64_tr_b16 a[124:125], v211 offset:17408\n"
"v_mfma_f32_16x16x32_bf16 v[48:51], a[148:151], a[188:191], v[48:51]\n"
"ds_read_b64_tr_b16 a[126:127], v211 offset:21760\n"
"v_mfma_f32_16x16x32_bf16 v[52:55], a[152:155], a[184:187], v[52:55]\n"
"ds_read_b64_tr_b16 a[128:129], v210 offset:64\n"
"v_mfma_f32_16x16x32_bf16 v[52:55], a[156:159], a[188:191], v[52:55]\n"
"ds_read_b64_tr_b16 a[130:131], v210 offset:4416\n"
"v_mfma_f32_16x16x32_bf16 v[56:59], a[160:163], a[184:187], v[56:59]\n"
"ds_read_b64_tr_b16 a[132:133], v210 offset:17472\n"
"buffer_load_dwordx4 a[24:27], v250, s[36:39], s54 offen offset:2048 nt\n"
"v_mfma_f32_16x16x32_bf16 v[56:59], a[164:167], a[188:191], v[56:59]\n"
"ds_read_b64_tr_b16 a[134:135], v210 offset:21824\n"
"v_mfma_f32_16x16x32_bf16 v[60:63], a[168:171], a[184:187], v[60:63]\n"
"ds_read_b64_tr_b16 a[136:137], v211 offset:64\n"
"v_mfma_f32_16x16x32_bf16 v[60:63], a[172:175], a[188:191], v[60:63]\n"
"ds_read_b64_tr_b16 a[138:139], v211 offset:4416\n"
"v_mfma_f32_16x16x32_bf16 v[80:83], a[144:147], a[192:195], v[80:83]\n"
"ds_read_b64_tr_b16 a[140:141], v211 offset:17472\n"
"v_mfma_f32_16x16x32_bf16 v[80:83], a[148:151], a[196:199], v[80:83]\n"
"ds_read_b64_tr_b16 a[142:143], v211 offset:21824\n"
"v_mfma_f32_16x16x32_bf16 v[84:87], a[152:155], a[192:195], v[84:87]\n"
"buffer_load_dwordx4 a[28:31], v250, s[36:39], s54 offen offset:3072 nt\n"
"s_add_u32 s53, s53, 8192\n"
"s_and_b32 s53, s53, 258047\n"
"s_lshl_b32 s38, s38, 1\n"
"s_add_u32 s54, s53, 4096\n"
"v_mfma_f32_16x16x32_bf16 v[84:87], a[156:159], a[196:199], v[84:87]\n"
"v_mfma_f32_16x16x32_bf16 v[88:91], a[160:163], a[192:195], v[88:91]\n"
"v_mfma_f32_16x16x32_bf16 v[88:91], a[164:167], a[196:199], v[88:91]\n"
"v_mfma_f32_16x16x32_bf16 v[92:95], a[168:171], a[192:195], v[92:95]\n"
"v_mfma_f32_16x16x32_bf16 v[92:95], a[172:175], a[196:199], v[92:95]\n"
"v_mfma_f32_16x16x32_bf16 v[112:115], a[144:147], a[200:203], v[112:115]\n"
"s_waitcnt lgkmcnt(0)\n"
"s_waitcnt vmcnt(19)\n"
"s_barrier\n"
"v_mfma_f32_16x16x32_bf16 v[112:115], a[148:151], a[204:207], v[112:115]\n"
"s_add_u32 m0, s49, 56576\n"
"v_mfma_f32_16x16x32_bf16 v[116:119], a[152:155], a[200:203], v[116:119]\n"
"buffer_load_dwordx4 v212, s[32:35], s52 offen lds\n"
"v_mfma_f32_16x16x32_bf16 v[116:119], a[156:159], a[204:207], v[116:119]\n"
"v_mfma_f32_16x16x32_bf16 v[120:123], a[160:163], a[200:203], v[120:123]\n"
"v_mfma_f32_16x16x32_bf16 v[120:123], a[164:167], a[204:207], v[120:123]\n"
"v_mfma_f32_16x16x32_bf16 v[124:127], a[168:171], a[200:203], v[124:127]\n"
"v_mfma_f32_16x16x32_bf16 v[124:127], a[172:175], a[204:207], v[124:127]\n"
"s_add_u32 m0, s49, 57664\n"
"v_mfma_f32_16x16x32_bf16 v[144:147], a[144:147], a[208:211], v[144:147]\n"
"buffer_load_dwordx4 v213, s[32:35], s52 offen lds\n"
"v_mfma_f32_16x16x32_bf16 v[144:147], a[148:151], a[212:215], v[144:147]\n"
"v_mfma_f32_16x16x32_bf16 v[148:151], a[152:155], a[208:211], v[148:151]\n"
"v_mfma_f32_16x16x32_bf16 v[148:151], a[156:159], a[212:215], v[148:151]\n"
"v_mfma_f32_16x16x32_bf16 v[152:155], a[160:163], a[208:211], v[152:155]\n"
"v_mfma_f32_16x16x32_bf16 v[152:155], a[164:167], a[212:215], v[152:155]\n"
"s_add_u32 m0, s49, 58752\n"
"v_mfma_f32_16x16x32_bf16 v[156:159], a[168:171], a[208:211], v[156:159]\n"
"buffer_load_dwordx4 v214, s[32:35], s52 offen lds\n"
"v_mfma_f32_16x16x32_bf16 v[156:159], a[172:175], a[212:215], v[156:159]\n"
"v_mfma_f32_16x16x32_bf16 v[176:179], a[144:147], a[216:219], v[176:179]\n"
"v_mfma_f32_16x16x32_bf16 v[176:179], a[148:151], a[220:223], v[176:179]\n"
"v_mfma_f32_16x16x32_bf16 v[180:183], a[152:155], a[216:219], v[180:183]\n"
"v_mfma_f32_16x16x32_bf16 v[180:183], a[156:159], a[220:223], v[180:183]\n"
"s_add_u32 m0, s49, 59840\n"
"v_mfma_f32_16x16x32_bf16 v[184:187], a[160:163], a[216:219], v[184:187]\n"
"buffer_load_dwordx4 v215, s[32:35], s52 offen lds\n"
"s_add_u32 s52, s52, 262144\n"
"v_mfma_f32_16x16x32_bf16 v[184:187], a[164:167], a[220:223], v[184:187]\n"
"v_mfma_f32_16x16x32_bf16 v[188:191], a[168:171], a[216:219], v[188:191]\n"
"v_mfma_f32_16x16x32_bf16 v[188:191], a[172:175], a[220:223], v[188:191]\n"
"v_mfma_f32_16x16x32_bf16 v[192:195], a[112:115], a[224:227], v[192:195]\n"
"v_mfma_f32_16x16x32_bf16 v[192:195], a[116:119], a[228:231], v[192:195]\n"
"s_add_u32 m0, s49, 73984\n"
"v_mfma_f32_16x16x32_bf16 v[196:199], a[120:123], a[224:227], v[196:199]\n"
"buffer_load_dwordx4 v212, s[32:35], s52 offen lds\n"
"v_mfma_f32_16x16x32_bf16 v[196:199], a[124:127], a[228:231], v[196:199]\n"
"v_mfma_f32_16x16x32_bf16 v[200:203], a[128:131], a[224:227], v[200:203]\n"
"v_mfma_f32_16x16x32_bf16 v[200:203], a[132:135], a[228:231], v[200:203]\n"
"v_mfma_f32_16x16x32_bf16 v[204:207], a[136:139], a[224:227], v[204:207]\n"
"v_mfma_f32_16x16x32_bf16 v[204:207], a[140:143], a[228:231], v[204:207]\n"
"s_add_u32 m0, s49, 75072\n"
"v_mfma_f32_16x16x32_bf16 v[0:3], a[112:115], a[176:179], v[0:3]\n"
"ds_read_b128 a[224:227], v208 offset:54400\n"
"buffer_load_dwordx4 v213, s[32:35], s52 offen lds\n"
"v_mfma_f32_16x16x32_bf16 v[0:3], a[116:119], a[180:183], v[0:3]\n"
"ds_read_b128 a[228:231], v208 offset:54464\n"
"v_mfma_f32_16x16x32_bf16 v[4:7], a[120:123], a[176:179], v[4:7]\n"
"v_mfma_f32_16x16x32_bf16 v[4:7], a[124:127], a[180:183], v[4:7]\n"
"v_mfma_f32_16x16x32_bf16 v[8:11], a[128:131], a[176:179], v[8:11]\n"
"v_mfma_f32_16x16x32_bf16 v[8:11], a[132:135], a[180:183], v[8:11]\n"
"s_add_u32 m0, s49, 76160\n"
"v_mfma_f32_16x16x32_bf16 v[12:15], a[136:139], a[176:179], v[12:15]\n"
"buffer_load_dwordx4 v214, s[32:35], s52 offen lds\n"
"v_mfma_f32_16x16x32_bf16 v[12:15], a[140:143], a[180:183], v[12:15]\n"
"v_mfma_f32_16x16x32_bf16 v[32:35], a[112:115], a[184:187], v[32:35]\n"
"ds_read_b128 a[176:179], v208 offset:28288\n"
"v_mfma_f32_16x16x32_bf16 v[32:35], a[116:119], a[188:191], v[32:35]\n"
"ds_read_b128 a[180:183], v208 offset:28352\n"
"v_mfma_f32_16x16x32_bf16 v[36:39], a[120:123], a[184:187], v[36:39]\n"
"v_mfma_f32_16x16x32_bf16 v[36:39], a[124:127], a[188:191], v[36:39]\n"
"s_add_u32 m0, s49, 77248\n"
"v_mfma_f32_16x16x32_bf16 v[40:43], a[128:131], a[184:187], v[40:43]\n"
"buffer_load_dwordx4 v215, s[32:35], s52 offen lds\n"
"s_add_u32 s52, s52, 262144\n"
"v_mfma_f32_16x16x32_bf16 v[40:43], a[132:135], a[188:191], v[40:43]\n"
"v_mfma_f32_16x16x32_bf16 v[44:47], a[136:139], a[184:187], v[44:47]\n"
"v_mfma_f32_16x16x32_bf16 v[44:47], a[140:143], a[188:191], v[44:47]\n"
"v_mfma_f32_16x16x32_bf16 v[64:67], a[112:115], a[192:195], v[64:67]\n"
"ds_read_b128 a[184:187], v208 offset:30464\n"
"v_mfma_f32_16x16x32_bf16 v[64:67], a[116:119], a[196:199], v[64:67]\n"
"ds_read_b128 a[188:191], v208 offset:30528\n"
"s_add_u32 m0, s48, 0\n"
"v_mfma_f32_16x16x32_bf16 v[68:71], a[120:123], a[192:195], v[68:71]\n"
"buffer_load_dwordx4 v216, s[28:31], s51 offen lds\n"
"v_mfma_f32_16x16x32_bf16 v[68:71], a[124:127], a[196:199], v[68:71]\n"
"v_mfma_f32_16x16x32_bf16 v[72:75], a[128:131], a[192:195], v[72:75]\n"
"v_mfma_f32_16x16x32_bf16 v[72:75], a[132:135], a[196:199], v[72:75]\n"
"s_waitcnt vmcnt(18)\n"
"s_barrier\n"
"v_mfma_f32_16x16x32_bf16 v[76:79], a[136:139], a[192:195], v[76:79]\n"
"ds_read_b64_tr_b16 a[144:145], v210 offset:34944\n"
"v_mfma_f32_16x16x32_bf16 v[76:79], a[140:143], a[196:199], v[76:79]\n"
"s_add_u32 m0, s48, 1088\n"
"ds_read_b64_tr_b16 a[146:147], v210 offset:39296\n"
"v_mfma_f32_16x16x32_bf16 v[96:99], a[112:115], a[200:203], v[96:99]\n"
"ds_read_b128 a[192:195], v208 offset:32640\n"
"buffer_load_dwordx4 v217, s[28:31], s51 offen lds\n"
"ds_read_b64_tr_b16 a[148:149], v210 offset:52352\n"
"v_mfma_f32_16x16x32_bf16 v[96:99], a[116:119], a[204:207], v[96:99]\n"
"ds_read_b128 a[196:199], v208 offset:32704\n"
"ds_read_b64_tr_b16 a[150:151], v210 offset:56704\n"
"v_mfma_f32_16x16x32_bf16 v[100:103], a[120:123], a[200:203], v[100:103]\n"
"ds_read_b64_tr_b16 a[152:153], v211 offset:34944\n"
"v_mfma_f32_16x16x32_bf16 v[100:103], a[124:127], a[204:207], v[100:103]\n"
"ds_read_b64_tr_b16 a[154:155], v211 offset:39296\n"
"v_mfma_f32_16x16x32_bf16 v[104:107], a[128:131], a[200:203], v[104:107]\n"
"ds_read_b64_tr_b16 a[156:157], v211 offset:52352\n"
"v_mfma_f32_16x16x32_bf16 v[104:107], a[132:135], a[204:207], v[104:107]\n"
"s_add_u32 m0, s48, 2176\n"
"ds_read_b64_tr_b16 a[158:159], v211 offset:56704\n"
"v_mfma_f32_16x16x32_bf16 v[108:111], a[136:139], a[200:203], v[108:111]\n"
"buffer_load_dwordx4 v218, s[28:31], s51 offen lds\n"
"ds_read_b64_tr_b16 a[160:161], v210 offset:35008\n"
"v_mfma_f32_16x16x32_bf16 v[108:111], a[140:143], a[204:207], v[108:111]\n"
"ds_read_b64_tr_b16 a[162:163], v210 offset:39360\n"
"v_mfma_f32_16x16x32_bf16 v[128:131], a[112:115], a[208:211], v[128:131]\n"
"ds_read_b128 a[200:203], v208 offset:34816\n"
"ds_read_b64_tr_b16 a[164:165], v210 offset:52416\n"
"v_mfma_f32_16x16x32_bf16 v[128:131], a[116:119], a[212:215], v[128:131]\n"
"ds_read_b128 a[204:207], v208 offset:34880\n"
"ds_read_b64_tr_b16 a[166:167], v210 offset:56768\n"
"v_mfma_f32_16x16x32_bf16 v[132:135], a[120:123], a[208:211], v[132:135]\n"
"ds_read_b64_tr_b16 a[168:169], v211 offset:35008\n"
"v_mfma_f32_16x16x32_bf16 v[132:135], a[124:127], a[212:215], v[132:135]\n"
"s_add_u32 m0, s48, 3264\n"
"ds_read_b64_tr_b16 a[170:171], v211 offset:39360\n"
"v_mfma_f32_16x16x32_bf16 v[136:139], a[128:131], a[208:211], v[136:139]\n"
"buffer_load_dwordx4 v219, s[28:31], s51 offen lds\n"
"ds_read_b64_tr_b16 a[172:173], v211 offset:52416\n"
"v_mfma_f32_16x16x32_bf16 v[136:139], a[132:135], a[212:215], v[136:139]\n"
"ds_read_b64_tr_b16 a[174:175], v211 offset:56768\n"
"v_mfma_f32_16x16x32_bf16 v[140:143], a[136:139], a[208:211], v[140:143]\n"
"v_mfma_f32_16x16x32_bf16 v[140:143], a[140:143], a[212:215], v[140:143]\n"
"v_mfma_f32_16x16x32_bf16 v[160:163], a[112:115], a[216:219], v[160:163]\n"
"ds_read_b128 a[208:211], v208 offset:36992\n"
"v_mfma_f32_16x16x32_bf16 v[160:163], a[116:119], a[220:223], v[160:163]\n"
"ds_read_b128 a[212:215], v208 offset:37056\n"
"s_add_u32 m0, s48, 4352\n"
"v_mfma_f32_16x16x32_bf16 v[164:167], a[120:123], a[216:219], v[164:167]\n"
"buffer_load_dwordx4 v220, s[28:31], s51 offen lds\n"
"v_mfma_f32_16x16x32_bf16 v[164:167], a[124:127], a[220:223], v[164:167]\n"
"v_mfma_f32_16x16x32_bf16 v[168:171], a[128:131], a[216:219], v[168:171]\n"
"v_mfma_f32_16x16x32_bf16 v[168:171], a[132:135], a[220:223], v[168:171]\n"
"v_mfma_f32_16x16x32_bf16 v[172:175], a[136:139], a[216:219], v[172:175]\n"
"s_waitcnt lgkmcnt(2)\n"
"v_mfma_f32_16x16x32_bf16 v[172:175], a[140:143], a[220:223], v[172:175]\n"
"v_mfma_f32_16x16x32_bf16 v[16:19], a[144:147], a[176:179], v[16:19]\n"
"ds_read_b128 a[216:219], v208 offset:39168\n"
"s_add_u32 m0, s48, 5440\n"
"v_mfma_f32_16x16x32_bf16 v[16:19], a[148:151], a[180:183], v[16:19]\n"
"ds_read_b128 a[220:223], v208 offset:39232\n"
"buffer_load_dwordx4 v221, s[28:31], s51 offen lds\n"
"v_mfma_f32_16x16x32_bf16 v[20:23], a[152:155], a[176:179], v[20:23]\n"
"ds_read_b64_tr_b16 a[112:113], v210 offset:34816\n"
"v_mfma_f32_16x16x32_bf16 v[20:23], a[156:159], a[180:183], v[20:23]\n"
"ds_read_b64_tr_b16 a[114:115], v210 offset:39168\n"
"v_mfma_f32_16x16x32_bf16 v[24:27], a[160:163], a[176:179], v[24:27]\n"
"ds_read_b64_tr_b16 a[116:117], v210 offset:52224\n"
"v_mfma_f32_16x16x32_bf16 v[24:27], a[164:167], a[180:183], v[24:27]\n"
"ds_read_b64_tr_b16 a[118:119], v210 offset:56576\n"
"s_add_u32 m0, s48, 6528\n"
"v_mfma_f32_16x16x32_bf16 v[28:31], a[168:171], a[176:179], v[28:31]\n"
"ds_read_b64_tr_b16 a[120:121], v211 offset:34816\n"
"buffer_load_dwordx4 v222, s[28:31], s51 offen lds\n"
"s_add_u32 s51, s51, 128\n"
"v_mfma_f32_16x16x32_bf16 v[28:31], a[172:175], a[180:183], v[28:31]\n"
"ds_read_b64_tr_b16 a[122:123], v211 offset:39168\n"
"v_mfma_f32_16x16x32_bf16 v[48:51], a[144:147], a[184:187], v[48:51]\n"
"ds_read_b64_tr_b16 a[124:125], v211 offset:52224\n"
"v_mfma_f32_16x16x32_bf16 v[48:51], a[148:151], a[188:191], v[48:51]\n"
"ds_read_b64_tr_b16 a[126:127], v211 offset:56576\n"
"v_mfma_f32_16x16x32_bf16 v[52:55], a[152:155], a[184:187], v[52:55]\n"
"ds_read_b64_tr_b16 a[128:129], v210 offset:34880\n"
"v_mfma_f32_16x16x32_bf16 v[52:55], a[156:159], a[188:191], v[52:55]\n"
"ds_read_b64_tr_b16 a[130:131], v210 offset:39232\n"
"v_mfma_f32_16x16x32_bf16 v[56:59], a[160:163], a[184:187], v[56:59]\n"
"ds_read_b64_tr_b16 a[132:133], v210 offset:52288\n"
"buffer_load_dwordx4 a[32:35], v250, s[36:39], s53 offen nt\n"
"v_mfma_f32_16x16x32_bf16 v[56:59], a[164:167], a[188:191], v[56:59]\n"
"ds_read_b64_tr_b16 a[134:135], v210 offset:56640\n"
"v_mfma_f32_16x16x32_bf16 v[60:63], a[168:171], a[184:187], v[60:63]\n"
"ds_read_b64_tr_b16 a[136:137], v211 offset:34880\n"
"v_mfma_f32_16x16x32_bf16 v[60:63], a[172:175], a[188:191], v[60:63]\n"
"ds_read_b64_tr_b16 a[138:139], v211 offset:39232\n"
"v_mfma_f32_16x16x32_bf16 v[80:83], a[144:147], a[192:195], v[80:83]\n"
"ds_read_b64_tr_b16 a[140:141], v211 offset:52288\n"
"v_mfma_f32_16x16x32_bf16 v[80:83], a[148:151], a[196:199], v[80:83]\n"
"ds_read_b64_tr_b16 a[142:143], v211 offset:56640\n"
"v_mfma_f32_16x16x32_bf16 v[84:87], a[152:155], a[192:195], v[84:87]\n"
"buffer_load_dwordx4 a[36:39], v250, s[36:39], s53 offen offset:1024 nt\n"
"v_mfma_f32_16x16x32_bf16 v[84:87], a[156:159], a[196:199], v[84:87]\n"
"v_mfma_f32_16x16x32_bf16 v[88:91], a[160:163], a[192:195], v[88:91]\n"
"v_mfma_f32_16x16x32_bf16 v[88:91], a[164:167], a[196:199], v[88:91]\n"
"v_mfma_f32_16x16x32_bf16 v[92:95], a[168:171], a[192:195], v[92:95]\n"
"v_mfma_f32_16x16x32_bf16 v[92:95], a[172:175], a[196:199], v[92:95]\n"
"v_mfma_f32_16x16x32_bf16 v[112:115], a[144:147], a[200:203], v[112:115]\n"
"s_waitcnt lgkmcnt(0)\n"
"s_waitcnt vmcnt(19)\n"
"s_barrier\n"
"v_mfma_f32_16x16x32_bf16 v[112:115], a[148:151], a[204:207], v[112:115]\n"
"s_add_u32 m0, s49, 91392\n"
"v_mfma_f32_16x16x32_bf16 v[116:119], a[152:155], a[200:203], v[116:119]\n"
"buffer_load_dwordx4 v212, s[32:35], s52 offen lds\n"
"v_mfma_f32_16x16x32_bf16 v[116:119], a[156:159], a[204:207], v[116:119]\n"
"v_mfma_f32_16x16x32_bf16 v[120:123], a[160:163], a[200:203], v[120:123]\n"
"v_mfma_f32_16x16x32_bf16 v[120:123], a[164:167], a[204:207], v[120:123]\n"
"v_mfma_f32_16x16x32_bf16 v[124:127], a[168:171], a[200:203], v[124:127]\n"
"v_mfma_f32_16x16x32_bf16 v[124:127], a[172:175], a[204:207], v[124:127]\n"
"s_add_u32 m0, s49, 92480\n"
"v_mfma_f32_16x16x32_bf16 v[144:147], a[144:147], a[208:211], v[144:147]\n"
"buffer_load_dwordx4 v213, s[32:35], s52 offen lds\n"
"v_mfma_f32_16x16x32_bf16 v[144:147], a[148:151], a[212:215], v[144:147]\n"
"v_mfma_f32_16x16x32_bf16 v[148:151], a[152:155], a[208:211], v[148:151]\n"
"v_mfma_f32_16x16x32_bf16 v[148:151], a[156:159], a[212:215], v[148:151]\n"
"v_mfma_f32_16x16x32_bf16 v[152:155], a[160:163], a[208:211], v[152:155]\n"
"v_mfma_f32_16x16x32_bf16 v[152:155], a[164:167], a[212:215], v[152:155]\n"
"s_add_u32 m0, s49, 93568\n"
"v_mfma_f32_16x16x32_bf16 v[156:159], a[168:171], a[208:211], v[156:159]\n"
"buffer_load_dwordx4 v214, s[32:35], s52 offen lds\n"
"v_mfma_f32_16x16x32_bf16 v[156:159], a[172:175], a[212:215], v[156:159]\n"
"v_mfma_f32_16x16x32_bf16 v[176:179], a[144:147], a[216:219], v[176:179]\n"
"v_mfma_f32_16x16x32_bf16 v[176:179], a[148:151], a[220:223], v[176:179]\n"
"v_mfma_f32_16x16x32_bf16 v[180:183], a[152:155], a[216:219], v[180:183]\n"
"v_mfma_f32_16x16x32_bf16 v[180:183], a[156:159], a[220:223], v[180:183]\n"
"s_add_u32 m0, s49, 94656\n"
"v_mfma_f32_16x16x32_bf16 v[184:187], a[160:163], a[216:219], v[184:187]\n"
"buffer_load_dwordx4 v215, s[32:35], s52 offen lds\n"
"s_add_u32 s52, s52, 262144\n"
"v_mfma_f32_16x16x32_bf16 v[184:187], a[164:167], a[220:223], v[184:187]\n"
"v_mfma_f32_16x16x32_bf16 v[188:191], a[168:171], a[216:219], v[188:191]\n"
"v_mfma_f32_16x16x32_bf16 v[188:191], a[172:175], a[220:223], v[188:191]\n"
"v_mfma_f32_16x16x32_bf16 v[192:195], a[112:115], a[224:227], v[192:195]\n"
"v_mfma_f32_16x16x32_bf16 v[192:195], a[116:119], a[228:231], v[192:195]\n"
"s_add_u32 m0, s49, 108800\n"
"v_mfma_f32_16x16x32_bf16 v[196:199], a[120:123], a[224:227], v[196:199]\n"
"buffer_load_dwordx4 v212, s[32:35], s52 offen lds\n"
"v_mfma_f32_16x16x32_bf16 v[196:199], a[124:127], a[228:231], v[196:199]\n"
"v_mfma_f32_16x16x32_bf16 v[200:203], a[128:131], a[224:227], v[200:203]\n"
"v_mfma_f32_16x16x32_bf16 v[200:203], a[132:135], a[228:231], v[200:203]\n"
"v_mfma_f32_16x16x32_bf16 v[204:207], a[136:139], a[224:227], v[204:207]\n"
"v_mfma_f32_16x16x32_bf16 v[204:207], a[140:143], a[228:231], v[204:207]\n"
"s_add_u32 m0, s49, 109888\n"
"v_mfma_f32_16x16x32_bf16 v[0:3], a[112:115], a[176:179], v[0:3]\n"
"ds_read_b128 a[224:227], v209 offset:26112\n"
"buffer_load_dwordx4 v213, s[32:35], s52 offen lds\n"
"v_mfma_f32_16x16x32_bf16 v[0:3], a[116:119], a[180:183], v[0:3]\n"
"ds_read_b128 a[228:231], v209 offset:26176\n"
"v_mfma_f32_16x16x32_bf16 v[4:7], a[120:123], a[176:179], v[4:7]\n"
"v_mfma_f32_16x16x32_bf16 v[4:7], a[124:127], a[180:183], v[4:7]\n"
"v_mfma_f32_16x16x32_bf16 v[8:11], a[128:131], a[176:179], v[8:11]\n"
"v_mfma_f32_16x16x32_bf16 v[8:11], a[132:135], a[180:183], v[8:11]\n"
"s_add_u32 m0, s49, 110976\n"
"v_mfma_f32_16x16x32_bf16 v[12:15], a[136:139], a[176:179], v[12:15]\n"
"buffer_load_dwordx4 v214, s[32:35], s52 offen lds\n"
"v_mfma_f32_16x16x32_bf16 v[12:15], a[140:143], a[180:183], v[12:15]\n"
"v_mfma_f32_16x16x32_bf16 v[32:35], a[112:115], a[184:187], v[32:35]\n"
"ds_read_b128 a[176:179], v209\n"
"v_mfma_f32_16x16x32_bf16 v[32:35], a[116:119], a[188:191], v[32:35]\n"
"ds_read_b128 a[180:183], v209 offset:64\n"
"v_mfma_f32_16x16x32_bf16 v[36:39], a[120:123], a[184:187], v[36:39]\n"
"v_mfma_f32_16x16x32_bf16 v[36:39], a[124:127], a[188:191], v[36:39]\n"
"s_add_u32 m0, s49, 112064\n"
"v_mfma_f32_16x16x32_bf16 v[40:43], a[128:131], a[184:187], v[40:43]\n"
"buffer_load_dwordx4 v215, s[32:35], s52 offen lds\n"
"s_add_u32 s52, s52, 262144\n"
"v_mfma_f32_16x16x32_bf16 v[40:43], a[132:135], a[188:191], v[40:43]\n"
"v_mfma_f32_16x16x32_bf16 v[44:47], a[136:139], a[184:187], v[44:47]\n"
"v_mfma_f32_16x16x32_bf16 v[44:47], a[140:143], a[188:191], v[44:47]\n"
"v_mfma_f32_16x16x32_bf16 v[64:67], a[112:115], a[192:195], v[64:67]\n"
"ds_read_b128 a[184:187], v209 offset:2176\n"
"v_mfma_f32_16x16x32_bf16 v[64:67], a[116:119], a[196:199], v[64:67]\n"
"ds_read_b128 a[188:191], v209 offset:2240\n"
"s_add_u32 m0, s48, 28288\n"
"v_mfma_f32_16x16x32_bf16 v[68:71], a[120:123], a[192:195], v[68:71]\n"
"buffer_load_dwordx4 v216, s[28:31], s51 offen lds\n"
"v_mfma_f32_16x16x32_bf16 v[68:71], a[124:127], a[196:199], v[68:71]\n"
"v_mfma_f32_16x16x32_bf16 v[72:75], a[128:131], a[192:195], v[72:75]\n"
"v_mfma_f32_16x16x32_bf16 v[72:75], a[132:135], a[196:199], v[72:75]\n"
"s_waitcnt vmcnt(18)\n"
"s_barrier\n"
"v_mfma_f32_16x16x32_bf16 v[76:79], a[136:139], a[192:195], v[76:79]\n"
"ds_read_b64_tr_b16 a[144:145], v210 offset:128\n"
"v_mfma_f32_16x16x32_bf16 v[76:79], a[140:143], a[196:199], v[76:79]\n"
"s_add_u32 m0, s48, 29376\n"
"ds_read_b64_tr_b16 a[146:147], v210 offset:4480\n"
"v_mfma_f32_16x16x32_bf16 v[96:99], a[112:115], a[200:203], v[96:99]\n"
"ds_read_b128 a[192:195], v209 offset:4352\n"
"buffer_load_dwordx4 v217, s[28:31], s51 offen lds\n"
"ds_read_b64_tr_b16 a[148:149], v210 offset:17536\n"
"v_mfma_f32_16x16x32_bf16 v[96:99], a[116:119], a[204:207], v[96:99]\n"
"ds_read_b128 a[196:199], v209 offset:4416\n"
"ds_read_b64_tr_b16 a[150:151], v210 offset:21888\n"
"v_mfma_f32_16x16x32_bf16 v[100:103], a[120:123], a[200:203], v[100:103]\n"
"ds_read_b64_tr_b16 a[152:153], v211 offset:128\n"
"v_mfma_f32_16x16x32_bf16 v[100:103], a[124:127], a[204:207], v[100:103]\n"
"ds_read_b64_tr_b16 a[154:155], v211 offset:4480\n"
"v_mfma_f32_16x16x32_bf16 v[104:107], a[128:131], a[200:203], v[104:107]\n"
"ds_read_b64_tr_b16 a[156:157], v211 offset:17536\n"
"v_mfma_f32_16x16x32_bf16 v[104:107], a[132:135], a[204:207], v[104:107]\n"
"s_add_u32 m0, s48, 30464\n"
"ds_read_b64_tr_b16 a[158:159], v211 offset:21888\n"
"v_mfma_f32_16x16x32_bf16 v[108:111], a[136:139], a[200:203], v[108:111]\n"
"buffer_load_dwordx4 v218, s[28:31], s51 offen lds\n"
"ds_read_b64_tr_b16 a[160:161], v210 offset:192\n"
"v_mfma_f32_16x16x32_bf16 v[108:111], a[140:143], a[204:207], v[108:111]\n"
"ds_read_b64_tr_b16 a[162:163], v210 offset:4544\n"
"v_mfma_f32_16x16x32_bf16 v[128:131], a[112:115], a[208:211], v[128:131]\n"
"ds_read_b128 a[200:203], v209 offset:6528\n"
"ds_read_b64_tr_b16 a[164:165], v210 offset:17600\n"
"v_mfma_f32_16x16x32_bf16 v[128:131], a[116:119], a[212:215], v[128:131]\n"
"ds_read_b128 a[204:207], v209 offset:6592\n"
"ds_read_b64_tr_b16 a[166:167], v210 offset:21952\n"
"v_mfma_f32_16x16x32_bf16 v[132:135], a[120:123], a[208:211], v[132:135]\n"
"ds_read_b64_tr_b16 a[168:169], v211 offset:192\n"
"v_mfma_f32_16x16x32_bf16 v[132:135], a[124:127], a[212:215], v[132:135]\n"
"s_add_u32 m0, s48, 31552\n"
"ds_read_b64_tr_b16 a[170:171], v211 offset:4544\n"
"v_mfma_f32_16x16x32_bf16 v[136:139], a[128:131], a[208:211], v[136:139]\n"
"buffer_load_dwordx4 v219, s[28:31], s51 offen lds\n"
"ds_read_b64_tr_b16 a[172:173], v211 offset:17600\n"
"v_mfma_f32_16x16x32_bf16 v[136:139], a[132:135], a[212:215], v[136:139]\n"
"ds_read_b64_tr_b16 a[174:175], v211 offset:21952\n"
"v_mfma_f32_16x16x32_bf16 v[140:143], a[136:139], a[208:211], v[140:143]\n"
"v_mfma_f32_16x16x32_bf16 v[140:143], a[140:143], a[212:215], v[140:143]\n"
"v_mfma_f32_16x16x32_bf16 v[160:163], a[112:115], a[216:219], v[160:163]\n"
"ds_read_b128 a[208:211], v209 offset:8704\n"
"v_mfma_f32_16x16x32_bf16 v[160:163], a[116:119], a[220:223], v[160:163]\n"
"ds_read_b128 a[212:215], v209 offset:8768\n"
"s_add_u32 m0, s48, 32640\n"
"v_mfma_f32_16x16x32_bf16 v[164:167], a[120:123], a[216:219], v[164:167]\n"
"buffer_load_dwordx4 v220, s[28:31], s51 offen lds\n"
"v_mfma_f32_16x16x32_bf16 v[164:167], a[124:127], a[220:223], v[164:167]\n"
"v_mfma_f32_16x16x32_bf16 v[168:171], a[128:131], a[216:219], v[168:171]\n"
"v_mfma_f32_16x16x32_bf16 v[168:171], a[132:135], a[220:223], v[168:171]\n"
"v_mfma_f32_16x16x32_bf16 v[172:175], a[136:139], a[216:219], v[172:175]\n"
"s_waitcnt lgkmcnt(2)\n"
"v_mfma_f32_16x16x32_bf16 v[172:175], a[140:143], a[220:223], v[172:175]\n"
"v_mfma_f32_16x16x32_bf16 v[16:19], a[144:147], a[176:179], v[16:19]\n"
"ds_read_b128 a[216:219], v209 offset:10880\n"
"s_add_u32 m0, s48, 33728\n"
"v_mfma_f32_16x16x32_bf16 v[16:19], a[148:151], a[180:183], v[16:19]\n"
"ds_read_b128 a[220:223], v209 offset:10944\n"
"buffer_load_dwordx4 v221, s[28:31], s51 offen lds\n"
"v_mfma_f32_16x16x32_bf16 v[20:23], a[152:155], a[176:179], v[20:23]\n"
"ds_read_b64_tr_b16 a[112:113], v210\n"
"v_mfma_f32_16x16x32_bf16 v[20:23], a[156:159], a[180:183], v[20:23]\n"
"ds_read_b64_tr_b16 a[114:115], v210 offset:4352\n"
"v_mfma_f32_16x16x32_bf16 v[24:27], a[160:163], a[176:179], v[24:27]\n"
"ds_read_b64_tr_b16 a[116:117], v210 offset:17408\n"
"v_mfma_f32_16x16x32_bf16 v[24:27], a[164:167], a[180:183], v[24:27]\n"
"ds_read_b64_tr_b16 a[118:119], v210 offset:21760\n"
"s_add_u32 m0, s48, 34816\n"
"v_mfma_f32_16x16x32_bf16 v[28:31], a[168:171], a[176:179], v[28:31]\n"
"ds_read_b64_tr_b16 a[120:121], v211\n"
"buffer_load_dwordx4 v222, s[28:31], s51 offen lds\n"
"s_add_u32 s51, s51, 128\n"
"v_mfma_f32_16x16x32_bf16 v[28:31], a[172:175], a[180:183], v[28:31]\n"
"ds_read_b64_tr_b16 a[122:123], v211 offset:4352\n"
"v_mfma_f32_16x16x32_bf16 v[48:51], a[144:147], a[184:187], v[48:51]\n"
"ds_read_b64_tr_b16 a[124:125], v211 offset:17408\n"
"v_mfma_f32_16x16x32_bf16 v[48:51], a[148:151], a[188:191], v[48:51]\n"
"ds_read_b64_tr_b16 a[126:127], v211 offset:21760\n"
"v_mfma_f32_16x16x32_bf16 v[52:55], a[152:155], a[184:187], v[52:55]\n"
"ds_read_b64_tr_b16 a[128:129], v210 offset:64\n"
"v_mfma_f32_16x16x32_bf16 v[52:55], a[156:159], a[188:191], v[52:55]\n"
"ds_read_b64_tr_b16 a[130:131], v210 offset:4416\n"
"v_mfma_f32_16x16x32_bf16 v[56:59], a[160:163], a[184:187], v[56:59]\n"
"ds_read_b64_tr_b16 a[132:133], v210 offset:17472\n"
"buffer_load_dwordx4 a[40:43], v250, s[36:39], s53 offen offset:2048 nt\n"
"v_mfma_f32_16x16x32_bf16 v[56:59], a[164:167], a[188:191], v[56:59]\n"
"ds_read_b64_tr_b16 a[134:135], v210 offset:21824\n"
"v_mfma_f32_16x16x32_bf16 v[60:63], a[168:171], a[184:187], v[60:63]\n"
"ds_read_b64_tr_b16 a[136:137], v211 offset:64\n"
"v_mfma_f32_16x16x32_bf16 v[60:63], a[172:175], a[188:191], v[60:63]\n"
"ds_read_b64_tr_b16 a[138:139], v211 offset:4416\n"
"v_mfma_f32_16x16x32_bf16 v[80:83], a[144:147], a[192:195], v[80:83]\n"
"ds_read_b64_tr_b16 a[140:141], v211 offset:17472\n"
"v_mfma_f32_16x16x32_bf16 v[80:83], a[148:151], a[196:199], v[80:83]\n"
"ds_read_b64_tr_b16 a[142:143], v211 offset:21824\n"
"v_mfma_f32_16x16x32_bf16 v[84:87], a[152:155], a[192:195], v[84:87]\n"
"buffer_load_dwordx4 a[44:47], v250, s[36:39], s53 offen offset:3072 nt\n"
"v_mfma_f32_16x16x32_bf16 v[84:87], a[156:159], a[196:199], v[84:87]\n"
"v_mfma_f32_16x16x32_bf16 v[88:91], a[160:163], a[192:195], v[88:91]\n"
"v_mfma_f32_16x16x32_bf16 v[88:91], a[164:167], a[196:199], v[88:91]\n"
"v_mfma_f32_16x16x32_bf16 v[92:95], a[168:171], a[192:195], v[92:95]\n"
"v_mfma_f32_16x16x32_bf16 v[92:95], a[172:175], a[196:199], v[92:95]\n"
"v_mfma_f32_16x16x32_bf16 v[112:115], a[144:147], a[200:203], v[112:115]\n"
"s_waitcnt lgkmcnt(0)\n"
"s_waitcnt vmcnt(19)\n"
"s_barrier\n"
"v_mfma_f32_16x16x32_bf16 v[112:115], a[148:151], a[204:207], v[112:115]\n"
"s_add_u32 m0, s49, 56576\n"
"v_mfma_f32_16x16x32_bf16 v[116:119], a[152:155], a[200:203], v[116:119]\n"
"buffer_load_dwordx4 v212, s[32:35], s52 offen lds\n"
"v_mfma_f32_16x16x32_bf16 v[116:119], a[156:159], a[204:207], v[116:119]\n"
"v_mfma_f32_16x16x32_bf16 v[120:123], a[160:163], a[200:203], v[120:123]\n"
"v_mfma_f32_16x16x32_bf16 v[120:123], a[164:167], a[204:207], v[120:123]\n"
"v_mfma_f32_16x16x32_bf16 v[124:127], a[168:171], a[200:203], v[124:127]\n"
"v_mfma_f32_16x16x32_bf16 v[124:127], a[172:175], a[204:207], v[124:127]\n"
"s_add_u32 m0, s49, 57664\n"
"v_mfma_f32_16x16x32_bf16 v[144:147], a[144:147], a[208:211], v[144:147]\n"
"buffer_load_dwordx4 v213, s[32:35], s52 offen lds\n"
"v_mfma_f32_16x16x32_bf16 v[144:147], a[148:151], a[212:215], v[144:147]\n"
"v_mfma_f32_16x16x32_bf16 v[148:151], a[152:155], a[208:211], v[148:151]\n"
"v_mfma_f32_16x16x32_bf16 v[148:151], a[156:159], a[212:215], v[148:151]\n"
"v_mfma_f32_16x16x32_bf16 v[152:155], a[160:163], a[208:211], v[152:155]\n"
"v_mfma_f32_16x16x32_bf16 v[152:155], a[164:167], a[212:215], v[152:155]\n"
"s_add_u32 m0, s49, 58752\n"
"v_mfma_f32_16x16x32_bf16 v[156:159], a[168:171], a[208:211], v[156:159]\n"
"buffer_load_dwordx4 v214, s[32:35], s52 offen lds\n"
"v_mfma_f32_16x16x32_bf16 v[156:159], a[172:175], a[212:215], v[156:159]\n"
"v_mfma_f32_16x16x32_bf16 v[176:179], a[144:147], a[216:219], v[176:179]\n"
"v_mfma_f32_16x16x32_bf16 v[176:179], a[148:151], a[220:223], v[176:179]\n"
"v_mfma_f32_16x16x32_bf16 v[180:183], a[152:155], a[216:219], v[180:183]\n"
"v_mfma_f32_16x16x32_bf16 v[180:183], a[156:159], a[220:223], v[180:183]\n"
"s_add_u32 m0, s49, 59840\n"
"v_mfma_f32_16x16x32_bf16 v[184:187], a[160:163], a[216:219], v[184:187]\n"
"buffer_load_dwordx4 v215, s[32:35], s52 offen lds\n"
"s_add_u32 s52, s52, 262144\n"
"v_mfma_f32_16x16x32_bf16 v[184:187], a[164:167], a[220:223], v[184:187]\n"
"v_mfma_f32_16x16x32_bf16 v[188:191], a[168:171], a[216:219], v[188:191]\n"
"v_mfma_f32_16x16x32_bf16 v[188:191], a[172:175], a[220:223], v[188:191]\n"
"v_mfma_f32_16x16x32_bf16 v[192:195], a[112:115], a[224:227], v[192:195]\n"
"v_mfma_f32_16x16x32_bf16 v[192:195], a[116:119], a[228:231], v[192:195]\n"
"s_add_u32 m0, s49, 73984\n"
"v_mfma_f32_16x16x32_bf16 v[196:199], a[120:123], a[224:227], v[196:199]\n"
"buffer_load_dwordx4 v212, s[32:35], s52 offen lds\n"
"v_mfma_f32_16x16x32_bf16 v[196:199], a[124:127], a[228:231], v[196:199]\n"
"v_mfma_f32_16x16x32_bf16 v[200:203], a[128:131], a[224:227], v[200:203]\n"
"v_mfma_f32_16x16x32_bf16 v[200:203], a[132:135], a[228:231], v[200:203]\n"
"v_mfma_f32_16x16x32_bf16 v[204:207], a[136:139], a[224:227], v[204:207]\n"
"v_mfma_f32_16x16x32_bf16 v[204:207], a[140:143], a[228:231], v[204:207]\n"
"s_add_u32 m0, s49, 75072\n"
"v_mfma_f32_16x16x32_bf16 v[0:3], a[112:115], a[176:179], v[0:3]\n"
"ds_read_b128 a[224:227], v208 offset:26112\n"
"buffer_load_dwordx4 v213, s[32:35], s52 offen lds\n"
"v_mfma_f32_16x16x32_bf16 v[0:3], a[116:119], a[180:183], v[0:3]\n"
"ds_read_b128 a[228:231], v208 offset:26176\n"
"v_mfma_f32_16x16x32_bf16 v[4:7], a[120:123], a[176:179], v[4:7]\n"
"v_mfma_f32_16x16x32_bf16 v[4:7], a[124:127], a[180:183], v[4:7]\n"
"v_mfma_f32_16x16x32_bf16 v[8:11], a[128:131], a[176:179], v[8:11]\n"
"v_mfma_f32_16x16x32_bf16 v[8:11], a[132:135], a[180:183], v[8:11]\n"
"s_add_u32 m0, s49, 76160\n"
"v_mfma_f32_16x16x32_bf16 v[12:15], a[136:139], a[176:179], v[12:15]\n"
"buffer_load_dwordx4 v214, s[32:35], s52 offen lds\n"
"v_mfma_f32_16x16x32_bf16 v[12:15], a[140:143], a[180:183], v[12:15]\n"
"v_mfma_f32_16x16x32_bf16 v[32:35], a[112:115], a[184:187], v[32:35]\n"
"ds_read_b128 a[176:179], v208\n"
"v_mfma_f32_16x16x32_bf16 v[32:35], a[116:119], a[188:191], v[32:35]\n"
"ds_read_b128 a[180:183], v208 offset:64\n"
"v_mfma_f32_16x16x32_bf16 v[36:39], a[120:123], a[184:187], v[36:39]\n"
"v_mfma_f32_16x16x32_bf16 v[36:39], a[124:127], a[188:191], v[36:39]\n"
"s_add_u32 m0, s49, 77248\n"
"v_mfma_f32_16x16x32_bf16 v[40:43], a[128:131], a[184:187], v[40:43]\n"
"buffer_load_dwordx4 v215, s[32:35], s52 offen lds\n"
"s_add_u32 s52, s52, 262144\n"
"v_mfma_f32_16x16x32_bf16 v[40:43], a[132:135], a[188:191], v[40:43]\n"
"v_mfma_f32_16x16x32_bf16 v[44:47], a[136:139], a[184:187], v[44:47]\n"
"v_mfma_f32_16x16x32_bf16 v[44:47], a[140:143], a[188:191], v[44:47]\n"
"v_mfma_f32_16x16x32_bf16 v[64:67], a[112:115], a[192:195], v[64:67]\n"
"ds_read_b128 a[184:187], v208 offset:2176\n"
"v_mfma_f32_16x16x32_bf16 v[64:67], a[116:119], a[196:199], v[64:67]\n"
"ds_read_b128 a[188:191], v208 offset:2240\n"
"s_add_u32 m0, s48, 126208\n"
"v_mfma_f32_16x16x32_bf16 v[68:71], a[120:123], a[192:195], v[68:71]\n"
"buffer_load_dwordx4 v216, s[28:31], s51 offen lds\n"
"v_mfma_f32_16x16x32_bf16 v[68:71], a[124:127], a[196:199], v[68:71]\n"
"v_mfma_f32_16x16x32_bf16 v[72:75], a[128:131], a[192:195], v[72:75]\n"
"v_mfma_f32_16x16x32_bf16 v[72:75], a[132:135], a[196:199], v[72:75]\n"
"s_waitcnt vmcnt(18)\n"
"s_barrier\n"
"v_mfma_f32_16x16x32_bf16 v[76:79], a[136:139], a[192:195], v[76:79]\n"
"ds_read_b64_tr_b16 a[144:145], v210 offset:34944\n"
"v_mfma_f32_16x16x32_bf16 v[76:79], a[140:143], a[196:199], v[76:79]\n"
"s_add_u32 m0, s48, 127296\n"
"ds_read_b64_tr_b16 a[146:147], v210 offset:39296\n"
"v_mfma_f32_16x16x32_bf16 v[96:99], a[112:115], a[200:203], v[96:99]\n"
"ds_read_b128 a[192:195], v208 offset:4352\n"
"buffer_load_dwordx4 v217, s[28:31], s51 offen lds\n"
"ds_read_b64_tr_b16 a[148:149], v210 offset:52352\n"
"v_mfma_f32_16x16x32_bf16 v[96:99], a[116:119], a[204:207], v[96:99]\n"
"ds_read_b128 a[196:199], v208 offset:4416\n"
"ds_read_b64_tr_b16 a[150:151], v210 offset:56704\n"
"v_mfma_f32_16x16x32_bf16 v[100:103], a[120:123], a[200:203], v[100:103]\n"
"ds_read_b64_tr_b16 a[152:153], v211 offset:34944\n"
"v_mfma_f32_16x16x32_bf16 v[100:103], a[124:127], a[204:207], v[100:103]\n"
"ds_read_b64_tr_b16 a[154:155], v211 offset:39296\n"
"v_mfma_f32_16x16x32_bf16 v[104:107], a[128:131], a[200:203], v[104:107]\n"
"ds_read_b64_tr_b16 a[156:157], v211 offset:52352\n"
"v_mfma_f32_16x16x32_bf16 v[104:107], a[132:135], a[204:207], v[104:107]\n"
"s_add_u32 m0, s48, 128384\n"
"ds_read_b64_tr_b16 a[158:159], v211 offset:56704\n"
"v_mfma_f32_16x16x32_bf16 v[108:111], a[136:139], a[200:203], v[108:111]\n"
"buffer_load_dwordx4 v218, s[28:31], s51 offen lds\n"
"ds_read_b64_tr_b16 a[160:161], v210 offset:35008\n"
"v_mfma_f32_16x16x32_bf16 v[108:111], a[140:143], a[204:207], v[108:111]\n"
"ds_read_b64_tr_b16 a[162:163], v210 offset:39360\n"
"v_mfma_f32_16x16x32_bf16 v[128:131], a[112:115], a[208:211], v[128:131]\n"
"ds_read_b128 a[200:203], v208 offset:6528\n"
"ds_read_b64_tr_b16 a[164:165], v210 offset:52416\n"
"v_mfma_f32_16x16x32_bf16 v[128:131], a[116:119], a[212:215], v[128:131]\n"
"ds_read_b128 a[204:207], v208 offset:6592\n"
"ds_read_b64_tr_b16 a[166:167], v210 offset:56768\n"
"v_mfma_f32_16x16x32_bf16 v[132:135], a[120:123], a[208:211], v[132:135]\n"
"ds_read_b64_tr_b16 a[168:169], v211 offset:35008\n"
"v_mfma_f32_16x16x32_bf16 v[132:135], a[124:127], a[212:215], v[132:135]\n"
"s_add_u32 m0, s48, 129472\n"
"ds_read_b64_tr_b16 a[170:171], v211 offset:39360\n"
"v_mfma_f32_16x16x32_bf16 v[136:139], a[128:131], a[208:211], v[136:139]\n"
"buffer_load_dwordx4 v219, s[28:31], s51 offen lds\n"
"ds_read_b64_tr_b16 a[172:173], v211 offset:52416\n"
"v_mfma_f32_16x16x32_bf16 v[136:139], a[132:135], a[212:215], v[136:139]\n"
"ds_read_b64_tr_b16 a[174:175], v211 offset:56768\n"
"v_mfma_f32_16x16x32_bf16 v[140:143], a[136:139], a[208:211], v[140:143]\n"
"v_mfma_f32_16x16x32_bf16 v[140:143], a[140:143], a[212:215], v[140:143]\n"
"v_mfma_f32_16x16x32_bf16 v[160:163], a[112:115], a[216:219], v[160:163]\n"
"ds_read_b128 a[208:211], v208 offset:8704\n"
"v_mfma_f32_16x16x32_bf16 v[160:163], a[116:119], a[220:223], v[160:163]\n"
"ds_read_b128 a[212:215], v208 offset:8768\n"
"s_add_u32 m0, s48, 130560\n"
"v_mfma_f32_16x16x32_bf16 v[164:167], a[120:123], a[216:219], v[164:167]\n"
"buffer_load_dwordx4 v220, s[28:31], s51 offen lds\n"
"v_mfma_f32_16x16x32_bf16 v[164:167], a[124:127], a[220:223], v[164:167]\n"
"v_mfma_f32_16x16x32_bf16 v[168:171], a[128:131], a[216:219], v[168:171]\n"
"v_mfma_f32_16x16x32_bf16 v[168:171], a[132:135], a[220:223], v[168:171]\n"
"v_mfma_f32_16x16x32_bf16 v[172:175], a[136:139], a[216:219], v[172:175]\n"
"s_waitcnt lgkmcnt(2)\n"
"v_mfma_f32_16x16x32_bf16 v[172:175], a[140:143], a[220:223], v[172:175]\n"
"v_mfma_f32_16x16x32_bf16 v[16:19], a[144:147], a[176:179], v[16:19]\n"
"ds_read_b128 a[216:219], v208 offset:10880\n"
"s_add_u32 m0, s48, 131648\n"
"v_mfma_f32_16x16x32_bf16 v[16:19], a[148:151], a[180:183], v[16:19]\n"
"ds_read_b128 a[220:223], v208 offset:10944\n"
"buffer_load_dwordx4 v221, s[28:31], s51 offen lds\n"
"v_mfma_f32_16x16x32_bf16 v[20:23], a[152:155], a[176:179], v[20:23]\n"
"ds_read_b64_tr_b16 a[112:113], v210 offset:34816\n"
"v_mfma_f32_16x16x32_bf16 v[20:23], a[156:159], a[180:183], v[20:23]\n"
"ds_read_b64_tr_b16 a[114:115], v210 offset:39168\n"
"v_mfma_f32_16x16x32_bf16 v[24:27], a[160:163], a[176:179], v[24:27]\n"
"ds_read_b64_tr_b16 a[116:117], v210 offset:52224\n"
"v_mfma_f32_16x16x32_bf16 v[24:27], a[164:167], a[180:183], v[24:27]\n"
"ds_read_b64_tr_b16 a[118:119], v210 offset:56576\n"
"s_add_u32 m0, s48, 132736\n"
"v_mfma_f32_16x16x32_bf16 v[28:31], a[168:171], a[176:179], v[28:31]\n"
"ds_read_b64_tr_b16 a[120:121], v211 offset:34816\n"
"buffer_load_dwordx4 v222, s[28:31], s51 offen lds\n"
"s_add_u32 s51, s51, 128\n"
"v_mfma_f32_16x16x32_bf16 v[28:31], a[172:175], a[180:183], v[28:31]\n"
"ds_read_b64_tr_b16 a[122:123], v211 offset:39168\n"
"v_mfma_f32_16x16x32_bf16 v[48:51], a[144:147], a[184:187], v[48:51]\n"
"ds_read_b64_tr_b16 a[124:125], v211 offset:52224\n"
"v_mfma_f32_16x16x32_bf16 v[48:51], a[148:151], a[188:191], v[48:51]\n"
"ds_read_b64_tr_b16 a[126:127], v211 offset:56576\n"
"v_mfma_f32_16x16x32_bf16 v[52:55], a[152:155], a[184:187], v[52:55]\n"
"ds_read_b64_tr_b16 a[128:129], v210 offset:34880\n"
"v_mfma_f32_16x16x32_bf16 v[52:55], a[156:159], a[188:191], v[52:55]\n"
"ds_read_b64_tr_b16 a[130:131], v210 offset:39232\n"
"v_mfma_f32_16x16x32_bf16 v[56:59], a[160:163], a[184:187], v[56:59]\n"
"ds_read_b64_tr_b16 a[132:133], v210 offset:52288\n"
"buffer_load_dwordx4 a[48:51], v250, s[36:39], s54 offen nt\n"
"v_mfma_f32_16x16x32_bf16 v[56:59], a[164:167], a[188:191], v[56:59]\n"
"ds_read_b64_tr_b16 a[134:135], v210 offset:56640\n"
"v_mfma_f32_16x16x32_bf16 v[60:63], a[168:171], a[184:187], v[60:63]\n"
"ds_read_b64_tr_b16 a[136:137], v211 offset:34880\n"
"v_mfma_f32_16x16x32_bf16 v[60:63], a[172:175], a[188:191], v[60:63]\n"
"ds_read_b64_tr_b16 a[138:139], v211 offset:39232\n"
"v_mfma_f32_16x16x32_bf16 v[80:83], a[144:147], a[192:195], v[80:83]\n"
"ds_read_b64_tr_b16 a[140:141], v211 offset:52288\n"
"v_mfma_f32_16x16x32_bf16 v[80:83], a[148:151], a[196:199], v[80:83]\n"
"ds_read_b64_tr_b16 a[142:143], v211 offset:56640\n"
"v_mfma_f32_16x16x32_bf16 v[84:87], a[152:155], a[192:195], v[84:87]\n"
"buffer_load_dwordx4 a[52:55], v250, s[36:39], s54 offen offset:1024 nt\n"
"v_mfma_f32_16x16x32_bf16 v[84:87], a[156:159], a[196:199], v[84:87]\n"
"v_mfma_f32_16x16x32_bf16 v[88:91], a[160:163], a[192:195], v[88:91]\n"
"v_mfma_f32_16x16x32_bf16 v[88:91], a[164:167], a[196:199], v[88:91]\n"
"v_mfma_f32_16x16x32_bf16 v[92:95], a[168:171], a[192:195], v[92:95]\n"
"v_mfma_f32_16x16x32_bf16 v[92:95], a[172:175], a[196:199], v[92:95]\n"
"v_mfma_f32_16x16x32_bf16 v[112:115], a[144:147], a[200:203], v[112:115]\n"
"s_waitcnt lgkmcnt(0)\n"
"s_waitcnt vmcnt(19)\n"
"s_barrier\n"
"v_mfma_f32_16x16x32_bf16 v[112:115], a[148:151], a[204:207], v[112:115]\n"
"s_add_u32 m0, s49, 91392\n"
"v_mfma_f32_16x16x32_bf16 v[116:119], a[152:155], a[200:203], v[116:119]\n"
"buffer_load_dwordx4 v212, s[32:35], s52 offen lds\n"
"v_mfma_f32_16x16x32_bf16 v[116:119], a[156:159], a[204:207], v[116:119]\n"
"v_mfma_f32_16x16x32_bf16 v[120:123], a[160:163], a[200:203], v[120:123]\n"
"v_mfma_f32_16x16x32_bf16 v[120:123], a[164:167], a[204:207], v[120:123]\n"
"v_mfma_f32_16x16x32_bf16 v[124:127], a[168:171], a[200:203], v[124:127]\n"
"v_mfma_f32_16x16x32_bf16 v[124:127], a[172:175], a[204:207], v[124:127]\n"
"s_add_u32 m0, s49, 92480\n"
"v_mfma_f32_16x16x32_bf16 v[144:147], a[144:147], a[208:211], v[144:147]\n"
"buffer_load_dwordx4 v213, s[32:35], s52 offen lds\n"
"v_mfma_f32_16x16x32_bf16 v[144:147], a[148:151], a[212:215], v[144:147]\n"
"v_mfma_f32_16x16x32_bf16 v[148:151], a[152:155], a[208:211], v[148:151]\n"
"v_mfma_f32_16x16x32_bf16 v[148:151], a[156:159], a[212:215], v[148:151]\n"
"v_mfma_f32_16x16x32_bf16 v[152:155], a[160:163], a[208:211], v[152:155]\n"
"v_mfma_f32_16x16x32_bf16 v[152:155], a[164:167], a[212:215], v[152:155]\n"
"s_add_u32 m0, s49, 93568\n"
"v_mfma_f32_16x16x32_bf16 v[156:159], a[168:171], a[208:211], v[156:159]\n"
"buffer_load_dwordx4 v214, s[32:35], s52 offen lds\n"
"v_mfma_f32_16x16x32_bf16 v[156:159], a[172:175], a[212:215], v[156:159]\n"
"v_mfma_f32_16x16x32_bf16 v[176:179], a[144:147], a[216:219], v[176:179]\n"
"v_mfma_f32_16x16x32_bf16 v[176:179], a[148:151], a[220:223], v[176:179]\n"
"v_mfma_f32_16x16x32_bf16 v[180:183], a[152:155], a[216:219], v[180:183]\n"
"v_mfma_f32_16x16x32_bf16 v[180:183], a[156:159], a[220:223], v[180:183]\n"
"s_add_u32 m0, s49, 94656\n"
"v_mfma_f32_16x16x32_bf16 v[184:187], a[160:163], a[216:219], v[184:187]\n"
"buffer_load_dwordx4 v215, s[32:35], s52 offen lds\n"
"s_add_u32 s52, s52, 262144\n"
"v_mfma_f32_16x16x32_bf16 v[184:187], a[164:167], a[220:223], v[184:187]\n"
"v_mfma_f32_16x16x32_bf16 v[188:191], a[168:171], a[216:219], v[188:191]\n"
"v_mfma_f32_16x16x32_bf16 v[188:191], a[172:175], a[220:223], v[188:191]\n"
"v_mfma_f32_16x16x32_bf16 v[192:195], a[112:115], a[224:227], v[192:195]\n"
"v_mfma_f32_16x16x32_bf16 v[192:195], a[116:119], a[228:231], v[192:195]\n"
"s_add_u32 m0, s49, 108800\n"
"v_mfma_f32_16x16x32_bf16 v[196:199], a[120:123], a[224:227], v[196:199]\n"
"buffer_load_dwordx4 v212, s[32:35], s52 offen lds\n"
"v_mfma_f32_16x16x32_bf16 v[196:199], a[124:127], a[228:231], v[196:199]\n"
"v_mfma_f32_16x16x32_bf16 v[200:203], a[128:131], a[224:227], v[200:203]\n"
"v_mfma_f32_16x16x32_bf16 v[200:203], a[132:135], a[228:231], v[200:203]\n"
"v_mfma_f32_16x16x32_bf16 v[204:207], a[136:139], a[224:227], v[204:207]\n"
"v_mfma_f32_16x16x32_bf16 v[204:207], a[140:143], a[228:231], v[204:207]\n"
"s_add_u32 m0, s49, 109888\n"
"v_mfma_f32_16x16x32_bf16 v[0:3], a[112:115], a[176:179], v[0:3]\n"
"ds_read_b128 a[224:227], v208 offset:54400\n"
"buffer_load_dwordx4 v213, s[32:35], s52 offen lds\n"
"v_mfma_f32_16x16x32_bf16 v[0:3], a[116:119], a[180:183], v[0:3]\n"
"ds_read_b128 a[228:231], v208 offset:54464\n"
"v_mfma_f32_16x16x32_bf16 v[4:7], a[120:123], a[176:179], v[4:7]\n"
"v_mfma_f32_16x16x32_bf16 v[4:7], a[124:127], a[180:183], v[4:7]\n"
"v_mfma_f32_16x16x32_bf16 v[8:11], a[128:131], a[176:179], v[8:11]\n"
"v_mfma_f32_16x16x32_bf16 v[8:11], a[132:135], a[180:183], v[8:11]\n"
"s_add_u32 m0, s49, 110976\n"
"v_mfma_f32_16x16x32_bf16 v[12:15], a[136:139], a[176:179], v[12:15]\n"
"buffer_load_dwordx4 v214, s[32:35], s52 offen lds\n"
"v_mfma_f32_16x16x32_bf16 v[12:15], a[140:143], a[180:183], v[12:15]\n"
"v_mfma_f32_16x16x32_bf16 v[32:35], a[112:115], a[184:187], v[32:35]\n"
"ds_read_b128 a[176:179], v208 offset:28288\n"
"v_mfma_f32_16x16x32_bf16 v[32:35], a[116:119], a[188:191], v[32:35]\n"
"ds_read_b128 a[180:183], v208 offset:28352\n"
"v_mfma_f32_16x16x32_bf16 v[36:39], a[120:123], a[184:187], v[36:39]\n"
"v_mfma_f32_16x16x32_bf16 v[36:39], a[124:127], a[188:191], v[36:39]\n"
"s_add_u32 m0, s49, 112064\n"
"v_mfma_f32_16x16x32_bf16 v[40:43], a[128:131], a[184:187], v[40:43]\n"
"buffer_load_dwordx4 v215, s[32:35], s52 offen lds\n"
"s_add_u32 s52, s52, 262144\n"
"v_mfma_f32_16x16x32_bf16 v[40:43], a[132:135], a[188:191], v[40:43]\n"
"v_mfma_f32_16x16x32_bf16 v[44:47], a[136:139], a[184:187], v[44:47]\n"
"v_mfma_f32_16x16x32_bf16 v[44:47], a[140:143], a[188:191], v[44:47]\n"
"v_mfma_f32_16x16x32_bf16 v[64:67], a[112:115], a[192:195], v[64:67]\n"
"ds_read_b128 a[184:187], v208 offset:30464\n"
"v_mfma_f32_16x16x32_bf16 v[64:67], a[116:119], a[196:199], v[64:67]\n"
"ds_read_b128 a[188:191], v208 offset:30528\n"
"s_add_u32 m0, s48, 0\n"
"v_mfma_f32_16x16x32_bf16 v[68:71], a[120:123], a[192:195], v[68:71]\n"
"buffer_load_dwordx4 v216, s[28:31], s51 offen lds\n"
"v_mfma_f32_16x16x32_bf16 v[68:71], a[124:127], a[196:199], v[68:71]\n"
"v_mfma_f32_16x16x32_bf16 v[72:75], a[128:131], a[192:195], v[72:75]\n"
"v_mfma_f32_16x16x32_bf16 v[72:75], a[132:135], a[196:199], v[72:75]\n"
"s_waitcnt vmcnt(18)\n"
"s_barrier\n"
"v_mfma_f32_16x16x32_bf16 v[76:79], a[136:139], a[192:195], v[76:79]\n"
"ds_read_b64_tr_b16 a[144:145], v210 offset:128\n"
"v_mfma_f32_16x16x32_bf16 v[76:79], a[140:143], a[196:199], v[76:79]\n"
"s_add_u32 m0, s48, 1088\n"
"ds_read_b64_tr_b16 a[146:147], v210 offset:4480\n"
"v_mfma_f32_16x16x32_bf16 v[96:99], a[112:115], a[200:203], v[96:99]\n"
"ds_read_b128 a[192:195], v208 offset:32640\n"
"buffer_load_dwordx4 v217, s[28:31], s51 offen lds\n"
"ds_read_b64_tr_b16 a[148:149], v210 offset:17536\n"
"v_mfma_f32_16x16x32_bf16 v[96:99], a[116:119], a[204:207], v[96:99]\n"
"ds_read_b128 a[196:199], v208 offset:32704\n"
"ds_read_b64_tr_b16 a[150:151], v210 offset:21888\n"
"v_mfma_f32_16x16x32_bf16 v[100:103], a[120:123], a[200:203], v[100:103]\n"
"ds_read_b64_tr_b16 a[152:153], v211 offset:128\n"
"v_mfma_f32_16x16x32_bf16 v[100:103], a[124:127], a[204:207], v[100:103]\n"
"ds_read_b64_tr_b16 a[154:155], v211 offset:4480\n"
"v_mfma_f32_16x16x32_bf16 v[104:107], a[128:131], a[200:203], v[104:107]\n"
"ds_read_b64_tr_b16 a[156:157], v211 offset:17536\n"
"v_mfma_f32_16x16x32_bf16 v[104:107], a[132:135], a[204:207], v[104:107]\n"
"s_add_u32 m0, s48, 2176\n"
"ds_read_b64_tr_b16 a[158:159], v211 offset:21888\n"
"v_mfma_f32_16x16x32_bf16 v[108:111], a[136:139], a[200:203], v[108:111]\n"
"buffer_load_dwordx4 v218, s[28:31], s51 offen lds\n"
"ds_read_b64_tr_b16 a[160:161], v210 offset:192\n"
"v_mfma_f32_16x16x32_bf16 v[108:111], a[140:143], a[204:207], v[108:111]\n"
"ds_read_b64_tr_b16 a[162:163], v210 offset:4544\n"
"v_mfma_f32_16x16x32_bf16 v[128:131], a[112:115], a[208:211], v[128:131]\n"
"ds_read_b128 a[200:203], v208 offset:34816\n"
"ds_read_b64_tr_b16 a[164:165], v210 offset:17600\n"
"v_mfma_f32_16x16x32_bf16 v[128:131], a[116:119], a[212:215], v[128:131]\n"
"ds_read_b128 a[204:207], v208 offset:34880\n"
"ds_read_b64_tr_b16 a[166:167], v210 offset:21952\n"
"v_mfma_f32_16x16x32_bf16 v[132:135], a[120:123], a[208:211], v[132:135]\n"
"ds_read_b64_tr_b16 a[168:169], v211 offset:192\n"
"v_mfma_f32_16x16x32_bf16 v[132:135], a[124:127], a[212:215], v[132:135]\n"
"s_add_u32 m0, s48, 3264\n"
"ds_read_b64_tr_b16 a[170:171], v211 offset:4544\n"
"v_mfma_f32_16x16x32_bf16 v[136:139], a[128:131], a[208:211], v[136:139]\n"
"buffer_load_dwordx4 v219, s[28:31], s51 offen lds\n"
"ds_read_b64_tr_b16 a[172:173], v211 offset:17600\n"
"v_mfma_f32_16x16x32_bf16 v[136:139], a[132:135], a[212:215], v[136:139]\n"
"ds_read_b64_tr_b16 a[174:175], v211 offset:21952\n"
"v_mfma_f32_16x16x32_bf16 v[140:143], a[136:139], a[208:211], v[140:143]\n"
"v_mfma_f32_16x16x32_bf16 v[140:143], a[140:143], a[212:215], v[140:143]\n"
"v_mfma_f32_16x16x32_bf16 v[160:163], a[112:115], a[216:219], v[160:163]\n"
"ds_read_b128 a[208:211], v208 offset:36992\n"
"v_mfma_f32_16x16x32_bf16 v[160:163], a[116:119], a[220:223], v[160:163]\n"
"ds_read_b128 a[212:215], v208 offset:37056\n"
"s_add_u32 m0, s48, 4352\n"
"v_mfma_f32_16x16x32_bf16 v[164:167], a[120:123], a[216:219], v[164:167]\n"
"buffer_load_dwordx4 v220, s[28:31], s51 offen lds\n"
"v_mfma_f32_16x16x32_bf16 v[164:167], a[124:127], a[220:223], v[164:167]\n"
"v_mfma_f32_16x16x32_bf16 v[168:171], a[128:131], a[216:219], v[168:171]\n"
"v_mfma_f32_16x16x32_bf16 v[168:171], a[132:135], a[220:223], v[168:171]\n"
"v_mfma_f32_16x16x32_bf16 v[172:175], a[136:139], a[216:219], v[172:175]\n"
"s_waitcnt lgkmcnt(2)\n"
"v_mfma_f32_16x16x32_bf16 v[172:175], a[140:143], a[220:223], v[172:175]\n"
"v_mfma_f32_16x16x32_bf16 v[16:19], a[144:147], a[176:179], v[16:19]\n"
"ds_read_b128 a[216:219], v208 offset:39168\n"
"s_add_u32 m0, s48, 5440\n"
"v_mfma_f32_16x16x32_bf16 v[16:19], a[148:151], a[180:183], v[16:19]\n"
"ds_read_b128 a[220:223], v208 offset:39232\n"
"buffer_load_dwordx4 v221, s[28:31], s51 offen lds\n"
"v_mfma_f32_16x16x32_bf16 v[20:23], a[152:155], a[176:179], v[20:23]\n"
"ds_read_b64_tr_b16 a[112:113], v210\n"
"v_mfma_f32_16x16x32_bf16 v[20:23], a[156:159], a[180:183], v[20:23]\n"
"ds_read_b64_tr_b16 a[114:115], v210 offset:4352\n"
"v_mfma_f32_16x16x32_bf16 v[24:27], a[160:163], a[176:179], v[24:27]\n"
"ds_read_b64_tr_b16 a[116:117], v210 offset:17408\n"
"v_mfma_f32_16x16x32_bf16 v[24:27], a[164:167], a[180:183], v[24:27]\n"
"ds_read_b64_tr_b16 a[118:119], v210 offset:21760\n"
"s_add_u32 m0, s48, 6528\n"
"v_mfma_f32_16x16x32_bf16 v[28:31], a[168:171], a[176:179], v[28:31]\n"
"ds_read_b64_tr_b16 a[120:121], v211\n"
"buffer_load_dwordx4 v222, s[28:31], s51 offen lds\n"
"s_add_u32 s51, s51, 128\n"
"v_mfma_f32_16x16x32_bf16 v[28:31], a[172:175], a[180:183], v[28:31]\n"
"ds_read_b64_tr_b16 a[122:123], v211 offset:4352\n"
"v_mfma_f32_16x16x32_bf16 v[48:51], a[144:147], a[184:187], v[48:51]\n"
"ds_read_b64_tr_b16 a[124:125], v211 offset:17408\n"
"v_mfma_f32_16x16x32_bf16 v[48:51], a[148:151], a[188:191], v[48:51]\n"
"ds_read_b64_tr_b16 a[126:127], v211 offset:21760\n"
"v_mfma_f32_16x16x32_bf16 v[52:55], a[152:155], a[184:187], v[52:55]\n"
"ds_read_b64_tr_b16 a[128:129], v210 offset:64\n"
"v_mfma_f32_16x16x32_bf16 v[52:55], a[156:159], a[188:191], v[52:55]\n"
"ds_read_b64_tr_b16 a[130:131], v210 offset:4416\n"
"v_mfma_f32_16x16x32_bf16 v[56:59], a[160:163], a[184:187], v[56:59]\n"
"ds_read_b64_tr_b16 a[132:133], v210 offset:17472\n"
"buffer_load_dwordx4 a[56:59], v250, s[36:39], s54 offen offset:2048 nt\n"
"v_mfma_f32_16x16x32_bf16 v[56:59], a[164:167], a[188:191], v[56:59]\n"
"ds_read_b64_tr_b16 a[134:135], v210 offset:21824\n"
"v_mfma_f32_16x16x32_bf16 v[60:63], a[168:171], a[184:187], v[60:63]\n"
"ds_read_b64_tr_b16 a[136:137], v211 offset:64\n"
"v_mfma_f32_16x16x32_bf16 v[60:63], a[172:175], a[188:191], v[60:63]\n"
"ds_read_b64_tr_b16 a[138:139], v211 offset:4416\n"
"v_mfma_f32_16x16x32_bf16 v[80:83], a[144:147], a[192:195], v[80:83]\n"
"ds_read_b64_tr_b16 a[140:141], v211 offset:17472\n"
"v_mfma_f32_16x16x32_bf16 v[80:83], a[148:151], a[196:199], v[80:83]\n"
"ds_read_b64_tr_b16 a[142:143], v211 offset:21824\n"
"v_mfma_f32_16x16x32_bf16 v[84:87], a[152:155], a[192:195], v[84:87]\n"
"buffer_load_dwordx4 a[60:63], v250, s[36:39], s54 offen offset:3072 nt\n"
"s_add_u32 s53, s53, 8192\n"
"s_and_b32 s53, s53, 258047\n"
"s_lshl_b32 s38, s38, 1\n"
"s_add_u32 s54, s53, 4096\n"
"v_mfma_f32_16x16x32_bf16 v[84:87], a[156:159], a[196:199], v[84:87]\n"
"v_mfma_f32_16x16x32_bf16 v[88:91], a[160:163], a[192:195], v[88:91]\n"
"v_mfma_f32_16x16x32_bf16 v[88:91], a[164:167], a[196:199], v[88:91]\n"
"v_mfma_f32_16x16x32_bf16 v[92:95], a[168:171], a[192:195], v[92:95]\n"
"v_mfma_f32_16x16x32_bf16 v[92:95], a[172:175], a[196:199], v[92:95]\n"
"v_mfma_f32_16x16x32_bf16 v[112:115], a[144:147], a[200:203], v[112:115]\n"
"s_waitcnt lgkmcnt(0)\n"
"s_waitcnt vmcnt(19)\n"
"s_barrier\n"
"v_mfma_f32_16x16x32_bf16 v[112:115], a[148:151], a[204:207], v[112:115]\n"
"s_add_u32 m0, s49, 56576\n"
"v_mfma_f32_16x16x32_bf16 v[116:119], a[152:155], a[200:203], v[116:119]\n"
"buffer_load_dwordx4 v212, s[32:35], s52 offen lds\n"
"v_mfma_f32_16x16x32_bf16 v[116:119], a[156:159], a[204:207], v[116:119]\n"
"v_mfma_f32_16x16x32_bf16 v[120:123], a[160:163], a[200:203], v[120:123]\n"
"v_mfma_f32_16x16x32_bf16 v[120:123], a[164:167], a[204:207], v[120:123]\n"
"v_mfma_f32_16x16x32_bf16 v[124:127], a[168:171], a[200:203], v[124:127]\n"
"v_mfma_f32_16x16x32_bf16 v[124:127], a[172:175], a[204:207], v[124:127]\n"
"s_add_u32 m0, s49, 57664\n"
"v_mfma_f32_16x16x32_bf16 v[144:147], a[144:147], a[208:211], v[144:147]\n"
"buffer_load_dwordx4 v213, s[32:35], s52 offen lds\n"
"v_mfma_f32_16x16x32_bf16 v[144:147], a[148:151], a[212:215], v[144:147]\n"
"v_mfma_f32_16x16x32_bf16 v[148:151], a[152:155], a[208:211], v[148:151]\n"
"v_mfma_f32_16x16x32_bf16 v[148:151], a[156:159], a[212:215], v[148:151]\n"
"v_mfma_f32_16x16x32_bf16 v[152:155], a[160:163], a[208:211], v[152:155]\n"
"v_mfma_f32_16x16x32_bf16 v[152:155], a[164:167], a[212:215], v[152:155]\n"
"s_add_u32 m0, s49, 58752\n"
"v_mfma_f32_16x16x32_bf16 v[156:159], a[168:171], a[208:211], v[156:159]\n"
"buffer_load_dwordx4 v214, s[32:35], s52 offen lds\n"
"v_mfma_f32_16x16x32_bf16 v[156:159], a[172:175], a[212:215], v[156:159]\n"
"v_mfma_f32_16x16x32_bf16 v[176:179], a[144:147], a[216:219], v[176:179]\n"
"v_mfma_f32_16x16x32_bf16 v[176:179], a[148:151], a[220:223], v[176:179]\n"
"v_mfma_f32_16x16x32_bf16 v[180:183], a[152:155], a[216:219], v[180:183]\n"
"v_mfma_f32_16x16x32_bf16 v[180:183], a[156:159], a[220:223], v[180:183]\n"
"s_add_u32 m0, s49, 59840\n"
"v_mfma_f32_16x16x32_bf16 v[184:187], a[160:163], a[216:219], v[184:187]\n"
"buffer_load_dwordx4 v215, s[32:35], s52 offen lds\n"
"s_add_u32 s52, s52, 262144\n"
"v_mfma_f32_16x16x32_bf16 v[184:187], a[164:167], a[220:223], v[184:187]\n"
"v_mfma_f32_16x16x32_bf16 v[188:191], a[168:171], a[216:219], v[188:191]\n"
"v_mfma_f32_16x16x32_bf16 v[188:191], a[172:175], a[220:223], v[188:191]\n"
"v_mfma_f32_16x16x32_bf16 v[192:195], a[112:115], a[224:227], v[192:195]\n"
"v_mfma_f32_16x16x32_bf16 v[192:195], a[116:119], a[228:231], v[192:195]\n"
"s_add_u32 m0, s49, 73984\n"
"v_mfma_f32_16x16x32_bf16 v[196:199], a[120:123], a[224:227], v[196:199]\n"
"buffer_load_dwordx4 v212, s[32:35], s52 offen lds\n"
"v_mfma_f32_16x16x32_bf16 v[196:199], a[124:127], a[228:231], v[196:199]\n"
"v_mfma_f32_16x16x32_bf16 v[200:203], a[128:131], a[224:227], v[200:203]\n"
"v_mfma_f32_16x16x32_bf16 v[200:203], a[132:135], a[228:231], v[200:203]\n"
"v_mfma_f32_16x16x32_bf16 v[204:207], a[136:139], a[224:227], v[204:207]\n"
"v_mfma_f32_16x16x32_bf16 v[204:207], a[140:143], a[228:231], v[204:207]\n"
"s_add_u32 m0, s49, 75072\n"
"v_mfma_f32_16x16x32_bf16 v[0:3], a[112:115], a[176:179], v[0:3]\n"
"ds_read_b128 a[224:227], v209 offset:26112\n"
"buffer_load_dwordx4 v213, s[32:35], s52 offen lds\n"
"v_mfma_f32_16x16x32_bf16 v[0:3], a[116:119], a[180:183], v[0:3]\n"
"ds_read_b128 a[228:231], v209 offset:26176\n"
"v_mfma_f32_16x16x32_bf16 v[4:7], a[120:123], a[176:179], v[4:7]\n"
"v_mfma_f32_16x16x32_bf16 v[4:7], a[124:127], a[180:183], v[4:7]\n"
"v_mfma_f32_16x16x32_bf16 v[8:11], a[128:131], a[176:179], v[8:11]\n"
"v_mfma_f32_16x16x32_bf16 v[8:11], a[132:135], a[180:183], v[8:11]\n"
"s_add_u32 m0, s49, 76160\n"
"v_mfma_f32_16x16x32_bf16 v[12:15], a[136:139], a[176:179], v[12:15]\n"
"buffer_load_dwordx4 v214, s[32:35], s52 offen lds\n"
"v_mfma_f32_16x16x32_bf16 v[12:15], a[140:143], a[180:183], v[12:15]\n"
"v_mfma_f32_16x16x32_bf16 v[32:35], a[112:115], a[184:187], v[32:35]\n"
"ds_read_b128 a[176:179], v209\n"
"v_mfma_f32_16x16x32_bf16 v[32:35], a[116:119], a[188:191], v[32:35]\n"
"ds_read_b128 a[180:183], v209 offset:64\n"
"v_mfma_f32_16x16x32_bf16 v[36:39], a[120:123], a[184:187], v[36:39]\n"
"v_mfma_f32_16x16x32_bf16 v[36:39], a[124:127], a[188:191], v[36:39]\n"
"s_add_u32 m0, s49, 77248\n"
"v_mfma_f32_16x16x32_bf16 v[40:43], a[128:131], a[184:187], v[40:43]\n"
"buffer_load_dwordx4 v215, s[32:35], s52 offen lds\n"
"s_add_u32 s52, s52, 262144\n"
"v_mfma_f32_16x16x32_bf16 v[40:43], a[132:135], a[188:191], v[40:43]\n"
"v_mfma_f32_16x16x32_bf16 v[44:47], a[136:139], a[184:187], v[44:47]\n"
"v_mfma_f32_16x16x32_bf16 v[44:47], a[140:143], a[188:191], v[44:47]\n"
"v_mfma_f32_16x16x32_bf16 v[64:67], a[112:115], a[192:195], v[64:67]\n"
"ds_read_b128 a[184:187], v209 offset:2176\n"
"v_mfma_f32_16x16x32_bf16 v[64:67], a[116:119], a[196:199], v[64:67]\n"
"ds_read_b128 a[188:191], v209 offset:2240\n"
"s_add_u32 m0, s48, 28288\n"
"v_mfma_f32_16x16x32_bf16 v[68:71], a[120:123], a[192:195], v[68:71]\n"
"buffer_load_dwordx4 v216, s[28:31], s51 offen lds\n"
"v_mfma_f32_16x16x32_bf16 v[68:71], a[124:127], a[196:199], v[68:71]\n"
"v_mfma_f32_16x16x32_bf16 v[72:75], a[128:131], a[192:195], v[72:75]\n"
"v_mfma_f32_16x16x32_bf16 v[72:75], a[132:135], a[196:199], v[72:75]\n"
"s_waitcnt vmcnt(18)\n"
"s_barrier\n"
"v_mfma_f32_16x16x32_bf16 v[76:79], a[136:139], a[192:195], v[76:79]\n"
"ds_read_b64_tr_b16 a[144:145], v210 offset:34944\n"
"v_mfma_f32_16x16x32_bf16 v[76:79], a[140:143], a[196:199], v[76:79]\n"
"s_add_u32 m0, s48, 29376\n"
"ds_read_b64_tr_b16 a[146:147], v210 offset:39296\n"
"v_mfma_f32_16x16x32_bf16 v[96:99], a[112:115], a[200:203], v[96:99]\n"
"ds_read_b128 a[192:195], v209 offset:4352\n"
"buffer_load_dwordx4 v217, s[28:31], s51 offen lds\n"
"ds_read_b64_tr_b16 a[148:149], v210 offset:52352\n"
"v_mfma_f32_16x16x32_bf16 v[96:99], a[116:119], a[204:207], v[96:99]\n"
"ds_read_b128 a[196:199], v209 offset:4416\n"
"ds_read_b64_tr_b16 a[150:151], v210 offset:56704\n"
"v_mfma_f32_16x16x32_bf16 v[100:103], a[120:123], a[200:203], v[100:103]\n"
"ds_read_b64_tr_b16 a[152:153], v211 offset:34944\n"
"v_mfma_f32_16x16x32_bf16 v[100:103], a[124:127], a[204:207], v[100:103]\n"
"ds_read_b64_tr_b16 a[154:155], v211 offset:39296\n"
"v_mfma_f32_16x16x32_bf16 v[104:107], a[128:131], a[200:203], v[104:107]\n"
"ds_read_b64_tr_b16 a[156:157], v211 offset:52352\n"
"v_mfma_f32_16x16x32_bf16 v[104:107], a[132:135], a[204:207], v[104:107]\n"
"s_add_u32 m0, s48, 30464\n"
"ds_read_b64_tr_b16 a[158:159], v211 offset:56704\n"
"v_mfma_f32_16x16x32_bf16 v[108:111], a[136:139], a[200:203], v[108:111]\n"
"buffer_load_dwordx4 v218, s[28:31], s51 offen lds\n"
"ds_read_b64_tr_b16 a[160:161], v210 offset:35008\n"
"v_mfma_f32_16x16x32_bf16 v[108:111], a[140:143], a[204:207], v[108:111]\n"
"ds_read_b64_tr_b16 a[162:163], v210 offset:39360\n"
"v_mfma_f32_16x16x32_bf16 v[128:131], a[112:115], a[208:211], v[128:131]\n"
"ds_read_b128 a[200:203], v209 offset:6528\n"
"ds_read_b64_tr_b16 a[164:165], v210 offset:52416\n"
"v_mfma_f32_16x16x32_bf16 v[128:131], a[116:119], a[212:215], v[128:131]\n"
"ds_read_b128 a[204:207], v209 offset:6592\n"
"ds_read_b64_tr_b16 a[166:167], v210 offset:56768\n"
"v_mfma_f32_16x16x32_bf16 v[132:135], a[120:123], a[208:211], v[132:135]\n"
"ds_read_b64_tr_b16 a[168:169], v211 offset:35008\n"
"v_mfma_f32_16x16x32_bf16 v[132:135], a[124:127], a[212:215], v[132:135]\n"
"s_add_u32 m0, s48, 31552\n"
"ds_read_b64_tr_b16 a[170:171], v211 offset:39360\n"
"v_mfma_f32_16x16x32_bf16 v[136:139], a[128:131], a[208:211], v[136:139]\n"
"buffer_load_dwordx4 v219, s[28:31], s51 offen lds\n"
"ds_read_b64_tr_b16 a[172:173], v211 offset:52416\n"
"v_mfma_f32_16x16x32_bf16 v[136:139], a[132:135], a[212:215], v[136:139]\n"
"ds_read_b64_tr_b16 a[174:175], v211 offset:56768\n"
"v_mfma_f32_16x16x32_bf16 v[140:143], a[136:139], a[208:211], v[140:143]\n"
"v_mfma_f32_16x16x32_bf16 v[140:143], a[140:143], a[212:215], v[140:143]\n"
"v_mfma_f32_16x16x32_bf16 v[160:163], a[112:115], a[216:219], v[160:163]\n"
"ds_read_b128 a[208:211], v209 offset:8704\n"
"v_mfma_f32_16x16x32_bf16 v[160:163], a[116:119], a[220:223], v[160:163]\n"
"ds_read_b128 a[212:215], v209 offset:8768\n"
"s_add_u32 m0, s48, 32640\n"
"v_mfma_f32_16x16x32_bf16 v[164:167], a[120:123], a[216:219], v[164:167]\n"
"buffer_load_dwordx4 v220, s[28:31], s51 offen lds\n"
"v_mfma_f32_16x16x32_bf16 v[164:167], a[124:127], a[220:223], v[164:167]\n"
"v_mfma_f32_16x16x32_bf16 v[168:171], a[128:131], a[216:219], v[168:171]\n"
"v_mfma_f32_16x16x32_bf16 v[168:171], a[132:135], a[220:223], v[168:171]\n"
"v_mfma_f32_16x16x32_bf16 v[172:175], a[136:139], a[216:219], v[172:175]\n"
"s_waitcnt lgkmcnt(2)\n"
"v_mfma_f32_16x16x32_bf16 v[172:175], a[140:143], a[220:223], v[172:175]\n"
"v_mfma_f32_16x16x32_bf16 v[16:19], a[144:147], a[176:179], v[16:19]\n"
"ds_read_b128 a[216:219], v209 offset:10880\n"
"s_add_u32 m0, s48, 33728\n"
"v_mfma_f32_16x16x32_bf16 v[16:19], a[148:151], a[180:183], v[16:19]\n"
"ds_read_b128 a[220:223], v209 offset:10944\n"
"buffer_load_dwordx4 v221, s[28:31], s51 offen lds\n"
"v_mfma_f32_16x16x32_bf16 v[20:23], a[152:155], a[176:179], v[20:23]\n"
"ds_read_b64_tr_b16 a[112:113], v210 offset:34816\n"
"v_mfma_f32_16x16x32_bf16 v[20:23], a[156:159], a[180:183], v[20:23]\n"
"ds_read_b64_tr_b16 a[114:115], v210 offset:39168\n"
"v_mfma_f32_16x16x32_bf16 v[24:27], a[160:163], a[176:179], v[24:27]\n"
"ds_read_b64_tr_b16 a[116:117], v210 offset:52224\n"
"v_mfma_f32_16x16x32_bf16 v[24:27], a[164:167], a[180:183], v[24:27]\n"
"ds_read_b64_tr_b16 a[118:119], v210 offset:56576\n"
"s_add_u32 m0, s48, 34816\n"
"v_mfma_f32_16x16x32_bf16 v[28:31], a[168:171], a[176:179], v[28:31]\n"
"ds_read_b64_tr_b16 a[120:121], v211 offset:34816\n"
"buffer_load_dwordx4 v222, s[28:31], s51 offen lds\n"
"s_add_u32 s51, s51, 128\n"
"v_mfma_f32_16x16x32_bf16 v[28:31], a[172:175], a[180:183], v[28:31]\n"
"ds_read_b64_tr_b16 a[122:123], v211 offset:39168\n"
"v_mfma_f32_16x16x32_bf16 v[48:51], a[144:147], a[184:187], v[48:51]\n"
"ds_read_b64_tr_b16 a[124:125], v211 offset:52224\n"
"v_mfma_f32_16x16x32_bf16 v[48:51], a[148:151], a[188:191], v[48:51]\n"
"ds_read_b64_tr_b16 a[126:127], v211 offset:56576\n"
"v_mfma_f32_16x16x32_bf16 v[52:55], a[152:155], a[184:187], v[52:55]\n"
"ds_read_b64_tr_b16 a[128:129], v210 offset:34880\n"
"v_mfma_f32_16x16x32_bf16 v[52:55], a[156:159], a[188:191], v[52:55]\n"
"ds_read_b64_tr_b16 a[130:131], v210 offset:39232\n"
"v_mfma_f32_16x16x32_bf16 v[56:59], a[160:163], a[184:187], v[56:59]\n"
"ds_read_b64_tr_b16 a[132:133], v210 offset:52288\n"
"buffer_load_dwordx4 a[64:67], v250, s[36:39], s53 offen nt\n"
"v_mfma_f32_16x16x32_bf16 v[56:59], a[164:167], a[188:191], v[56:59]\n"
"ds_read_b64_tr_b16 a[134:135], v210 offset:56640\n"
"v_mfma_f32_16x16x32_bf16 v[60:63], a[168:171], a[184:187], v[60:63]\n"
"ds_read_b64_tr_b16 a[136:137], v211 offset:34880\n"
"v_mfma_f32_16x16x32_bf16 v[60:63], a[172:175], a[188:191], v[60:63]\n"
"ds_read_b64_tr_b16 a[138:139], v211 offset:39232\n"
"v_mfma_f32_16x16x32_bf16 v[80:83], a[144:147], a[192:195], v[80:83]\n"
"ds_read_b64_tr_b16 a[140:141], v211 offset:52288\n"
"v_mfma_f32_16x16x32_bf16 v[80:83], a[148:151], a[196:199], v[80:83]\n"
"ds_read_b64_tr_b16 a[142:143], v211 offset:56640\n"
"v_mfma_f32_16x16x32_bf16 v[84:87], a[152:155], a[192:195], v[84:87]\n"
"buffer_load_dwordx4 a[68:71], v250, s[36:39], s53 offen offset:1024 nt\n"
"v_mfma_f32_16x16x32_bf16 v[84:87], a[156:159], a[196:199], v[84:87]\n"
"v_mfma_f32_16x16x32_bf16 v[88:91], a[160:163], a[192:195], v[88:91]\n"
"v_mfma_f32_16x16x32_bf16 v[88:91], a[164:167], a[196:199], v[88:91]\n"
"v_mfma_f32_16x16x32_bf16 v[92:95], a[168:171], a[192:195], v[92:95]\n"
"v_mfma_f32_16x16x32_bf16 v[92:95], a[172:175], a[196:199], v[92:95]\n"
"v_mfma_f32_16x16x32_bf16 v[112:115], a[144:147], a[200:203], v[112:115]\n"
"s_waitcnt lgkmcnt(0)\n"
"s_waitcnt vmcnt(19)\n"
"s_barrier\n"
"v_mfma_f32_16x16x32_bf16 v[112:115], a[148:151], a[204:207], v[112:115]\n"
"s_add_u32 m0, s49, 91392\n"
"v_mfma_f32_16x16x32_bf16 v[116:119], a[152:155], a[200:203], v[116:119]\n"
"buffer_load_dwordx4 v212, s[32:35], s52 offen lds\n"
"v_mfma_f32_16x16x32_bf16 v[116:119], a[156:159], a[204:207], v[116:119]\n"
"v_mfma_f32_16x16x32_bf16 v[120:123], a[160:163], a[200:203], v[120:123]\n"
"v_mfma_f32_16x16x32_bf16 v[120:123], a[164:167], a[204:207], v[120:123]\n"
"v_mfma_f32_16x16x32_bf16 v[124:127], a[168:171], a[200:203], v[124:127]\n"
"v_mfma_f32_16x16x32_bf16 v[124:127], a[172:175], a[204:207], v[124:127]\n"
"s_add_u32 m0, s49, 92480\n"
"v_mfma_f32_16x16x32_bf16 v[144:147], a[144:147], a[208:211], v[144:147]\n"
"buffer_load_dwordx4 v213, s[32:35], s52 offen lds\n"
"v_mfma_f32_16x16x32_bf16 v[144:147], a[148:151], a[212:215], v[144:147]\n"
"v_mfma_f32_16x16x32_bf16 v[148:151], a[152:155], a[208:211], v[148:151]\n"
"v_mfma_f32_16x16x32_bf16 v[148:151], a[156:159], a[212:215], v[148:151]\n"
"v_mfma_f32_16x16x32_bf16 v[152:155], a[160:163], a[208:211], v[152:155]\n"
"v_mfma_f32_16x16x32_bf16 v[152:155], a[164:167], a[212:215], v[152:155]\n"
"s_add_u32 m0, s49, 93568\n"
"v_mfma_f32_16x16x32_bf16 v[156:159], a[168:171], a[208:211], v[156:159]\n"
"buffer_load_dwordx4 v214, s[32:35], s52 offen lds\n"
"v_mfma_f32_16x16x32_bf16 v[156:159], a[172:175], a[212:215], v[156:159]\n"
"v_mfma_f32_16x16x32_bf16 v[176:179], a[144:147], a[216:219], v[176:179]\n"
"v_mfma_f32_16x16x32_bf16 v[176:179], a[148:151], a[220:223], v[176:179]\n"
"v_mfma_f32_16x16x32_bf16 v[180:183], a[152:155], a[216:219], v[180:183]\n"
"v_mfma_f32_16x16x32_bf16 v[180:183], a[156:159], a[220:223], v[180:183]\n"
"s_add_u32 m0, s49, 94656\n"
"v_mfma_f32_16x16x32_bf16 v[184:187], a[160:163], a[216:219], v[184:187]\n"
"buffer_load_dwordx4 v215, s[32:35], s52 offen lds\n"
"s_add_u32 s52, s52, 262144\n"
"v_mfma_f32_16x16x32_bf16 v[184:187], a[164:167], a[220:223], v[184:187]\n"
"v_mfma_f32_16x16x32_bf16 v[188:191], a[168:171], a[216:219], v[188:191]\n"
"v_mfma_f32_16x16x32_bf16 v[188:191], a[172:175], a[220:223], v[188:191]\n"
"v_mfma_f32_16x16x32_bf16 v[192:195], a[112:115], a[224:227], v[192:195]\n"
"v_mfma_f32_16x16x32_bf16 v[192:195], a[116:119], a[228:231], v[192:195]\n"
"s_add_u32 m0, s49, 108800\n"
"v_mfma_f32_16x16x32_bf16 v[196:199], a[120:123], a[224:227], v[196:199]\n"
"buffer_load_dwordx4 v212, s[32:35], s52 offen lds\n"
"v_mfma_f32_16x16x32_bf16 v[196:199], a[124:127], a[228:231], v[196:199]\n"
"v_mfma_f32_16x16x32_bf16 v[200:203], a[128:131], a[224:227], v[200:203]\n"
"v_mfma_f32_16x16x32_bf16 v[200:203], a[132:135], a[228:231], v[200:203]\n"
"v_mfma_f32_16x16x32_bf16 v[204:207], a[136:139], a[224:227], v[204:207]\n"
"v_mfma_f32_16x16x32_bf16 v[204:207], a[140:143], a[228:231], v[204:207]\n"
"s_add_u32 m0, s49, 109888\n"
"v_mfma_f32_16x16x32_bf16 v[0:3], a[112:115], a[176:179], v[0:3]\n"
"ds_read_b128 a[224:227], v208 offset:26112\n"
"buffer_load_dwordx4 v213, s[32:35], s52 offen lds\n"
"v_mfma_f32_16x16x32_bf16 v[0:3], a[116:119], a[180:183], v[0:3]\n"
"ds_read_b128 a[228:231], v208 offset:26176\n"
"v_mfma_f32_16x16x32_bf16 v[4:7], a[120:123], a[176:179], v[4:7]\n"
"v_mfma_f32_16x16x32_bf16 v[4:7], a[124:127], a[180:183], v[4:7]\n"
"v_mfma_f32_16x16x32_bf16 v[8:11], a[128:131], a[176:179], v[8:11]\n"
"v_mfma_f32_16x16x32_bf16 v[8:11], a[132:135], a[180:183], v[8:11]\n"
"s_add_u32 m0, s49, 110976\n"
"v_mfma_f32_16x16x32_bf16 v[12:15], a[136:139], a[176:179], v[12:15]\n"
"buffer_load_dwordx4 v214, s[32:35], s52 offen lds\n"
"v_mfma_f32_16x16x32_bf16 v[12:15], a[140:143], a[180:183], v[12:15]\n"
"v_mfma_f32_16x16x32_bf16 v[32:35], a[112:115], a[184:187], v[32:35]\n"
"ds_read_b128 a[176:179], v208\n"
"v_mfma_f32_16x16x32_bf16 v[32:35], a[116:119], a[188:191], v[32:35]\n"
"ds_read_b128 a[180:183], v208 offset:64\n"
"v_mfma_f32_16x16x32_bf16 v[36:39], a[120:123], a[184:187], v[36:39]\n"
"v_mfma_f32_16x16x32_bf16 v[36:39], a[124:127], a[188:191], v[36:39]\n"
"s_add_u32 m0, s49, 112064\n"
"v_mfma_f32_16x16x32_bf16 v[40:43], a[128:131], a[184:187], v[40:43]\n"
"buffer_load_dwordx4 v215, s[32:35], s52 offen lds\n"
"s_add_u32 s52, s52, 262144\n"
"v_mfma_f32_16x16x32_bf16 v[40:43], a[132:135], a[188:191], v[40:43]\n"
"v_mfma_f32_16x16x32_bf16 v[44:47], a[136:139], a[184:187], v[44:47]\n"
"v_mfma_f32_16x16x32_bf16 v[44:47], a[140:143], a[188:191], v[44:47]\n"
"v_mfma_f32_16x16x32_bf16 v[64:67], a[112:115], a[192:195], v[64:67]\n"
"ds_read_b128 a[184:187], v208 offset:2176\n"
"v_mfma_f32_16x16x32_bf16 v[64:67], a[116:119], a[196:199], v[64:67]\n"
"ds_read_b128 a[188:191], v208 offset:2240\n"
"s_add_u32 m0, s48, 126208\n"
"v_mfma_f32_16x16x32_bf16 v[68:71], a[120:123], a[192:195], v[68:71]\n"
"buffer_load_dwordx4 v216, s[28:31], s51 offen lds\n"
"v_mfma_f32_16x16x32_bf16 v[68:71], a[124:127], a[196:199], v[68:71]\n"
"v_mfma_f32_16x16x32_bf16 v[72:75], a[128:131], a[192:195], v[72:75]\n"
"v_mfma_f32_16x16x32_bf16 v[72:75], a[132:135], a[196:199], v[72:75]\n"
"s_waitcnt vmcnt(18)\n"
"s_barrier\n"
"v_mfma_f32_16x16x32_bf16 v[76:79], a[136:139], a[192:195], v[76:79]\n"
"ds_read_b64_tr_b16 a[144:145], v210 offset:128\n"
"v_mfma_f32_16x16x32_bf16 v[76:79], a[140:143], a[196:199], v[76:79]\n"
"s_add_u32 m0, s48, 127296\n"
"ds_read_b64_tr_b16 a[146:147], v210 offset:4480\n"
"v_mfma_f32_16x16x32_bf16 v[96:99], a[112:115], a[200:203], v[96:99]\n"
"ds_read_b128 a[192:195], v208 offset:4352\n"
"buffer_load_dwordx4 v217, s[28:31], s51 offen lds\n"
"ds_read_b64_tr_b16 a[148:149], v210 offset:17536\n"
"v_mfma_f32_16x16x32_bf16 v[96:99], a[116:119], a[204:207], v[96:99]\n"
"ds_read_b128 a[196:199], v208 offset:4416\n"
"ds_read_b64_tr_b16 a[150:151], v210 offset:21888\n"
"v_mfma_f32_16x16x32_bf16 v[100:103], a[120:123], a[200:203], v[100:103]\n"
"ds_read_b64_tr_b16 a[152:153], v211 offset:128\n"
"v_mfma_f32_16x16x32_bf16 v[100:103], a[124:127], a[204:207], v[100:103]\n"
"ds_read_b64_tr_b16 a[154:155], v211 offset:4480\n"
"v_mfma_f32_16x16x32_bf16 v[104:107], a[128:131], a[200:203], v[104:107]\n"
"ds_read_b64_tr_b16 a[156:157], v211 offset:17536\n"
"v_mfma_f32_16x16x32_bf16 v[104:107], a[132:135], a[204:207], v[104:107]\n"
"s_add_u32 m0, s48, 128384\n"
"ds_read_b64_tr_b16 a[158:159], v211 offset:21888\n"
"v_mfma_f32_16x16x32_bf16 v[108:111], a[136:139], a[200:203], v[108:111]\n"
"buffer_load_dwordx4 v218, s[28:31], s51 offen lds\n"
"ds_read_b64_tr_b16 a[160:161], v210 offset:192\n"
"v_mfma_f32_16x16x32_bf16 v[108:111], a[140:143], a[204:207], v[108:111]\n"
"ds_read_b64_tr_b16 a[162:163], v210 offset:4544\n"
"v_mfma_f32_16x16x32_bf16 v[128:131], a[112:115], a[208:211], v[128:131]\n"
"ds_read_b128 a[200:203], v208 offset:6528\n"
"ds_read_b64_tr_b16 a[164:165], v210 offset:17600\n"
"v_mfma_f32_16x16x32_bf16 v[128:131], a[116:119], a[212:215], v[128:131]\n"
"ds_read_b128 a[204:207], v208 offset:6592\n"
"ds_read_b64_tr_b16 a[166:167], v210 offset:21952\n"
"v_mfma_f32_16x16x32_bf16 v[132:135], a[120:123], a[208:211], v[132:135]\n"
"ds_read_b64_tr_b16 a[168:169], v211 offset:192\n"
"v_mfma_f32_16x16x32_bf16 v[132:135], a[124:127], a[212:215], v[132:135]\n"
"s_add_u32 m0, s48, 129472\n"
"ds_read_b64_tr_b16 a[170:171], v211 offset:4544\n"
"v_mfma_f32_16x16x32_bf16 v[136:139], a[128:131], a[208:211], v[136:139]\n"
"buffer_load_dwordx4 v219, s[28:31], s51 offen lds\n"
"ds_read_b64_tr_b16 a[172:173], v211 offset:17600\n"
"v_mfma_f32_16x16x32_bf16 v[136:139], a[132:135], a[212:215], v[136:139]\n"
"ds_read_b64_tr_b16 a[174:175], v211 offset:21952\n"
"v_mfma_f32_16x16x32_bf16 v[140:143], a[136:139], a[208:211], v[140:143]\n"
"v_mfma_f32_16x16x32_bf16 v[140:143], a[140:143], a[212:215], v[140:143]\n"
"v_mfma_f32_16x16x32_bf16 v[160:163], a[112:115], a[216:219], v[160:163]\n"
"ds_read_b128 a[208:211], v208 offset:8704\n"
"v_mfma_f32_16x16x32_bf16 v[160:163], a[116:119], a[220:223], v[160:163]\n"
"ds_read_b128 a[212:215], v208 offset:8768\n"
"s_add_u32 m0, s48, 130560\n"
"v_mfma_f32_16x16x32_bf16 v[164:167], a[120:123], a[216:219], v[164:167]\n"
"buffer_load_dwordx4 v220, s[28:31], s51 offen lds\n"
"v_mfma_f32_16x16x32_bf16 v[164:167], a[124:127], a[220:223], v[164:167]\n"
"v_mfma_f32_16x16x32_bf16 v[168:171], a[128:131], a[216:219], v[168:171]\n"
"v_mfma_f32_16x16x32_bf16 v[168:171], a[132:135], a[220:223], v[168:171]\n"
"v_mfma_f32_16x16x32_bf16 v[172:175], a[136:139], a[216:219], v[172:175]\n"
"s_waitcnt lgkmcnt(2)\n"
"v_mfma_f32_16x16x32_bf16 v[172:175], a[140:143], a[220:223], v[172:175]\n"
"v_mfma_f32_16x16x32_bf16 v[16:19], a[144:147], a[176:179], v[16:19]\n"
"ds_read_b128 a[216:219], v208 offset:10880\n"
"s_add_u32 m0, s48, 131648\n"
"v_mfma_f32_16x16x32_bf16 v[16:19], a[148:151], a[180:183], v[16:19]\n"
"ds_read_b128 a[220:223], v208 offset:10944\n"
"buffer_load_dwordx4 v221, s[28:31], s51 offen lds\n"
"v_mfma_f32_16x16x32_bf16 v[20:23], a[152:155], a[176:179], v[20:23]\n"
"ds_read_b64_tr_b16 a[112:113], v210\n"
"v_mfma_f32_16x16x32_bf16 v[20:23], a[156:159], a[180:183], v[20:23]\n"
"ds_read_b64_tr_b16 a[114:115], v210 offset:4352\n"
"v_mfma_f32_16x16x32_bf16 v[24:27], a[160:163], a[176:179], v[24:27]\n"
"ds_read_b64_tr_b16 a[116:117], v210 offset:17408\n"
"v_mfma_f32_16x16x32_bf16 v[24:27], a[164:167], a[180:183], v[24:27]\n"
"ds_read_b64_tr_b16 a[118:119], v210 offset:21760\n"
"s_add_u32 m0, s48, 132736\n"
"v_mfma_f32_16x16x32_bf16 v[28:31], a[168:171], a[176:179], v[28:31]\n"
"ds_read_b64_tr_b16 a[120:121], v211\n"
"buffer_load_dwordx4 v222, s[28:31], s51 offen lds\n"
"s_add_u32 s51, s51, 128\n"
"v_mfma_f32_16x16x32_bf16 v[28:31], a[172:175], a[180:183], v[28:31]\n"
"ds_read_b64_tr_b16 a[122:123], v211 offset:4352\n"
"v_mfma_f32_16x16x32_bf16 v[48:51], a[144:147], a[184:187], v[48:51]\n"
"ds_read_b64_tr_b16 a[124:125], v211 offset:17408\n"
"v_mfma_f32_16x16x32_bf16 v[48:51], a[148:151], a[188:191], v[48:51]\n"
"ds_read_b64_tr_b16 a[126:127], v211 offset:21760\n"
"v_mfma_f32_16x16x32_bf16 v[52:55], a[152:155], a[184:187], v[52:55]\n"
"ds_read_b64_tr_b16 a[128:129], v210 offset:64\n"
"v_mfma_f32_16x16x32_bf16 v[52:55], a[156:159], a[188:191], v[52:55]\n"
"ds_read_b64_tr_b16 a[130:131], v210 offset:4416\n"
"v_mfma_f32_16x16x32_bf16 v[56:59], a[160:163], a[184:187], v[56:59]\n"
"ds_read_b64_tr_b16 a[132:133], v210 offset:17472\n"
"buffer_load_dwordx4 a[72:75], v250, s[36:39], s53 offen offset:2048 nt\n"
"v_mfma_f32_16x16x32_bf16 v[56:59], a[164:167], a[188:191], v[56:59]\n"
"ds_read_b64_tr_b16 a[134:135], v210 offset:21824\n"
"v_mfma_f32_16x16x32_bf16 v[60:63], a[168:171], a[184:187], v[60:63]\n"
"ds_read_b64_tr_b16 a[136:137], v211 offset:64\n"
"v_mfma_f32_16x16x32_bf16 v[60:63], a[172:175], a[188:191], v[60:63]\n"
"ds_read_b64_tr_b16 a[138:139], v211 offset:4416\n"
"v_mfma_f32_16x16x32_bf16 v[80:83], a[144:147], a[192:195], v[80:83]\n"
"ds_read_b64_tr_b16 a[140:141], v211 offset:17472\n"
"v_mfma_f32_16x16x32_bf16 v[80:83], a[148:151], a[196:199], v[80:83]\n"
"ds_read_b64_tr_b16 a[142:143], v211 offset:21824\n"
"v_mfma_f32_16x16x32_bf16 v[84:87], a[152:155], a[192:195], v[84:87]\n"
"buffer_load_dwordx4 a[76:79], v250, s[36:39], s53 offen offset:3072 nt\n"
"v_mfma_f32_16x16x32_bf16 v[84:87], a[156:159], a[196:199], v[84:87]\n"
"v_mfma_f32_16x16x32_bf16 v[88:91], a[160:163], a[192:195], v[88:91]\n"
"v_mfma_f32_16x16x32_bf16 v[88:91], a[164:167], a[196:199], v[88:91]\n"
"v_mfma_f32_16x16x32_bf16 v[92:95], a[168:171], a[192:195], v[92:95]\n"
"v_mfma_f32_16x16x32_bf16 v[92:95], a[172:175], a[196:199], v[92:95]\n"
"v_mfma_f32_16x16x32_bf16 v[112:115], a[144:147], a[200:203], v[112:115]\n"
"s_waitcnt lgkmcnt(0)\n"
"s_waitcnt vmcnt(19)\n"
"s_barrier\n"
"v_mfma_f32_16x16x32_bf16 v[112:115], a[148:151], a[204:207], v[112:115]\n"
"s_add_u32 m0, s49, 56576\n"
"v_mfma_f32_16x16x32_bf16 v[116:119], a[152:155], a[200:203], v[116:119]\n"
"buffer_load_dwordx4 v212, s[32:35], s52 offen lds\n"
"v_mfma_f32_16x16x32_bf16 v[116:119], a[156:159], a[204:207], v[116:119]\n"
"v_mfma_f32_16x16x32_bf16 v[120:123], a[160:163], a[200:203], v[120:123]\n"
"v_mfma_f32_16x16x32_bf16 v[120:123], a[164:167], a[204:207], v[120:123]\n"
"v_mfma_f32_16x16x32_bf16 v[124:127], a[168:171], a[200:203], v[124:127]\n"
"s_add_u32 m0, s49, 57664\n"
"v_mfma_f32_16x16x32_bf16 v[124:127], a[172:175], a[204:207], v[124:127]\n"
"buffer_load_dwordx4 v213, s[32:35], s52 offen lds\n"
"v_mfma_f32_16x16x32_bf16 v[144:147], a[144:147], a[208:211], v[144:147]\n"
"v_mfma_f32_16x16x32_bf16 v[144:147], a[148:151], a[212:215], v[144:147]\n"
"v_mfma_f32_16x16x32_bf16 v[148:151], a[152:155], a[208:211], v[148:151]\n"
"v_mfma_f32_16x16x32_bf16 v[148:151], a[156:159], a[212:215], v[148:151]\n"
"v_mfma_f32_16x16x32_bf16 v[152:155], a[160:163], a[208:211], v[152:155]\n"
"s_add_u32 m0, s49, 58752\n"
"v_mfma_f32_16x16x32_bf16 v[152:155], a[164:167], a[212:215], v[152:155]\n"
"buffer_load_dwordx4 v214, s[32:35], s52 offen lds\n"
"v_mfma_f32_16x16x32_bf16 v[156:159], a[168:171], a[208:211], v[156:159]\n"
"v_mfma_f32_16x16x32_bf16 v[156:159], a[172:175], a[212:215], v[156:159]\n"
"v_mfma_f32_16x16x32_bf16 v[176:179], a[144:147], a[216:219], v[176:179]\n"
"v_mfma_f32_16x16x32_bf16 v[176:179], a[148:151], a[220:223], v[176:179]\n"
"v_mfma_f32_16x16x32_bf16 v[180:183], a[152:155], a[216:219], v[180:183]\n"
"s_add_u32 m0, s49, 59840\n"
"v_mfma_f32_16x16x32_bf16 v[180:183], a[156:159], a[220:223], v[180:183]\n"
"buffer_load_dwordx4 v215, s[32:35], s52 offen lds\n"
"s_add_u32 s52, s52, 262144\n"
"v_mfma_f32_16x16x32_bf16 v[184:187], a[160:163], a[216:219], v[184:187]\n"
"v_mfma_f32_16x16x32_bf16 v[184:187], a[164:167], a[220:223], v[184:187]\n"
"v_mfma_f32_16x16x32_bf16 v[188:191], a[168:171], a[216:219], v[188:191]\n"
"v_mfma_f32_16x16x32_bf16 v[188:191], a[172:175], a[220:223], v[188:191]\n"
"s_add_u32 m0, s49, 73984\n"
"v_mfma_f32_16x16x32_bf16 v[192:195], a[112:115], a[224:227], v[192:195]\n"
"buffer_load_dwordx4 v212, s[32:35], s52 offen lds\n"
"v_mfma_f32_16x16x32_bf16 v[192:195], a[116:119], a[228:231], v[192:195]\n"
"v_mfma_f32_16x16x32_bf16 v[196:199], a[120:123], a[224:227], v[196:199]\n"
"v_mfma_f32_16x16x32_bf16 v[196:199], a[124:127], a[228:231], v[196:199]\n"
"v_mfma_f32_16x16x32_bf16 v[200:203], a[128:131], a[224:227], v[200:203]\n"
"v_mfma_f32_16x16x32_bf16 v[200:203], a[132:135], a[228:231], v[200:203]\n"
"s_add_u32 m0, s49, 75072\n"
"v_mfma_f32_16x16x32_bf16 v[204:207], a[136:139], a[224:227], v[204:207]\n"
"buffer_load_dwordx4 v213, s[32:35], s52 offen lds\n"
"v_mfma_f32_16x16x32_bf16 v[204:207], a[140:143], a[228:231], v[204:207]\n"
"v_mfma_f32_16x16x32_bf16 v[0:3], a[112:115], a[176:179], v[0:3]\n"
"ds_read_b128 a[224:227], v208 offset:54400\n"
"v_mfma_f32_16x16x32_bf16 v[0:3], a[116:119], a[180:183], v[0:3]\n"
"ds_read_b128 a[228:231], v208 offset:54464\n"
"v_mfma_f32_16x16x32_bf16 v[4:7], a[120:123], a[176:179], v[4:7]\n"
"v_mfma_f32_16x16x32_bf16 v[4:7], a[124:127], a[180:183], v[4:7]\n"
"s_add_u32 m0, s49, 76160\n"
"v_mfma_f32_16x16x32_bf16 v[8:11], a[128:131], a[176:179], v[8:11]\n"
"buffer_load_dwordx4 v214, s[32:35], s52 offen lds\n"
"v_mfma_f32_16x16x32_bf16 v[8:11], a[132:135], a[180:183], v[8:11]\n"
"v_mfma_f32_16x16x32_bf16 v[12:15], a[136:139], a[176:179], v[12:15]\n"
"v_mfma_f32_16x16x32_bf16 v[12:15], a[140:143], a[180:183], v[12:15]\n"
"v_mfma_f32_16x16x32_bf16 v[32:35], a[112:115], a[184:187], v[32:35]\n"
"ds_read_b128 a[176:179], v208 offset:28288\n"
"s_add_u32 m0, s49, 77248\n"
"v_mfma_f32_16x16x32_bf16 v[32:35], a[116:119], a[188:191], v[32:35]\n"
"ds_read_b128 a[180:183], v208 offset:28352\n"
"buffer_load_dwordx4 v215, s[32:35], s52 offen lds\n"
"s_add_u32 s52, s52, 262144\n"
"v_mfma_f32_16x16x32_bf16 v[36:39], a[120:123], a[184:187], v[36:39]\n"
"v_mfma_f32_16x16x32_bf16 v[36:39], a[124:127], a[188:191], v[36:39]\n"
"v_mfma_f32_16x16x32_bf16 v[40:43], a[128:131], a[184:187], v[40:43]\n"
"v_mfma_f32_16x16x32_bf16 v[40:43], a[132:135], a[188:191], v[40:43]\n"
"v_mfma_f32_16x16x32_bf16 v[44:47], a[136:139], a[184:187], v[44:47]\n"
"s_add_u32 m0, s48, 0\n"
"v_mfma_f32_16x16x32_bf16 v[44:47], a[140:143], a[188:191], v[44:47]\n"
"buffer_load_dwordx4 v216, s[28:31], s51 offen lds\n"
"v_mfma_f32_16x16x32_bf16 v[64:67], a[112:115], a[192:195], v[64:67]\n"
"ds_read_b128 a[184:187], v208 offset:30464\n"
"v_mfma_f32_16x16x32_bf16 v[64:67], a[116:119], a[196:199], v[64:67]\n"
"ds_read_b128 a[188:191], v208 offset:30528\n"
"v_mfma_f32_16x16x32_bf16 v[68:71], a[120:123], a[192:195], v[68:71]\n"
"v_mfma_f32_16x16x32_bf16 v[68:71], a[124:127], a[196:199], v[68:71]\n"
"v_mfma_f32_16x16x32_bf16 v[72:75], a[128:131], a[192:195], v[72:75]\n"
"v_mfma_f32_16x16x32_bf16 v[72:75], a[132:135], a[196:199], v[72:75]\n"
"s_waitcnt vmcnt(18)\n"
"s_barrier\n"
"v_mfma_f32_16x16x32_bf16 v[76:79], a[136:139], a[192:195], v[76:79]\n"
"s_add_u32 m0, s48, 1088\n"
"ds_read_b64_tr_b16 a[144:145], v210 offset:34944\n"
"v_mfma_f32_16x16x32_bf16 v[76:79], a[140:143], a[196:199], v[76:79]\n"
"buffer_load_dwordx4 v217, s[28:31], s51 offen lds\n"
"ds_read_b64_tr_b16 a[146:147], v210 offset:39296\n"
"v_mfma_f32_16x16x32_bf16 v[96:99], a[112:115], a[200:203], v[96:99]\n"
"ds_read_b128 a[192:195], v208 offset:32640\n"
"ds_read_b64_tr_b16 a[148:149], v210 offset:52352\n"
"v_mfma_f32_16x16x32_bf16 v[96:99], a[116:119], a[204:207], v[96:99]\n"
"ds_read_b128 a[196:199], v208 offset:32704\n"
"s_add_u32 m0, s48, 2176\n"
"ds_read_b64_tr_b16 a[150:151], v210 offset:56704\n"
"v_mfma_f32_16x16x32_bf16 v[100:103], a[120:123], a[200:203], v[100:103]\n"
"buffer_load_dwordx4 v218, s[28:31], s51 offen lds\n"
"ds_read_b64_tr_b16 a[152:153], v211 offset:34944\n"
"v_mfma_f32_16x16x32_bf16 v[100:103], a[124:127], a[204:207], v[100:103]\n"
"ds_read_b64_tr_b16 a[154:155], v211 offset:39296\n"
"v_mfma_f32_16x16x32_bf16 v[104:107], a[128:131], a[200:203], v[104:107]\n"
"ds_read_b64_tr_b16 a[156:157], v211 offset:52352\n"
"v_mfma_f32_16x16x32_bf16 v[104:107], a[132:135], a[204:207], v[104:107]\n"
"ds_read_b64_tr_b16 a[158:159], v211 offset:56704\n"
"v_mfma_f32_16x16x32_bf16 v[108:111], a[136:139], a[200:203], v[108:111]\n"
"ds_read_b64_tr_b16 a[160:161], v210 offset:35008\n"
"v_mfma_f32_16x16x32_bf16 v[108:111], a[140:143], a[204:207], v[108:111]\n"
"s_add_u32 m0, s48, 3264\n"
"ds_read_b64_tr_b16 a[162:163], v210 offset:39360\n"
"v_mfma_f32_16x16x32_bf16 v[128:131], a[112:115], a[208:211], v[128:131]\n"
"ds_read_b128 a[200:203], v208 offset:34816\n"
"buffer_load_dwordx4 v219, s[28:31], s51 offen lds\n"
"ds_read_b64_tr_b16 a[164:165], v210 offset:52416\n"
"v_mfma_f32_16x16x32_bf16 v[128:131], a[116:119], a[212:215], v[128:131]\n"
"ds_read_b128 a[204:207], v208 offset:34880\n"
"ds_read_b64_tr_b16 a[166:167], v210 offset:56768\n"
"v_mfma_f32_16x16x32_bf16 v[132:135], a[120:123], a[208:211], v[132:135]\n"
"ds_read_b64_tr_b16 a[168:169], v211 offset:35008\n"
"v_mfma_f32_16x16x32_bf16 v[132:135], a[124:127], a[212:215], v[132:135]\n"
"ds_read_b64_tr_b16 a[170:171], v211 offset:39360\n"
"v_mfma_f32_16x16x32_bf16 v[136:139], a[128:131], a[208:211], v[136:139]\n"
"ds_read_b64_tr_b16 a[172:173], v211 offset:52416\n"
"v_mfma_f32_16x16x32_bf16 v[136:139], a[132:135], a[212:215], v[136:139]\n"
"s_add_u32 m0, s48, 4352\n"
"ds_read_b64_tr_b16 a[174:175], v211 offset:56768\n"
"v_mfma_f32_16x16x32_bf16 v[140:143], a[136:139], a[208:211], v[140:143]\n"
"buffer_load_dwordx4 v220, s[28:31], s51 offen lds\n"
"v_mfma_f32_16x16x32_bf16 v[140:143], a[140:143], a[212:215], v[140:143]\n"
"v_mfma_f32_16x16x32_bf16 v[160:163], a[112:115], a[216:219], v[160:163]\n"
"ds_read_b128 a[208:211], v208 offset:36992\n"
"v_mfma_f32_16x16x32_bf16 v[160:163], a[116:119], a[220:223], v[160:163]\n"
"ds_read_b128 a[212:215], v208 offset:37056\n"
"v_mfma_f32_16x16x32_bf16 v[164:167], a[120:123], a[216:219], v[164:167]\n"
"s_add_u32 m0, s48, 5440\n"
"v_mfma_f32_16x16x32_bf16 v[164:167], a[124:127], a[220:223], v[164:167]\n"
"buffer_load_dwordx4 v221, s[28:31], s51 offen lds\n"
"v_mfma_f32_16x16x32_bf16 v[168:171], a[128:131], a[216:219], v[168:171]\n"
"v_mfma_f32_16x16x32_bf16 v[168:171], a[132:135], a[220:223], v[168:171]\n"
"v_mfma_f32_16x16x32_bf16 v[172:175], a[136:139], a[216:219], v[172:175]\n"
"s_waitcnt lgkmcnt(2)\n"
"v_mfma_f32_16x16x32_bf16 v[172:175], a[140:143], a[220:223], v[172:175]\n"
"v_mfma_f32_16x16x32_bf16 v[16:19], a[144:147], a[176:179], v[16:19]\n"
"ds_read_b128 a[216:219], v208 offset:39168\n"
"s_add_u32 m0, s48, 6528\n"
"v_mfma_f32_16x16x32_bf16 v[16:19], a[148:151], a[180:183], v[16:19]\n"
"ds_read_b128 a[220:223], v208 offset:39232\n"
"buffer_load_dwordx4 v222, s[28:31], s51 offen lds\n"
"s_add_u32 s51, s51, 128\n"
"v_mfma_f32_16x16x32_bf16 v[20:23], a[152:155], a[176:179], v[20:23]\n"
"ds_read_b64_tr_b16 a[112:113], v210 offset:34816\n"
"v_mfma_f32_16x16x32_bf16 v[20:23], a[156:159], a[180:183], v[20:23]\n"
"ds_read_b64_tr_b16 a[114:115], v210 offset:39168\n"
"v_mfma_f32_16x16x32_bf16 v[24:27], a[160:163], a[176:179], v[24:27]\n"
"ds_read_b64_tr_b16 a[116:117], v210 offset:52224\n"
"v_mfma_f32_16x16x32_bf16 v[24:27], a[164:167], a[180:183], v[24:27]\n"
"ds_read_b64_tr_b16 a[118:119], v210 offset:56576\n"
"v_mfma_f32_16x16x32_bf16 v[28:31], a[168:171], a[176:179], v[28:31]\n"
"ds_read_b64_tr_b16 a[120:121], v211 offset:34816\n"
"v_mfma_f32_16x16x32_bf16 v[28:31], a[172:175], a[180:183], v[28:31]\n"
"ds_read_b64_tr_b16 a[122:123], v211 offset:39168\n"
"buffer_load_dwordx4 a[80:83], v250, s[36:39], s54 offen nt\n"
"v_mfma_f32_16x16x32_bf16 v[48:51], a[144:147], a[184:187], v[48:51]\n"
"ds_read_b64_tr_b16 a[124:125], v211 offset:52224\n"
"v_mfma_f32_16x16x32_bf16 v[48:51], a[148:151], a[188:191], v[48:51]\n"
"ds_read_b64_tr_b16 a[126:127], v211 offset:56576\n"
"v_mfma_f32_16x16x32_bf16 v[52:55], a[152:155], a[184:187], v[52:55]\n"
"ds_read_b64_tr_b16 a[128:129], v210 offset:34880\n"
"v_mfma_f32_16x16x32_bf16 v[52:55], a[156:159], a[188:191], v[52:55]\n"
"ds_read_b64_tr_b16 a[130:131], v210 offset:39232\n"
"v_mfma_f32_16x16x32_bf16 v[56:59], a[160:163], a[184:187], v[56:59]\n"
"ds_read_b64_tr_b16 a[132:133], v210 offset:52288\n"
"buffer_load_dwordx4 a[84:87], v250, s[36:39], s54 offen offset:1024 nt\n"
"v_mfma_f32_16x16x32_bf16 v[56:59], a[164:167], a[188:191], v[56:59]\n"
"ds_read_b64_tr_b16 a[134:135], v210 offset:56640\n"
"v_mfma_f32_16x16x32_bf16 v[60:63], a[168:171], a[184:187], v[60:63]\n"
"ds_read_b64_tr_b16 a[136:137], v211 offset:34880\n"
"v_mfma_f32_16x16x32_bf16 v[60:63], a[172:175], a[188:191], v[60:63]\n"
"ds_read_b64_tr_b16 a[138:139], v211 offset:39232\n"
"v_mfma_f32_16x16x32_bf16 v[80:83], a[144:147], a[192:195], v[80:83]\n"
"ds_read_b64_tr_b16 a[140:141], v211 offset:52288\n"
"v_mfma_f32_16x16x32_bf16 v[80:83], a[148:151], a[196:199], v[80:83]\n"
"ds_read_b64_tr_b16 a[142:143], v211 offset:56640\n"
"v_mfma_f32_16x16x32_bf16 v[84:87], a[152:155], a[192:195], v[84:87]\n"
"buffer_load_dwordx4 a[88:91], v250, s[36:39], s54 offen offset:2048 nt\n"
"v_mfma_f32_16x16x32_bf16 v[84:87], a[156:159], a[196:199], v[84:87]\n"
"v_mfma_f32_16x16x32_bf16 v[88:91], a[160:163], a[192:195], v[88:91]\n"
"v_mfma_f32_16x16x32_bf16 v[88:91], a[164:167], a[196:199], v[88:91]\n"
"v_mfma_f32_16x16x32_bf16 v[92:95], a[168:171], a[192:195], v[92:95]\n"
"v_mfma_f32_16x16x32_bf16 v[92:95], a[172:175], a[196:199], v[92:95]\n"
"v_mfma_f32_16x16x32_bf16 v[112:115], a[144:147], a[200:203], v[112:115]\n"
"s_waitcnt lgkmcnt(0)\n"
"s_waitcnt vmcnt(20)\n"
"s_barrier\n"
"v_mfma_f32_16x16x32_bf16 v[112:115], a[148:151], a[204:207], v[112:115]\n"
"s_add_u32 m0, s49, 91392\n"
"v_mfma_f32_16x16x32_bf16 v[116:119], a[152:155], a[200:203], v[116:119]\n"
"buffer_load_dwordx4 v212, s[32:35], s52 offen lds\n"
"v_mfma_f32_16x16x32_bf16 v[116:119], a[156:159], a[204:207], v[116:119]\n"
"v_mfma_f32_16x16x32_bf16 v[120:123], a[160:163], a[200:203], v[120:123]\n"
"v_mfma_f32_16x16x32_bf16 v[120:123], a[164:167], a[204:207], v[120:123]\n"
"v_mfma_f32_16x16x32_bf16 v[124:127], a[168:171], a[200:203], v[124:127]\n"
"s_add_u32 m0, s49, 92480\n"
"v_mfma_f32_16x16x32_bf16 v[124:127], a[172:175], a[204:207], v[124:127]\n"
"buffer_load_dwordx4 v213, s[32:35], s52 offen lds\n"
"v_mfma_f32_16x16x32_bf16 v[144:147], a[144:147], a[208:211], v[144:147]\n"
"v_mfma_f32_16x16x32_bf16 v[144:147], a[148:151], a[212:215], v[144:147]\n"
"v_mfma_f32_16x16x32_bf16 v[148:151], a[152:155], a[208:211], v[148:151]\n"
"v_mfma_f32_16x16x32_bf16 v[148:151], a[156:159], a[212:215], v[148:151]\n"
"v_mfma_f32_16x16x32_bf16 v[152:155], a[160:163], a[208:211], v[152:155]\n"
"s_add_u32 m0, s49, 93568\n"
"v_mfma_f32_16x16x32_bf16 v[152:155], a[164:167], a[212:215], v[152:155]\n"
"buffer_load_dwordx4 v214, s[32:35], s52 offen lds\n"
"v_mfma_f32_16x16x32_bf16 v[156:159], a[168:171], a[208:211], v[156:159]\n"
"v_mfma_f32_16x16x32_bf16 v[156:159], a[172:175], a[212:215], v[156:159]\n"
"v_mfma_f32_16x16x32_bf16 v[176:179], a[144:147], a[216:219], v[176:179]\n"
"v_mfma_f32_16x16x32_bf16 v[176:179], a[148:151], a[220:223], v[176:179]\n"
"v_mfma_f32_16x16x32_bf16 v[180:183], a[152:155], a[216:219], v[180:183]\n"
"s_add_u32 m0, s49, 94656\n"
"v_mfma_f32_16x16x32_bf16 v[180:183], a[156:159], a[220:223], v[180:183]\n"
"buffer_load_dwordx4 v215, s[32:35], s52 offen lds\n"
"s_add_u32 s52, s52, 262144\n"
"v_mfma_f32_16x16x32_bf16 v[184:187], a[160:163], a[216:219], v[184:187]\n"
"v_mfma_f32_16x16x32_bf16 v[184:187], a[164:167], a[220:223], v[184:187]\n"
"v_mfma_f32_16x16x32_bf16 v[188:191], a[168:171], a[216:219], v[188:191]\n"
"v_mfma_f32_16x16x32_bf16 v[188:191], a[172:175], a[220:223], v[188:191]\n"
"s_add_u32 m0, s49, 108800\n"
"v_mfma_f32_16x16x32_bf16 v[192:195], a[112:115], a[224:227], v[192:195]\n"
"buffer_load_dwordx4 v212, s[32:35], s52 offen lds\n"
"v_mfma_f32_16x16x32_bf16 v[192:195], a[116:119], a[228:231], v[192:195]\n"
"v_mfma_f32_16x16x32_bf16 v[196:199], a[120:123], a[224:227], v[196:199]\n"
"v_mfma_f32_16x16x32_bf16 v[196:199], a[124:127], a[228:231], v[196:199]\n"
"v_mfma_f32_16x16x32_bf16 v[200:203], a[128:131], a[224:227], v[200:203]\n"
"v_mfma_f32_16x16x32_bf16 v[200:203], a[132:135], a[228:231], v[200:203]\n"
"s_add_u32 m0, s49, 109888\n"
"v_mfma_f32_16x16x32_bf16 v[204:207], a[136:139], a[224:227], v[204:207]\n"
"buffer_load_dwordx4 v213, s[32:35], s52 offen lds\n"
"v_mfma_f32_16x16x32_bf16 v[204:207], a[140:143], a[228:231], v[204:207]\n"
"v_mfma_f32_16x16x32_bf16 v[0:3], a[112:115], a[176:179], v[0:3]\n"
"ds_read_b128 a[224:227], v209 offset:26112\n"
"v_mfma_f32_16x16x32_bf16 v[0:3], a[116:119], a[180:183], v[0:3]\n"
"ds_read_b128 a[228:231], v209 offset:26176\n"
"v_mfma_f32_16x16x32_bf16 v[4:7], a[120:123], a[176:179], v[4:7]\n"
"v_mfma_f32_16x16x32_bf16 v[4:7], a[124:127], a[180:183], v[4:7]\n"
"s_add_u32 m0, s49, 110976\n"
"v_mfma_f32_16x16x32_bf16 v[8:11], a[128:131], a[176:179], v[8:11]\n"
"buffer_load_dwordx4 v214, s[32:35], s52 offen lds\n"
"v_mfma_f32_16x16x32_bf16 v[8:11], a[132:135], a[180:183], v[8:11]\n"
"v_mfma_f32_16x16x32_bf16 v[12:15], a[136:139], a[176:179], v[12:15]\n"
"v_mfma_f32_16x16x32_bf16 v[12:15], a[140:143], a[180:183], v[12:15]\n"
"v_mfma_f32_16x16x32_bf16 v[32:35], a[112:115], a[184:187], v[32:35]\n"
"ds_read_b128 a[176:179], v209\n"
"s_add_u32 m0, s49, 112064\n"
"v_mfma_f32_16x16x32_bf16 v[32:35], a[116:119], a[188:191], v[32:35]\n"
"ds_read_b128 a[180:183], v209 offset:64\n"
"buffer_load_dwordx4 v215, s[32:35], s52 offen lds\n"
"s_add_u32 s52, s52, 262144\n"
"v_mfma_f32_16x16x32_bf16 v[36:39], a[120:123], a[184:187], v[36:39]\n"
"v_mfma_f32_16x16x32_bf16 v[36:39], a[124:127], a[188:191], v[36:39]\n"
"v_mfma_f32_16x16x32_bf16 v[40:43], a[128:131], a[184:187], v[40:43]\n"
"v_mfma_f32_16x16x32_bf16 v[40:43], a[132:135], a[188:191], v[40:43]\n"
"v_mfma_f32_16x16x32_bf16 v[44:47], a[136:139], a[184:187], v[44:47]\n"
"s_add_u32 m0, s48, 28288\n"
"v_mfma_f32_16x16x32_bf16 v[44:47], a[140:143], a[188:191], v[44:47]\n"
"buffer_load_dwordx4 v216, s[28:31], s51 offen lds\n"
"v_mfma_f32_16x16x32_bf16 v[64:67], a[112:115], a[192:195], v[64:67]\n"
"ds_read_b128 a[184:187], v209 offset:2176\n"
"v_mfma_f32_16x16x32_bf16 v[64:67], a[116:119], a[196:199], v[64:67]\n"
"ds_read_b128 a[188:191], v209 offset:2240\n"
"v_mfma_f32_16x16x32_bf16 v[68:71], a[120:123], a[192:195], v[68:71]\n"
"v_mfma_f32_16x16x32_bf16 v[68:71], a[124:127], a[196:199], v[68:71]\n"
"v_mfma_f32_16x16x32_bf16 v[72:75], a[128:131], a[192:195], v[72:75]\n"
"v_mfma_f32_16x16x32_bf16 v[72:75], a[132:135], a[196:199], v[72:75]\n"
"s_waitcnt vmcnt(19)\n"
"s_barrier\n"
"v_mfma_f32_16x16x32_bf16 v[76:79], a[136:139], a[192:195], v[76:79]\n"
"s_add_u32 m0, s48, 29376\n"
"ds_read_b64_tr_b16 a[144:145], v210 offset:128\n"
"v_mfma_f32_16x16x32_bf16 v[76:79], a[140:143], a[196:199], v[76:79]\n"
"buffer_load_dwordx4 v217, s[28:31], s51 offen lds\n"
"ds_read_b64_tr_b16 a[146:147], v210 offset:4480\n"
"v_mfma_f32_16x16x32_bf16 v[96:99], a[112:115], a[200:203], v[96:99]\n"
"ds_read_b128 a[192:195], v209 offset:4352\n"
"ds_read_b64_tr_b16 a[148:149], v210 offset:17536\n"
"v_mfma_f32_16x16x32_bf16 v[96:99], a[116:119], a[204:207], v[96:99]\n"
"ds_read_b128 a[196:199], v209 offset:4416\n"
"s_add_u32 m0, s48, 30464\n"
"ds_read_b64_tr_b16 a[150:151], v210 offset:21888\n"
"v_mfma_f32_16x16x32_bf16 v[100:103], a[120:123], a[200:203], v[100:103]\n"
"buffer_load_dwordx4 v218, s[28:31], s51 offen lds\n"
"ds_read_b64_tr_b16 a[152:153], v211 offset:128\n"
"v_mfma_f32_16x16x32_bf16 v[100:103], a[124:127], a[204:207], v[100:103]\n"
"ds_read_b64_tr_b16 a[154:155], v211 offset:4480\n"
"v_mfma_f32_16x16x32_bf16 v[104:107], a[128:131], a[200:203], v[104:107]\n"
"ds_read_b64_tr_b16 a[156:157], v211 offset:17536\n"
"v_mfma_f32_16x16x32_bf16 v[104:107], a[132:135], a[204:207], v[104:107]\n"
"ds_read_b64_tr_b16 a[158:159], v211 offset:21888\n"
"v_mfma_f32_16x16x32_bf16 v[108:111], a[136:139], a[200:203], v[108:111]\n"
"ds_read_b64_tr_b16 a[160:161], v210 offset:192\n"
"v_mfma_f32_16x16x32_bf16 v[108:111], a[140:143], a[204:207], v[108:111]\n"
"s_add_u32 m0, s48, 31552\n"
"ds_read_b64_tr_b16 a[162:163], v210 offset:4544\n"
"v_mfma_f32_16x16x32_bf16 v[128:131], a[112:115], a[208:211], v[128:131]\n"
"ds_read_b128 a[200:203], v209 offset:6528\n"
"buffer_load_dwordx4 v219, s[28:31], s51 offen lds\n"
"ds_read_b64_tr_b16 a[164:165], v210 offset:17600\n"
"v_mfma_f32_16x16x32_bf16 v[128:131], a[116:119], a[212:215], v[128:131]\n"
"ds_read_b128 a[204:207], v209 offset:6592\n"
"ds_read_b64_tr_b16 a[166:167], v210 offset:21952\n"
"v_mfma_f32_16x16x32_bf16 v[132:135], a[120:123], a[208:211], v[132:135]\n"
"ds_read_b64_tr_b16 a[168:169], v211 offset:192\n"
"v_mfma_f32_16x16x32_bf16 v[132:135], a[124:127], a[212:215], v[132:135]\n"
"ds_read_b64_tr_b16 a[170:171], v211 offset:4544\n"
"v_mfma_f32_16x16x32_bf16 v[136:139], a[128:131], a[208:211], v[136:139]\n"
"ds_read_b64_tr_b16 a[172:173], v211 offset:17600\n"
"v_mfma_f32_16x16x32_bf16 v[136:139], a[132:135], a[212:215], v[136:139]\n"
"s_add_u32 m0, s48, 32640\n"
"ds_read_b64_tr_b16 a[174:175], v211 offset:21952\n"
"v_mfma_f32_16x16x32_bf16 v[140:143], a[136:139], a[208:211], v[140:143]\n"
"buffer_load_dwordx4 v220, s[28:31], s51 offen lds\n"
"v_mfma_f32_16x16x32_bf16 v[140:143], a[140:143], a[212:215], v[140:143]\n"
"v_mfma_f32_16x16x32_bf16 v[160:163], a[112:115], a[216:219], v[160:163]\n"
"ds_read_b128 a[208:211], v209 offset:8704\n"
"v_mfma_f32_16x16x32_bf16 v[160:163], a[116:119], a[220:223], v[160:163]\n"
"ds_read_b128 a[212:215], v209 offset:8768\n"
"v_mfma_f32_16x16x32_bf16 v[164:167], a[120:123], a[216:219], v[164:167]\n"
"s_add_u32 m0, s48, 33728\n"
"v_mfma_f32_16x16x32_bf16 v[164:167], a[124:127], a[220:223], v[164:167]\n"
"buffer_load_dwordx4 v221, s[28:31], s51 offen lds\n"
"v_mfma_f32_16x16x32_bf16 v[168:171], a[128:131], a[216:219], v[168:171]\n"
"v_mfma_f32_16x16x32_bf16 v[168:171], a[132:135], a[220:223], v[168:171]\n"
"v_mfma_f32_16x16x32_bf16 v[172:175], a[136:139], a[216:219], v[172:175]\n"
"s_waitcnt lgkmcnt(2)\n"
"v_mfma_f32_16x16x32_bf16 v[172:175], a[140:143], a[220:223], v[172:175]\n"
"v_mfma_f32_16x16x32_bf16 v[16:19], a[144:147], a[176:179], v[16:19]\n"
"ds_read_b128 a[216:219], v209 offset:10880\n"
"s_add_u32 m0, s48, 34816\n"
"v_mfma_f32_16x16x32_bf16 v[16:19], a[148:151], a[180:183], v[16:19]\n"
"ds_read_b128 a[220:223], v209 offset:10944\n"
"buffer_load_dwordx4 v222, s[28:31], s51 offen lds\n"
"s_add_u32 s51, s51, 128\n"
"v_mfma_f32_16x16x32_bf16 v[20:23], a[152:155], a[176:179], v[20:23]\n"
"ds_read_b64_tr_b16 a[112:113], v210\n"
"v_mfma_f32_16x16x32_bf16 v[20:23], a[156:159], a[180:183], v[20:23]\n"
"ds_read_b64_tr_b16 a[114:115], v210 offset:4352\n"
"v_mfma_f32_16x16x32_bf16 v[24:27], a[160:163], a[176:179], v[24:27]\n"
"ds_read_b64_tr_b16 a[116:117], v210 offset:17408\n"
"v_mfma_f32_16x16x32_bf16 v[24:27], a[164:167], a[180:183], v[24:27]\n"
"ds_read_b64_tr_b16 a[118:119], v210 offset:21760\n"
"v_mfma_f32_16x16x32_bf16 v[28:31], a[168:171], a[176:179], v[28:31]\n"
"ds_read_b64_tr_b16 a[120:121], v211\n"
"v_mfma_f32_16x16x32_bf16 v[28:31], a[172:175], a[180:183], v[28:31]\n"
"ds_read_b64_tr_b16 a[122:123], v211 offset:4352\n"
"buffer_load_dwordx4 a[92:95], v250, s[36:39], s54 offen offset:3072 nt\n"
"s_add_u32 s53, s53, 8192\n"
"s_and_b32 s53, s53, 258047\n"
"s_lshl_b32 s38, s38, 1\n"
"s_add_u32 s54, s53, 4096\n"
"v_mfma_f32_16x16x32_bf16 v[48:51], a[144:147], a[184:187], v[48:51]\n"
"ds_read_b64_tr_b16 a[124:125], v211 offset:17408\n"
"v_mfma_f32_16x16x32_bf16 v[48:51], a[148:151], a[188:191], v[48:51]\n"
"ds_read_b64_tr_b16 a[126:127], v211 offset:21760\n"
"v_mfma_f32_16x16x32_bf16 v[52:55], a[152:155], a[184:187], v[52:55]\n"
"ds_read_b64_tr_b16 a[128:129], v210 offset:64\n"
"v_mfma_f32_16x16x32_bf16 v[52:55], a[156:159], a[188:191], v[52:55]\n"
"ds_read_b64_tr_b16 a[130:131], v210 offset:4416\n"
"v_mfma_f32_16x16x32_bf16 v[56:59], a[160:163], a[184:187], v[56:59]\n"
"ds_read_b64_tr_b16 a[132:133], v210 offset:17472\n"
"buffer_load_dwordx4 a[96:99], v250, s[36:39], s53 offen nt\n"
"v_mfma_f32_16x16x32_bf16 v[56:59], a[164:167], a[188:191], v[56:59]\n"
"ds_read_b64_tr_b16 a[134:135], v210 offset:21824\n"
"v_mfma_f32_16x16x32_bf16 v[60:63], a[168:171], a[184:187], v[60:63]\n"
"ds_read_b64_tr_b16 a[136:137], v211 offset:64\n"
"v_mfma_f32_16x16x32_bf16 v[60:63], a[172:175], a[188:191], v[60:63]\n"
"ds_read_b64_tr_b16 a[138:139], v211 offset:4416\n"
"v_mfma_f32_16x16x32_bf16 v[80:83], a[144:147], a[192:195], v[80:83]\n"
"ds_read_b64_tr_b16 a[140:141], v211 offset:17472\n"
"v_mfma_f32_16x16x32_bf16 v[80:83], a[148:151], a[196:199], v[80:83]\n"
"ds_read_b64_tr_b16 a[142:143], v211 offset:21824\n"
"v_mfma_f32_16x16x32_bf16 v[84:87], a[152:155], a[192:195], v[84:87]\n"
"buffer_load_dwordx4 a[100:103], v250, s[36:39], s53 offen offset:1024 nt\n"
"v_mfma_f32_16x16x32_bf16 v[84:87], a[156:159], a[196:199], v[84:87]\n"
"v_mfma_f32_16x16x32_bf16 v[88:91], a[160:163], a[192:195], v[88:91]\n"
"v_mfma_f32_16x16x32_bf16 v[88:91], a[164:167], a[196:199], v[88:91]\n"
"v_mfma_f32_16x16x32_bf16 v[92:95], a[168:171], a[192:195], v[92:95]\n"
"v_mfma_f32_16x16x32_bf16 v[92:95], a[172:175], a[196:199], v[92:95]\n"
"v_mfma_f32_16x16x32_bf16 v[112:115], a[144:147], a[200:203], v[112:115]\n"
"s_waitcnt lgkmcnt(0)\n"
"s_waitcnt vmcnt(21)\n"
"s_barrier\n"
"v_mfma_f32_16x16x32_bf16 v[112:115], a[148:151], a[204:207], v[112:115]\n"
"s_add_u32 m0, s49, 56576\n"
"v_mfma_f32_16x16x32_bf16 v[116:119], a[152:155], a[200:203], v[116:119]\n"
"buffer_load_dwordx4 v212, s[32:35], s52 offen lds\n"
"v_mfma_f32_16x16x32_bf16 v[116:119], a[156:159], a[204:207], v[116:119]\n"
"v_mfma_f32_16x16x32_bf16 v[120:123], a[160:163], a[200:203], v[120:123]\n"
"v_mfma_f32_16x16x32_bf16 v[120:123], a[164:167], a[204:207], v[120:123]\n"
"v_mfma_f32_16x16x32_bf16 v[124:127], a[168:171], a[200:203], v[124:127]\n"
"s_add_u32 m0, s49, 57664\n"
"v_mfma_f32_16x16x32_bf16 v[124:127], a[172:175], a[204:207], v[124:127]\n"
"buffer_load_dwordx4 v213, s[32:35], s52 offen lds\n"
"v_mfma_f32_16x16x32_bf16 v[144:147], a[144:147], a[208:211], v[144:147]\n"
"v_mfma_f32_16x16x32_bf16 v[144:147], a[148:151], a[212:215], v[144:147]\n"
"v_mfma_f32_16x16x32_bf16 v[148:151], a[152:155], a[208:211], v[148:151]\n"
"v_mfma_f32_16x16x32_bf16 v[148:151], a[156:159], a[212:215], v[148:151]\n"
"v_mfma_f32_16x16x32_bf16 v[152:155], a[160:163], a[208:211], v[152:155]\n"
"s_add_u32 m0, s49, 58752\n"
"v_mfma_f32_16x16x32_bf16 v[152:155], a[164:167], a[212:215], v[152:155]\n"
"buffer_load_dwordx4 v214, s[32:35], s52 offen lds\n"
"v_mfma_f32_16x16x32_bf16 v[156:159], a[168:171], a[208:211], v[156:159]\n"
"v_mfma_f32_16x16x32_bf16 v[156:159], a[172:175], a[212:215], v[156:159]\n"
"v_mfma_f32_16x16x32_bf16 v[176:179], a[144:147], a[216:219], v[176:179]\n"
"v_mfma_f32_16x16x32_bf16 v[176:179], a[148:151], a[220:223], v[176:179]\n"
"v_mfma_f32_16x16x32_bf16 v[180:183], a[152:155], a[216:219], v[180:183]\n"
"s_add_u32 m0, s49, 59840\n"
"v_mfma_f32_16x16x32_bf16 v[180:183], a[156:159], a[220:223], v[180:183]\n"
"buffer_load_dwordx4 v215, s[32:35], s52 offen lds\n"
"s_add_u32 s52, s52, 262144\n"
"v_mfma_f32_16x16x32_bf16 v[184:187], a[160:163], a[216:219], v[184:187]\n"
"v_mfma_f32_16x16x32_bf16 v[184:187], a[164:167], a[220:223], v[184:187]\n"
"v_mfma_f32_16x16x32_bf16 v[188:191], a[168:171], a[216:219], v[188:191]\n"
"v_mfma_f32_16x16x32_bf16 v[188:191], a[172:175], a[220:223], v[188:191]\n"
"s_add_u32 m0, s49, 73984\n"
"v_mfma_f32_16x16x32_bf16 v[192:195], a[112:115], a[224:227], v[192:195]\n"
"buffer_load_dwordx4 v212, s[32:35], s52 offen lds\n"
"v_mfma_f32_16x16x32_bf16 v[192:195], a[116:119], a[228:231], v[192:195]\n"
"v_mfma_f32_16x16x32_bf16 v[196:199], a[120:123], a[224:227], v[196:199]\n"
"v_mfma_f32_16x16x32_bf16 v[196:199], a[124:127], a[228:231], v[196:199]\n"
"v_mfma_f32_16x16x32_bf16 v[200:203], a[128:131], a[224:227], v[200:203]\n"
"v_mfma_f32_16x16x32_bf16 v[200:203], a[132:135], a[228:231], v[200:203]\n"
"s_add_u32 m0, s49, 75072\n"
"v_mfma_f32_16x16x32_bf16 v[204:207], a[136:139], a[224:227], v[204:207]\n"
"buffer_load_dwordx4 v213, s[32:35], s52 offen lds\n"
"v_mfma_f32_16x16x32_bf16 v[204:207], a[140:143], a[228:231], v[204:207]\n"
"v_mfma_f32_16x16x32_bf16 v[0:3], a[112:115], a[176:179], v[0:3]\n"
"ds_read_b128 a[224:227], v208 offset:26112\n"
"v_mfma_f32_16x16x32_bf16 v[0:3], a[116:119], a[180:183], v[0:3]\n"
"ds_read_b128 a[228:231], v208 offset:26176\n"
"v_mfma_f32_16x16x32_bf16 v[4:7], a[120:123], a[176:179], v[4:7]\n"
"v_mfma_f32_16x16x32_bf16 v[4:7], a[124:127], a[180:183], v[4:7]\n"
"s_add_u32 m0, s49, 76160\n"
"v_mfma_f32_16x16x32_bf16 v[8:11], a[128:131], a[176:179], v[8:11]\n"
"buffer_load_dwordx4 v214, s[32:35], s52 offen lds\n"
"v_mfma_f32_16x16x32_bf16 v[8:11], a[132:135], a[180:183], v[8:11]\n"
"v_mfma_f32_16x16x32_bf16 v[12:15], a[136:139], a[176:179], v[12:15]\n"
"v_mfma_f32_16x16x32_bf16 v[12:15], a[140:143], a[180:183], v[12:15]\n"
"v_mfma_f32_16x16x32_bf16 v[32:35], a[112:115], a[184:187], v[32:35]\n"
"ds_read_b128 a[176:179], v208\n"
"s_add_u32 m0, s49, 77248\n"
"v_mfma_f32_16x16x32_bf16 v[32:35], a[116:119], a[188:191], v[32:35]\n"
"ds_read_b128 a[180:183], v208 offset:64\n"
"buffer_load_dwordx4 v215, s[32:35], s52 offen lds\n"
"s_add_u32 s52, s52, 262144\n"
"v_mfma_f32_16x16x32_bf16 v[36:39], a[120:123], a[184:187], v[36:39]\n"
"v_mfma_f32_16x16x32_bf16 v[36:39], a[124:127], a[188:191], v[36:39]\n"
"v_mfma_f32_16x16x32_bf16 v[40:43], a[128:131], a[184:187], v[40:43]\n"
"v_mfma_f32_16x16x32_bf16 v[40:43], a[132:135], a[188:191], v[40:43]\n"
"v_mfma_f32_16x16x32_bf16 v[44:47], a[136:139], a[184:187], v[44:47]\n"
"s_add_u32 m0, s48, 126208\n"
"v_mfma_f32_16x16x32_bf16 v[44:47], a[140:143], a[188:191], v[44:47]\n"
"buffer_load_dwordx4 v216, s[28:31], s51 offen lds\n"
"v_mfma_f32_16x16x32_bf16 v[64:67], a[112:115], a[192:195], v[64:67]\n"
"ds_read_b128 a[184:187], v208 offset:2176\n"
"v_mfma_f32_16x16x32_bf16 v[64:67], a[116:119], a[196:199], v[64:67]\n"
"ds_read_b128 a[188:191], v208 offset:2240\n"
"v_mfma_f32_16x16x32_bf16 v[68:71], a[120:123], a[192:195], v[68:71]\n"
"v_mfma_f32_16x16x32_bf16 v[68:71], a[124:127], a[196:199], v[68:71]\n"
"v_mfma_f32_16x16x32_bf16 v[72:75], a[128:131], a[192:195], v[72:75]\n"
"v_mfma_f32_16x16x32_bf16 v[72:75], a[132:135], a[196:199], v[72:75]\n"
"s_waitcnt vmcnt(19)\n"
"s_barrier\n"
"v_mfma_f32_16x16x32_bf16 v[76:79], a[136:139], a[192:195], v[76:79]\n"
"s_add_u32 m0, s48, 127296\n"
"ds_read_b64_tr_b16 a[144:145], v210 offset:34944\n"
"v_mfma_f32_16x16x32_bf16 v[76:79], a[140:143], a[196:199], v[76:79]\n"
"buffer_load_dwordx4 v217, s[28:31], s51 offen lds\n"
"ds_read_b64_tr_b16 a[146:147], v210 offset:39296\n"
"v_mfma_f32_16x16x32_bf16 v[96:99], a[112:115], a[200:203], v[96:99]\n"
"ds_read_b128 a[192:195], v208 offset:4352\n"
"ds_read_b64_tr_b16 a[148:149], v210 offset:52352\n"
"v_mfma_f32_16x16x32_bf16 v[96:99], a[116:119], a[204:207], v[96:99]\n"
"ds_read_b128 a[196:199], v208 offset:4416\n"
"s_add_u32 m0, s48, 128384\n"
"ds_read_b64_tr_b16 a[150:151], v210 offset:56704\n"
"v_mfma_f32_16x16x32_bf16 v[100:103], a[120:123], a[200:203], v[100:103]\n"
"buffer_load_dwordx4 v218, s[28:31], s51 offen lds\n"
"ds_read_b64_tr_b16 a[152:153], v211 offset:34944\n"
"v_mfma_f32_16x16x32_bf16 v[100:103], a[124:127], a[204:207], v[100:103]\n"
"ds_read_b64_tr_b16 a[154:155], v211 offset:39296\n"
"v_mfma_f32_16x16x32_bf16 v[104:107], a[128:131], a[200:203], v[104:107]\n"
"ds_read_b64_tr_b16 a[156:157], v211 offset:52352\n"
"v_mfma_f32_16x16x32_bf16 v[104:107], a[132:135], a[204:207], v[104:107]\n"
"ds_read_b64_tr_b16 a[158:159], v211 offset:56704\n"
"v_mfma_f32_16x16x32_bf16 v[108:111], a[136:139], a[200:203], v[108:111]\n"
"ds_read_b64_tr_b16 a[160:161], v210 offset:35008\n"
"v_mfma_f32_16x16x32_bf16 v[108:111], a[140:143], a[204:207], v[108:111]\n"
"s_add_u32 m0, s48, 129472\n"
"ds_read_b64_tr_b16 a[162:163], v210 offset:39360\n"
"v_mfma_f32_16x16x32_bf16 v[128:131], a[112:115], a[208:211], v[128:131]\n"
"ds_read_b128 a[200:203], v208 offset:6528\n"
"buffer_load_dwordx4 v219, s[28:31], s51 offen lds\n"
"ds_read_b64_tr_b16 a[164:165], v210 offset:52416\n"
"v_mfma_f32_16x16x32_bf16 v[128:131], a[116:119], a[212:215], v[128:131]\n"
"ds_read_b128 a[204:207], v208 offset:6592\n"
"ds_read_b64_tr_b16 a[166:167], v210 offset:56768\n"
"v_mfma_f32_16x16x32_bf16 v[132:135], a[120:123], a[208:211], v[132:135]\n"
"ds_read_b64_tr_b16 a[168:169], v211 offset:35008\n"
"v_mfma_f32_16x16x32_bf16 v[132:135], a[124:127], a[212:215], v[132:135]\n"
"ds_read_b64_tr_b16 a[170:171], v211 offset:39360\n"
"v_mfma_f32_16x16x32_bf16 v[136:139], a[128:131], a[208:211], v[136:139]\n"
"ds_read_b64_tr_b16 a[172:173], v211 offset:52416\n"
"v_mfma_f32_16x16x32_bf16 v[136:139], a[132:135], a[212:215], v[136:139]\n"
"s_add_u32 m0, s48, 130560\n"
"ds_read_b64_tr_b16 a[174:175], v211 offset:56768\n"
"v_mfma_f32_16x16x32_bf16 v[140:143], a[136:139], a[208:211], v[140:143]\n"
"buffer_load_dwordx4 v220, s[28:31], s51 offen lds\n"
"v_mfma_f32_16x16x32_bf16 v[140:143], a[140:143], a[212:215], v[140:143]\n"
"v_mfma_f32_16x16x32_bf16 v[160:163], a[112:115], a[216:219], v[160:163]\n"
"ds_read_b128 a[208:211], v208 offset:8704\n"
"v_mfma_f32_16x16x32_bf16 v[160:163], a[116:119], a[220:223], v[160:163]\n"
"ds_read_b128 a[212:215], v208 offset:8768\n"
"v_mfma_f32_16x16x32_bf16 v[164:167], a[120:123], a[216:219], v[164:167]\n"
"s_add_u32 m0, s48, 131648\n"
"v_mfma_f32_16x16x32_bf16 v[164:167], a[124:127], a[220:223], v[164:167]\n"
"buffer_load_dwordx4 v221, s[28:31], s51 offen lds\n"
"global_load_dword v223, v255, s[100:101]\n"
"v_mfma_f32_16x16x32_bf16 v[168:171], a[128:131], a[216:219], v[168:171]\n"
"v_mfma_f32_16x16x32_bf16 v[168:171], a[132:135], a[220:223], v[168:171]\n"
"v_mfma_f32_16x16x32_bf16 v[172:175], a[136:139], a[216:219], v[172:175]\n"
"s_waitcnt lgkmcnt(2)\n"
"v_mfma_f32_16x16x32_bf16 v[172:175], a[140:143], a[220:223], v[172:175]\n"
"v_mfma_f32_16x16x32_bf16 v[16:19], a[144:147], a[176:179], v[16:19]\n"
"ds_read_b128 a[216:219], v208 offset:10880\n"
"s_add_u32 m0, s48, 132736\n"
"v_mfma_f32_16x16x32_bf16 v[16:19], a[148:151], a[180:183], v[16:19]\n"
"ds_read_b128 a[220:223], v208 offset:10944\n"
"buffer_load_dwordx4 v222, s[28:31], s51 offen lds\n"
"s_add_u32 s51, s51, 128\n"
"v_mfma_f32_16x16x32_bf16 v[20:23], a[152:155], a[176:179], v[20:23]\n"
"ds_read_b64_tr_b16 a[112:113], v210 offset:34816\n"
"v_mfma_f32_16x16x32_bf16 v[20:23], a[156:159], a[180:183], v[20:23]\n"
"ds_read_b64_tr_b16 a[114:115], v210 offset:39168\n"
"v_mfma_f32_16x16x32_bf16 v[24:27], a[160:163], a[176:179], v[24:27]\n"
"ds_read_b64_tr_b16 a[116:117], v210 offset:52224\n"
"v_mfma_f32_16x16x32_bf16 v[24:27], a[164:167], a[180:183], v[24:27]\n"
"ds_read_b64_tr_b16 a[118:119], v210 offset:56576\n"
"v_mfma_f32_16x16x32_bf16 v[28:31], a[168:171], a[176:179], v[28:31]\n"
"ds_read_b64_tr_b16 a[120:121], v211 offset:34816\n"
"v_mfma_f32_16x16x32_bf16 v[28:31], a[172:175], a[180:183], v[28:31]\n"
"ds_read_b64_tr_b16 a[122:123], v211 offset:39168\n"
"buffer_load_dwordx4 a[104:107], v250, s[36:39], s53 offen offset:2048 nt\n"
"v_mfma_f32_16x16x32_bf16 v[48:51], a[144:147], a[184:187], v[48:51]\n"
"ds_read_b64_tr_b16 a[124:125], v211 offset:52224\n"
"v_mfma_f32_16x16x32_bf16 v[48:51], a[148:151], a[188:191], v[48:51]\n"
"ds_read_b64_tr_b16 a[126:127], v211 offset:56576\n"
"v_mfma_f32_16x16x32_bf16 v[52:55], a[152:155], a[184:187], v[52:55]\n"
"ds_read_b64_tr_b16 a[128:129], v210 offset:34880\n"
"v_mfma_f32_16x16x32_bf16 v[52:55], a[156:159], a[188:191], v[52:55]\n"
"ds_read_b64_tr_b16 a[130:131], v210 offset:39232\n"
"v_mfma_f32_16x16x32_bf16 v[56:59], a[160:163], a[184:187], v[56:59]\n"
"ds_read_b64_tr_b16 a[132:133], v210 offset:52288\n"
"buffer_load_dwordx4 a[108:111], v250, s[36:39], s53 offen offset:3072 nt\n"
"v_mfma_f32_16x16x32_bf16 v[56:59], a[164:167], a[188:191], v[56:59]\n"
"ds_read_b64_tr_b16 a[134:135], v210 offset:56640\n"
"v_mfma_f32_16x16x32_bf16 v[60:63], a[168:171], a[184:187], v[60:63]\n"
"ds_read_b64_tr_b16 a[136:137], v211 offset:34880\n"
"v_mfma_f32_16x16x32_bf16 v[60:63], a[172:175], a[188:191], v[60:63]\n"
"ds_read_b64_tr_b16 a[138:139], v211 offset:39232\n"
"v_mfma_f32_16x16x32_bf16 v[80:83], a[144:147], a[192:195], v[80:83]\n"
"ds_read_b64_tr_b16 a[140:141], v211 offset:52288\n"
"v_mfma_f32_16x16x32_bf16 v[80:83], a[148:151], a[196:199], v[80:83]\n"
"ds_read_b64_tr_b16 a[142:143], v211 offset:56640\n"
"v_mfma_f32_16x16x32_bf16 v[84:87], a[152:155], a[192:195], v[84:87]\n"
"buffer_load_dwordx4 a[240:243], v250, s[36:39], s54 offen nt\n"
"v_mfma_f32_16x16x32_bf16 v[84:87], a[156:159], a[196:199], v[84:87]\n"
"v_mfma_f32_16x16x32_bf16 v[88:91], a[160:163], a[192:195], v[88:91]\n"
"v_mfma_f32_16x16x32_bf16 v[88:91], a[164:167], a[196:199], v[88:91]\n"
"v_mfma_f32_16x16x32_bf16 v[92:95], a[168:171], a[192:195], v[92:95]\n"
"v_mfma_f32_16x16x32_bf16 v[92:95], a[172:175], a[196:199], v[92:95]\n"
"v_mfma_f32_16x16x32_bf16 v[112:115], a[144:147], a[200:203], v[112:115]\n"
"s_waitcnt lgkmcnt(0)\n"
"s_waitcnt vmcnt(22)\n"
"s_barrier\n"
"v_mfma_f32_16x16x32_bf16 v[112:115], a[148:151], a[204:207], v[112:115]\n"
"s_add_u32 m0, s49, 91392\n"
"v_mfma_f32_16x16x32_bf16 v[116:119], a[152:155], a[200:203], v[116:119]\n"
"buffer_load_dwordx4 v212, s[32:35], s52 offen lds\n"
"v_mfma_f32_16x16x32_bf16 v[116:119], a[156:159], a[204:207], v[116:119]\n"
"v_mfma_f32_16x16x32_bf16 v[120:123], a[160:163], a[200:203], v[120:123]\n"
"v_mfma_f32_16x16x32_bf16 v[120:123], a[164:167], a[204:207], v[120:123]\n"
"v_mfma_f32_16x16x32_bf16 v[124:127], a[168:171], a[200:203], v[124:127]\n"
"v_mfma_f32_16x16x32_bf16 v[124:127], a[172:175], a[204:207], v[124:127]\n"
"s_add_u32 m0, s49, 92480\n"
"v_mfma_f32_16x16x32_bf16 v[144:147], a[144:147], a[208:211], v[144:147]\n"
"buffer_load_dwordx4 v213, s[32:35], s52 offen lds\n"
"v_mfma_f32_16x16x32_bf16 v[144:147], a[148:151], a[212:215], v[144:147]\n"
"v_mfma_f32_16x16x32_bf16 v[148:151], a[152:155], a[208:211], v[148:151]\n"
"v_mfma_f32_16x16x32_bf16 v[148:151], a[156:159], a[212:215], v[148:151]\n"
"v_mfma_f32_16x16x32_bf16 v[152:155], a[160:163], a[208:211], v[152:155]\n"
"v_mfma_f32_16x16x32_bf16 v[152:155], a[164:167], a[212:215], v[152:155]\n"
"s_add_u32 m0, s49, 93568\n"
"v_mfma_f32_16x16x32_bf16 v[156:159], a[168:171], a[208:211], v[156:159]\n"
"buffer_load_dwordx4 v214, s[32:35], s52 offen lds\n"
"v_mfma_f32_16x16x32_bf16 v[156:159], a[172:175], a[212:215], v[156:159]\n"
"v_mfma_f32_16x16x32_bf16 v[176:179], a[144:147], a[216:219], v[176:179]\n"
"v_mfma_f32_16x16x32_bf16 v[176:179], a[148:151], a[220:223], v[176:179]\n"
"v_mfma_f32_16x16x32_bf16 v[180:183], a[152:155], a[216:219], v[180:183]\n"
"v_mfma_f32_16x16x32_bf16 v[180:183], a[156:159], a[220:223], v[180:183]\n"
"s_add_u32 m0, s49, 94656\n"
"v_mfma_f32_16x16x32_bf16 v[184:187], a[160:163], a[216:219], v[184:187]\n"
"buffer_load_dwordx4 v215, s[32:35], s52 offen lds\n"
"s_add_u32 s52, s52, 262144\n"
"v_mfma_f32_16x16x32_bf16 v[184:187], a[164:167], a[220:223], v[184:187]\n"
"v_mfma_f32_16x16x32_bf16 v[188:191], a[168:171], a[216:219], v[188:191]\n"
"v_mfma_f32_16x16x32_bf16 v[188:191], a[172:175], a[220:223], v[188:191]\n"
"v_mfma_f32_16x16x32_bf16 v[192:195], a[112:115], a[224:227], v[192:195]\n"
"v_mfma_f32_16x16x32_bf16 v[192:195], a[116:119], a[228:231], v[192:195]\n"
"s_add_u32 m0, s49, 108800\n"
"v_mfma_f32_16x16x32_bf16 v[196:199], a[120:123], a[224:227], v[196:199]\n"
"buffer_load_dwordx4 v212, s[32:35], s52 offen lds\n"
"v_mfma_f32_16x16x32_bf16 v[196:199], a[124:127], a[228:231], v[196:199]\n"
"v_mfma_f32_16x16x32_bf16 v[200:203], a[128:131], a[224:227], v[200:203]\n"
"v_mfma_f32_16x16x32_bf16 v[200:203], a[132:135], a[228:231], v[200:203]\n"
"v_mfma_f32_16x16x32_bf16 v[204:207], a[136:139], a[224:227], v[204:207]\n"
"v_mfma_f32_16x16x32_bf16 v[204:207], a[140:143], a[228:231], v[204:207]\n"
"s_add_u32 m0, s49, 109888\n"
"v_mfma_f32_16x16x32_bf16 v[0:3], a[112:115], a[176:179], v[0:3]\n"
"ds_read_b128 a[224:227], v208 offset:54400\n"
"buffer_load_dwordx4 v213, s[32:35], s52 offen lds\n"
"v_mfma_f32_16x16x32_bf16 v[0:3], a[116:119], a[180:183], v[0:3]\n"
"ds_read_b128 a[228:231], v208 offset:54464\n"
"v_mfma_f32_16x16x32_bf16 v[4:7], a[120:123], a[176:179], v[4:7]\n"
"v_mfma_f32_16x16x32_bf16 v[4:7], a[124:127], a[180:183], v[4:7]\n"
"v_mfma_f32_16x16x32_bf16 v[8:11], a[128:131], a[176:179], v[8:11]\n"
"v_mfma_f32_16x16x32_bf16 v[8:11], a[132:135], a[180:183], v[8:11]\n"
"s_add_u32 m0, s49, 110976\n"
"v_mfma_f32_16x16x32_bf16 v[12:15], a[136:139], a[176:179], v[12:15]\n"
"buffer_load_dwordx4 v214, s[32:35], s52 offen lds\n"
"v_mfma_f32_16x16x32_bf16 v[12:15], a[140:143], a[180:183], v[12:15]\n"
"v_mfma_f32_16x16x32_bf16 v[32:35], a[112:115], a[184:187], v[32:35]\n"
"ds_read_b128 a[176:179], v208 offset:28288\n"
"v_mfma_f32_16x16x32_bf16 v[32:35], a[116:119], a[188:191], v[32:35]\n"
"ds_read_b128 a[180:183], v208 offset:28352\n"
"v_mfma_f32_16x16x32_bf16 v[36:39], a[120:123], a[184:187], v[36:39]\n"
"v_mfma_f32_16x16x32_bf16 v[36:39], a[124:127], a[188:191], v[36:39]\n"
"s_add_u32 m0, s49, 112064\n"
"v_mfma_f32_16x16x32_bf16 v[40:43], a[128:131], a[184:187], v[40:43]\n"
"buffer_load_dwordx4 v215, s[32:35], s52 offen lds\n"
"s_add_u32 s52, s52, 262144\n"
"v_mfma_f32_16x16x32_bf16 v[40:43], a[132:135], a[188:191], v[40:43]\n"
"v_mfma_f32_16x16x32_bf16 v[44:47], a[136:139], a[184:187], v[44:47]\n"
"v_mfma_f32_16x16x32_bf16 v[44:47], a[140:143], a[188:191], v[44:47]\n"
"v_mfma_f32_16x16x32_bf16 v[64:67], a[112:115], a[192:195], v[64:67]\n"
"ds_read_b128 a[184:187], v208 offset:30464\n"
"v_mfma_f32_16x16x32_bf16 v[64:67], a[116:119], a[196:199], v[64:67]\n"
"ds_read_b128 a[188:191], v208 offset:30528\n"
"v_mfma_f32_16x16x32_bf16 v[68:71], a[120:123], a[192:195], v[68:71]\n"
"buffer_load_dwordx4 a[244:247], v250, s[36:39], s54 offen offset:1024 nt\n"
"v_mfma_f32_16x16x32_bf16 v[68:71], a[124:127], a[196:199], v[68:71]\n"
"v_mfma_f32_16x16x32_bf16 v[72:75], a[128:131], a[192:195], v[72:75]\n"
"v_mfma_f32_16x16x32_bf16 v[72:75], a[132:135], a[196:199], v[72:75]\n"
"s_waitcnt vmcnt(20)\n"
"s_barrier\n"
"v_mfma_f32_16x16x32_bf16 v[76:79], a[136:139], a[192:195], v[76:79]\n"
"ds_read_b64_tr_b16 a[144:145], v210 offset:128\n"
"v_mfma_f32_16x16x32_bf16 v[76:79], a[140:143], a[196:199], v[76:79]\n"
"ds_read_b64_tr_b16 a[146:147], v210 offset:4480\n"
"v_mfma_f32_16x16x32_bf16 v[96:99], a[112:115], a[200:203], v[96:99]\n"
"ds_read_b128 a[192:195], v208 offset:32640\n"
"buffer_load_dwordx4 a[248:251], v250, s[36:39], s54 offen offset:2048 nt\n"
"ds_read_b64_tr_b16 a[148:149], v210 offset:17536\n"
"v_mfma_f32_16x16x32_bf16 v[96:99], a[116:119], a[204:207], v[96:99]\n"
"ds_read_b128 a[196:199], v208 offset:32704\n"
"ds_read_b64_tr_b16 a[150:151], v210 offset:21888\n"
"v_mfma_f32_16x16x32_bf16 v[100:103], a[120:123], a[200:203], v[100:103]\n"
"ds_read_b64_tr_b16 a[152:153], v211 offset:128\n"
"v_mfma_f32_16x16x32_bf16 v[100:103], a[124:127], a[204:207], v[100:103]\n"
"ds_read_b64_tr_b16 a[154:155], v211 offset:4480\n"
"v_mfma_f32_16x16x32_bf16 v[104:107], a[128:131], a[200:203], v[104:107]\n"
"ds_read_b64_tr_b16 a[156:157], v211 offset:17536\n"
"v_mfma_f32_16x16x32_bf16 v[104:107], a[132:135], a[204:207], v[104:107]\n"
"ds_read_b64_tr_b16 a[158:159], v211 offset:21888\n"
"v_mfma_f32_16x16x32_bf16 v[108:111], a[136:139], a[200:203], v[108:111]\n"
"buffer_load_dwordx4 a[252:255], v250, s[36:39], s54 offen offset:3072 nt\n"
"s_add_u32 s53, s53, 8192\n"
"s_and_b32 s53, s53, 258047\n"
"s_lshl_b32 s38, s38, 1\n"
"s_add_u32 s54, s53, 4096\n"
"ds_read_b64_tr_b16 a[160:161], v210 offset:192\n"
"v_mfma_f32_16x16x32_bf16 v[108:111], a[140:143], a[204:207], v[108:111]\n"
"ds_read_b64_tr_b16 a[162:163], v210 offset:4544\n"
"v_mfma_f32_16x16x32_bf16 v[128:131], a[112:115], a[208:211], v[128:131]\n"
"ds_read_b128 a[200:203], v208 offset:34816\n"
"ds_read_b64_tr_b16 a[164:165], v210 offset:17600\n"
"v_mfma_f32_16x16x32_bf16 v[128:131], a[116:119], a[212:215], v[128:131]\n"
"ds_read_b128 a[204:207], v208 offset:34880\n"
"ds_read_b64_tr_b16 a[166:167], v210 offset:21952\n"
"v_mfma_f32_16x16x32_bf16 v[132:135], a[120:123], a[208:211], v[132:135]\n"
"ds_read_b64_tr_b16 a[168:169], v211 offset:192\n"
"v_mfma_f32_16x16x32_bf16 v[132:135], a[124:127], a[212:215], v[132:135]\n"
"ds_read_b64_tr_b16 a[170:171], v211 offset:4544\n"
"v_mfma_f32_16x16x32_bf16 v[136:139], a[128:131], a[208:211], v[136:139]\n"
"ds_read_b64_tr_b16 a[172:173], v211 offset:17600\n"
"v_mfma_f32_16x16x32_bf16 v[136:139], a[132:135], a[212:215], v[136:139]\n"
"ds_read_b64_tr_b16 a[174:175], v211 offset:21952\n"
"v_mfma_f32_16x16x32_bf16 v[140:143], a[136:139], a[208:211], v[140:143]\n"
"v_mfma_f32_16x16x32_bf16 v[140:143], a[140:143], a[212:215], v[140:143]\n"
"v_mfma_f32_16x16x32_bf16 v[160:163], a[112:115], a[216:219], v[160:163]\n"
"ds_read_b128 a[208:211], v208 offset:36992\n"
"v_mfma_f32_16x16x32_bf16 v[160:163], a[116:119], a[220:223], v[160:163]\n"
"ds_read_b128 a[212:215], v208 offset:37056\n"
"v_mfma_f32_16x16x32_bf16 v[164:167], a[120:123], a[216:219], v[164:167]\n"
"v_mfma_f32_16x16x32_bf16 v[164:167], a[124:127], a[220:223], v[164:167]\n"
"v_mfma_f32_16x16x32_bf16 v[168:171], a[128:131], a[216:219], v[168:171]\n"
"v_mfma_f32_16x16x32_bf16 v[168:171], a[132:135], a[220:223], v[168:171]\n"
"v_mfma_f32_16x16x32_bf16 v[172:175], a[136:139], a[216:219], v[172:175]\n"
"s_waitcnt lgkmcnt(2)\n"
"v_mfma_f32_16x16x32_bf16 v[172:175], a[140:143], a[220:223], v[172:175]\n"
"v_mfma_f32_16x16x32_bf16 v[16:19], a[144:147], a[176:179], v[16:19]\n"
"ds_read_b128 a[216:219], v208 offset:39168\n"
"v_mfma_f32_16x16x32_bf16 v[16:19], a[148:151], a[180:183], v[16:19]\n"
"ds_read_b128 a[220:223], v208 offset:39232\n"
"v_mfma_f32_16x16x32_bf16 v[20:23], a[152:155], a[176:179], v[20:23]\n"
"ds_read_b64_tr_b16 a[112:113], v210\n"
"v_mfma_f32_16x16x32_bf16 v[20:23], a[156:159], a[180:183], v[20:23]\n"
"ds_read_b64_tr_b16 a[114:115], v210 offset:4352\n"
"v_mfma_f32_16x16x32_bf16 v[24:27], a[160:163], a[176:179], v[24:27]\n"
"ds_read_b64_tr_b16 a[116:117], v210 offset:17408\n"
"v_mfma_f32_16x16x32_bf16 v[24:27], a[164:167], a[180:183], v[24:27]\n"
"ds_read_b64_tr_b16 a[118:119], v210 offset:21760\n"
"v_mfma_f32_16x16x32_bf16 v[28:31], a[168:171], a[176:179], v[28:31]\n"
"ds_read_b64_tr_b16 a[120:121], v211\n"
"v_mfma_f32_16x16x32_bf16 v[28:31], a[172:175], a[180:183], v[28:31]\n"
"ds_read_b64_tr_b16 a[122:123], v211 offset:4352\n"
"v_mfma_f32_16x16x32_bf16 v[48:51], a[144:147], a[184:187], v[48:51]\n"
"ds_read_b64_tr_b16 a[124:125], v211 offset:17408\n"
"v_mfma_f32_16x16x32_bf16 v[48:51], a[148:151], a[188:191], v[48:51]\n"
"ds_read_b64_tr_b16 a[126:127], v211 offset:21760\n"
"v_mfma_f32_16x16x32_bf16 v[52:55], a[152:155], a[184:187], v[52:55]\n"
"ds_read_b64_tr_b16 a[128:129], v210 offset:64\n"
"v_mfma_f32_16x16x32_bf16 v[52:55], a[156:159], a[188:191], v[52:55]\n"
"ds_read_b64_tr_b16 a[130:131], v210 offset:4416\n"
"v_mfma_f32_16x16x32_bf16 v[56:59], a[160:163], a[184:187], v[56:59]\n"
"ds_read_b64_tr_b16 a[132:133], v210 offset:17472\n"
"v_mfma_f32_16x16x32_bf16 v[56:59], a[164:167], a[188:191], v[56:59]\n"
"ds_read_b64_tr_b16 a[134:135], v210 offset:21824\n"
"v_mfma_f32_16x16x32_bf16 v[60:63], a[168:171], a[184:187], v[60:63]\n"
"ds_read_b64_tr_b16 a[136:137], v211 offset:64\n"
"v_mfma_f32_16x16x32_bf16 v[60:63], a[172:175], a[188:191], v[60:63]\n"
"ds_read_b64_tr_b16 a[138:139], v211 offset:4416\n"
"v_mfma_f32_16x16x32_bf16 v[80:83], a[144:147], a[192:195], v[80:83]\n"
"ds_read_b64_tr_b16 a[140:141], v211 offset:17472\n"
"v_mfma_f32_16x16x32_bf16 v[80:83], a[148:151], a[196:199], v[80:83]\n"
"ds_read_b64_tr_b16 a[142:143], v211 offset:21824\n"
"v_mfma_f32_16x16x32_bf16 v[84:87], a[152:155], a[192:195], v[84:87]\n"
"v_mfma_f32_16x16x32_bf16 v[84:87], a[156:159], a[196:199], v[84:87]\n"
"v_mfma_f32_16x16x32_bf16 v[88:91], a[160:163], a[192:195], v[88:91]\n"
"v_mfma_f32_16x16x32_bf16 v[88:91], a[164:167], a[196:199], v[88:91]\n"
"v_mfma_f32_16x16x32_bf16 v[92:95], a[168:171], a[192:195], v[92:95]\n"
"v_mfma_f32_16x16x32_bf16 v[92:95], a[172:175], a[196:199], v[92:95]\n"
"v_mfma_f32_16x16x32_bf16 v[112:115], a[144:147], a[200:203], v[112:115]\n"
"s_waitcnt lgkmcnt(0)\n"
"s_waitcnt vmcnt(14)\n"
"s_barrier\n"
"v_mfma_f32_16x16x32_bf16 v[112:115], a[148:151], a[204:207], v[112:115]\n"
"v_mfma_f32_16x16x32_bf16 v[116:119], a[152:155], a[200:203], v[116:119]\n"
"ds_write_b128 v252, a[0:3]\n"
"v_mfma_f32_16x16x32_bf16 v[116:119], a[156:159], a[204:207], v[116:119]\n"
"v_mfma_f32_16x16x32_bf16 v[120:123], a[160:163], a[200:203], v[120:123]\n"
"v_mfma_f32_16x16x32_bf16 v[120:123], a[164:167], a[204:207], v[120:123]\n"
"ds_write_b128 v252, a[4:7] offset:1088\n"
"v_mfma_f32_16x16x32_bf16 v[124:127], a[168:171], a[200:203], v[124:127]\n"
"v_mfma_f32_16x16x32_bf16 v[124:127], a[172:175], a[204:207], v[124:127]\n"
"v_mfma_f32_16x16x32_bf16 v[144:147], a[144:147], a[208:211], v[144:147]\n"
"ds_write_b128 v252, a[8:11] offset:2176\n"
"v_mfma_f32_16x16x32_bf16 v[144:147], a[148:151], a[212:215], v[144:147]\n"
"v_mfma_f32_16x16x32_bf16 v[148:151], a[152:155], a[208:211], v[148:151]\n"
"v_mfma_f32_16x16x32_bf16 v[148:151], a[156:159], a[212:215], v[148:151]\n"
"ds_write_b128 v252, a[12:15] offset:3264\n"
"v_mfma_f32_16x16x32_bf16 v[152:155], a[160:163], a[208:211], v[152:155]\n"
"v_mfma_f32_16x16x32_bf16 v[152:155], a[164:167], a[212:215], v[152:155]\n"
"v_mfma_f32_16x16x32_bf16 v[156:159], a[168:171], a[208:211], v[156:159]\n"
"ds_write_b128 v252, a[16:19] offset:4352\n"
"v_mfma_f32_16x16x32_bf16 v[156:159], a[172:175], a[212:215], v[156:159]\n"
"v_mfma_f32_16x16x32_bf16 v[176:179], a[144:147], a[216:219], v[176:179]\n"
"v_mfma_f32_16x16x32_bf16 v[176:179], a[148:151], a[220:223], v[176:179]\n"
"ds_write_b128 v252, a[20:23] offset:5440\n"
"v_mfma_f32_16x16x32_bf16 v[180:183], a[152:155], a[216:219], v[180:183]\n"
"v_mfma_f32_16x16x32_bf16 v[180:183], a[156:159], a[220:223], v[180:183]\n"
"v_mfma_f32_16x16x32_bf16 v[184:187], a[160:163], a[216:219], v[184:187]\n"
"ds_write_b128 v252, a[24:27] offset:6528\n"
"v_mfma_f32_16x16x32_bf16 v[184:187], a[164:167], a[220:223], v[184:187]\n"
"v_mfma_f32_16x16x32_bf16 v[188:191], a[168:171], a[216:219], v[188:191]\n"
"v_mfma_f32_16x16x32_bf16 v[188:191], a[172:175], a[220:223], v[188:191]\n"
"ds_write_b128 v252, a[28:31] offset:7616\n"
"v_mfma_f32_16x16x32_bf16 v[192:195], a[112:115], a[224:227], v[192:195]\n"
"v_mfma_f32_16x16x32_bf16 v[192:195], a[116:119], a[228:231], v[192:195]\n"
"v_mfma_f32_16x16x32_bf16 v[196:199], a[120:123], a[224:227], v[196:199]\n"
"ds_write_b128 v252, a[32:35] offset:34304\n"
"v_mfma_f32_16x16x32_bf16 v[196:199], a[124:127], a[228:231], v[196:199]\n"
"v_mfma_f32_16x16x32_bf16 v[200:203], a[128:131], a[224:227], v[200:203]\n"
"v_mfma_f32_16x16x32_bf16 v[200:203], a[132:135], a[228:231], v[200:203]\n"
"ds_write_b128 v252, a[36:39] offset:35392\n"
"v_mfma_f32_16x16x32_bf16 v[204:207], a[136:139], a[224:227], v[204:207]\n"
"v_mfma_f32_16x16x32_bf16 v[204:207], a[140:143], a[228:231], v[204:207]\n"
"v_mfma_f32_16x16x32_bf16 v[0:3], a[112:115], a[176:179], v[0:3]\n"
"ds_read_b128 a[224:227], v209 offset:26112\n"
"ds_write_b128 v252, a[40:43] offset:36480\n"
"v_mfma_f32_16x16x32_bf16 v[0:3], a[116:119], a[180:183], v[0:3]\n"
"ds_read_b128 a[228:231], v209 offset:26176\n"
"v_mfma_f32_16x16x32_bf16 v[4:7], a[120:123], a[176:179], v[4:7]\n"
"v_mfma_f32_16x16x32_bf16 v[4:7], a[124:127], a[180:183], v[4:7]\n"
"ds_write_b128 v252, a[44:47] offset:37568\n"
"v_mfma_f32_16x16x32_bf16 v[8:11], a[128:131], a[176:179], v[8:11]\n"
"v_mfma_f32_16x16x32_bf16 v[8:11], a[132:135], a[180:183], v[8:11]\n"
"v_mfma_f32_16x16x32_bf16 v[12:15], a[136:139], a[176:179], v[12:15]\n"
"ds_write_b128 v252, a[48:51] offset:38656\n"
"v_mfma_f32_16x16x32_bf16 v[12:15], a[140:143], a[180:183], v[12:15]\n"
"v_mfma_f32_16x16x32_bf16 v[32:35], a[112:115], a[184:187], v[32:35]\n"
"ds_read_b128 a[176:179], v209\n"
"v_mfma_f32_16x16x32_bf16 v[32:35], a[116:119], a[188:191], v[32:35]\n"
"ds_read_b128 a[180:183], v209 offset:64\n"
"ds_write_b128 v252, a[52:55] offset:39744\n"
"v_mfma_f32_16x16x32_bf16 v[36:39], a[120:123], a[184:187], v[36:39]\n"
"v_mfma_f32_16x16x32_bf16 v[36:39], a[124:127], a[188:191], v[36:39]\n"
"v_mfma_f32_16x16x32_bf16 v[40:43], a[128:131], a[184:187], v[40:43]\n"
"ds_write_b128 v252, a[56:59] offset:40832\n"
"v_mfma_f32_16x16x32_bf16 v[40:43], a[132:135], a[188:191], v[40:43]\n"
"v_mfma_f32_16x16x32_bf16 v[44:47], a[136:139], a[184:187], v[44:47]\n"
"v_mfma_f32_16x16x32_bf16 v[44:47], a[140:143], a[188:191], v[44:47]\n"
"ds_write_b128 v252, a[60:63] offset:41920\n"
"v_mfma_f32_16x16x32_bf16 v[64:67], a[112:115], a[192:195], v[64:67]\n"
"ds_read_b128 a[184:187], v209 offset:2176\n"
"v_mfma_f32_16x16x32_bf16 v[64:67], a[116:119], a[196:199], v[64:67]\n"
"ds_read_b128 a[188:191], v209 offset:2240\n"
"v_mfma_f32_16x16x32_bf16 v[68:71], a[120:123], a[192:195], v[68:71]\n"
"v_mfma_f32_16x16x32_bf16 v[68:71], a[124:127], a[196:199], v[68:71]\n"
"v_mfma_f32_16x16x32_bf16 v[72:75], a[128:131], a[192:195], v[72:75]\n"
"v_mfma_f32_16x16x32_bf16 v[72:75], a[132:135], a[196:199], v[72:75]\n"
"s_waitcnt vmcnt(3)\n"
"s_barrier\n"
"v_mfma_f32_16x16x32_bf16 v[76:79], a[136:139], a[192:195], v[76:79]\n"
"ds_read_b64_tr_b16 a[144:145], v210 offset:34944\n"
"v_mfma_f32_16x16x32_bf16 v[76:79], a[140:143], a[196:199], v[76:79]\n"
"ds_read_b64_tr_b16 a[146:147], v210 offset:39296\n"
"v_mfma_f32_16x16x32_bf16 v[96:99], a[112:115], a[200:203], v[96:99]\n"
"ds_read_b128 a[192:195], v209 offset:4352\n"
"ds_read_b64_tr_b16 a[148:149], v210 offset:52352\n"
"v_mfma_f32_16x16x32_bf16 v[96:99], a[116:119], a[204:207], v[96:99]\n"
"ds_read_b128 a[196:199], v209 offset:4416\n"
"ds_read_b64_tr_b16 a[150:151], v210 offset:56704\n"
"v_mfma_f32_16x16x32_bf16 v[100:103], a[120:123], a[200:203], v[100:103]\n"
"ds_read_b64_tr_b16 a[152:153], v211 offset:34944\n"
"v_mfma_f32_16x16x32_bf16 v[100:103], a[124:127], a[204:207], v[100:103]\n"
"ds_read_b64_tr_b16 a[154:155], v211 offset:39296\n"
"v_mfma_f32_16x16x32_bf16 v[104:107], a[128:131], a[200:203], v[104:107]\n"
"ds_read_b64_tr_b16 a[156:157], v211 offset:52352\n"
"v_mfma_f32_16x16x32_bf16 v[104:107], a[132:135], a[204:207], v[104:107]\n"
"ds_read_b64_tr_b16 a[158:159], v211 offset:56704\n"
"v_mfma_f32_16x16x32_bf16 v[108:111], a[136:139], a[200:203], v[108:111]\n"
"ds_read_b64_tr_b16 a[160:161], v210 offset:35008\n"
"v_mfma_f32_16x16x32_bf16 v[108:111], a[140:143], a[204:207], v[108:111]\n"
"ds_read_b64_tr_b16 a[162:163], v210 offset:39360\n"
"v_mfma_f32_16x16x32_bf16 v[128:131], a[112:115], a[208:211], v[128:131]\n"
"ds_read_b128 a[200:203], v209 offset:6528\n"
"ds_read_b64_tr_b16 a[164:165], v210 offset:52416\n"
"v_mfma_f32_16x16x32_bf16 v[128:131], a[116:119], a[212:215], v[128:131]\n"
"ds_read_b128 a[204:207], v209 offset:6592\n"
"ds_read_b64_tr_b16 a[166:167], v210 offset:56768\n"
"v_mfma_f32_16x16x32_bf16 v[132:135], a[120:123], a[208:211], v[132:135]\n"
"ds_read_b64_tr_b16 a[168:169], v211 offset:35008\n"
"v_mfma_f32_16x16x32_bf16 v[132:135], a[124:127], a[212:215], v[132:135]\n"
"ds_read_b64_tr_b16 a[170:171], v211 offset:39360\n"
"v_mfma_f32_16x16x32_bf16 v[136:139], a[128:131], a[208:211], v[136:139]\n"
"ds_read_b64_tr_b16 a[172:173], v211 offset:52416\n"
"v_mfma_f32_16x16x32_bf16 v[136:139], a[132:135], a[212:215], v[136:139]\n"
"ds_read_b64_tr_b16 a[174:175], v211 offset:56768\n"
"v_mfma_f32_16x16x32_bf16 v[140:143], a[136:139], a[208:211], v[140:143]\n"
"v_mfma_f32_16x16x32_bf16 v[140:143], a[140:143], a[212:215], v[140:143]\n"
"v_mfma_f32_16x16x32_bf16 v[160:163], a[112:115], a[216:219], v[160:163]\n"
"ds_read_b128 a[208:211], v209 offset:8704\n"
"v_mfma_f32_16x16x32_bf16 v[160:163], a[116:119], a[220:223], v[160:163]\n"
"ds_read_b128 a[212:215], v209 offset:8768\n"
"v_mfma_f32_16x16x32_bf16 v[164:167], a[120:123], a[216:219], v[164:167]\n"
"v_mfma_f32_16x16x32_bf16 v[164:167], a[124:127], a[220:223], v[164:167]\n"
"v_mfma_f32_16x16x32_bf16 v[168:171], a[128:131], a[216:219], v[168:171]\n"
"v_mfma_f32_16x16x32_bf16 v[168:171], a[132:135], a[220:223], v[168:171]\n"
"v_mfma_f32_16x16x32_bf16 v[172:175], a[136:139], a[216:219], v[172:175]\n"
"s_waitcnt lgkmcnt(2)\n"
"v_mfma_f32_16x16x32_bf16 v[172:175], a[140:143], a[220:223], v[172:175]\n"
"v_mfma_f32_16x16x32_bf16 v[16:19], a[144:147], a[176:179], v[16:19]\n"
"ds_read_b128 a[216:219], v209 offset:10880\n"
"v_mfma_f32_16x16x32_bf16 v[16:19], a[148:151], a[180:183], v[16:19]\n"
"ds_read_b128 a[220:223], v209 offset:10944\n"
"v_mfma_f32_16x16x32_bf16 v[20:23], a[152:155], a[176:179], v[20:23]\n"
"ds_read_b64_tr_b16 a[112:113], v210 offset:34816\n"
"v_mfma_f32_16x16x32_bf16 v[20:23], a[156:159], a[180:183], v[20:23]\n"
"ds_read_b64_tr_b16 a[114:115], v210 offset:39168\n"
"v_mfma_f32_16x16x32_bf16 v[24:27], a[160:163], a[176:179], v[24:27]\n"
"ds_read_b64_tr_b16 a[116:117], v210 offset:52224\n"
"v_mfma_f32_16x16x32_bf16 v[24:27], a[164:167], a[180:183], v[24:27]\n"
"ds_read_b64_tr_b16 a[118:119], v210 offset:56576\n"
"v_mfma_f32_16x16x32_bf16 v[28:31], a[168:171], a[176:179], v[28:31]\n"
"ds_read_b64_tr_b16 a[120:121], v211 offset:34816\n"
"v_mfma_f32_16x16x32_bf16 v[28:31], a[172:175], a[180:183], v[28:31]\n"
"ds_read_b64_tr_b16 a[122:123], v211 offset:39168\n"
"v_mfma_f32_16x16x32_bf16 v[48:51], a[144:147], a[184:187], v[48:51]\n"
"ds_read_b64_tr_b16 a[124:125], v211 offset:52224\n"
"v_mfma_f32_16x16x32_bf16 v[48:51], a[148:151], a[188:191], v[48:51]\n"
"ds_read_b64_tr_b16 a[126:127], v211 offset:56576\n"
"v_mfma_f32_16x16x32_bf16 v[52:55], a[152:155], a[184:187], v[52:55]\n"
"ds_read_b64_tr_b16 a[128:129], v210 offset:34880\n"
"v_mfma_f32_16x16x32_bf16 v[52:55], a[156:159], a[188:191], v[52:55]\n"
"ds_read_b64_tr_b16 a[130:131], v210 offset:39232\n"
"v_mfma_f32_16x16x32_bf16 v[56:59], a[160:163], a[184:187], v[56:59]\n"
"ds_read_b64_tr_b16 a[132:133], v210 offset:52288\n"
"v_mfma_f32_16x16x32_bf16 v[56:59], a[164:167], a[188:191], v[56:59]\n"
"ds_read_b64_tr_b16 a[134:135], v210 offset:56640\n"
"v_mfma_f32_16x16x32_bf16 v[60:63], a[168:171], a[184:187], v[60:63]\n"
"ds_read_b64_tr_b16 a[136:137], v211 offset:34880\n"
"v_mfma_f32_16x16x32_bf16 v[60:63], a[172:175], a[188:191], v[60:63]\n"
"ds_read_b64_tr_b16 a[138:139], v211 offset:39232\n"
"v_mfma_f32_16x16x32_bf16 v[80:83], a[144:147], a[192:195], v[80:83]\n"
"ds_read_b64_tr_b16 a[140:141], v211 offset:52288\n"
"v_mfma_f32_16x16x32_bf16 v[80:83], a[148:151], a[196:199], v[80:83]\n"
"ds_read_b64_tr_b16 a[142:143], v211 offset:56640\n"
"v_mfma_f32_16x16x32_bf16 v[84:87], a[152:155], a[192:195], v[84:87]\n"
"v_mfma_f32_16x16x32_bf16 v[84:87], a[156:159], a[196:199], v[84:87]\n"
"v_mfma_f32_16x16x32_bf16 v[88:91], a[160:163], a[192:195], v[88:91]\n"
"v_mfma_f32_16x16x32_bf16 v[88:91], a[164:167], a[196:199], v[88:91]\n"
"v_mfma_f32_16x16x32_bf16 v[92:95], a[168:171], a[192:195], v[92:95]\n"
"v_mov_b32 v216, 0\n"
"v_mfma_f32_16x16x32_bf16 v[92:95], a[172:175], a[196:199], v[92:95]\n"
"v_mfma_f32_16x16x32_bf16 v[112:115], a[144:147], a[200:203], v[112:115]\n"
"s_waitcnt lgkmcnt(0)\n"
"s_barrier\n"
"v_mfma_f32_16x16x32_bf16 v[112:115], a[148:151], a[204:207], v[112:115]\n"
"v_mov_b32 v217, 0\n"
"v_mfma_f32_16x16x32_bf16 v[116:119], a[152:155], a[200:203], v[116:119]\n"
"ds_write_b128 v253, a[64:67]\n"
"v_mfma_f32_16x16x32_bf16 v[116:119], a[156:159], a[204:207], v[116:119]\n"
"v_mfma_f32_16x16x32_bf16 v[120:123], a[160:163], a[200:203], v[120:123]\n"
"v_mov_b32 v218, 0\n"
"v_mfma_f32_16x16x32_bf16 v[120:123], a[164:167], a[204:207], v[120:123]\n"
"v_mfma_f32_16x16x32_bf16 v[124:127], a[168:171], a[200:203], v[124:127]\n"
"v_mov_b32 v219, 0\n"
"v_mfma_f32_16x16x32_bf16 v[124:127], a[172:175], a[204:207], v[124:127]\n"
"ds_write_b128 v253, a[68:71] offset:1088\n"
"v_mfma_f32_16x16x32_bf16 v[144:147], a[144:147], a[208:211], v[144:147]\n"
"v_mfma_f32_16x16x32_bf16 v[144:147], a[148:151], a[212:215], v[144:147]\n"
"v_mov_b32 v220, 0\n"
"v_mfma_f32_16x16x32_bf16 v[148:151], a[152:155], a[208:211], v[148:151]\n"
"v_mfma_f32_16x16x32_bf16 v[148:151], a[156:159], a[212:215], v[148:151]\n"
"v_mov_b32 v221, 0\n"
"v_mfma_f32_16x16x32_bf16 v[152:155], a[160:163], a[208:211], v[152:155]\n"
"ds_write_b128 v253, a[72:75] offset:2176\n"
"v_mfma_f32_16x16x32_bf16 v[152:155], a[164:167], a[212:215], v[152:155]\n"
"v_mfma_f32_16x16x32_bf16 v[156:159], a[168:171], a[208:211], v[156:159]\n"
"v_mov_b32 v222, 0\n"
"v_mfma_f32_16x16x32_bf16 v[156:159], a[172:175], a[212:215], v[156:159]\n"
"v_mfma_f32_16x16x32_bf16 v[176:179], a[144:147], a[216:219], v[176:179]\n"
"v_mov_b32 v223, 0\n"
"v_mfma_f32_16x16x32_bf16 v[176:179], a[148:151], a[220:223], v[176:179]\n"
"ds_write_b128 v253, a[76:79] offset:3264\n"
"v_mfma_f32_16x16x32_bf16 v[180:183], a[152:155], a[216:219], v[180:183]\n"
"v_mfma_f32_16x16x32_bf16 v[180:183], a[156:159], a[220:223], v[180:183]\n"
"v_mov_b32 v224, 0\n"
"v_mfma_f32_16x16x32_bf16 v[184:187], a[160:163], a[216:219], v[184:187]\n"
"v_mfma_f32_16x16x32_bf16 v[184:187], a[164:167], a[220:223], v[184:187]\n"
"v_mov_b32 v225, 0\n"
"v_mfma_f32_16x16x32_bf16 v[188:191], a[168:171], a[216:219], v[188:191]\n"
"ds_write_b128 v253, a[80:83] offset:4352\n"
"v_mfma_f32_16x16x32_bf16 v[188:191], a[172:175], a[220:223], v[188:191]\n"
"v_mfma_f32_16x16x32_bf16 v[192:195], a[112:115], a[224:227], v[192:195]\n"
"v_mov_b32 v226, 0\n"
"v_mfma_f32_16x16x32_bf16 v[192:195], a[116:119], a[228:231], v[192:195]\n"
"v_mfma_f32_16x16x32_bf16 v[196:199], a[120:123], a[224:227], v[196:199]\n"
"v_mov_b32 v227, 0\n"
"v_mfma_f32_16x16x32_bf16 v[196:199], a[124:127], a[228:231], v[196:199]\n"
"ds_write_b128 v253, a[84:87] offset:5440\n"
"v_mfma_f32_16x16x32_bf16 v[200:203], a[128:131], a[224:227], v[200:203]\n"
"v_mfma_f32_16x16x32_bf16 v[200:203], a[132:135], a[228:231], v[200:203]\n"
"v_mov_b32 v228, 0\n"
"v_mfma_f32_16x16x32_bf16 v[204:207], a[136:139], a[224:227], v[204:207]\n"
"v_mfma_f32_16x16x32_bf16 v[204:207], a[140:143], a[228:231], v[204:207]\n"
"v_mov_b32 v229, 0\n"
"v_mfma_f32_16x16x32_bf16 v[0:3], a[112:115], a[176:179], v[0:3]\n"
"ds_write_b128 v253, a[88:91] offset:6528\n"
"v_mfma_f32_16x16x32_bf16 v[0:3], a[116:119], a[180:183], v[0:3]\n"
"v_mfma_f32_16x16x32_bf16 v[4:7], a[120:123], a[176:179], v[4:7]\n"
"v_mov_b32 v230, 0\n"
"v_mfma_f32_16x16x32_bf16 v[4:7], a[124:127], a[180:183], v[4:7]\n"
"v_mfma_f32_16x16x32_bf16 v[8:11], a[128:131], a[176:179], v[8:11]\n"
"v_mov_b32 v231, 0\n"
"v_mfma_f32_16x16x32_bf16 v[8:11], a[132:135], a[180:183], v[8:11]\n"
"ds_write_b128 v253, a[92:95] offset:7616\n"
"v_mfma_f32_16x16x32_bf16 v[12:15], a[136:139], a[176:179], v[12:15]\n"
"v_mfma_f32_16x16x32_bf16 v[12:15], a[140:143], a[180:183], v[12:15]\n"
"v_mov_b32 v232, 0\n"
"v_mfma_f32_16x16x32_bf16 v[32:35], a[112:115], a[184:187], v[32:35]\n"
"v_mfma_f32_16x16x32_bf16 v[32:35], a[116:119], a[188:191], v[32:35]\n"
"v_mov_b32 v233, 0\n"
"v_mfma_f32_16x16x32_bf16 v[36:39], a[120:123], a[184:187], v[36:39]\n"
"ds_write_b128 v253, a[96:99] offset:34304\n"
"v_mfma_f32_16x16x32_bf16 v[36:39], a[124:127], a[188:191], v[36:39]\n"
"v_mfma_f32_16x16x32_bf16 v[40:43], a[128:131], a[184:187], v[40:43]\n"
"v_mov_b32 v234, 0\n"
"v_mfma_f32_16x16x32_bf16 v[40:43], a[132:135], a[188:191], v[40:43]\n"
"v_mfma_f32_16x16x32_bf16 v[44:47], a[136:139], a[184:187], v[44:47]\n"
"v_mov_b32 v235, 0\n"
"v_mfma_f32_16x16x32_bf16 v[44:47], a[140:143], a[188:191], v[44:47]\n"
"ds_write_b128 v253, a[100:103] offset:35392\n"
"v_mfma_f32_16x16x32_bf16 v[64:67], a[112:115], a[192:195], v[64:67]\n"
"v_mfma_f32_16x16x32_bf16 v[64:67], a[116:119], a[196:199], v[64:67]\n"
"v_mov_b32 v236, 0\n"
"v_mfma_f32_16x16x32_bf16 v[68:71], a[120:123], a[192:195], v[68:71]\n"
"v_mfma_f32_16x16x32_bf16 v[68:71], a[124:127], a[196:199], v[68:71]\n"
"v_mov_b32 v237, 0\n"
"v_mfma_f32_16x16x32_bf16 v[72:75], a[128:131], a[192:195], v[72:75]\n"
"ds_write_b128 v253, a[104:107] offset:36480\n"
"v_mfma_f32_16x16x32_bf16 v[72:75], a[132:135], a[196:199], v[72:75]\n"
"v_mfma_f32_16x16x32_bf16 v[76:79], a[136:139], a[192:195], v[76:79]\n"
"v_mov_b32 v238, 0\n"
"v_mfma_f32_16x16x32_bf16 v[76:79], a[140:143], a[196:199], v[76:79]\n"
"v_mfma_f32_16x16x32_bf16 v[96:99], a[112:115], a[200:203], v[96:99]\n"
"v_mov_b32 v239, 0\n"
"v_mfma_f32_16x16x32_bf16 v[96:99], a[116:119], a[204:207], v[96:99]\n"
"ds_write_b128 v253, a[108:111] offset:37568\n"
"v_mfma_f32_16x16x32_bf16 v[100:103], a[120:123], a[200:203], v[100:103]\n"
"v_mfma_f32_16x16x32_bf16 v[100:103], a[124:127], a[204:207], v[100:103]\n"
"v_mov_b32 v240, 0\n"
"v_mfma_f32_16x16x32_bf16 v[104:107], a[128:131], a[200:203], v[104:107]\n"
"v_mfma_f32_16x16x32_bf16 v[104:107], a[132:135], a[204:207], v[104:107]\n"
"v_mov_b32 v241, 0\n"
"v_mfma_f32_16x16x32_bf16 v[108:111], a[136:139], a[200:203], v[108:111]\n"
"ds_write_b128 v253, a[240:243] offset:38656\n"
"v_mfma_f32_16x16x32_bf16 v[108:111], a[140:143], a[204:207], v[108:111]\n"
"v_mfma_f32_16x16x32_bf16 v[128:131], a[112:115], a[208:211], v[128:131]\n"
"v_mov_b32 v242, 0\n"
"v_mfma_f32_16x16x32_bf16 v[128:131], a[116:119], a[212:215], v[128:131]\n"
"v_mfma_f32_16x16x32_bf16 v[132:135], a[120:123], a[208:211], v[132:135]\n"
"v_mov_b32 v243, 0\n"
"v_mfma_f32_16x16x32_bf16 v[132:135], a[124:127], a[212:215], v[132:135]\n"
"s_waitcnt vmcnt(2)\n"
"ds_write_b128 v253, a[244:247] offset:39744\n"
"v_mfma_f32_16x16x32_bf16 v[136:139], a[128:131], a[208:211], v[136:139]\n"
"v_mfma_f32_16x16x32_bf16 v[136:139], a[132:135], a[212:215], v[136:139]\n"
"v_mfma_f32_16x16x32_bf16 v[140:143], a[136:139], a[208:211], v[140:143]\n"
"v_mfma_f32_16x16x32_bf16 v[140:143], a[140:143], a[212:215], v[140:143]\n"
"v_mfma_f32_16x16x32_bf16 v[160:163], a[112:115], a[216:219], v[160:163]\n"
"s_waitcnt vmcnt(1)\n"
"ds_write_b128 v253, a[248:251] offset:40832\n"
"v_mfma_f32_16x16x32_bf16 v[160:163], a[116:119], a[220:223], v[160:163]\n"
"v_mfma_f32_16x16x32_bf16 v[164:167], a[120:123], a[216:219], v[164:167]\n"
"v_mfma_f32_16x16x32_bf16 v[164:167], a[124:127], a[220:223], v[164:167]\n"
"v_mfma_f32_16x16x32_bf16 v[168:171], a[128:131], a[216:219], v[168:171]\n"
"v_mfma_f32_16x16x32_bf16 v[168:171], a[132:135], a[220:223], v[168:171]\n"
"s_waitcnt vmcnt(0)\n"
"ds_write_b128 v253, a[252:255] offset:41920\n"
"v_mfma_f32_16x16x32_bf16 v[172:175], a[136:139], a[216:219], v[172:175]\n"
"v_mfma_f32_16x16x32_bf16 v[172:175], a[140:143], a[220:223], v[172:175]\n"
"s_nop 3\n"
"v_accvgpr_read_b32 v252, a232\n"
"v_accvgpr_read_b32 v253, a233\n"
"s_nop 0\n"
"v_readfirstlane_b32 s74, v252\n"
"v_readfirstlane_b32 s75, v253\n"
"s_nop 0\n"
"s_add_u32 s74, s74, 2135587861\n"
"s_addc_u32 s75, s75, 2654435769\n"
"s_lshl_b32 s25, s21, 11\n"
"s_add_u32 s25, s25, 154496\n"
"v_add_u32 v252, s25, v251\n"
"v_pk_mul_f32 v[0:1], v[0:1], s[56:57]\n"
"v_pk_mul_f32 v[2:3], v[2:3], s[56:57]\n"
"v_cvt_pk_bf16_f32 v0, v0, v1\n"
"v_cvt_pk_bf16_f32 v1, v2, v3\n"
"v_pk_mul_f32 v[4:5], v[4:5], s[56:57]\n"
"v_pk_mul_f32 v[6:7], v[6:7], s[56:57]\n"
"v_cvt_pk_bf16_f32 v2, v4, v5\n"
"v_cvt_pk_bf16_f32 v3, v6, v7\n"
"v_pk_mul_f32 v[8:9], v[8:9], s[56:57]\n"
"v_pk_mul_f32 v[10:11], v[10:11], s[56:57]\n"
"v_cvt_pk_bf16_f32 v4, v8, v9\n"
"v_cvt_pk_bf16_f32 v5, v10, v11\n"
"v_pk_mul_f32 v[12:13], v[12:13], s[56:57]\n"
"v_pk_mul_f32 v[14:15], v[14:15], s[56:57]\n"
"v_cvt_pk_bf16_f32 v6, v12, v13\n"
"v_cvt_pk_bf16_f32 v7, v14, v15\n"
"v_pk_mul_f32 v[16:17], v[16:17], s[56:57]\n"
"v_pk_mul_f32 v[18:19], v[18:19], s[56:57]\n"
"v_cvt_pk_bf16_f32 v8, v16, v17\n"
"v_cvt_pk_bf16_f32 v9, v18, v19\n"
"v_pk_mul_f32 v[20:21], v[20:21], s[56:57]\n"
"v_pk_mul_f32 v[22:23], v[22:23], s[56:57]\n"
"v_cvt_pk_bf16_f32 v10, v20, v21\n"
"v_cvt_pk_bf16_f32 v11, v22, v23\n"
"v_pk_mul_f32 v[24:25], v[24:25], s[56:57]\n"
"v_pk_mul_f32 v[26:27], v[26:27], s[56:57]\n"
"v_cvt_pk_bf16_f32 v12, v24, v25\n"
"v_cvt_pk_bf16_f32 v13, v26, v27\n"
"v_pk_mul_f32 v[28:29], v[28:29], s[56:57]\n"
"v_pk_mul_f32 v[30:31], v[30:31], s[56:57]\n"
"v_cvt_pk_bf16_f32 v14, v28, v29\n"
"v_cvt_pk_bf16_f32 v15, v30, v31\n"
"v_pk_mul_f32 v[32:33], v[32:33], s[56:57]\n"
"v_pk_mul_f32 v[34:35], v[34:35], s[56:57]\n"
"v_cvt_pk_bf16_f32 v16, v32, v33\n"
"v_cvt_pk_bf16_f32 v17, v34, v35\n"
"v_pk_mul_f32 v[36:37], v[36:37], s[56:57]\n"
"v_pk_mul_f32 v[38:39], v[38:39], s[56:57]\n"
"v_cvt_pk_bf16_f32 v18, v36, v37\n"
"v_cvt_pk_bf16_f32 v19, v38, v39\n"
"v_pk_mul_f32 v[40:41], v[40:41], s[56:57]\n"
"v_pk_mul_f32 v[42:43], v[42:43], s[56:57]\n"
"v_cvt_pk_bf16_f32 v20, v40, v41\n"
"v_cvt_pk_bf16_f32 v21, v42, v43\n"
"v_pk_mul_f32 v[44:45], v[44:45], s[56:57]\n"
"v_pk_mul_f32 v[46:47], v[46:47], s[56:57]\n"
"v_cvt_pk_bf16_f32 v22, v44, v45\n"
"v_cvt_pk_bf16_f32 v23, v46, v47\n"
"v_pk_mul_f32 v[48:49], v[48:49], s[56:57]\n"
"v_pk_mul_f32 v[50:51], v[50:51], s[56:57]\n"
"v_cvt_pk_bf16_f32 v24, v48, v49\n"
"v_cvt_pk_bf16_f32 v25, v50, v51\n"
"v_pk_mul_f32 v[52:53], v[52:53], s[56:57]\n"
"v_pk_mul_f32 v[54:55], v[54:55], s[56:57]\n"
"v_cvt_pk_bf16_f32 v26, v52, v53\n"
"v_cvt_pk_bf16_f32 v27, v54, v55\n"
"v_pk_mul_f32 v[56:57], v[56:57], s[56:57]\n"
"v_pk_mul_f32 v[58:59], v[58:59], s[56:57]\n"
"v_cvt_pk_bf16_f32 v28, v56, v57\n"
"v_cvt_pk_bf16_f32 v29, v58, v59\n"
"v_pk_mul_f32 v[60:61], v[60:61], s[56:57]\n"
"v_pk_mul_f32 v[62:63], v[62:63], s[56:57]\n"
"v_cvt_pk_bf16_f32 v30, v60, v61\n"
"v_cvt_pk_bf16_f32 v31, v62, v63\n"
"v_pk_mul_f32 v[64:65], v[64:65], s[56:57]\n"
"v_pk_mul_f32 v[66:67], v[66:67], s[56:57]\n"
"v_cvt_pk_bf16_f32 v32, v64, v65\n"
"v_cvt_pk_bf16_f32 v33, v66, v67\n"
"v_pk_mul_f32 v[68:69], v[68:69], s[56:57]\n"
"v_pk_mul_f32 v[70:71], v[70:71], s[56:57]\n"
"v_cvt_pk_bf16_f32 v34, v68, v69\n"
"v_cvt_pk_bf16_f32 v35, v70, v71\n"
"v_pk_mul_f32 v[72:73], v[72:73], s[56:57]\n"
"v_pk_mul_f32 v[74:75], v[74:75], s[56:57]\n"
"v_cvt_pk_bf16_f32 v36, v72, v73\n"
"v_cvt_pk_bf16_f32 v37, v74, v75\n"
"v_pk_mul_f32 v[76:77], v[76:77], s[56:57]\n"
"v_pk_mul_f32 v[78:79], v[78:79], s[56:57]\n"
"v_cvt_pk_bf16_f32 v38, v76, v77\n"
"v_cvt_pk_bf16_f32 v39, v78, v79\n"
"v_pk_mul_f32 v[80:81], v[80:81], s[56:57]\n"
"v_pk_mul_f32 v[82:83], v[82:83], s[56:57]\n"
"v_cvt_pk_bf16_f32 v40, v80, v81\n"
"v_cvt_pk_bf16_f32 v41, v82, v83\n"
"v_pk_mul_f32 v[84:85], v[84:85], s[56:57]\n"
"v_pk_mul_f32 v[86:87], v[86:87], s[56:57]\n"
"v_cvt_pk_bf16_f32 v42, v84, v85\n"
"v_cvt_pk_bf16_f32 v43, v86, v87\n"
"v_pk_mul_f32 v[88:89], v[88:89], s[56:57]\n"
"v_pk_mul_f32 v[90:91], v[90:91], s[56:57]\n"
"v_cvt_pk_bf16_f32 v44, v88, v89\n"
"v_cvt_pk_bf16_f32 v45, v90, v91\n"
"v_pk_mul_f32 v[92:93], v[92:93], s[56:57]\n"
"v_pk_mul_f32 v[94:95], v[94:95], s[56:57]\n"
"v_cvt_pk_bf16_f32 v46, v92, v93\n"
"v_cvt_pk_bf16_f32 v47, v94, v95\n"
"v_pk_mul_f32 v[96:97], v[96:97], s[56:57]\n"
"v_pk_mul_f32 v[98:99], v[98:99], s[56:57]\n"
"v_cvt_pk_bf16_f32 v48, v96, v97\n"
"v_cvt_pk_bf16_f32 v49, v98, v99\n"
"v_pk_mul_f32 v[100:101], v[100:101], s[56:57]\n"
"v_pk_mul_f32 v[102:103], v[102:103], s[56:57]\n"
"v_cvt_pk_bf16_f32 v50, v100, v101\n"
"v_cvt_pk_bf16_f32 v51, v102, v103\n"
"v_pk_mul_f32 v[104:105], v[104:105], s[56:57]\n"
"v_pk_mul_f32 v[106:107], v[106:107], s[56:57]\n"
"v_cvt_pk_bf16_f32 v52, v104, v105\n"
"v_cvt_pk_bf16_f32 v53, v106, v107\n"
"v_pk_mul_f32 v[108:109], v[108:109], s[56:57]\n"
"v_pk_mul_f32 v[110:111], v[110:111], s[56:57]\n"
"v_cvt_pk_bf16_f32 v54, v108, v109\n"
"v_cvt_pk_bf16_f32 v55, v110, v111\n"
"v_pk_mul_f32 v[112:113], v[112:113], s[56:57]\n"
"v_pk_mul_f32 v[114:115], v[114:115], s[56:57]\n"
"v_cvt_pk_bf16_f32 v56, v112, v113\n"
"v_cvt_pk_bf16_f32 v57, v114, v115\n"
"v_pk_mul_f32 v[116:117], v[116:117], s[56:57]\n"
"v_pk_mul_f32 v[118:119], v[118:119], s[56:57]\n"
"v_cvt_pk_bf16_f32 v58, v116, v117\n"
"v_cvt_pk_bf16_f32 v59, v118, v119\n"
"v_pk_mul_f32 v[120:121], v[120:121], s[56:57]\n"
"v_pk_mul_f32 v[122:123], v[122:123], s[56:57]\n"
"v_cvt_pk_bf16_f32 v60, v120, v121\n"
"v_cvt_pk_bf16_f32 v61, v122, v123\n"
"v_pk_mul_f32 v[124:125], v[124:125], s[56:57]\n"
"v_pk_mul_f32 v[126:127], v[126:127], s[56:57]\n"
"v_cvt_pk_bf16_f32 v62, v124, v125\n"
"v_cvt_pk_bf16_f32 v63, v126, v127\n"
"v_pk_mul_f32 v[128:129], v[128:129], s[56:57]\n"
"v_pk_mul_f32 v[130:131], v[130:131], s[56:57]\n"
"v_cvt_pk_bf16_f32 v64, v128, v129\n"
"v_cvt_pk_bf16_f32 v65, v130, v131\n"
"v_pk_mul_f32 v[132:133], v[132:133], s[56:57]\n"
"v_pk_mul_f32 v[134:135], v[134:135], s[56:57]\n"
"v_cvt_pk_bf16_f32 v66, v132, v133\n"
"v_cvt_pk_bf16_f32 v67, v134, v135\n"
"v_pk_mul_f32 v[136:137], v[136:137], s[56:57]\n"
"v_pk_mul_f32 v[138:139], v[138:139], s[56:57]\n"
"v_cvt_pk_bf16_f32 v68, v136, v137\n"
"v_cvt_pk_bf16_f32 v69, v138, v139\n"
"v_pk_mul_f32 v[140:141], v[140:141], s[56:57]\n"
"v_pk_mul_f32 v[142:143], v[142:143], s[56:57]\n"
"v_cvt_pk_bf16_f32 v70, v140, v141\n"
"v_cvt_pk_bf16_f32 v71, v142, v143\n"
"v_pk_mul_f32 v[144:145], v[144:145], s[56:57]\n"
"v_pk_mul_f32 v[146:147], v[146:147], s[56:57]\n"
"v_cvt_pk_bf16_f32 v72, v144, v145\n"
"v_cvt_pk_bf16_f32 v73, v146, v147\n"
"v_pk_mul_f32 v[148:149], v[148:149], s[56:57]\n"
"v_pk_mul_f32 v[150:151], v[150:151], s[56:57]\n"
"v_cvt_pk_bf16_f32 v74, v148, v149\n"
"v_cvt_pk_bf16_f32 v75, v150, v151\n"
"v_pk_mul_f32 v[152:153], v[152:153], s[56:57]\n"
"v_pk_mul_f32 v[154:155], v[154:155], s[56:57]\n"
"v_cvt_pk_bf16_f32 v76, v152, v153\n"
"v_cvt_pk_bf16_f32 v77, v154, v155\n"
"v_pk_mul_f32 v[156:157], v[156:157], s[56:57]\n"
"v_pk_mul_f32 v[158:159], v[158:159], s[56:57]\n"
"v_cvt_pk_bf16_f32 v78, v156, v157\n"
"v_cvt_pk_bf16_f32 v79, v158, v159\n"
"v_pk_mul_f32 v[160:161], v[160:161], s[56:57]\n"
"v_pk_mul_f32 v[162:163], v[162:163], s[56:57]\n"
"v_cvt_pk_bf16_f32 v80, v160, v161\n"
"v_cvt_pk_bf16_f32 v81, v162, v163\n"
"v_pk_mul_f32 v[164:165], v[164:165], s[56:57]\n"
"v_pk_mul_f32 v[166:167], v[166:167], s[56:57]\n"
"v_cvt_pk_bf16_f32 v82, v164, v165\n"
"v_cvt_pk_bf16_f32 v83, v166, v167\n"
"v_pk_mul_f32 v[168:169], v[168:169], s[56:57]\n"
"v_pk_mul_f32 v[170:171], v[170:171], s[56:57]\n"
"v_cvt_pk_bf16_f32 v84, v168, v169\n"
"v_cvt_pk_bf16_f32 v85, v170, v171\n"
"v_pk_mul_f32 v[172:173], v[172:173], s[56:57]\n"
"v_pk_mul_f32 v[174:175], v[174:175], s[56:57]\n"
"v_cvt_pk_bf16_f32 v86, v172, v173\n"
"v_cvt_pk_bf16_f32 v87, v174, v175\n"
"v_pk_mul_f32 v[176:177], v[176:177], s[56:57]\n"
"v_pk_mul_f32 v[178:179], v[178:179], s[56:57]\n"
"v_cvt_pk_bf16_f32 v88, v176, v177\n"
"v_cvt_pk_bf16_f32 v89, v178, v179\n"
"v_pk_mul_f32 v[180:181], v[180:181], s[56:57]\n"
"v_pk_mul_f32 v[182:183], v[182:183], s[56:57]\n"
"v_cvt_pk_bf16_f32 v90, v180, v181\n"
"v_cvt_pk_bf16_f32 v91, v182, v183\n"
"v_pk_mul_f32 v[184:185], v[184:185], s[56:57]\n"
"v_pk_mul_f32 v[186:187], v[186:187], s[56:57]\n"
"v_cvt_pk_bf16_f32 v92, v184, v185\n"
"v_cvt_pk_bf16_f32 v93, v186, v187\n"
"v_pk_mul_f32 v[188:189], v[188:189], s[56:57]\n"
"v_pk_mul_f32 v[190:191], v[190:191], s[56:57]\n"
"v_cvt_pk_bf16_f32 v94, v188, v189\n"
"v_cvt_pk_bf16_f32 v95, v190, v191\n"
"v_pk_mul_f32 v[192:193], v[192:193], s[56:57]\n"
"v_pk_mul_f32 v[194:195], v[194:195], s[56:57]\n"
"v_cvt_pk_bf16_f32 v192, v192, v193\n"
"v_cvt_pk_bf16_f32 v193, v194, v195\n"
"v_pk_mul_f32 v[196:197], v[196:197], s[56:57]\n"
"v_pk_mul_f32 v[198:199], v[198:199], s[56:57]\n"
"v_cvt_pk_bf16_f32 v194, v196, v197\n"
"v_cvt_pk_bf16_f32 v195, v198, v199\n"
"v_pk_mul_f32 v[200:201], v[200:201], s[56:57]\n"
"v_pk_mul_f32 v[202:203], v[202:203], s[56:57]\n"
"v_cvt_pk_bf16_f32 v196, v200, v201\n"
"v_cvt_pk_bf16_f32 v197, v202, v203\n"
"v_pk_mul_f32 v[204:205], v[204:205], s[56:57]\n"
"v_pk_mul_f32 v[206:207], v[206:207], s[56:57]\n"
"v_cvt_pk_bf16_f32 v198, v204, v205\n"
"v_cvt_pk_bf16_f32 v199, v206, v207\n"
"s_mov_b32 exec_lo, 0xffff0000\n"
"s_mov_b32 exec_hi, 0xffff0000\n"
"v_swap_b32 v192, v194\n"
"v_swap_b32 v193, v195\n"
"v_swap_b32 v196, v198\n"
"v_swap_b32 v197, v199\n"
"v_swap_b32 v0, v2\n"
"v_swap_b32 v1, v3\n"
"v_swap_b32 v4, v6\n"
"v_swap_b32 v5, v7\n"
"v_swap_b32 v8, v10\n"
"v_swap_b32 v9, v11\n"
"v_swap_b32 v12, v14\n"
"v_swap_b32 v13, v15\n"
"v_swap_b32 v16, v18\n"
"v_swap_b32 v17, v19\n"
"v_swap_b32 v20, v22\n"
"v_swap_b32 v21, v23\n"
"v_swap_b32 v24, v26\n"
"v_swap_b32 v25, v27\n"
"v_swap_b32 v28, v30\n"
"v_swap_b32 v29, v31\n"
"v_swap_b32 v32, v34\n"
"v_swap_b32 v33, v35\n"
"v_swap_b32 v36, v38\n"
"v_swap_b32 v37, v39\n"
"v_swap_b32 v40, v42\n"
"v_swap_b32 v41, v43\n"
"v_swap_b32 v44, v46\n"
"v_swap_b32 v45, v47\n"
"v_swap_b32 v48, v50\n"
"v_swap_b32 v49, v51\n"
"v_swap_b32 v52, v54\n"
"v_swap_b32 v53, v55\n"
"v_swap_b32 v56, v58\n"
"v_swap_b32 v57, v59\n"
"v_swap_b32 v60, v62\n"
"v_swap_b32 v61, v63\n"
"v_swap_b32 v64, v66\n"
"v_swap_b32 v65, v67\n"
"v_swap_b32 v68, v70\n"
"v_swap_b32 v69, v71\n"
"v_swap_b32 v72, v74\n"
"v_swap_b32 v73, v75\n"
"v_swap_b32 v76, v78\n"
"v_swap_b32 v77, v79\n"
"v_swap_b32 v80, v82\n"
"v_swap_b32 v81, v83\n"
"v_swap_b32 v84, v86\n"
"v_swap_b32 v85, v87\n"
"v_swap_b32 v88, v90\n"
"v_swap_b32 v89, v91\n"
"v_swap_b32 v92, v94\n"
"v_swap_b32 v93, v95\n"
"s_mov_b64 exec, -1\n"
"ds_write_b128 v252, v[192:195]\n"
"ds_write_b128 v252, v[196:199] offset:1024\n"
"v_accvgpr_write_b32 a0, v0\n"
"v_accvgpr_write_b32 a1, v1\n"
"v_accvgpr_write_b32 a2, v2\n"
"v_accvgpr_write_b32 a3, v3\n"
"v_accvgpr_write_b32 a4, v4\n"
"v_accvgpr_write_b32 a5, v5\n"
"v_accvgpr_write_b32 a6, v6\n"
"v_accvgpr_write_b32 a7, v7\n"
"v_accvgpr_write_b32 a8, v8\n"
"v_accvgpr_write_b32 a9, v9\n"
"v_accvgpr_write_b32 a10, v10\n"
"v_accvgpr_write_b32 a11, v11\n"
"v_accvgpr_write_b32 a12, v12\n"
"v_accvgpr_write_b32 a13, v13\n"
"v_accvgpr_write_b32 a14, v14\n"
"v_accvgpr_write_b32 a15, v15\n"
"v_accvgpr_write_b32 a16, v16\n"
"v_accvgpr_write_b32 a17, v17\n"
"v_accvgpr_write_b32 a18, v18\n"
"v_accvgpr_write_b32 a19, v19\n"
"v_accvgpr_write_b32 a20, v20\n"
"v_accvgpr_write_b32 a21, v21\n"
"v_accvgpr_write_b32 a22, v22\n"
"v_accvgpr_write_b32 a23, v23\n"
"v_accvgpr_write_b32 a24, v24\n"
"v_accvgpr_write_b32 a25, v25\n"
"v_accvgpr_write_b32 a26, v26\n"
"v_accvgpr_write_b32 a27, v27\n"
"v_accvgpr_write_b32 a28, v28\n"
"v_accvgpr_write_b32 a29, v29\n"
"v_accvgpr_write_b32 a30, v30\n"
"v_accvgpr_write_b32 a31, v31\n"
"v_accvgpr_write_b32 a32, v32\n"
"v_accvgpr_write_b32 a33, v33\n"
"v_accvgpr_write_b32 a34, v34\n"
"v_accvgpr_write_b32 a35, v35\n"
"v_accvgpr_write_b32 a36, v36\n"
"v_accvgpr_write_b32 a37, v37\n"
"v_accvgpr_write_b32 a38, v38\n"
"v_accvgpr_write_b32 a39, v39\n"
"v_accvgpr_write_b32 a40, v40\n"
"v_accvgpr_write_b32 a41, v41\n"
"v_accvgpr_write_b32 a42, v42\n"
"v_accvgpr_write_b32 a43, v43\n"
"v_accvgpr_write_b32 a44, v44\n"
"v_accvgpr_write_b32 a45, v45\n"
"v_accvgpr_write_b32 a46, v46\n"
"v_accvgpr_write_b32 a47, v47\n"
"v_accvgpr_write_b32 a48, v48\n"
"v_accvgpr_write_b32 a49, v49\n"
"v_accvgpr_write_b32 a50, v50\n"
"v_accvgpr_write_b32 a51, v51\n"
"v_accvgpr_write_b32 a52, v52\n"
"v_accvgpr_write_b32 a53, v53\n"
"v_accvgpr_write_b32 a54, v54\n"
"v_accvgpr_write_b32 a55, v55\n"
"v_accvgpr_write_b32 a56, v56\n"
"v_accvgpr_write_b32 a57, v57\n"
"v_accvgpr_write_b32 a58, v58\n"
"v_accvgpr_write_b32 a59, v59\n"
"v_accvgpr_write_b32 a60, v60\n"
"v_accvgpr_write_b32 a61, v61\n"
"v_accvgpr_write_b32 a62, v62\n"
"v_accvgpr_write_b32 a63, v63\n"
"v_accvgpr_write_b32 a64, v64\n"
"v_accvgpr_write_b32 a65, v65\n"
"v_accvgpr_write_b32 a66, v66\n"
"v_accvgpr_write_b32 a67, v67\n"
"v_accvgpr_write_b32 a68, v68\n"
"v_accvgpr_write_b32 a69, v69\n"
"v_accvgpr_write_b32 a70, v70\n"
"v_accvgpr_write_b32 a71, v71\n"
"v_accvgpr_write_b32 a72, v72\n"
"v_accvgpr_write_b32 a73, v73\n"
"v_accvgpr_write_b32 a74, v74\n"
"v_accvgpr_write_b32 a75, v75\n"
"v_accvgpr_write_b32 a76, v76\n"
"v_accvgpr_write_b32 a77, v77\n"
"v_accvgpr_write_b32 a78, v78\n"
"v_accvgpr_write_b32 a79, v79\n"
"v_accvgpr_write_b32 a80, v80\n"
"v_accvgpr_write_b32 a81, v81\n"
"v_accvgpr_write_b32 a82, v82\n"
"v_accvgpr_write_b32 a83, v83\n"
"v_accvgpr_write_b32 a84, v84\n"
"v_accvgpr_write_b32 a85, v85\n"
"v_accvgpr_write_b32 a86, v86\n"
"v_accvgpr_write_b32 a87, v87\n"
"v_accvgpr_write_b32 a88, v88\n"
"v_accvgpr_write_b32 a89, v89\n"
"v_accvgpr_write_b32 a90, v90\n"
"v_accvgpr_write_b32 a91, v91\n"
"v_accvgpr_write_b32 a92, v92\n"
"v_accvgpr_write_b32 a93, v93\n"
"v_accvgpr_write_b32 a94, v94\n"
"v_accvgpr_write_b32 a95, v95\n"
"s_lshl_b32 s25, s22, 12\n"
"s_add_u32 s25, s25, 154496\n"
"v_add_u32 v252, s25, v251\n"
"s_waitcnt lgkmcnt(0)\n"
"s_barrier\n"
"ds_read_b128 a[96:99], v252\n"
"ds_read_b128 a[100:103], v252 offset:1024\n"
"ds_read_b128 a[104:107], v252 offset:2048\n"
"ds_read_b128 a[108:111], v252 offset:3072\n"
"L1_attn_h0:\n"
"s_bfe_u32 s35, s20, 196611\n"
"s_mul_i32 s35, s35, 128\n"
"s_add_u32 s35, s35, 0\n"
"s_mov_b32 s66, 1160\n"
"ds_read_b128 a[112:115], v244\n"
"ds_read_b128 a[128:131], v244 offset:4352\n"
"ds_read_b128 a[116:119], v244 offset:64\n"
"ds_read_b128 a[132:135], v244 offset:4416\n"
"ds_read_b128 a[120:123], v244 offset:128\n"
"ds_read_b128 a[136:139], v244 offset:4480\n"
"ds_read_b128 a[124:127], v244 offset:192\n"
"ds_read_b128 a[140:143], v244 offset:4544\n"
"s_waitcnt lgkmcnt(7)\n"
"v_mfma_f32_16x16x32_bf16 v[192:195], a[112:115], a[0:3], v[216:219]\n"
"s_waitcnt lgkmcnt(5)\n"
"v_mfma_f32_16x16x32_bf16 v[192:195], a[116:119], a[4:7], v[192:195]\n"
"s_waitcnt lgkmcnt(3)\n"
"v_mfma_f32_16x16x32_bf16 v[192:195], a[120:123], a[8:11], v[192:195]\n"
"s_waitcnt lgkmcnt(1)\n"
"v_mfma_f32_16x16x32_bf16 v[192:195], a[124:127], a[12:15], v[192:195]\n"
"v_mfma_f32_16x16x32_bf16 v[196:199], a[128:131], a[0:3], v[216:219]\n"
"v_mfma_f32_16x16x32_bf16 v[196:199], a[132:135], a[4:7], v[196:199]\n"
"v_mfma_f32_16x16x32_bf16 v[196:199], a[136:139], a[8:11], v[196:199]\n"
"s_waitcnt lgkmcnt(0)\n"
"v_mfma_f32_16x16x32_bf16 v[196:199], a[140:143], a[12:15], v[196:199]\n"
"s_nop 7\n"
"v_mfma_f32_16x16x32_bf16 v[200:203], a[112:115], a[16:19], v[220:223]\n"
"v_exp_f32 v192, v192\n"
"v_mfma_f32_16x16x32_bf16 v[200:203], a[116:119], a[20:23], v[200:203]\n"
"v_exp_f32 v193, v193\n"
"v_mfma_f32_16x16x32_bf16 v[200:203], a[120:123], a[24:27], v[200:203]\n"
"v_exp_f32 v194, v194\n"
"v_mfma_f32_16x16x32_bf16 v[200:203], a[124:127], a[28:31], v[200:203]\n"
"v_exp_f32 v195, v195\n"
"v_mfma_f32_16x16x32_bf16 v[204:207], a[128:131], a[16:19], v[220:223]\n"
"v_exp_f32 v196, v196\n"
"v_mfma_f32_16x16x32_bf16 v[204:207], a[132:135], a[20:23], v[204:207]\n"
"v_exp_f32 v197, v197\n"
"v_mfma_f32_16x16x32_bf16 v[204:207], a[136:139], a[24:27], v[204:207]\n"
"v_exp_f32 v198, v198\n"
"v_mfma_f32_16x16x32_bf16 v[204:207], a[140:143], a[28:31], v[204:207]\n"
"v_exp_f32 v199, v199\n"
"ds_read_b64_tr_b16 a[144:145], v246\n"
"ds_read_b64_tr_b16 a[146:147], v246 offset:4224\n"
"ds_read_b64_tr_b16 a[148:149], v246 offset:32\n"
"v_cvt_pk_bf16_f32 v208, v192, v193\n"
"ds_read_b64_tr_b16 a[150:151], v246 offset:4256\n"
"ds_read_b64_tr_b16 a[152:153], v246 offset:64\n"
"ds_read_b64_tr_b16 a[154:155], v246 offset:4288\n"
"v_cvt_pk_bf16_f32 v209, v194, v195\n"
"ds_read_b64_tr_b16 a[156:157], v246 offset:96\n"
"ds_read_b64_tr_b16 a[158:159], v246 offset:4320\n"
"ds_read_b64_tr_b16 a[160:161], v248\n"
"v_cvt_pk_bf16_f32 v210, v196, v197\n"
"ds_read_b64_tr_b16 a[162:163], v248 offset:4224\n"
"ds_read_b64_tr_b16 a[164:165], v248 offset:32\n"
"ds_read_b64_tr_b16 a[166:167], v248 offset:4256\n"
"v_cvt_pk_bf16_f32 v211, v198, v199\n"
"ds_read_b64_tr_b16 a[168:169], v248 offset:64\n"
"ds_read_b64_tr_b16 a[170:171], v248 offset:4288\n"
"v_mfma_f32_16x16x32_bf16 v[192:195], a[112:115], a[32:35], v[224:227]\n"
"ds_read_b64_tr_b16 a[172:173], v248 offset:96\n"
"ds_read_b64_tr_b16 a[174:175], v248 offset:4320\n"
"v_mfma_f32_16x16x32_bf16 v[192:195], a[116:119], a[36:39], v[192:195]\n"
"v_exp_f32 v200, v200\n"
"v_mfma_f32_16x16x32_bf16 v[192:195], a[120:123], a[40:43], v[192:195]\n"
"s_memrealtime s[28:29]\n"
"v_exp_f32 v201, v201\n"
"v_mfma_f32_16x16x32_bf16 v[192:195], a[124:127], a[44:47], v[192:195]\n"
"v_exp_f32 v202, v202\n"
"v_mfma_f32_16x16x32_bf16 v[196:199], a[128:131], a[32:35], v[224:227]\n"
"v_exp_f32 v203, v203\n"
"v_mfma_f32_16x16x32_bf16 v[196:199], a[132:135], a[36:39], v[196:199]\n"
"v_exp_f32 v204, v204\n"
"v_mfma_f32_16x16x32_bf16 v[196:199], a[136:139], a[40:43], v[196:199]\n"
"v_exp_f32 v205, v205\n"
"v_mfma_f32_16x16x32_bf16 v[196:199], a[140:143], a[44:47], v[196:199]\n"
"s_waitcnt lgkmcnt(14)\n"
"v_exp_f32 v206, v206\n"
"v_mfma_f32_16x16x32_bf16 v[0:3], a[144:147], v[208:211], 0\n"
"s_waitcnt lgkmcnt(12)\n"
"v_exp_f32 v207, v207\n"
"v_mfma_f32_16x16x32_bf16 v[4:7], a[148:151], v[208:211], 0\n"
"s_waitcnt lgkmcnt(10)\n"
"v_cvt_pk_bf16_f32 v212, v200, v201\n"
"v_cvt_pk_bf16_f32 v213, v202, v203\n"
"v_mfma_f32_16x16x32_bf16 v[8:11], a[152:155], v[208:211], 0\n"
"s_waitcnt lgkmcnt(8)\n"
"v_cvt_pk_bf16_f32 v214, v204, v205\n"
"v_cvt_pk_bf16_f32 v215, v206, v207\n"
"v_mfma_f32_16x16x32_bf16 v[12:15], a[156:159], v[208:211], 0\n"
"s_waitcnt lgkmcnt(6)\n"
"v_mfma_f32_16x16x32_bf16 v[16:19], a[160:163], v[208:211], 0\n"
"s_waitcnt lgkmcnt(4)\n"
"v_mfma_f32_16x16x32_bf16 v[20:23], a[164:167], v[208:211], 0\n"
"s_waitcnt lgkmcnt(2)\n"
"v_mfma_f32_16x16x32_bf16 v[24:27], a[168:171], v[208:211], 0\n"
"s_waitcnt lgkmcnt(0)\n"
"v_mfma_f32_16x16x32_bf16 v[28:31], a[172:175], v[208:211], 0\n"
"v_mfma_f32_16x16x32_bf16 a[208:211], a[236:239], v[208:211], 0\n"
"v_mfma_f32_16x16x32_bf16 v[200:203], a[112:115], a[48:51], v[228:231]\n"
"v_exp_f32 v192, v192\n"
"v_mfma_f32_16x16x32_bf16 v[200:203], a[116:119], a[52:55], v[200:203]\n"
"v_exp_f32 v193, v193\n"
"v_mfma_f32_16x16x32_bf16 v[200:203], a[120:123], a[56:59], v[200:203]\n"
"v_exp_f32 v194, v194\n"
"v_mfma_f32_16x16x32_bf16 v[200:203], a[124:127], a[60:63], v[200:203]\n"
"v_exp_f32 v195, v195\n"
"v_mfma_f32_16x16x32_bf16 v[204:207], a[128:131], a[48:51], v[228:231]\n"
"v_exp_f32 v196, v196\n"
"v_mfma_f32_16x16x32_bf16 v[204:207], a[132:135], a[52:55], v[204:207]\n"
"v_exp_f32 v197, v197\n"
"v_mfma_f32_16x16x32_bf16 v[204:207], a[136:139], a[56:59], v[204:207]\n"
"v_exp_f32 v198, v198\n"
"v_mfma_f32_16x16x32_bf16 v[204:207], a[140:143], a[60:63], v[204:207]\n"
"v_exp_f32 v199, v199\n"
"v_mfma_f32_16x16x32_bf16 v[32:35], a[144:147], v[212:215], 0\n"
"v_mfma_f32_16x16x32_bf16 v[36:39], a[148:151], v[212:215], 0\n"
"v_cvt_pk_bf16_f32 v208, v192, v193\n"
"v_cvt_pk_bf16_f32 v209, v194, v195\n"
"v_mfma_f32_16x16x32_bf16 v[40:43], a[152:155], v[212:215], 0\n"
"v_cvt_pk_bf16_f32 v210, v196, v197\n"
"v_cvt_pk_bf16_f32 v211, v198, v199\n"
"v_mfma_f32_16x16x32_bf16 v[44:47], a[156:159], v[212:215], 0\n"
"s_lshl_b32 s33, s28, 3\n"
"v_mfma_f32_16x16x32_bf16 v[48:51], a[160:163], v[212:215], 0\n"
"s_mov_b32 s32, s35\n"
"v_mfma_f32_16x16x32_bf16 v[52:55], a[164:167], v[212:215], 0\n"
"s_max_i32 s30, s32, -400\n"
"v_mfma_f32_16x16x32_bf16 v[56:59], a[168:171], v[212:215], 0\n"
"s_add_i32 s30, s30, s33\n"
"v_mfma_f32_16x16x32_bf16 v[60:63], a[172:175], v[212:215], 0\n"
"s_add_i32 s30, s30, s66\n"
"v_mfma_f32_16x16x32_bf16 a[212:215], a[236:239], v[212:215], 0\n"
"v_mfma_f32_16x16x32_bf16 v[192:195], a[112:115], a[64:67], v[232:235]\n"
"v_exp_f32 v200, v200\n"
"v_mfma_f32_16x16x32_bf16 v[192:195], a[116:119], a[68:71], v[192:195]\n"
"v_exp_f32 v201, v201\n"
"v_mfma_f32_16x16x32_bf16 v[192:195], a[120:123], a[72:75], v[192:195]\n"
"v_exp_f32 v202, v202\n"
"v_mfma_f32_16x16x32_bf16 v[192:195], a[124:127], a[76:79], v[192:195]\n"
"v_exp_f32 v203, v203\n"
"v_mfma_f32_16x16x32_bf16 v[196:199], a[128:131], a[64:67], v[232:235]\n"
"v_exp_f32 v204, v204\n"
"v_mfma_f32_16x16x32_bf16 v[196:199], a[132:135], a[68:71], v[196:199]\n"
"v_exp_f32 v205, v205\n"
"v_mfma_f32_16x16x32_bf16 v[196:199], a[136:139], a[72:75], v[196:199]\n"
"v_exp_f32 v206, v206\n"
"v_mfma_f32_16x16x32_bf16 v[196:199], a[140:143], a[76:79], v[196:199]\n"
"v_exp_f32 v207, v207\n"
"v_mfma_f32_16x16x32_bf16 v[64:67], a[144:147], v[208:211], 0\n"
"v_mfma_f32_16x16x32_bf16 v[68:71], a[148:151], v[208:211], 0\n"
"v_cvt_pk_bf16_f32 v212, v200, v201\n"
"v_cvt_pk_bf16_f32 v213, v202, v203\n"
"v_mfma_f32_16x16x32_bf16 v[72:75], a[152:155], v[208:211], 0\n"
"v_cvt_pk_bf16_f32 v214, v204, v205\n"
"v_cvt_pk_bf16_f32 v215, v206, v207\n"
"v_mfma_f32_16x16x32_bf16 v[76:79], a[156:159], v[208:211], 0\n"
"s_sub_i32 s66, s66, 15\n"
"v_mfma_f32_16x16x32_bf16 v[80:83], a[160:163], v[208:211], 0\n"
"s_max_i32 s32, s32, 0\n"
"v_mfma_f32_16x16x32_bf16 v[84:87], a[164:167], v[208:211], 0\n"
"s_mul_i32 s32, s32, 13\n"
"v_mfma_f32_16x16x32_bf16 v[88:91], a[168:171], v[208:211], 0\n"
"s_lshr_b32 s32, s32, 8\n"
"v_mfma_f32_16x16x32_bf16 v[92:95], a[172:175], v[208:211], 0\n"
"s_min_u32 s31, s32, 24\n"
"v_mfma_f32_16x16x32_bf16 a[216:219], a[236:239], v[208:211], 0\n"
"1:\n"
"s_sub_u32 s31, s31, 1\n"
"s_cbranch_scc1 2f\n"
"s_nop 7\n"
"s_branch 1b\n"
"2:\n"
"s_barrier\n"
"v_mfma_f32_16x16x32_bf16 v[200:203], a[112:115], a[80:83], v[236:239]\n"
"v_exp_f32 v192, v192\n"
"v_mfma_f32_16x16x32_bf16 v[200:203], a[116:119], a[84:87], v[200:203]\n"
"v_exp_f32 v193, v193\n"
"v_mfma_f32_16x16x32_bf16 v[200:203], a[120:123], a[88:91], v[200:203]\n"
"v_exp_f32 v194, v194\n"
"v_mfma_f32_16x16x32_bf16 v[200:203], a[124:127], a[92:95], v[200:203]\n"
"v_exp_f32 v195, v195\n"
"v_mfma_f32_16x16x32_bf16 v[204:207], a[128:131], a[80:83], v[236:239]\n"
"v_exp_f32 v196, v196\n"
"v_mfma_f32_16x16x32_bf16 v[204:207], a[132:135], a[84:87], v[204:207]\n"
"v_exp_f32 v197, v197\n"
"v_mfma_f32_16x16x32_bf16 v[204:207], a[136:139], a[88:91], v[204:207]\n"
"v_exp_f32 v198, v198\n"
"v_mfma_f32_16x16x32_bf16 v[204:207], a[140:143], a[92:95], v[204:207]\n"
"v_exp_f32 v199, v199\n"
"v_mfma_f32_16x16x32_bf16 v[96:99], a[144:147], v[212:215], 0\n"
"v_mfma_f32_16x16x32_bf16 v[100:103], a[148:151], v[212:215], 0\n"
"s_add_u32 m0, s50, 0\n"
"v_cvt_pk_bf16_f32 v208, v192, v193\n"
"v_mfma_f32_16x16x32_bf16 v[104:107], a[152:155], v[212:215], 0\n"
"buffer_load_dwordx4 v250, s[36:39], s53 offen nt lds\n"
"v_mfma_f32_16x16x32_bf16 v[108:111], a[156:159], v[212:215], 0\n"
"s_add_u32 m0, s50, 64\n"
"v_cvt_pk_bf16_f32 v209, v194, v195\n"
"v_mfma_f32_16x16x32_bf16 v[112:115], a[160:163], v[212:215], 0\n"
"buffer_load_dwordx4 v250, s[36:39], s53 offen offset:1024 nt lds\n"
"v_mfma_f32_16x16x32_bf16 v[116:119], a[164:167], v[212:215], 0\n"
"s_add_u32 m0, s50, 128\n"
"v_cvt_pk_bf16_f32 v210, v196, v197\n"
"v_mfma_f32_16x16x32_bf16 v[120:123], a[168:171], v[212:215], 0\n"
"buffer_load_dwordx4 v250, s[36:39], s53 offen offset:2048 nt lds\n"
"v_mfma_f32_16x16x32_bf16 v[124:127], a[172:175], v[212:215], 0\n"
"s_add_u32 m0, s50, 192\n"
"v_cvt_pk_bf16_f32 v211, v198, v199\n"
"v_mfma_f32_16x16x32_bf16 a[220:223], a[236:239], v[212:215], 0\n"
"buffer_load_dwordx4 v250, s[36:39], s53 offen offset:3072 nt lds\n"
"v_mfma_f32_16x16x32_bf16 v[192:195], a[112:115], a[96:99], v[240:243]\n"
"v_exp_f32 v200, v200\n"
"v_mfma_f32_16x16x32_bf16 v[192:195], a[116:119], a[100:103], v[192:195]\n"
"ds_read_b128 a[112:115], v244 offset:34304\n"
"ds_read_b128 a[116:119], v244 offset:34368\n"
"v_mfma_f32_16x16x32_bf16 v[192:195], a[120:123], a[104:107], v[192:195]\n"
"v_exp_f32 v201, v201\n"
"v_mfma_f32_16x16x32_bf16 v[192:195], a[124:127], a[108:111], v[192:195]\n"
"ds_read_b128 a[120:123], v244 offset:34432\n"
"ds_read_b128 a[124:127], v244 offset:34496\n"
"v_mfma_f32_16x16x32_bf16 v[196:199], a[128:131], a[96:99], v[240:243]\n"
"v_exp_f32 v202, v202\n"
"v_mfma_f32_16x16x32_bf16 v[196:199], a[132:135], a[100:103], v[196:199]\n"
"ds_read_b128 a[128:131], v244 offset:38656\n"
"ds_read_b128 a[132:135], v244 offset:38720\n"
"v_mfma_f32_16x16x32_bf16 v[196:199], a[136:139], a[104:107], v[196:199]\n"
"v_exp_f32 v203, v203\n"
"v_mfma_f32_16x16x32_bf16 v[196:199], a[140:143], a[108:111], v[196:199]\n"
"ds_read_b128 a[136:139], v244 offset:38784\n"
"ds_read_b128 a[140:143], v244 offset:38848\n"
"v_mfma_f32_16x16x32_bf16 v[128:131], a[144:147], v[208:211], 0\n"
"v_exp_f32 v204, v204\n"
"v_mfma_f32_16x16x32_bf16 v[132:135], a[148:151], v[208:211], 0\n"
"v_exp_f32 v205, v205\n"
"v_mfma_f32_16x16x32_bf16 v[136:139], a[152:155], v[208:211], 0\n"
"v_exp_f32 v206, v206\n"
"v_mfma_f32_16x16x32_bf16 v[140:143], a[156:159], v[208:211], 0\n"
"v_exp_f32 v207, v207\n"
"v_mfma_f32_16x16x32_bf16 v[144:147], a[160:163], v[208:211], 0\n"
"v_cvt_pk_bf16_f32 v212, v200, v201\n"
"v_cvt_pk_bf16_f32 v213, v202, v203\n"
"v_mfma_f32_16x16x32_bf16 v[148:151], a[164:167], v[208:211], 0\n"
"v_cvt_pk_bf16_f32 v214, v204, v205\n"
"v_cvt_pk_bf16_f32 v215, v206, v207\n"
"v_mfma_f32_16x16x32_bf16 v[152:155], a[168:171], v[208:211], 0\n"
"v_mfma_f32_16x16x32_bf16 v[156:159], a[172:175], v[208:211], 0\n"
"v_mfma_f32_16x16x32_bf16 a[224:227], a[236:239], v[208:211], 0\n"
"s_waitcnt lgkmcnt(7)\n"
"v_mfma_f32_16x16x32_bf16 v[200:203], a[112:115], a[0:3], v[216:219]\n"
"s_waitcnt lgkmcnt(6)\n"
"v_exp_f32 v192, v192\n"
"v_mfma_f32_16x16x32_bf16 v[200:203], a[116:119], a[4:7], v[200:203]\n"
"s_waitcnt lgkmcnt(5)\n"
"v_exp_f32 v193, v193\n"
"v_mfma_f32_16x16x32_bf16 v[200:203], a[120:123], a[8:11], v[200:203]\n"
"s_waitcnt lgkmcnt(4)\n"
"v_exp_f32 v194, v194\n"
"v_mfma_f32_16x16x32_bf16 v[200:203], a[124:127], a[12:15], v[200:203]\n"
"s_waitcnt lgkmcnt(3)\n"
"v_exp_f32 v195, v195\n"
"v_mfma_f32_16x16x32_bf16 v[204:207], a[128:131], a[0:3], v[216:219]\n"
"s_waitcnt lgkmcnt(2)\n"
"v_exp_f32 v196, v196\n"
"v_mfma_f32_16x16x32_bf16 v[204:207], a[132:135], a[4:7], v[204:207]\n"
"s_waitcnt lgkmcnt(1)\n"
"v_exp_f32 v197, v197\n"
"v_mfma_f32_16x16x32_bf16 v[204:207], a[136:139], a[8:11], v[204:207]\n"
"s_waitcnt lgkmcnt(0)\n"
"v_exp_f32 v198, v198\n"
"v_mfma_f32_16x16x32_bf16 v[204:207], a[140:143], a[12:15], v[204:207]\n"
"v_exp_f32 v199, v199\n"
"v_mfma_f32_16x16x32_bf16 v[160:163], a[144:147], v[212:215], 0\n"
"s_add_u32 s53, s53, 8192\n"
"s_and_b32 s53, s53, 258047\n"
"v_mfma_f32_16x16x32_bf16 v[164:167], a[148:151], v[212:215], 0\n"
"s_add_u32 m0, s50, 4352\n"
"v_cvt_pk_bf16_f32 v208, v192, v193\n"
"v_mfma_f32_16x16x32_bf16 v[168:171], a[152:155], v[212:215], 0\n"
"buffer_load_dwordx4 v250, s[36:39], s54 offen nt lds\n"
"v_mfma_f32_16x16x32_bf16 v[172:175], a[156:159], v[212:215], 0\n"
"s_add_u32 m0, s50, 4416\n"
"v_cvt_pk_bf16_f32 v209, v194, v195\n"
"v_mfma_f32_16x16x32_bf16 v[176:179], a[160:163], v[212:215], 0\n"
"buffer_load_dwordx4 v250, s[36:39], s54 offen offset:1024 nt lds\n"
"v_mfma_f32_16x16x32_bf16 v[180:183], a[164:167], v[212:215], 0\n"
"s_add_u32 m0, s50, 4480\n"
"v_cvt_pk_bf16_f32 v210, v196, v197\n"
"v_mfma_f32_16x16x32_bf16 v[184:187], a[168:171], v[212:215], 0\n"
"buffer_load_dwordx4 v250, s[36:39], s54 offen offset:2048 nt lds\n"
"v_mfma_f32_16x16x32_bf16 v[188:191], a[172:175], v[212:215], 0\n"
"s_add_u32 m0, s50, 4544\n"
"v_cvt_pk_bf16_f32 v211, v198, v199\n"
"v_mfma_f32_16x16x32_bf16 a[228:231], a[236:239], v[212:215], 0\n"
"buffer_load_dwordx4 v250, s[36:39], s54 offen offset:3072 nt lds\n"
"s_lshl_b32 s38, s38, 1\n"
"s_add_u32 s54, s53, 4096\n"
"v_mfma_f32_16x16x32_bf16 v[192:195], a[112:115], a[16:19], v[220:223]\n"
"v_exp_f32 v200, v200\n"
"v_mfma_f32_16x16x32_bf16 v[192:195], a[116:119], a[20:23], v[192:195]\n"
"v_exp_f32 v201, v201\n"
"v_mfma_f32_16x16x32_bf16 v[192:195], a[120:123], a[24:27], v[192:195]\n"
"v_exp_f32 v202, v202\n"
"v_mfma_f32_16x16x32_bf16 v[192:195], a[124:127], a[28:31], v[192:195]\n"
"v_exp_f32 v203, v203\n"
"v_mfma_f32_16x16x32_bf16 v[196:199], a[128:131], a[16:19], v[220:223]\n"
"v_exp_f32 v204, v204\n"
"v_mfma_f32_16x16x32_bf16 v[196:199], a[132:135], a[20:23], v[196:199]\n"
"v_exp_f32 v205, v205\n"
"v_mfma_f32_16x16x32_bf16 v[196:199], a[136:139], a[24:27], v[196:199]\n"
"v_exp_f32 v206, v206\n"
"v_mfma_f32_16x16x32_bf16 v[196:199], a[140:143], a[28:31], v[196:199]\n"
"v_exp_f32 v207, v207\n"
"v_mfma_f32_16x16x32_bf16 a[176:179], a[144:147], v[208:211], 0\n"
"ds_read_b64_tr_b16 a[144:145], v246 offset:34304\n"
"ds_read_b64_tr_b16 a[146:147], v246 offset:38528\n"
"v_mfma_f32_16x16x32_bf16 a[180:183], a[148:151], v[208:211], 0\n"
"ds_read_b64_tr_b16 a[148:149], v246 offset:34336\n"
"v_cvt_pk_bf16_f32 v212, v200, v201\n"
"v_mfma_f32_16x16x32_bf16 a[184:187], a[152:155], v[208:211], 0\n"
"ds_read_b64_tr_b16 a[150:151], v246 offset:38560\n"
"ds_read_b64_tr_b16 a[152:153], v246 offset:34368\n"
"v_mfma_f32_16x16x32_bf16 a[188:191], a[156:159], v[208:211], 0\n"
"ds_read_b64_tr_b16 a[154:155], v246 offset:38592\n"
"v_cvt_pk_bf16_f32 v213, v202, v203\n"
"v_mfma_f32_16x16x32_bf16 a[192:195], a[160:163], v[208:211], 0\n"
"ds_read_b64_tr_b16 a[156:157], v246 offset:34400\n"
"ds_read_b64_tr_b16 a[158:159], v246 offset:38624\n"
"v_mfma_f32_16x16x32_bf16 a[196:199], a[164:167], v[208:211], 0\n"
"ds_read_b64_tr_b16 a[160:161], v248 offset:34304\n"
"v_cvt_pk_bf16_f32 v214, v204, v205\n"
"v_mfma_f32_16x16x32_bf16 a[200:203], a[168:171], v[208:211], 0\n"
"ds_read_b64_tr_b16 a[162:163], v248 offset:38528\n"
"ds_read_b64_tr_b16 a[164:165], v248 offset:34336\n"
"v_mfma_f32_16x16x32_bf16 a[204:207], a[172:175], v[208:211], 0\n"
"ds_read_b64_tr_b16 a[166:167], v248 offset:38560\n"
"v_cvt_pk_bf16_f32 v215, v206, v207\n"
"v_mfma_f32_16x16x32_bf16 a[232:235], a[236:239], v[208:211], 0\n"
"ds_read_b64_tr_b16 a[168:169], v248 offset:34368\n"
"ds_read_b64_tr_b16 a[170:171], v248 offset:38592\n"
"v_mfma_f32_16x16x32_bf16 v[200:203], a[112:115], a[32:35], v[224:227]\n"
"ds_read_b64_tr_b16 a[172:173], v248 offset:34400\n"
"ds_read_b64_tr_b16 a[174:175], v248 offset:38624\n"
"v_mfma_f32_16x16x32_bf16 v[200:203], a[116:119], a[36:39], v[200:203]\n"
"v_exp_f32 v192, v192\n"
"v_mfma_f32_16x16x32_bf16 v[200:203], a[120:123], a[40:43], v[200:203]\n"
"s_memrealtime s[28:29]\n"
"v_exp_f32 v193, v193\n"
"v_mfma_f32_16x16x32_bf16 v[200:203], a[124:127], a[44:47], v[200:203]\n"
"v_exp_f32 v194, v194\n"
"v_mfma_f32_16x16x32_bf16 v[204:207], a[128:131], a[32:35], v[224:227]\n"
"v_exp_f32 v195, v195\n"
"v_mfma_f32_16x16x32_bf16 v[204:207], a[132:135], a[36:39], v[204:207]\n"
"v_exp_f32 v196, v196\n"
"v_mfma_f32_16x16x32_bf16 v[204:207], a[136:139], a[40:43], v[204:207]\n"
"v_exp_f32 v197, v197\n"
"v_mfma_f32_16x16x32_bf16 v[204:207], a[140:143], a[44:47], v[204:207]\n"
"s_waitcnt lgkmcnt(14)\n"
"v_exp_f32 v198, v198\n"
"v_mfma_f32_16x16x32_bf16 v[0:3], a[144:147], v[212:215], v[0:3]\n"
"s_waitcnt lgkmcnt(12)\n"
"v_exp_f32 v199, v199\n"
"v_mfma_f32_16x16x32_bf16 v[4:7], a[148:151], v[212:215], v[4:7]\n"
"s_waitcnt lgkmcnt(10)\n"
"v_cvt_pk_bf16_f32 v208, v192, v193\n"
"v_cvt_pk_bf16_f32 v209, v194, v195\n"
"v_mfma_f32_16x16x32_bf16 v[8:11], a[152:155], v[212:215], v[8:11]\n"
"s_waitcnt lgkmcnt(8)\n"
"v_cvt_pk_bf16_f32 v210, v196, v197\n"
"v_cvt_pk_bf16_f32 v211, v198, v199\n"
"v_mfma_f32_16x16x32_bf16 v[12:15], a[156:159], v[212:215], v[12:15]\n"
"s_waitcnt lgkmcnt(6)\n"
"v_mfma_f32_16x16x32_bf16 v[16:19], a[160:163], v[212:215], v[16:19]\n"
"s_waitcnt lgkmcnt(4)\n"
"v_mfma_f32_16x16x32_bf16 v[20:23], a[164:167], v[212:215], v[20:23]\n"
"s_waitcnt lgkmcnt(2)\n"
"v_mfma_f32_16x16x32_bf16 v[24:27], a[168:171], v[212:215], v[24:27]\n"
"s_waitcnt lgkmcnt(0)\n"
"v_mfma_f32_16x16x32_bf16 v[28:31], a[172:175], v[212:215], v[28:31]\n"
"v_mfma_f32_16x16x32_bf16 a[208:211], a[236:239], v[212:215], a[208:211]\n"
"v_mfma_f32_16x16x32_bf16 v[192:195], a[112:115], a[48:51], v[228:231]\n"
"v_exp_f32 v200, v200\n"
"v_mfma_f32_16x16x32_bf16 v[192:195], a[116:119], a[52:55], v[192:195]\n"
"v_exp_f32 v201, v201\n"
"v_mfma_f32_16x16x32_bf16 v[192:195], a[120:123], a[56:59], v[192:195]\n"
"v_exp_f32 v202, v202\n"
"v_mfma_f32_16x16x32_bf16 v[192:195], a[124:127], a[60:63], v[192:195]\n"
"v_exp_f32 v203, v203\n"
"v_mfma_f32_16x16x32_bf16 v[196:199], a[128:131], a[48:51], v[228:231]\n"
"v_exp_f32 v204, v204\n"
"v_mfma_f32_16x16x32_bf16 v[196:199], a[132:135], a[52:55], v[196:199]\n"
"v_exp_f32 v205, v205\n"
"v_mfma_f32_16x16x32_bf16 v[196:199], a[136:139], a[56:59], v[196:199]\n"
"v_exp_f32 v206, v206\n"
"v_mfma_f32_16x16x32_bf16 v[196:199], a[140:143], a[60:63], v[196:199]\n"
"v_exp_f32 v207, v207\n"
"v_mfma_f32_16x16x32_bf16 v[32:35], a[144:147], v[208:211], v[32:35]\n"
"v_mfma_f32_16x16x32_bf16 v[36:39], a[148:151], v[208:211], v[36:39]\n"
"v_cvt_pk_bf16_f32 v212, v200, v201\n"
"v_cvt_pk_bf16_f32 v213, v202, v203\n"
"v_mfma_f32_16x16x32_bf16 v[40:43], a[152:155], v[208:211], v[40:43]\n"
"v_cvt_pk_bf16_f32 v214, v204, v205\n"
"v_cvt_pk_bf16_f32 v215, v206, v207\n"
"v_mfma_f32_16x16x32_bf16 v[44:47], a[156:159], v[208:211], v[44:47]\n"
"s_lshl_b32 s33, s28, 3\n"
"v_mfma_f32_16x16x32_bf16 v[48:51], a[160:163], v[208:211], v[48:51]\n"
"s_sub_i32 s32, s30, s33\n"
"v_mfma_f32_16x16x32_bf16 v[52:55], a[164:167], v[208:211], v[52:55]\n"
"s_max_i32 s30, s32, -400\n"
"v_mfma_f32_16x16x32_bf16 v[56:59], a[168:171], v[208:211], v[56:59]\n"
"s_add_i32 s30, s30, s33\n"
"v_mfma_f32_16x16x32_bf16 v[60:63], a[172:175], v[208:211], v[60:63]\n"
"s_add_i32 s30, s30, s66\n"
"v_mfma_f32_16x16x32_bf16 a[212:215], a[236:239], v[208:211], a[212:215]\n"
"v_mfma_f32_16x16x32_bf16 v[200:203], a[112:115], a[64:67], v[232:235]\n"
"v_exp_f32 v192, v192\n"
"v_mfma_f32_16x16x32_bf16 v[200:203], a[116:119], a[68:71], v[200:203]\n"
"v_exp_f32 v193, v193\n"
"v_mfma_f32_16x16x32_bf16 v[200:203], a[120:123], a[72:75], v[200:203]\n"
"v_exp_f32 v194, v194\n"
"v_mfma_f32_16x16x32_bf16 v[200:203], a[124:127], a[76:79], v[200:203]\n"
"v_exp_f32 v195, v195\n"
"v_mfma_f32_16x16x32_bf16 v[204:207], a[128:131], a[64:67], v[232:235]\n"
"v_exp_f32 v196, v196\n"
"v_mfma_f32_16x16x32_bf16 v[204:207], a[132:135], a[68:71], v[204:207]\n"
"v_exp_f32 v197, v197\n"
"v_mfma_f32_16x16x32_bf16 v[204:207], a[136:139], a[72:75], v[204:207]\n"
"v_exp_f32 v198, v198\n"
"v_mfma_f32_16x16x32_bf16 v[204:207], a[140:143], a[76:79], v[204:207]\n"
"v_exp_f32 v199, v199\n"
"v_mfma_f32_16x16x32_bf16 v[64:67], a[144:147], v[212:215], v[64:67]\n"
"v_mfma_f32_16x16x32_bf16 v[68:71], a[148:151], v[212:215], v[68:71]\n"
"v_cvt_pk_bf16_f32 v208, v192, v193\n"
"v_cvt_pk_bf16_f32 v209, v194, v195\n"
"v_mfma_f32_16x16x32_bf16 v[72:75], a[152:155], v[212:215], v[72:75]\n"
"v_cvt_pk_bf16_f32 v210, v196, v197\n"
"v_cvt_pk_bf16_f32 v211, v198, v199\n"
"v_mfma_f32_16x16x32_bf16 v[76:79], a[156:159], v[212:215], v[76:79]\n"
"s_sub_i32 s66, s66, 15\n"
"v_mfma_f32_16x16x32_bf16 v[80:83], a[160:163], v[212:215], v[80:83]\n"
"s_max_i32 s32, s32, 0\n"
"v_mfma_f32_16x16x32_bf16 v[84:87], a[164:167], v[212:215], v[84:87]\n"
"s_mul_i32 s32, s32, 13\n"
"v_mfma_f32_16x16x32_bf16 v[88:91], a[168:171], v[212:215], v[88:91]\n"
"s_lshr_b32 s32, s32, 8\n"
"v_mfma_f32_16x16x32_bf16 v[92:95], a[172:175], v[212:215], v[92:95]\n"
"s_min_u32 s31, s32, 24\n"
"v_mfma_f32_16x16x32_bf16 a[216:219], a[236:239], v[212:215], a[216:219]\n"
"1:\n"
"s_sub_u32 s31, s31, 1\n"
"s_cbranch_scc1 2f\n"
"s_nop 7\n"
"s_branch 1b\n"
"2:\n"
"s_barrier\n"
"v_mfma_f32_16x16x32_bf16 v[192:195], a[112:115], a[80:83], v[236:239]\n"
"v_exp_f32 v200, v200\n"
"v_mfma_f32_16x16x32_bf16 v[192:195], a[116:119], a[84:87], v[192:195]\n"
"ds_read_b128 a[112:115], v245\n"
"ds_read_b128 a[116:119], v245 offset:64\n"
"v_mfma_f32_16x16x32_bf16 v[192:195], a[120:123], a[88:91], v[192:195]\n"
"v_exp_f32 v201, v201\n"
"v_mfma_f32_16x16x32_bf16 v[192:195], a[124:127], a[92:95], v[192:195]\n"
"ds_read_b128 a[120:123], v245 offset:128\n"
"ds_read_b128 a[124:127], v245 offset:192\n"
"v_mfma_f32_16x16x32_bf16 v[196:199], a[128:131], a[80:83], v[236:239]\n"
"v_exp_f32 v202, v202\n"
"v_mfma_f32_16x16x32_bf16 v[196:199], a[132:135], a[84:87], v[196:199]\n"
"ds_read_b128 a[128:131], v245 offset:4352\n"
"ds_read_b128 a[132:135], v245 offset:4416\n"
"v_mfma_f32_16x16x32_bf16 v[196:199], a[136:139], a[88:91], v[196:199]\n"
"v_exp_f32 v203, v203\n"
"v_mfma_f32_16x16x32_bf16 v[196:199], a[140:143], a[92:95], v[196:199]\n"
"ds_read_b128 a[136:139], v245 offset:4480\n"
"ds_read_b128 a[140:143], v245 offset:4544\n"
"v_mfma_f32_16x16x32_bf16 v[96:99], a[144:147], v[208:211], v[96:99]\n"
"v_exp_f32 v204, v204\n"
"v_mfma_f32_16x16x32_bf16 v[100:103], a[148:151], v[208:211], v[100:103]\n"
"v_exp_f32 v205, v205\n"
"v_mfma_f32_16x16x32_bf16 v[104:107], a[152:155], v[208:211], v[104:107]\n"
"v_exp_f32 v206, v206\n"
"v_mfma_f32_16x16x32_bf16 v[108:111], a[156:159], v[208:211], v[108:111]\n"
"v_exp_f32 v207, v207\n"
"v_mfma_f32_16x16x32_bf16 v[112:115], a[160:163], v[208:211], v[112:115]\n"
"v_cvt_pk_bf16_f32 v212, v200, v201\n"
"v_cvt_pk_bf16_f32 v213, v202, v203\n"
"v_mfma_f32_16x16x32_bf16 v[116:119], a[164:167], v[208:211], v[116:119]\n"
"v_cvt_pk_bf16_f32 v214, v204, v205\n"
"v_cvt_pk_bf16_f32 v215, v206, v207\n"
"v_mfma_f32_16x16x32_bf16 v[120:123], a[168:171], v[208:211], v[120:123]\n"
"v_mfma_f32_16x16x32_bf16 v[124:127], a[172:175], v[208:211], v[124:127]\n"
"v_mfma_f32_16x16x32_bf16 a[220:223], a[236:239], v[208:211], a[220:223]\n"
"s_waitcnt lgkmcnt(7)\n"
"v_mfma_f32_16x16x32_bf16 v[200:203], a[112:115], a[0:3], v[216:219]\n"
"s_waitcnt lgkmcnt(6)\n"
"v_exp_f32 v192, v192\n"
"v_mfma_f32_16x16x32_bf16 v[200:203], a[116:119], a[4:7], v[200:203]\n"
"s_waitcnt lgkmcnt(5)\n"
"v_exp_f32 v193, v193\n"
"v_mfma_f32_16x16x32_bf16 v[200:203], a[120:123], a[8:11], v[200:203]\n"
"s_waitcnt lgkmcnt(4)\n"
"v_exp_f32 v194, v194\n"
"v_mfma_f32_16x16x32_bf16 v[200:203], a[124:127], a[12:15], v[200:203]\n"
"s_waitcnt lgkmcnt(3)\n"
"v_exp_f32 v195, v195\n"
"v_mfma_f32_16x16x32_bf16 v[204:207], a[128:131], a[0:3], v[216:219]\n"
"s_waitcnt lgkmcnt(2)\n"
"v_exp_f32 v196, v196\n"
"v_mfma_f32_16x16x32_bf16 v[204:207], a[132:135], a[4:7], v[204:207]\n"
"s_waitcnt lgkmcnt(1)\n"
"v_exp_f32 v197, v197\n"
"v_mfma_f32_16x16x32_bf16 v[204:207], a[136:139], a[8:11], v[204:207]\n"
"s_waitcnt lgkmcnt(0)\n"
"v_exp_f32 v198, v198\n"
"v_mfma_f32_16x16x32_bf16 v[204:207], a[140:143], a[12:15], v[204:207]\n"
"v_exp_f32 v199, v199\n"
"v_mfma_f32_16x16x32_bf16 v[128:131], a[144:147], v[212:215], v[128:131]\n"
"v_mfma_f32_16x16x32_bf16 v[132:135], a[148:151], v[212:215], v[132:135]\n"
"s_add_u32 m0, s50, 34304\n"
"v_cvt_pk_bf16_f32 v208, v192, v193\n"
"v_mfma_f32_16x16x32_bf16 v[136:139], a[152:155], v[212:215], v[136:139]\n"
"buffer_load_dwordx4 v250, s[36:39], s53 offen nt lds\n"
"v_mfma_f32_16x16x32_bf16 v[140:143], a[156:159], v[212:215], v[140:143]\n"
"s_add_u32 m0, s50, 34368\n"
"v_cvt_pk_bf16_f32 v209, v194, v195\n"
"v_mfma_f32_16x16x32_bf16 v[144:147], a[160:163], v[212:215], v[144:147]\n"
"buffer_load_dwordx4 v250, s[36:39], s53 offen offset:1024 nt lds\n"
"v_mfma_f32_16x16x32_bf16 v[148:151], a[164:167], v[212:215], v[148:151]\n"
"s_add_u32 m0, s50, 34432\n"
"v_cvt_pk_bf16_f32 v210, v196, v197\n"
"v_mfma_f32_16x16x32_bf16 v[152:155], a[168:171], v[212:215], v[152:155]\n"
"buffer_load_dwordx4 v250, s[36:39], s53 offen offset:2048 nt lds\n"
"v_mfma_f32_16x16x32_bf16 v[156:159], a[172:175], v[212:215], v[156:159]\n"
"s_add_u32 m0, s50, 34496\n"
"v_cvt_pk_bf16_f32 v211, v198, v199\n"
"v_mfma_f32_16x16x32_bf16 a[224:227], a[236:239], v[212:215], a[224:227]\n"
"buffer_load_dwordx4 v250, s[36:39], s53 offen offset:3072 nt lds\n"
"v_mfma_f32_16x16x32_bf16 v[192:195], a[112:115], a[16:19], v[220:223]\n"
"v_exp_f32 v200, v200\n"
"v_mfma_f32_16x16x32_bf16 v[192:195], a[116:119], a[20:23], v[192:195]\n"
"v_exp_f32 v201, v201\n"
"v_mfma_f32_16x16x32_bf16 v[192:195], a[120:123], a[24:27], v[192:195]\n"
"v_exp_f32 v202, v202\n"
"v_mfma_f32_16x16x32_bf16 v[192:195], a[124:127], a[28:31], v[192:195]\n"
"v_exp_f32 v203, v203\n"
"v_mfma_f32_16x16x32_bf16 v[196:199], a[128:131], a[16:19], v[220:223]\n"
"v_exp_f32 v204, v204\n"
"v_mfma_f32_16x16x32_bf16 v[196:199], a[132:135], a[20:23], v[196:199]\n"
"v_exp_f32 v205, v205\n"
"v_mfma_f32_16x16x32_bf16 v[196:199], a[136:139], a[24:27], v[196:199]\n"
"v_exp_f32 v206, v206\n"
"v_mfma_f32_16x16x32_bf16 v[196:199], a[140:143], a[28:31], v[196:199]\n"
"v_exp_f32 v207, v207\n"
"v_mfma_f32_16x16x32_bf16 v[160:163], a[144:147], v[208:211], v[160:163]\n"
"ds_read_b64_tr_b16 a[144:145], v247\n"
"ds_read_b64_tr_b16 a[146:147], v247 offset:4224\n"
"v_mfma_f32_16x16x32_bf16 v[164:167], a[148:151], v[208:211], v[164:167]\n"
"ds_read_b64_tr_b16 a[148:149], v247 offset:32\n"
"v_cvt_pk_bf16_f32 v212, v200, v201\n"
"v_mfma_f32_16x16x32_bf16 v[168:171], a[152:155], v[208:211], v[168:171]\n"
"ds_read_b64_tr_b16 a[150:151], v247 offset:4256\n"
"ds_read_b64_tr_b16 a[152:153], v247 offset:64\n"
"v_mfma_f32_16x16x32_bf16 v[172:175], a[156:159], v[208:211], v[172:175]\n"
"ds_read_b64_tr_b16 a[154:155], v247 offset:4288\n"
"v_cvt_pk_bf16_f32 v213, v202, v203\n"
"v_mfma_f32_16x16x32_bf16 v[176:179], a[160:163], v[208:211], v[176:179]\n"
"ds_read_b64_tr_b16 a[156:157], v247 offset:96\n"
"ds_read_b64_tr_b16 a[158:159], v247 offset:4320\n"
"v_mfma_f32_16x16x32_bf16 v[180:183], a[164:167], v[208:211], v[180:183]\n"
"ds_read_b64_tr_b16 a[160:161], v249\n"
"v_cvt_pk_bf16_f32 v214, v204, v205\n"
"v_mfma_f32_16x16x32_bf16 v[184:187], a[168:171], v[208:211], v[184:187]\n"
"ds_read_b64_tr_b16 a[162:163], v249 offset:4224\n"
"ds_read_b64_tr_b16 a[164:165], v249 offset:32\n"
"v_mfma_f32_16x16x32_bf16 v[188:191], a[172:175], v[208:211], v[188:191]\n"
"ds_read_b64_tr_b16 a[166:167], v249 offset:4256\n"
"v_cvt_pk_bf16_f32 v215, v206, v207\n"
"v_mfma_f32_16x16x32_bf16 a[228:231], a[236:239], v[208:211], a[228:231]\n"
"ds_read_b64_tr_b16 a[168:169], v249 offset:64\n"
"ds_read_b64_tr_b16 a[170:171], v249 offset:4288\n"
"v_mfma_f32_16x16x32_bf16 v[200:203], a[112:115], a[32:35], v[224:227]\n"
"ds_read_b64_tr_b16 a[172:173], v249 offset:96\n"
"ds_read_b64_tr_b16 a[174:175], v249 offset:4320\n"
"v_mfma_f32_16x16x32_bf16 v[200:203], a[116:119], a[36:39], v[200:203]\n"
"v_exp_f32 v192, v192\n"
"v_mfma_f32_16x16x32_bf16 v[200:203], a[120:123], a[40:43], v[200:203]\n"
"s_memrealtime s[28:29]\n"
"v_exp_f32 v193, v193\n"
"v_mfma_f32_16x16x32_bf16 v[200:203], a[124:127], a[44:47], v[200:203]\n"
"v_exp_f32 v194, v194\n"
"v_mfma_f32_16x16x32_bf16 v[204:207], a[128:131], a[32:35], v[224:227]\n"
"v_exp_f32 v195, v195\n"
"v_mfma_f32_16x16x32_bf16 v[204:207], a[132:135], a[36:39], v[204:207]\n"
"v_exp_f32 v196, v196\n"
"v_mfma_f32_16x16x32_bf16 v[204:207], a[136:139], a[40:43], v[204:207]\n"
"v_exp_f32 v197, v197\n"
"v_mfma_f32_16x16x32_bf16 v[204:207], a[140:143], a[44:47], v[204:207]\n"
"s_waitcnt lgkmcnt(14)\n"
"v_exp_f32 v198, v198\n"
"v_mfma_f32_16x16x32_bf16 v[0:3], a[144:147], v[212:215], v[0:3]\n"
"s_waitcnt lgkmcnt(12)\n"
"v_exp_f32 v199, v199\n"
"v_mfma_f32_16x16x32_bf16 v[4:7], a[148:151], v[212:215], v[4:7]\n"
"s_waitcnt lgkmcnt(10)\n"
"s_add_u32 m0, s50, 38656\n"
"v_cvt_pk_bf16_f32 v208, v192, v193\n"
"v_mfma_f32_16x16x32_bf16 v[8:11], a[152:155], v[212:215], v[8:11]\n"
"s_waitcnt lgkmcnt(8)\n"
"buffer_load_dwordx4 v250, s[36:39], s54 offen nt lds\n"
"s_add_u32 s53, s53, 8192\n"
"v_mfma_f32_16x16x32_bf16 v[12:15], a[156:159], v[212:215], v[12:15]\n"
"s_waitcnt lgkmcnt(6)\n"
"s_add_u32 m0, s50, 38720\n"
"v_cvt_pk_bf16_f32 v209, v194, v195\n"
"v_mfma_f32_16x16x32_bf16 v[16:19], a[160:163], v[212:215], v[16:19]\n"
"s_waitcnt lgkmcnt(4)\n"
"buffer_load_dwordx4 v250, s[36:39], s54 offen offset:1024 nt lds\n"
"s_and_b32 s53, s53, 258047\n"
"v_mfma_f32_16x16x32_bf16 v[20:23], a[164:167], v[212:215], v[20:23]\n"
"s_waitcnt lgkmcnt(2)\n"
"s_add_u32 m0, s50, 38784\n"
"v_cvt_pk_bf16_f32 v210, v196, v197\n"
"v_mfma_f32_16x16x32_bf16 v[24:27], a[168:171], v[212:215], v[24:27]\n"
"s_waitcnt lgkmcnt(0)\n"
"buffer_load_dwordx4 v250, s[36:39], s54 offen offset:2048 nt lds\n"
"v_mfma_f32_16x16x32_bf16 v[28:31], a[172:175], v[212:215], v[28:31]\n"
"s_add_u32 m0, s50, 38848\n"
"v_cvt_pk_bf16_f32 v211, v198, v199\n"
"v_mfma_f32_16x16x32_bf16 a[208:211], a[236:239], v[212:215], a[208:211]\n"
"buffer_load_dwordx4 v250, s[36:39], s54 offen offset:3072 nt lds\n"
"s_lshl_b32 s38, s38, 1\n"
"s_add_u32 s54, s53, 4096\n"
"v_mfma_f32_16x16x32_bf16 v[192:195], a[112:115], a[48:51], v[228:231]\n"
"v_exp_f32 v200, v200\n"
"v_mfma_f32_16x16x32_bf16 v[192:195], a[116:119], a[52:55], v[192:195]\n"
"v_exp_f32 v201, v201\n"
"v_mfma_f32_16x16x32_bf16 v[192:195], a[120:123], a[56:59], v[192:195]\n"
"v_exp_f32 v202, v202\n"
"v_mfma_f32_16x16x32_bf16 v[192:195], a[124:127], a[60:63], v[192:195]\n"
"v_exp_f32 v203, v203\n"
"v_mfma_f32_16x16x32_bf16 v[196:199], a[128:131], a[48:51], v[228:231]\n"
"v_exp_f32 v204, v204\n"
"v_mfma_f32_16x16x32_bf16 v[196:199], a[132:135], a[52:55], v[196:199]\n"
"v_exp_f32 v205, v205\n"
"v_mfma_f32_16x16x32_bf16 v[196:199], a[136:139], a[56:59], v[196:199]\n"
"v_exp_f32 v206, v206\n"
"v_mfma_f32_16x16x32_bf16 v[196:199], a[140:143], a[60:63], v[196:199]\n"
"v_exp_f32 v207, v207\n"
"v_mfma_f32_16x16x32_bf16 v[32:35], a[144:147], v[208:211], v[32:35]\n"
"v_mfma_f32_16x16x32_bf16 v[36:39], a[148:151], v[208:211], v[36:39]\n"
"v_cvt_pk_bf16_f32 v212, v200, v201\n"
"v_cvt_pk_bf16_f32 v213, v202, v203\n"
"v_mfma_f32_16x16x32_bf16 v[40:43], a[152:155], v[208:211], v[40:43]\n"
"v_cvt_pk_bf16_f32 v214, v204, v205\n"
"v_cvt_pk_bf16_f32 v215, v206, v207\n"
"v_mfma_f32_16x16x32_bf16 v[44:47], a[156:159], v[208:211], v[44:47]\n"
"s_lshl_b32 s33, s28, 3\n"
"v_mfma_f32_16x16x32_bf16 v[48:51], a[160:163], v[208:211], v[48:51]\n"
"s_sub_i32 s32, s30, s33\n"
"v_mfma_f32_16x16x32_bf16 v[52:55], a[164:167], v[208:211], v[52:55]\n"
"s_max_i32 s30, s32, -400\n"
"v_mfma_f32_16x16x32_bf16 v[56:59], a[168:171], v[208:211], v[56:59]\n"
"s_add_i32 s30, s30, s33\n"
"v_mfma_f32_16x16x32_bf16 v[60:63], a[172:175], v[208:211], v[60:63]\n"
"s_add_i32 s30, s30, s66\n"
"v_mfma_f32_16x16x32_bf16 a[212:215], a[236:239], v[208:211], a[212:215]\n"
"v_mfma_f32_16x16x32_bf16 v[200:203], a[112:115], a[64:67], v[232:235]\n"
"v_exp_f32 v192, v192\n"
"v_mfma_f32_16x16x32_bf16 v[200:203], a[116:119], a[68:71], v[200:203]\n"
"v_exp_f32 v193, v193\n"
"v_mfma_f32_16x16x32_bf16 v[200:203], a[120:123], a[72:75], v[200:203]\n"
"v_exp_f32 v194, v194\n"
"v_mfma_f32_16x16x32_bf16 v[200:203], a[124:127], a[76:79], v[200:203]\n"
"v_exp_f32 v195, v195\n"
"v_mfma_f32_16x16x32_bf16 v[204:207], a[128:131], a[64:67], v[232:235]\n"
"v_exp_f32 v196, v196\n"
"v_mfma_f32_16x16x32_bf16 v[204:207], a[132:135], a[68:71], v[204:207]\n"
"v_exp_f32 v197, v197\n"
"v_mfma_f32_16x16x32_bf16 v[204:207], a[136:139], a[72:75], v[204:207]\n"
"v_exp_f32 v198, v198\n"
"v_mfma_f32_16x16x32_bf16 v[204:207], a[140:143], a[76:79], v[204:207]\n"
"v_exp_f32 v199, v199\n"
"v_mfma_f32_16x16x32_bf16 v[64:67], a[144:147], v[212:215], v[64:67]\n"
"v_mfma_f32_16x16x32_bf16 v[68:71], a[148:151], v[212:215], v[68:71]\n"
"v_cvt_pk_bf16_f32 v208, v192, v193\n"
"v_cvt_pk_bf16_f32 v209, v194, v195\n"
"v_mfma_f32_16x16x32_bf16 v[72:75], a[152:155], v[212:215], v[72:75]\n"
"v_cvt_pk_bf16_f32 v210, v196, v197\n"
"v_cvt_pk_bf16_f32 v211, v198, v199\n"
"v_mfma_f32_16x16x32_bf16 v[76:79], a[156:159], v[212:215], v[76:79]\n"
"s_sub_i32 s66, s66, 15\n"
"v_mfma_f32_16x16x32_bf16 v[80:83], a[160:163], v[212:215], v[80:83]\n"
"s_max_i32 s32, s32, 0\n"
"v_mfma_f32_16x16x32_bf16 v[84:87], a[164:167], v[212:215], v[84:87]\n"
"s_mul_i32 s32, s32, 13\n"
"v_mfma_f32_16x16x32_bf16 v[88:91], a[168:171], v[212:215], v[88:91]\n"
"s_lshr_b32 s32, s32, 8\n"
"v_mfma_f32_16x16x32_bf16 v[92:95], a[172:175], v[212:215], v[92:95]\n"
"s_min_u32 s31, s32, 24\n"
"v_mfma_f32_16x16x32_bf16 a[216:219], a[236:239], v[212:215], a[216:219]\n"
"1:\n"
"s_sub_u32 s31, s31, 1\n"
"s_cbranch_scc1 2f\n"
"s_nop 7\n"
"s_branch 1b\n"
"2:\n"
"s_barrier\n"
"v_mfma_f32_16x16x32_bf16 v[192:195], a[112:115], a[80:83], v[236:239]\n"
"v_exp_f32 v200, v200\n"
"v_mfma_f32_16x16x32_bf16 v[192:195], a[116:119], a[84:87], v[192:195]\n"
"v_exp_f32 v201, v201\n"
"v_mfma_f32_16x16x32_bf16 v[192:195], a[120:123], a[88:91], v[192:195]\n"
"v_exp_f32 v202, v202\n"
"v_mfma_f32_16x16x32_bf16 v[192:195], a[124:127], a[92:95], v[192:195]\n"
"v_exp_f32 v203, v203\n"
"v_mfma_f32_16x16x32_bf16 v[196:199], a[128:131], a[80:83], v[236:239]\n"
"v_exp_f32 v204, v204\n"
"v_mfma_f32_16x16x32_bf16 v[196:199], a[132:135], a[84:87], v[196:199]\n"
"v_exp_f32 v205, v205\n"
"v_mfma_f32_16x16x32_bf16 v[196:199], a[136:139], a[88:91], v[196:199]\n"
"v_exp_f32 v206, v206\n"
"v_mfma_f32_16x16x32_bf16 v[196:199], a[140:143], a[92:95], v[196:199]\n"
"v_exp_f32 v207, v207\n"
"v_mfma_f32_16x16x32_bf16 v[96:99], a[144:147], v[208:211], v[96:99]\n"
"v_mfma_f32_16x16x32_bf16 v[100:103], a[148:151], v[208:211], v[100:103]\n"
"s_add_u32 m0, s50, 68608\n"
"v_cvt_pk_bf16_f32 v212, v200, v201\n"
"v_mfma_f32_16x16x32_bf16 v[104:107], a[152:155], v[208:211], v[104:107]\n"
"buffer_load_dwordx4 v250, s[36:39], s53 offen nt lds\n"
"v_mfma_f32_16x16x32_bf16 v[108:111], a[156:159], v[208:211], v[108:111]\n"
"s_add_u32 m0, s50, 68672\n"
"v_cvt_pk_bf16_f32 v213, v202, v203\n"
"v_mfma_f32_16x16x32_bf16 v[112:115], a[160:163], v[208:211], v[112:115]\n"
"buffer_load_dwordx4 v250, s[36:39], s53 offen offset:1024 nt lds\n"
"v_mfma_f32_16x16x32_bf16 v[116:119], a[164:167], v[208:211], v[116:119]\n"
"s_add_u32 m0, s50, 68736\n"
"v_cvt_pk_bf16_f32 v214, v204, v205\n"
"v_mfma_f32_16x16x32_bf16 v[120:123], a[168:171], v[208:211], v[120:123]\n"
"buffer_load_dwordx4 v250, s[36:39], s53 offen offset:2048 nt lds\n"
"v_mfma_f32_16x16x32_bf16 v[124:127], a[172:175], v[208:211], v[124:127]\n"
"s_add_u32 m0, s50, 68800\n"
"v_cvt_pk_bf16_f32 v215, v206, v207\n"
"v_mfma_f32_16x16x32_bf16 a[220:223], a[236:239], v[208:211], a[220:223]\n"
"buffer_load_dwordx4 v250, s[36:39], s53 offen offset:3072 nt lds\n"
"v_mfma_f32_16x16x32_bf16 v[200:203], a[112:115], a[96:99], v[240:243]\n"
"v_exp_f32 v192, v192\n"
"v_mfma_f32_16x16x32_bf16 v[200:203], a[116:119], a[100:103], v[200:203]\n"
"ds_read_b128 a[112:115], v245 offset:34304\n"
"ds_read_b128 a[116:119], v245 offset:34368\n"
"v_mfma_f32_16x16x32_bf16 v[200:203], a[120:123], a[104:107], v[200:203]\n"
"v_exp_f32 v193, v193\n"
"v_mfma_f32_16x16x32_bf16 v[200:203], a[124:127], a[108:111], v[200:203]\n"
"ds_read_b128 a[120:123], v245 offset:34432\n"
"ds_read_b128 a[124:127], v245 offset:34496\n"
"v_mfma_f32_16x16x32_bf16 v[204:207], a[128:131], a[96:99], v[240:243]\n"
"v_exp_f32 v194, v194\n"
"v_mfma_f32_16x16x32_bf16 v[204:207], a[132:135], a[100:103], v[204:207]\n"
"ds_read_b128 a[128:131], v245 offset:38656\n"
"ds_read_b128 a[132:135], v245 offset:38720\n"
"v_mfma_f32_16x16x32_bf16 v[204:207], a[136:139], a[104:107], v[204:207]\n"
"v_exp_f32 v195, v195\n"
"v_mfma_f32_16x16x32_bf16 v[204:207], a[140:143], a[108:111], v[204:207]\n"
"ds_read_b128 a[136:139], v245 offset:38784\n"
"ds_read_b128 a[140:143], v245 offset:38848\n"
"v_mfma_f32_16x16x32_bf16 v[128:131], a[144:147], v[212:215], v[128:131]\n"
"v_exp_f32 v196, v196\n"
"v_mfma_f32_16x16x32_bf16 v[132:135], a[148:151], v[212:215], v[132:135]\n"
"v_exp_f32 v197, v197\n"
"v_mfma_f32_16x16x32_bf16 v[136:139], a[152:155], v[212:215], v[136:139]\n"
"v_exp_f32 v198, v198\n"
"v_mfma_f32_16x16x32_bf16 v[140:143], a[156:159], v[212:215], v[140:143]\n"
"v_exp_f32 v199, v199\n"
"v_mfma_f32_16x16x32_bf16 v[144:147], a[160:163], v[212:215], v[144:147]\n"
"v_cvt_pk_bf16_f32 v208, v192, v193\n"
"v_cvt_pk_bf16_f32 v209, v194, v195\n"
"v_mfma_f32_16x16x32_bf16 v[148:151], a[164:167], v[212:215], v[148:151]\n"
"v_cvt_pk_bf16_f32 v210, v196, v197\n"
"v_cvt_pk_bf16_f32 v211, v198, v199\n"
"v_mfma_f32_16x16x32_bf16 v[152:155], a[168:171], v[212:215], v[152:155]\n"
"v_mfma_f32_16x16x32_bf16 v[156:159], a[172:175], v[212:215], v[156:159]\n"
"v_mfma_f32_16x16x32_bf16 a[224:227], a[236:239], v[212:215], a[224:227]\n"
"s_waitcnt lgkmcnt(7)\n"
"v_mfma_f32_16x16x32_bf16 v[192:195], a[112:115], a[0:3], v[216:219]\n"
"s_waitcnt lgkmcnt(6)\n"
"v_exp_f32 v200, v200\n"
"v_mfma_f32_16x16x32_bf16 v[192:195], a[116:119], a[4:7], v[192:195]\n"
"s_waitcnt lgkmcnt(5)\n"
"v_exp_f32 v201, v201\n"
"v_mfma_f32_16x16x32_bf16 v[192:195], a[120:123], a[8:11], v[192:195]\n"
"s_waitcnt lgkmcnt(4)\n"
"v_exp_f32 v202, v202\n"
"v_mfma_f32_16x16x32_bf16 v[192:195], a[124:127], a[12:15], v[192:195]\n"
"s_waitcnt lgkmcnt(3)\n"
"v_exp_f32 v203, v203\n"
"v_mfma_f32_16x16x32_bf16 v[196:199], a[128:131], a[0:3], v[216:219]\n"
"s_waitcnt lgkmcnt(2)\n"
"v_exp_f32 v204, v204\n"
"v_mfma_f32_16x16x32_bf16 v[196:199], a[132:135], a[4:7], v[196:199]\n"
"s_waitcnt lgkmcnt(1)\n"
"v_exp_f32 v205, v205\n"
"v_mfma_f32_16x16x32_bf16 v[196:199], a[136:139], a[8:11], v[196:199]\n"
"s_waitcnt lgkmcnt(0)\n"
"v_exp_f32 v206, v206\n"
"v_mfma_f32_16x16x32_bf16 v[196:199], a[140:143], a[12:15], v[196:199]\n"
"v_exp_f32 v207, v207\n"
"v_mfma_f32_16x16x32_bf16 v[160:163], a[144:147], v[208:211], v[160:163]\n"
"s_add_u32 s53, s53, 8192\n"
"s_and_b32 s53, s53, 258047\n"
"v_mfma_f32_16x16x32_bf16 v[164:167], a[148:151], v[208:211], v[164:167]\n"
"s_add_u32 m0, s50, 72960\n"
"v_cvt_pk_bf16_f32 v212, v200, v201\n"
"v_mfma_f32_16x16x32_bf16 v[168:171], a[152:155], v[208:211], v[168:171]\n"
"buffer_load_dwordx4 v250, s[36:39], s54 offen nt lds\n"
"v_mfma_f32_16x16x32_bf16 v[172:175], a[156:159], v[208:211], v[172:175]\n"
"s_add_u32 m0, s50, 73024\n"
"v_cvt_pk_bf16_f32 v213, v202, v203\n"
"v_mfma_f32_16x16x32_bf16 v[176:179], a[160:163], v[208:211], v[176:179]\n"
"buffer_load_dwordx4 v250, s[36:39], s54 offen offset:1024 nt lds\n"
"v_mfma_f32_16x16x32_bf16 v[180:183], a[164:167], v[208:211], v[180:183]\n"
"s_add_u32 m0, s50, 73088\n"
"v_cvt_pk_bf16_f32 v214, v204, v205\n"
"v_mfma_f32_16x16x32_bf16 v[184:187], a[168:171], v[208:211], v[184:187]\n"
"buffer_load_dwordx4 v250, s[36:39], s54 offen offset:2048 nt lds\n"
"v_mfma_f32_16x16x32_bf16 v[188:191], a[172:175], v[208:211], v[188:191]\n"
"s_add_u32 m0, s50, 73152\n"
"v_cvt_pk_bf16_f32 v215, v206, v207\n"
"v_mfma_f32_16x16x32_bf16 a[228:231], a[236:239], v[208:211], a[228:231]\n"
"buffer_load_dwordx4 v250, s[36:39], s54 offen offset:3072 nt lds\n"
"s_lshl_b32 s38, s38, 1\n"
"s_add_u32 s54, s53, 4096\n"
"v_mfma_f32_16x16x32_bf16 v[200:203], a[112:115], a[16:19], v[220:223]\n"
"v_exp_f32 v192, v192\n"
"v_mfma_f32_16x16x32_bf16 v[200:203], a[116:119], a[20:23], v[200:203]\n"
"v_exp_f32 v193, v193\n"
"v_mfma_f32_16x16x32_bf16 v[200:203], a[120:123], a[24:27], v[200:203]\n"
"v_exp_f32 v194, v194\n"
"v_mfma_f32_16x16x32_bf16 v[200:203], a[124:127], a[28:31], v[200:203]\n"
"v_exp_f32 v195, v195\n"
"v_mfma_f32_16x16x32_bf16 v[204:207], a[128:131], a[16:19], v[220:223]\n"
"v_exp_f32 v196, v196\n"
"v_mfma_f32_16x16x32_bf16 v[204:207], a[132:135], a[20:23], v[204:207]\n"
"v_exp_f32 v197, v197\n"
"v_mfma_f32_16x16x32_bf16 v[204:207], a[136:139], a[24:27], v[204:207]\n"
"v_exp_f32 v198, v198\n"
"v_mfma_f32_16x16x32_bf16 v[204:207], a[140:143], a[28:31], v[204:207]\n"
"v_exp_f32 v199, v199\n"
"v_mfma_f32_16x16x32_bf16 a[176:179], a[144:147], v[212:215], a[176:179]\n"
"ds_read_b64_tr_b16 a[144:145], v247 offset:34304\n"
"ds_read_b64_tr_b16 a[146:147], v247 offset:38528\n"
"v_mfma_f32_16x16x32_bf16 a[180:183], a[148:151], v[212:215], a[180:183]\n"
"ds_read_b64_tr_b16 a[148:149], v247 offset:34336\n"
"v_cvt_pk_bf16_f32 v208, v192, v193\n"
"v_mfma_f32_16x16x32_bf16 a[184:187], a[152:155], v[212:215], a[184:187]\n"
"ds_read_b64_tr_b16 a[150:151], v247 offset:38560\n"
"ds_read_b64_tr_b16 a[152:153], v247 offset:34368\n"
"v_mfma_f32_16x16x32_bf16 a[188:191], a[156:159], v[212:215], a[188:191]\n"
"ds_read_b64_tr_b16 a[154:155], v247 offset:38592\n"
"v_cvt_pk_bf16_f32 v209, v194, v195\n"
"v_mfma_f32_16x16x32_bf16 a[192:195], a[160:163], v[212:215], a[192:195]\n"
"ds_read_b64_tr_b16 a[156:157], v247 offset:34400\n"
"ds_read_b64_tr_b16 a[158:159], v247 offset:38624\n"
"v_mfma_f32_16x16x32_bf16 a[196:199], a[164:167], v[212:215], a[196:199]\n"
"ds_read_b64_tr_b16 a[160:161], v249 offset:34304\n"
"v_cvt_pk_bf16_f32 v210, v196, v197\n"
"v_mfma_f32_16x16x32_bf16 a[200:203], a[168:171], v[212:215], a[200:203]\n"
"ds_read_b64_tr_b16 a[162:163], v249 offset:38528\n"
"ds_read_b64_tr_b16 a[164:165], v249 offset:34336\n"
"v_mfma_f32_16x16x32_bf16 a[204:207], a[172:175], v[212:215], a[204:207]\n"
"ds_read_b64_tr_b16 a[166:167], v249 offset:38560\n"
"v_cvt_pk_bf16_f32 v211, v198, v199\n"
"v_mfma_f32_16x16x32_bf16 a[232:235], a[236:239], v[212:215], a[232:235]\n"
"ds_read_b64_tr_b16 a[168:169], v249 offset:34368\n"
"ds_read_b64_tr_b16 a[170:171], v249 offset:38592\n"
"v_mfma_f32_16x16x32_bf16 v[192:195], a[112:115], a[32:35], v[224:227]\n"
"ds_read_b64_tr_b16 a[172:173], v249 offset:34400\n"
"ds_read_b64_tr_b16 a[174:175], v249 offset:38624\n"
"v_mfma_f32_16x16x32_bf16 v[192:195], a[116:119], a[36:39], v[192:195]\n"
"v_exp_f32 v200, v200\n"
"v_mfma_f32_16x16x32_bf16 v[192:195], a[120:123], a[40:43], v[192:195]\n"
"s_memrealtime s[28:29]\n"
"v_exp_f32 v201, v201\n"
"v_mfma_f32_16x16x32_bf16 v[192:195], a[124:127], a[44:47], v[192:195]\n"
"v_exp_f32 v202, v202\n"
"v_mfma_f32_16x16x32_bf16 v[196:199], a[128:131], a[32:35], v[224:227]\n"
"v_exp_f32 v203, v203\n"
"v_mfma_f32_16x16x32_bf16 v[196:199], a[132:135], a[36:39], v[196:199]\n"
"v_exp_f32 v204, v204\n"
"v_mfma_f32_16x16x32_bf16 v[196:199], a[136:139], a[40:43], v[196:199]\n"
"v_exp_f32 v205, v205\n"
"v_mfma_f32_16x16x32_bf16 v[196:199], a[140:143], a[44:47], v[196:199]\n"
"s_waitcnt lgkmcnt(14)\n"
"v_exp_f32 v206, v206\n"
"v_mfma_f32_16x16x32_bf16 v[0:3], a[144:147], v[208:211], v[0:3]\n"
"s_waitcnt lgkmcnt(12)\n"
"v_exp_f32 v207, v207\n"
"v_mfma_f32_16x16x32_bf16 v[4:7], a[148:151], v[208:211], v[4:7]\n"
"s_waitcnt lgkmcnt(10)\n"
"v_cvt_pk_bf16_f32 v212, v200, v201\n"
"v_cvt_pk_bf16_f32 v213, v202, v203\n"
"v_mfma_f32_16x16x32_bf16 v[8:11], a[152:155], v[208:211], v[8:11]\n"
"s_waitcnt lgkmcnt(8)\n"
"v_cvt_pk_bf16_f32 v214, v204, v205\n"
"v_cvt_pk_bf16_f32 v215, v206, v207\n"
"v_mfma_f32_16x16x32_bf16 v[12:15], a[156:159], v[208:211], v[12:15]\n"
"s_waitcnt lgkmcnt(6)\n"
"v_mfma_f32_16x16x32_bf16 v[16:19], a[160:163], v[208:211], v[16:19]\n"
"s_waitcnt lgkmcnt(4)\n"
"v_mfma_f32_16x16x32_bf16 v[20:23], a[164:167], v[208:211], v[20:23]\n"
"s_waitcnt lgkmcnt(2)\n"
"v_mfma_f32_16x16x32_bf16 v[24:27], a[168:171], v[208:211], v[24:27]\n"
"s_waitcnt lgkmcnt(0)\n"
"v_mfma_f32_16x16x32_bf16 v[28:31], a[172:175], v[208:211], v[28:31]\n"
"v_mfma_f32_16x16x32_bf16 a[208:211], a[236:239], v[208:211], a[208:211]\n"
"v_mfma_f32_16x16x32_bf16 v[200:203], a[112:115], a[48:51], v[228:231]\n"
"v_exp_f32 v192, v192\n"
"v_mfma_f32_16x16x32_bf16 v[200:203], a[116:119], a[52:55], v[200:203]\n"
"v_exp_f32 v193, v193\n"
"v_mfma_f32_16x16x32_bf16 v[200:203], a[120:123], a[56:59], v[200:203]\n"
"v_exp_f32 v194, v194\n"
"v_mfma_f32_16x16x32_bf16 v[200:203], a[124:127], a[60:63], v[200:203]\n"
"v_exp_f32 v195, v195\n"
"v_mfma_f32_16x16x32_bf16 v[204:207], a[128:131], a[48:51], v[228:231]\n"
"v_exp_f32 v196, v196\n"
"v_mfma_f32_16x16x32_bf16 v[204:207], a[132:135], a[52:55], v[204:207]\n"
"v_exp_f32 v197, v197\n"
"v_mfma_f32_16x16x32_bf16 v[204:207], a[136:139], a[56:59], v[204:207]\n"
"v_exp_f32 v198, v198\n"
"v_mfma_f32_16x16x32_bf16 v[204:207], a[140:143], a[60:63], v[204:207]\n"
"v_exp_f32 v199, v199\n"
"v_mfma_f32_16x16x32_bf16 v[32:35], a[144:147], v[212:215], v[32:35]\n"
"v_mfma_f32_16x16x32_bf16 v[36:39], a[148:151], v[212:215], v[36:39]\n"
"v_cvt_pk_bf16_f32 v208, v192, v193\n"
"v_cvt_pk_bf16_f32 v209, v194, v195\n"
"v_mfma_f32_16x16x32_bf16 v[40:43], a[152:155], v[212:215], v[40:43]\n"
"v_cvt_pk_bf16_f32 v210, v196, v197\n"
"v_cvt_pk_bf16_f32 v211, v198, v199\n"
"v_mfma_f32_16x16x32_bf16 v[44:47], a[156:159], v[212:215], v[44:47]\n"
"s_lshl_b32 s33, s28, 3\n"
"v_mfma_f32_16x16x32_bf16 v[48:51], a[160:163], v[212:215], v[48:51]\n"
"s_sub_i32 s32, s30, s33\n"
"v_mfma_f32_16x16x32_bf16 v[52:55], a[164:167], v[212:215], v[52:55]\n"
"s_max_i32 s30, s32, -400\n"
"v_mfma_f32_16x16x32_bf16 v[56:59], a[168:171], v[212:215], v[56:59]\n"
"s_add_i32 s30, s30, s33\n"
"v_mfma_f32_16x16x32_bf16 v[60:63], a[172:175], v[212:215], v[60:63]\n"
"s_add_i32 s30, s30, s66\n"
"v_mfma_f32_16x16x32_bf16 a[212:215], a[236:239], v[212:215], a[212:215]\n"
"v_mfma_f32_16x16x32_bf16 v[192:195], a[112:115], a[64:67], v[232:235]\n"
"v_exp_f32 v200, v200\n"
"v_mfma_f32_16x16x32_bf16 v[192:195], a[116:119], a[68:71], v[192:195]\n"
"v_exp_f32 v201, v201\n"
"v_mfma_f32_16x16x32_bf16 v[192:195], a[120:123], a[72:75], v[192:195]\n"
"v_exp_f32 v202, v202\n"
"v_mfma_f32_16x16x32_bf16 v[192:195], a[124:127], a[76:79], v[192:195]\n"
"v_exp_f32 v203, v203\n"
"v_mfma_f32_16x16x32_bf16 v[196:199], a[128:131], a[64:67], v[232:235]\n"
"v_exp_f32 v204, v204\n"
"v_mfma_f32_16x16x32_bf16 v[196:199], a[132:135], a[68:71], v[196:199]\n"
"v_exp_f32 v205, v205\n"
"v_mfma_f32_16x16x32_bf16 v[196:199], a[136:139], a[72:75], v[196:199]\n"
"v_exp_f32 v206, v206\n"
"v_mfma_f32_16x16x32_bf16 v[196:199], a[140:143], a[76:79], v[196:199]\n"
"v_exp_f32 v207, v207\n"
"v_mfma_f32_16x16x32_bf16 v[64:67], a[144:147], v[208:211], v[64:67]\n"
"v_mfma_f32_16x16x32_bf16 v[68:71], a[148:151], v[208:211], v[68:71]\n"
"v_cvt_pk_bf16_f32 v212, v200, v201\n"
"v_cvt_pk_bf16_f32 v213, v202, v203\n"
"v_mfma_f32_16x16x32_bf16 v[72:75], a[152:155], v[208:211], v[72:75]\n"
"v_cvt_pk_bf16_f32 v214, v204, v205\n"
"v_cvt_pk_bf16_f32 v215, v206, v207\n"
"v_mfma_f32_16x16x32_bf16 v[76:79], a[156:159], v[208:211], v[76:79]\n"
"s_sub_i32 s66, s66, 15\n"
"v_mfma_f32_16x16x32_bf16 v[80:83], a[160:163], v[208:211], v[80:83]\n"
"s_max_i32 s32, s32, 0\n"
"v_mfma_f32_16x16x32_bf16 v[84:87], a[164:167], v[208:211], v[84:87]\n"
"s_mul_i32 s32, s32, 13\n"
"v_mfma_f32_16x16x32_bf16 v[88:91], a[168:171], v[208:211], v[88:91]\n"
"s_lshr_b32 s32, s32, 8\n"
"v_mfma_f32_16x16x32_bf16 v[92:95], a[172:175], v[208:211], v[92:95]\n"
"s_min_u32 s31, s32, 24\n"
"v_mfma_f32_16x16x32_bf16 a[216:219], a[236:239], v[208:211], a[216:219]\n"
"1:\n"
"s_sub_u32 s31, s31, 1\n"
"s_cbranch_scc1 2f\n"
"s_nop 7\n"
"s_branch 1b\n"
"2:\n"
"s_waitcnt vmcnt(16)\n"
"s_barrier\n"
"v_mfma_f32_16x16x32_bf16 v[200:203], a[112:115], a[80:83], v[236:239]\n"
"v_exp_f32 v192, v192\n"
"v_mfma_f32_16x16x32_bf16 v[200:203], a[116:119], a[84:87], v[200:203]\n"
"ds_read_b128 a[112:115], v244\n"
"ds_read_b128 a[116:119], v244 offset:64\n"
"v_mfma_f32_16x16x32_bf16 v[200:203], a[120:123], a[88:91], v[200:203]\n"
"v_exp_f32 v193, v193\n"
"v_mfma_f32_16x16x32_bf16 v[200:203], a[124:127], a[92:95], v[200:203]\n"
"ds_read_b128 a[120:123], v244 offset:128\n"
"ds_read_b128 a[124:127], v244 offset:192\n"
"v_mfma_f32_16x16x32_bf16 v[204:207], a[128:131], a[80:83], v[236:239]\n"
"v_exp_f32 v194, v194\n"
"v_mfma_f32_16x16x32_bf16 v[204:207], a[132:135], a[84:87], v[204:207]\n"
"ds_read_b128 a[128:131], v244 offset:4352\n"
"ds_read_b128 a[132:135], v244 offset:4416\n"
"v_mfma_f32_16x16x32_bf16 v[204:207], a[136:139], a[88:91], v[204:207]\n"
"v_exp_f32 v195, v195\n"
"v_mfma_f32_16x16x32_bf16 v[204:207], a[140:143], a[92:95], v[204:207]\n"
"ds_read_b128 a[136:139], v244 offset:4480\n"
"ds_read_b128 a[140:143], v244 offset:4544\n"
"v_mfma_f32_16x16x32_bf16 v[96:99], a[144:147], v[212:215], v[96:99]\n"
"v_exp_f32 v196, v196\n"
"v_mfma_f32_16x16x32_bf16 v[100:103], a[148:151], v[212:215], v[100:103]\n"
"v_exp_f32 v197, v197\n"
"v_mfma_f32_16x16x32_bf16 v[104:107], a[152:155], v[212:215], v[104:107]\n"
"v_exp_f32 v198, v198\n"
"v_mfma_f32_16x16x32_bf16 v[108:111], a[156:159], v[212:215], v[108:111]\n"
"v_exp_f32 v199, v199\n"
"v_mfma_f32_16x16x32_bf16 v[112:115], a[160:163], v[212:215], v[112:115]\n"
"v_cvt_pk_bf16_f32 v208, v192, v193\n"
"v_cvt_pk_bf16_f32 v209, v194, v195\n"
"v_mfma_f32_16x16x32_bf16 v[116:119], a[164:167], v[212:215], v[116:119]\n"
"v_cvt_pk_bf16_f32 v210, v196, v197\n"
"v_cvt_pk_bf16_f32 v211, v198, v199\n"
"v_mfma_f32_16x16x32_bf16 v[120:123], a[168:171], v[212:215], v[120:123]\n"
"v_mfma_f32_16x16x32_bf16 v[124:127], a[172:175], v[212:215], v[124:127]\n"
"v_mfma_f32_16x16x32_bf16 a[220:223], a[236:239], v[212:215], a[220:223]\n"
"s_waitcnt lgkmcnt(7)\n"
"v_mfma_f32_16x16x32_bf16 v[192:195], a[112:115], a[0:3], v[216:219]\n"
"s_waitcnt lgkmcnt(6)\n"
"v_exp_f32 v200, v200\n"
"v_mfma_f32_16x16x32_bf16 v[192:195], a[116:119], a[4:7], v[192:195]\n"
"s_waitcnt lgkmcnt(5)\n"
"v_exp_f32 v201, v201\n"
"v_mfma_f32_16x16x32_bf16 v[192:195], a[120:123], a[8:11], v[192:195]\n"
"s_waitcnt lgkmcnt(4)\n"
"v_exp_f32 v202, v202\n"
"v_mfma_f32_16x16x32_bf16 v[192:195], a[124:127], a[12:15], v[192:195]\n"
"s_waitcnt lgkmcnt(3)\n"
"v_exp_f32 v203, v203\n"
"v_mfma_f32_16x16x32_bf16 v[196:199], a[128:131], a[0:3], v[216:219]\n"
"s_waitcnt lgkmcnt(2)\n"
"v_exp_f32 v204, v204\n"
"v_mfma_f32_16x16x32_bf16 v[196:199], a[132:135], a[4:7], v[196:199]\n"
"s_waitcnt lgkmcnt(1)\n"
"v_exp_f32 v205, v205\n"
"v_mfma_f32_16x16x32_bf16 v[196:199], a[136:139], a[8:11], v[196:199]\n"
"s_waitcnt lgkmcnt(0)\n"
"v_exp_f32 v206, v206\n"
"v_mfma_f32_16x16x32_bf16 v[196:199], a[140:143], a[12:15], v[196:199]\n"
"v_exp_f32 v207, v207\n"
"v_mfma_f32_16x16x32_bf16 v[128:131], a[144:147], v[208:211], v[128:131]\n"
"v_mfma_f32_16x16x32_bf16 v[132:135], a[148:151], v[208:211], v[132:135]\n"
"s_add_u32 m0, s50, 102912\n"
"v_cvt_pk_bf16_f32 v212, v200, v201\n"
"v_mfma_f32_16x16x32_bf16 v[136:139], a[152:155], v[208:211], v[136:139]\n"
"buffer_load_dwordx4 v250, s[36:39], s53 offen nt lds\n"
"v_mfma_f32_16x16x32_bf16 v[140:143], a[156:159], v[208:211], v[140:143]\n"
"s_add_u32 m0, s50, 102976\n"
"v_cvt_pk_bf16_f32 v213, v202, v203\n"
"v_mfma_f32_16x16x32_bf16 v[144:147], a[160:163], v[208:211], v[144:147]\n"
"buffer_load_dwordx4 v250, s[36:39], s53 offen offset:1024 nt lds\n"
"v_mfma_f32_16x16x32_bf16 v[148:151], a[164:167], v[208:211], v[148:151]\n"
"s_add_u32 m0, s50, 103040\n"
"v_cvt_pk_bf16_f32 v214, v204, v205\n"
"v_mfma_f32_16x16x32_bf16 v[152:155], a[168:171], v[208:211], v[152:155]\n"
"buffer_load_dwordx4 v250, s[36:39], s53 offen offset:2048 nt lds\n"
"v_mfma_f32_16x16x32_bf16 v[156:159], a[172:175], v[208:211], v[156:159]\n"
"s_add_u32 m0, s50, 103104\n"
"v_cvt_pk_bf16_f32 v215, v206, v207\n"
"v_mfma_f32_16x16x32_bf16 a[224:227], a[236:239], v[208:211], a[224:227]\n"
"buffer_load_dwordx4 v250, s[36:39], s53 offen offset:3072 nt lds\n"
"s_mov_b32 s55, 6\n"
".p2align 6\n"
"L1_loop_h0:\n"
"v_mfma_f32_16x16x32_bf16 v[200:203], a[112:115], a[16:19], v[220:223]\n"
"v_exp_f32 v192, v192\n"
"v_mfma_f32_16x16x32_bf16 v[200:203], a[116:119], a[20:23], v[200:203]\n"
"v_exp_f32 v193, v193\n"
"v_mfma_f32_16x16x32_bf16 v[200:203], a[120:123], a[24:27], v[200:203]\n"
"v_exp_f32 v194, v194\n"
"v_mfma_f32_16x16x32_bf16 v[200:203], a[124:127], a[28:31], v[200:203]\n"
"v_exp_f32 v195, v195\n"
"v_mfma_f32_16x16x32_bf16 v[204:207], a[128:131], a[16:19], v[220:223]\n"
"v_exp_f32 v196, v196\n"
"v_mfma_f32_16x16x32_bf16 v[204:207], a[132:135], a[20:23], v[204:207]\n"
"v_exp_f32 v197, v197\n"
"v_mfma_f32_16x16x32_bf16 v[204:207], a[136:139], a[24:27], v[204:207]\n"
"v_exp_f32 v198, v198\n"
"v_mfma_f32_16x16x32_bf16 v[204:207], a[140:143], a[28:31], v[204:207]\n"
"v_exp_f32 v199, v199\n"
"v_mfma_f32_16x16x32_bf16 v[160:163], a[144:147], v[212:215], v[160:163]\n"
"ds_read_b64_tr_b16 a[144:145], v246\n"
"ds_read_b64_tr_b16 a[146:147], v246 offset:4224\n"
"v_mfma_f32_16x16x32_bf16 v[164:167], a[148:151], v[212:215], v[164:167]\n"
"ds_read_b64_tr_b16 a[148:149], v246 offset:32\n"
"v_cvt_pk_bf16_f32 v208, v192, v193\n"
"v_mfma_f32_16x16x32_bf16 v[168:171], a[152:155], v[212:215], v[168:171]\n"
"ds_read_b64_tr_b16 a[150:151], v246 offset:4256\n"
"ds_read_b64_tr_b16 a[152:153], v246 offset:64\n"
"v_mfma_f32_16x16x32_bf16 v[172:175], a[156:159], v[212:215], v[172:175]\n"
"ds_read_b64_tr_b16 a[154:155], v246 offset:4288\n"
"v_cvt_pk_bf16_f32 v209, v194, v195\n"
"v_mfma_f32_16x16x32_bf16 v[176:179], a[160:163], v[212:215], v[176:179]\n"
"ds_read_b64_tr_b16 a[156:157], v246 offset:96\n"
"ds_read_b64_tr_b16 a[158:159], v246 offset:4320\n"
"v_mfma_f32_16x16x32_bf16 v[180:183], a[164:167], v[212:215], v[180:183]\n"
"ds_read_b64_tr_b16 a[160:161], v248\n"
"v_cvt_pk_bf16_f32 v210, v196, v197\n"
"v_mfma_f32_16x16x32_bf16 v[184:187], a[168:171], v[212:215], v[184:187]\n"
"ds_read_b64_tr_b16 a[162:163], v248 offset:4224\n"
"ds_read_b64_tr_b16 a[164:165], v248 offset:32\n"
"v_mfma_f32_16x16x32_bf16 v[188:191], a[172:175], v[212:215], v[188:191]\n"
"ds_read_b64_tr_b16 a[166:167], v248 offset:4256\n"
"v_cvt_pk_bf16_f32 v211, v198, v199\n"
"v_mfma_f32_16x16x32_bf16 a[228:231], a[236:239], v[212:215], a[228:231]\n"
"ds_read_b64_tr_b16 a[168:169], v248 offset:64\n"
"ds_read_b64_tr_b16 a[170:171], v248 offset:4288\n"
"v_mfma_f32_16x16x32_bf16 v[192:195], a[112:115], a[32:35], v[224:227]\n"
"ds_read_b64_tr_b16 a[172:173], v248 offset:96\n"
"ds_read_b64_tr_b16 a[174:175], v248 offset:4320\n"
"v_mfma_f32_16x16x32_bf16 v[192:195], a[116:119], a[36:39], v[192:195]\n"
"v_exp_f32 v200, v200\n"
"v_mfma_f32_16x16x32_bf16 v[192:195], a[120:123], a[40:43], v[192:195]\n"
"s_memrealtime s[28:29]\n"
"v_exp_f32 v201, v201\n"
"v_mfma_f32_16x16x32_bf16 v[192:195], a[124:127], a[44:47], v[192:195]\n"
"v_exp_f32 v202, v202\n"
"v_mfma_f32_16x16x32_bf16 v[196:199], a[128:131], a[32:35], v[224:227]\n"
"v_exp_f32 v203, v203\n"
"v_mfma_f32_16x16x32_bf16 v[196:199], a[132:135], a[36:39], v[196:199]\n"
"v_exp_f32 v204, v204\n"
"v_mfma_f32_16x16x32_bf16 v[196:199], a[136:139], a[40:43], v[196:199]\n"
"v_exp_f32 v205, v205\n"
"v_mfma_f32_16x16x32_bf16 v[196:199], a[140:143], a[44:47], v[196:199]\n"
"s_waitcnt lgkmcnt(14)\n"
"v_exp_f32 v206, v206\n"
"v_mfma_f32_16x16x32_bf16 v[0:3], a[144:147], v[208:211], v[0:3]\n"
"s_waitcnt lgkmcnt(12)\n"
"v_exp_f32 v207, v207\n"
"v_mfma_f32_16x16x32_bf16 v[4:7], a[148:151], v[208:211], v[4:7]\n"
"s_waitcnt lgkmcnt(10)\n"
"s_add_u32 m0, s50, 107264\n"
"v_cvt_pk_bf16_f32 v212, v200, v201\n"
"v_mfma_f32_16x16x32_bf16 v[8:11], a[152:155], v[208:211], v[8:11]\n"
"s_waitcnt lgkmcnt(8)\n"
"buffer_load_dwordx4 v250, s[36:39], s54 offen nt lds\n"
"s_add_u32 s53, s53, 8192\n"
"v_mfma_f32_16x16x32_bf16 v[12:15], a[156:159], v[208:211], v[12:15]\n"
"s_waitcnt lgkmcnt(6)\n"
"s_add_u32 m0, s50, 107328\n"
"v_cvt_pk_bf16_f32 v213, v202, v203\n"
"v_mfma_f32_16x16x32_bf16 v[16:19], a[160:163], v[208:211], v[16:19]\n"
"s_waitcnt lgkmcnt(4)\n"
"buffer_load_dwordx4 v250, s[36:39], s54 offen offset:1024 nt lds\n"
"s_and_b32 s53, s53, 258047\n"
"v_mfma_f32_16x16x32_bf16 v[20:23], a[164:167], v[208:211], v[20:23]\n"
"s_waitcnt lgkmcnt(2)\n"
"s_add_u32 m0, s50, 107392\n"
"v_cvt_pk_bf16_f32 v214, v204, v205\n"
"v_mfma_f32_16x16x32_bf16 v[24:27], a[168:171], v[208:211], v[24:27]\n"
"s_waitcnt lgkmcnt(0)\n"
"buffer_load_dwordx4 v250, s[36:39], s54 offen offset:2048 nt lds\n"
"v_mfma_f32_16x16x32_bf16 v[28:31], a[172:175], v[208:211], v[28:31]\n"
"s_add_u32 m0, s50, 107456\n"
"v_cvt_pk_bf16_f32 v215, v206, v207\n"
"v_mfma_f32_16x16x32_bf16 a[208:211], a[236:239], v[208:211], a[208:211]\n"
"buffer_load_dwordx4 v250, s[36:39], s54 offen offset:3072 nt lds\n"
"s_lshl_b32 s38, s38, 1\n"
"s_add_u32 s54, s53, 4096\n"
"v_mfma_f32_16x16x32_bf16 v[200:203], a[112:115], a[48:51], v[228:231]\n"
"v_exp_f32 v192, v192\n"
"v_mfma_f32_16x16x32_bf16 v[200:203], a[116:119], a[52:55], v[200:203]\n"
"v_exp_f32 v193, v193\n"
"v_mfma_f32_16x16x32_bf16 v[200:203], a[120:123], a[56:59], v[200:203]\n"
"v_exp_f32 v194, v194\n"
"v_mfma_f32_16x16x32_bf16 v[200:203], a[124:127], a[60:63], v[200:203]\n"
"v_exp_f32 v195, v195\n"
"v_mfma_f32_16x16x32_bf16 v[204:207], a[128:131], a[48:51], v[228:231]\n"
"v_exp_f32 v196, v196\n"
"v_mfma_f32_16x16x32_bf16 v[204:207], a[132:135], a[52:55], v[204:207]\n"
"v_exp_f32 v197, v197\n"
"v_mfma_f32_16x16x32_bf16 v[204:207], a[136:139], a[56:59], v[204:207]\n"
"v_exp_f32 v198, v198\n"
"v_mfma_f32_16x16x32_bf16 v[204:207], a[140:143], a[60:63], v[204:207]\n"
"v_exp_f32 v199, v199\n"
"v_mfma_f32_16x16x32_bf16 v[32:35], a[144:147], v[212:215], v[32:35]\n"
"v_mfma_f32_16x16x32_bf16 v[36:39], a[148:151], v[212:215], v[36:39]\n"
"v_cvt_pk_bf16_f32 v208, v192, v193\n"
"v_cvt_pk_bf16_f32 v209, v194, v195\n"
"v_mfma_f32_16x16x32_bf16 v[40:43], a[152:155], v[212:215], v[40:43]\n"
"v_cvt_pk_bf16_f32 v210, v196, v197\n"
"v_cvt_pk_bf16_f32 v211, v198, v199\n"
"v_mfma_f32_16x16x32_bf16 v[44:47], a[156:159], v[212:215], v[44:47]\n"
"s_lshl_b32 s33, s28, 3\n"
"v_mfma_f32_16x16x32_bf16 v[48:51], a[160:163], v[212:215], v[48:51]\n"
"s_sub_i32 s32, s30, s33\n"
"v_mfma_f32_16x16x32_bf16 v[52:55], a[164:167], v[212:215], v[52:55]\n"
"s_max_i32 s30, s32, -400\n"
"v_mfma_f32_16x16x32_bf16 v[56:59], a[168:171], v[212:215], v[56:59]\n"
"s_add_i32 s30, s30, s33\n"
"v_mfma_f32_16x16x32_bf16 v[60:63], a[172:175], v[212:215], v[60:63]\n"
"s_add_i32 s30, s30, s66\n"
"v_mfma_f32_16x16x32_bf16 a[212:215], a[236:239], v[212:215], a[212:215]\n"
"v_mfma_f32_16x16x32_bf16 v[192:195], a[112:115], a[64:67], v[232:235]\n"
"v_exp_f32 v200, v200\n"
"v_mfma_f32_16x16x32_bf16 v[192:195], a[116:119], a[68:71], v[192:195]\n"
"v_exp_f32 v201, v201\n"
"v_mfma_f32_16x16x32_bf16 v[192:195], a[120:123], a[72:75], v[192:195]\n"
"v_exp_f32 v202, v202\n"
"v_mfma_f32_16x16x32_bf16 v[192:195], a[124:127], a[76:79], v[192:195]\n"
"v_exp_f32 v203, v203\n"
"v_mfma_f32_16x16x32_bf16 v[196:199], a[128:131], a[64:67], v[232:235]\n"
"v_exp_f32 v204, v204\n"
"v_mfma_f32_16x16x32_bf16 v[196:199], a[132:135], a[68:71], v[196:199]\n"
"v_exp_f32 v205, v205\n"
"v_mfma_f32_16x16x32_bf16 v[196:199], a[136:139], a[72:75], v[196:199]\n"
"v_exp_f32 v206, v206\n"
"v_mfma_f32_16x16x32_bf16 v[196:199], a[140:143], a[76:79], v[196:199]\n"
"v_exp_f32 v207, v207\n"
"v_mfma_f32_16x16x32_bf16 v[64:67], a[144:147], v[208:211], v[64:67]\n"
"v_mfma_f32_16x16x32_bf16 v[68:71], a[148:151], v[208:211], v[68:71]\n"
"v_cvt_pk_bf16_f32 v212, v200, v201\n"
"v_cvt_pk_bf16_f32 v213, v202, v203\n"
"v_mfma_f32_16x16x32_bf16 v[72:75], a[152:155], v[208:211], v[72:75]\n"
"v_cvt_pk_bf16_f32 v214, v204, v205\n"
"v_cvt_pk_bf16_f32 v215, v206, v207\n"
"v_mfma_f32_16x16x32_bf16 v[76:79], a[156:159], v[208:211], v[76:79]\n"
"s_sub_i32 s66, s66, 15\n"
"v_mfma_f32_16x16x32_bf16 v[80:83], a[160:163], v[208:211], v[80:83]\n"
"s_max_i32 s32, s32, 0\n"
"v_mfma_f32_16x16x32_bf16 v[84:87], a[164:167], v[208:211], v[84:87]\n"
"s_mul_i32 s32, s32, 13\n"
"v_mfma_f32_16x16x32_bf16 v[88:91], a[168:171], v[208:211], v[88:91]\n"
"s_lshr_b32 s32, s32, 8\n"
"v_mfma_f32_16x16x32_bf16 v[92:95], a[172:175], v[208:211], v[92:95]\n"
"s_min_u32 s31, s32, 24\n"
"v_mfma_f32_16x16x32_bf16 a[216:219], a[236:239], v[208:211], a[216:219]\n"
"1:\n"
"s_sub_u32 s31, s31, 1\n"
"s_cbranch_scc1 2f\n"
"s_nop 7\n"
"s_branch 1b\n"
"2:\n"
"s_waitcnt vmcnt(16)\n"
"s_barrier\n"
"v_mfma_f32_16x16x32_bf16 v[200:203], a[112:115], a[80:83], v[236:239]\n"
"v_exp_f32 v192, v192\n"
"v_mfma_f32_16x16x32_bf16 v[200:203], a[116:119], a[84:87], v[200:203]\n"
"v_exp_f32 v193, v193\n"
"v_mfma_f32_16x16x32_bf16 v[200:203], a[120:123], a[88:91], v[200:203]\n"
"v_exp_f32 v194, v194\n"
"v_mfma_f32_16x16x32_bf16 v[200:203], a[124:127], a[92:95], v[200:203]\n"
"v_exp_f32 v195, v195\n"
"v_mfma_f32_16x16x32_bf16 v[204:207], a[128:131], a[80:83], v[236:239]\n"
"v_exp_f32 v196, v196\n"
"v_mfma_f32_16x16x32_bf16 v[204:207], a[132:135], a[84:87], v[204:207]\n"
"v_exp_f32 v197, v197\n"
"v_mfma_f32_16x16x32_bf16 v[204:207], a[136:139], a[88:91], v[204:207]\n"
"v_exp_f32 v198, v198\n"
"v_mfma_f32_16x16x32_bf16 v[204:207], a[140:143], a[92:95], v[204:207]\n"
"v_exp_f32 v199, v199\n"
"v_mfma_f32_16x16x32_bf16 v[96:99], a[144:147], v[212:215], v[96:99]\n"
"v_mfma_f32_16x16x32_bf16 v[100:103], a[148:151], v[212:215], v[100:103]\n"
"s_add_u32 m0, s50, 0\n"
"v_cvt_pk_bf16_f32 v208, v192, v193\n"
"v_mfma_f32_16x16x32_bf16 v[104:107], a[152:155], v[212:215], v[104:107]\n"
"buffer_load_dwordx4 v250, s[36:39], s53 offen nt lds\n"
"v_mfma_f32_16x16x32_bf16 v[108:111], a[156:159], v[212:215], v[108:111]\n"
"s_add_u32 m0, s50, 64\n"
"v_cvt_pk_bf16_f32 v209, v194, v195\n"
"v_mfma_f32_16x16x32_bf16 v[112:115], a[160:163], v[212:215], v[112:115]\n"
"buffer_load_dwordx4 v250, s[36:39], s53 offen offset:1024 nt lds\n"
"v_mfma_f32_16x16x32_bf16 v[116:119], a[164:167], v[212:215], v[116:119]\n"
"s_add_u32 m0, s50, 128\n"
"v_cvt_pk_bf16_f32 v210, v196, v197\n"
"v_mfma_f32_16x16x32_bf16 v[120:123], a[168:171], v[212:215], v[120:123]\n"
"buffer_load_dwordx4 v250, s[36:39], s53 offen offset:2048 nt lds\n"
"v_mfma_f32_16x16x32_bf16 v[124:127], a[172:175], v[212:215], v[124:127]\n"
"s_add_u32 m0, s50, 192\n"
"v_cvt_pk_bf16_f32 v211, v198, v199\n"
"v_mfma_f32_16x16x32_bf16 a[220:223], a[236:239], v[212:215], a[220:223]\n"
"buffer_load_dwordx4 v250, s[36:39], s53 offen offset:3072 nt lds\n"
"v_mfma_f32_16x16x32_bf16 v[192:195], a[112:115], a[96:99], v[240:243]\n"
"v_exp_f32 v200, v200\n"
"v_mfma_f32_16x16x32_bf16 v[192:195], a[116:119], a[100:103], v[192:195]\n"
"ds_read_b128 a[112:115], v244 offset:34304\n"
"ds_read_b128 a[116:119], v244 offset:34368\n"
"v_mfma_f32_16x16x32_bf16 v[192:195], a[120:123], a[104:107], v[192:195]\n"
"v_exp_f32 v201, v201\n"
"v_mfma_f32_16x16x32_bf16 v[192:195], a[124:127], a[108:111], v[192:195]\n"
"ds_read_b128 a[120:123], v244 offset:34432\n"
"ds_read_b128 a[124:127], v244 offset:34496\n"
"v_mfma_f32_16x16x32_bf16 v[196:199], a[128:131], a[96:99], v[240:243]\n"
"v_exp_f32 v202, v202\n"
"v_mfma_f32_16x16x32_bf16 v[196:199], a[132:135], a[100:103], v[196:199]\n"
"ds_read_b128 a[128:131], v244 offset:38656\n"
"ds_read_b128 a[132:135], v244 offset:38720\n"
"v_mfma_f32_16x16x32_bf16 v[196:199], a[136:139], a[104:107], v[196:199]\n"
"v_exp_f32 v203, v203\n"
"v_mfma_f32_16x16x32_bf16 v[196:199], a[140:143], a[108:111], v[196:199]\n"
"ds_read_b128 a[136:139], v244 offset:38784\n"
"ds_read_b128 a[140:143], v244 offset:38848\n"
"v_mfma_f32_16x16x32_bf16 v[128:131], a[144:147], v[208:211], v[128:131]\n"
"v_exp_f32 v204, v204\n"
"v_mfma_f32_16x16x32_bf16 v[132:135], a[148:151], v[208:211], v[132:135]\n"
"v_exp_f32 v205, v205\n"
"v_mfma_f32_16x16x32_bf16 v[136:139], a[152:155], v[208:211], v[136:139]\n"
"v_exp_f32 v206, v206\n"
"v_mfma_f32_16x16x32_bf16 v[140:143], a[156:159], v[208:211], v[140:143]\n"
"v_exp_f32 v207, v207\n"
"v_mfma_f32_16x16x32_bf16 v[144:147], a[160:163], v[208:211], v[144:147]\n"
"v_cvt_pk_bf16_f32 v212, v200, v201\n"
"v_cvt_pk_bf16_f32 v213, v202, v203\n"
"v_mfma_f32_16x16x32_bf16 v[148:151], a[164:167], v[208:211], v[148:151]\n"
"v_cvt_pk_bf16_f32 v214, v204, v205\n"
"v_cvt_pk_bf16_f32 v215, v206, v207\n"
"v_mfma_f32_16x16x32_bf16 v[152:155], a[168:171], v[208:211], v[152:155]\n"
"v_mfma_f32_16x16x32_bf16 v[156:159], a[172:175], v[208:211], v[156:159]\n"
"v_mfma_f32_16x16x32_bf16 a[224:227], a[236:239], v[208:211], a[224:227]\n"
"s_waitcnt lgkmcnt(7)\n"
"v_mfma_f32_16x16x32_bf16 v[200:203], a[112:115], a[0:3], v[216:219]\n"
"s_waitcnt lgkmcnt(6)\n"
"v_exp_f32 v192, v192\n"
"v_mfma_f32_16x16x32_bf16 v[200:203], a[116:119], a[4:7], v[200:203]\n"
"s_waitcnt lgkmcnt(5)\n"
"v_exp_f32 v193, v193\n"
"v_mfma_f32_16x16x32_bf16 v[200:203], a[120:123], a[8:11], v[200:203]\n"
"s_waitcnt lgkmcnt(4)\n"
"v_exp_f32 v194, v194\n"
"v_mfma_f32_16x16x32_bf16 v[200:203], a[124:127], a[12:15], v[200:203]\n"
"s_waitcnt lgkmcnt(3)\n"
"v_exp_f32 v195, v195\n"
"v_mfma_f32_16x16x32_bf16 v[204:207], a[128:131], a[0:3], v[216:219]\n"
"s_waitcnt lgkmcnt(2)\n"
"v_exp_f32 v196, v196\n"
"v_mfma_f32_16x16x32_bf16 v[204:207], a[132:135], a[4:7], v[204:207]\n"
"s_waitcnt lgkmcnt(1)\n"
"v_exp_f32 v197, v197\n"
"v_mfma_f32_16x16x32_bf16 v[204:207], a[136:139], a[8:11], v[204:207]\n"
"s_waitcnt lgkmcnt(0)\n"
"v_exp_f32 v198, v198\n"
"v_mfma_f32_16x16x32_bf16 v[204:207], a[140:143], a[12:15], v[204:207]\n"
"v_exp_f32 v199, v199\n"
"v_mfma_f32_16x16x32_bf16 v[160:163], a[144:147], v[212:215], v[160:163]\n"
"s_add_u32 s53, s53, 8192\n"
"s_and_b32 s53, s53, 258047\n"
"v_mfma_f32_16x16x32_bf16 v[164:167], a[148:151], v[212:215], v[164:167]\n"
"s_add_u32 m0, s50, 4352\n"
"v_cvt_pk_bf16_f32 v208, v192, v193\n"
"v_mfma_f32_16x16x32_bf16 v[168:171], a[152:155], v[212:215], v[168:171]\n"
"buffer_load_dwordx4 v250, s[36:39], s54 offen nt lds\n"
"v_mfma_f32_16x16x32_bf16 v[172:175], a[156:159], v[212:215], v[172:175]\n"
"s_add_u32 m0, s50, 4416\n"
"v_cvt_pk_bf16_f32 v209, v194, v195\n"
"v_mfma_f32_16x16x32_bf16 v[176:179], a[160:163], v[212:215], v[176:179]\n"
"buffer_load_dwordx4 v250, s[36:39], s54 offen offset:1024 nt lds\n"
"v_mfma_f32_16x16x32_bf16 v[180:183], a[164:167], v[212:215], v[180:183]\n"
"s_add_u32 m0, s50, 4480\n"
"v_cvt_pk_bf16_f32 v210, v196, v197\n"
"v_mfma_f32_16x16x32_bf16 v[184:187], a[168:171], v[212:215], v[184:187]\n"
"buffer_load_dwordx4 v250, s[36:39], s54 offen offset:2048 nt lds\n"
"v_mfma_f32_16x16x32_bf16 v[188:191], a[172:175], v[212:215], v[188:191]\n"
"s_add_u32 m0, s50, 4544\n"
"v_cvt_pk_bf16_f32 v211, v198, v199\n"
"v_mfma_f32_16x16x32_bf16 a[228:231], a[236:239], v[212:215], a[228:231]\n"
"buffer_load_dwordx4 v250, s[36:39], s54 offen offset:3072 nt lds\n"
"s_lshl_b32 s38, s38, 1\n"
"s_add_u32 s54, s53, 4096\n"
"v_mfma_f32_16x16x32_bf16 v[192:195], a[112:115], a[16:19], v[220:223]\n"
"v_exp_f32 v200, v200\n"
"v_mfma_f32_16x16x32_bf16 v[192:195], a[116:119], a[20:23], v[192:195]\n"
"v_exp_f32 v201, v201\n"
"v_mfma_f32_16x16x32_bf16 v[192:195], a[120:123], a[24:27], v[192:195]\n"
"v_exp_f32 v202, v202\n"
"v_mfma_f32_16x16x32_bf16 v[192:195], a[124:127], a[28:31], v[192:195]\n"
"v_exp_f32 v203, v203\n"
"v_mfma_f32_16x16x32_bf16 v[196:199], a[128:131], a[16:19], v[220:223]\n"
"v_exp_f32 v204, v204\n"
"v_mfma_f32_16x16x32_bf16 v[196:199], a[132:135], a[20:23], v[196:199]\n"
"v_exp_f32 v205, v205\n"
"v_mfma_f32_16x16x32_bf16 v[196:199], a[136:139], a[24:27], v[196:199]\n"
"v_exp_f32 v206, v206\n"
"v_mfma_f32_16x16x32_bf16 v[196:199], a[140:143], a[28:31], v[196:199]\n"
"v_exp_f32 v207, v207\n"
"v_mfma_f32_16x16x32_bf16 a[176:179], a[144:147], v[208:211], a[176:179]\n"
"ds_read_b64_tr_b16 a[144:145], v246 offset:34304\n"
"ds_read_b64_tr_b16 a[146:147], v246 offset:38528\n"
"v_mfma_f32_16x16x32_bf16 a[180:183], a[148:151], v[208:211], a[180:183]\n"
"ds_read_b64_tr_b16 a[148:149], v246 offset:34336\n"
"v_cvt_pk_bf16_f32 v212, v200, v201\n"
"v_mfma_f32_16x16x32_bf16 a[184:187], a[152:155], v[208:211], a[184:187]\n"
"ds_read_b64_tr_b16 a[150:151], v246 offset:38560\n"
"ds_read_b64_tr_b16 a[152:153], v246 offset:34368\n"
"v_mfma_f32_16x16x32_bf16 a[188:191], a[156:159], v[208:211], a[188:191]\n"
"ds_read_b64_tr_b16 a[154:155], v246 offset:38592\n"
"v_cvt_pk_bf16_f32 v213, v202, v203\n"
"v_mfma_f32_16x16x32_bf16 a[192:195], a[160:163], v[208:211], a[192:195]\n"
"ds_read_b64_tr_b16 a[156:157], v246 offset:34400\n"
"ds_read_b64_tr_b16 a[158:159], v246 offset:38624\n"
"v_mfma_f32_16x16x32_bf16 a[196:199], a[164:167], v[208:211], a[196:199]\n"
"ds_read_b64_tr_b16 a[160:161], v248 offset:34304\n"
"v_cvt_pk_bf16_f32 v214, v204, v205\n"
"v_mfma_f32_16x16x32_bf16 a[200:203], a[168:171], v[208:211], a[200:203]\n"
"ds_read_b64_tr_b16 a[162:163], v248 offset:38528\n"
"ds_read_b64_tr_b16 a[164:165], v248 offset:34336\n"
"v_mfma_f32_16x16x32_bf16 a[204:207], a[172:175], v[208:211], a[204:207]\n"
"ds_read_b64_tr_b16 a[166:167], v248 offset:38560\n"
"v_cvt_pk_bf16_f32 v215, v206, v207\n"
"v_mfma_f32_16x16x32_bf16 a[232:235], a[236:239], v[208:211], a[232:235]\n"
"ds_read_b64_tr_b16 a[168:169], v248 offset:34368\n"
"ds_read_b64_tr_b16 a[170:171], v248 offset:38592\n"
"v_mfma_f32_16x16x32_bf16 v[200:203], a[112:115], a[32:35], v[224:227]\n"
"ds_read_b64_tr_b16 a[172:173], v248 offset:34400\n"
"ds_read_b64_tr_b16 a[174:175], v248 offset:38624\n"
"v_mfma_f32_16x16x32_bf16 v[200:203], a[116:119], a[36:39], v[200:203]\n"
"v_exp_f32 v192, v192\n"
"v_mfma_f32_16x16x32_bf16 v[200:203], a[120:123], a[40:43], v[200:203]\n"
"s_memrealtime s[28:29]\n"
"v_exp_f32 v193, v193\n"
"v_mfma_f32_16x16x32_bf16 v[200:203], a[124:127], a[44:47], v[200:203]\n"
"v_exp_f32 v194, v194\n"
"v_mfma_f32_16x16x32_bf16 v[204:207], a[128:131], a[32:35], v[224:227]\n"
"v_exp_f32 v195, v195\n"
"v_mfma_f32_16x16x32_bf16 v[204:207], a[132:135], a[36:39], v[204:207]\n"
"v_exp_f32 v196, v196\n"
"v_mfma_f32_16x16x32_bf16 v[204:207], a[136:139], a[40:43], v[204:207]\n"
"v_exp_f32 v197, v197\n"
"v_mfma_f32_16x16x32_bf16 v[204:207], a[140:143], a[44:47], v[204:207]\n"
"s_waitcnt lgkmcnt(14)\n"
"v_exp_f32 v198, v198\n"
"v_mfma_f32_16x16x32_bf16 v[0:3], a[144:147], v[212:215], v[0:3]\n"
"s_waitcnt lgkmcnt(12)\n"
"v_exp_f32 v199, v199\n"
"v_mfma_f32_16x16x32_bf16 v[4:7], a[148:151], v[212:215], v[4:7]\n"
"s_waitcnt lgkmcnt(10)\n"
"v_cvt_pk_bf16_f32 v208, v192, v193\n"
"v_cvt_pk_bf16_f32 v209, v194, v195\n"
"v_mfma_f32_16x16x32_bf16 v[8:11], a[152:155], v[212:215], v[8:11]\n"
"s_waitcnt lgkmcnt(8)\n"
"v_cvt_pk_bf16_f32 v210, v196, v197\n"
"v_cvt_pk_bf16_f32 v211, v198, v199\n"
"v_mfma_f32_16x16x32_bf16 v[12:15], a[156:159], v[212:215], v[12:15]\n"
"s_waitcnt lgkmcnt(6)\n"
"v_mfma_f32_16x16x32_bf16 v[16:19], a[160:163], v[212:215], v[16:19]\n"
"s_waitcnt lgkmcnt(4)\n"
"v_mfma_f32_16x16x32_bf16 v[20:23], a[164:167], v[212:215], v[20:23]\n"
"s_waitcnt lgkmcnt(2)\n"
"v_mfma_f32_16x16x32_bf16 v[24:27], a[168:171], v[212:215], v[24:27]\n"
"s_waitcnt lgkmcnt(0)\n"
"v_mfma_f32_16x16x32_bf16 v[28:31], a[172:175], v[212:215], v[28:31]\n"
"v_mfma_f32_16x16x32_bf16 a[208:211], a[236:239], v[212:215], a[208:211]\n"
"v_mfma_f32_16x16x32_bf16 v[192:195], a[112:115], a[48:51], v[228:231]\n"
"v_exp_f32 v200, v200\n"
"v_mfma_f32_16x16x32_bf16 v[192:195], a[116:119], a[52:55], v[192:195]\n"
"v_exp_f32 v201, v201\n"
"v_mfma_f32_16x16x32_bf16 v[192:195], a[120:123], a[56:59], v[192:195]\n"
"v_exp_f32 v202, v202\n"
"v_mfma_f32_16x16x32_bf16 v[192:195], a[124:127], a[60:63], v[192:195]\n"
"v_exp_f32 v203, v203\n"
"v_mfma_f32_16x16x32_bf16 v[196:199], a[128:131], a[48:51], v[228:231]\n"
"v_exp_f32 v204, v204\n"
"v_mfma_f32_16x16x32_bf16 v[196:199], a[132:135], a[52:55], v[196:199]\n"
"v_exp_f32 v205, v205\n"
"v_mfma_f32_16x16x32_bf16 v[196:199], a[136:139], a[56:59], v[196:199]\n"
"v_exp_f32 v206, v206\n"
"v_mfma_f32_16x16x32_bf16 v[196:199], a[140:143], a[60:63], v[196:199]\n"
"v_exp_f32 v207, v207\n"
"v_mfma_f32_16x16x32_bf16 v[32:35], a[144:147], v[208:211], v[32:35]\n"
"v_mfma_f32_16x16x32_bf16 v[36:39], a[148:151], v[208:211], v[36:39]\n"
"v_cvt_pk_bf16_f32 v212, v200, v201\n"
"v_cvt_pk_bf16_f32 v213, v202, v203\n"
"v_mfma_f32_16x16x32_bf16 v[40:43], a[152:155], v[208:211], v[40:43]\n"
"v_cvt_pk_bf16_f32 v214, v204, v205\n"
"v_cvt_pk_bf16_f32 v215, v206, v207\n"
"v_mfma_f32_16x16x32_bf16 v[44:47], a[156:159], v[208:211], v[44:47]\n"
"s_lshl_b32 s33, s28, 3\n"
"v_mfma_f32_16x16x32_bf16 v[48:51], a[160:163], v[208:211], v[48:51]\n"
"s_sub_i32 s32, s30, s33\n"
"v_mfma_f32_16x16x32_bf16 v[52:55], a[164:167], v[208:211], v[52:55]\n"
"s_max_i32 s30, s32, -400\n"
"v_mfma_f32_16x16x32_bf16 v[56:59], a[168:171], v[208:211], v[56:59]\n"
"s_add_i32 s30, s30, s33\n"
"v_mfma_f32_16x16x32_bf16 v[60:63], a[172:175], v[208:211], v[60:63]\n"
"s_add_i32 s30, s30, s66\n"
"v_mfma_f32_16x16x32_bf16 a[212:215], a[236:239], v[208:211], a[212:215]\n"
"v_mfma_f32_16x16x32_bf16 v[200:203], a[112:115], a[64:67], v[232:235]\n"
"v_exp_f32 v192, v192\n"
"v_mfma_f32_16x16x32_bf16 v[200:203], a[116:119], a[68:71], v[200:203]\n"
"v_exp_f32 v193, v193\n"
"v_mfma_f32_16x16x32_bf16 v[200:203], a[120:123], a[72:75], v[200:203]\n"
"v_exp_f32 v194, v194\n"
"v_mfma_f32_16x16x32_bf16 v[200:203], a[124:127], a[76:79], v[200:203]\n"
"v_exp_f32 v195, v195\n"
"v_mfma_f32_16x16x32_bf16 v[204:207], a[128:131], a[64:67], v[232:235]\n"
"v_exp_f32 v196, v196\n"
"v_mfma_f32_16x16x32_bf16 v[204:207], a[132:135], a[68:71], v[204:207]\n"
"v_exp_f32 v197, v197\n"
"v_mfma_f32_16x16x32_bf16 v[204:207], a[136:139], a[72:75], v[204:207]\n"
"v_exp_f32 v198, v198\n"
"v_mfma_f32_16x16x32_bf16 v[204:207], a[140:143], a[76:79], v[204:207]\n"
"v_exp_f32 v199, v199\n"
"v_mfma_f32_16x16x32_bf16 v[64:67], a[144:147], v[212:215], v[64:67]\n"
"v_mfma_f32_16x16x32_bf16 v[68:71], a[148:151], v[212:215], v[68:71]\n"
"v_cvt_pk_bf16_f32 v208, v192, v193\n"
"v_cvt_pk_bf16_f32 v209, v194, v195\n"
"v_mfma_f32_16x16x32_bf16 v[72:75], a[152:155], v[212:215], v[72:75]\n"
"v_cvt_pk_bf16_f32 v210, v196, v197\n"
"v_cvt_pk_bf16_f32 v211, v198, v199\n"
"v_mfma_f32_16x16x32_bf16 v[76:79], a[156:159], v[212:215], v[76:79]\n"
"s_sub_i32 s66, s66, 15\n"
"v_mfma_f32_16x16x32_bf16 v[80:83], a[160:163], v[212:215], v[80:83]\n"
"s_max_i32 s32, s32, 0\n"
"v_mfma_f32_16x16x32_bf16 v[84:87], a[164:167], v[212:215], v[84:87]\n"
"s_mul_i32 s32, s32, 13\n"
"v_mfma_f32_16x16x32_bf16 v[88:91], a[168:171], v[212:215], v[88:91]\n"
"s_lshr_b32 s32, s32, 8\n"
"v_mfma_f32_16x16x32_bf16 v[92:95], a[172:175], v[212:215], v[92:95]\n"
"s_min_u32 s31, s32, 24\n"
"v_mfma_f32_16x16x32_bf16 a[216:219], a[236:239], v[212:215], a[216:219]\n"
"1:\n"
"s_sub_u32 s31, s31, 1\n"
"s_cbranch_scc1 2f\n"
"s_nop 7\n"
"s_branch 1b\n"
"2:\n"
"s_waitcnt vmcnt(16)\n"
"s_barrier\n"
"s_cmp_lg_u32 s55, 0\n"
"s_cbranch_scc1 L1_npf_h0\n"
"buffer_load_dwordx4 a[240:243], v251, s[60:63], 0 offen\n"
"buffer_load_dwordx4 a[240:243], v251, s[60:63], 0 offen offset:2048\n"
"buffer_load_dwordx4 a[240:243], v254, s[60:63], 0 offen\n"
"buffer_load_dwordx4 a[240:243], v254, s[60:63], 0 offen offset:2048\n"
"global_load_dword a240, v255, s[100:101]\n"
"L1_npf_h0:\n"
"v_mfma_f32_16x16x32_bf16 v[192:195], a[112:115], a[80:83], v[236:239]\n"
"v_exp_f32 v200, v200\n"
"v_mfma_f32_16x16x32_bf16 v[192:195], a[116:119], a[84:87], v[192:195]\n"
"ds_read_b128 a[112:115], v245\n"
"ds_read_b128 a[116:119], v245 offset:64\n"
"v_mfma_f32_16x16x32_bf16 v[192:195], a[120:123], a[88:91], v[192:195]\n"
"v_exp_f32 v201, v201\n"
"v_mfma_f32_16x16x32_bf16 v[192:195], a[124:127], a[92:95], v[192:195]\n"
"ds_read_b128 a[120:123], v245 offset:128\n"
"ds_read_b128 a[124:127], v245 offset:192\n"
"v_mfma_f32_16x16x32_bf16 v[196:199], a[128:131], a[80:83], v[236:239]\n"
"v_exp_f32 v202, v202\n"
"v_mfma_f32_16x16x32_bf16 v[196:199], a[132:135], a[84:87], v[196:199]\n"
"ds_read_b128 a[128:131], v245 offset:4352\n"
"ds_read_b128 a[132:135], v245 offset:4416\n"
"v_mfma_f32_16x16x32_bf16 v[196:199], a[136:139], a[88:91], v[196:199]\n"
"v_exp_f32 v203, v203\n"
"v_mfma_f32_16x16x32_bf16 v[196:199], a[140:143], a[92:95], v[196:199]\n"
"ds_read_b128 a[136:139], v245 offset:4480\n"
"ds_read_b128 a[140:143], v245 offset:4544\n"
"v_mfma_f32_16x16x32_bf16 v[96:99], a[144:147], v[208:211], v[96:99]\n"
"v_exp_f32 v204, v204\n"
"v_mfma_f32_16x16x32_bf16 v[100:103], a[148:151], v[208:211], v[100:103]\n"
"v_exp_f32 v205, v205\n"
"v_mfma_f32_16x16x32_bf16 v[104:107], a[152:155], v[208:211], v[104:107]\n"
"v_exp_f32 v206, v206\n"
"v_mfma_f32_16x16x32_bf16 v[108:111], a[156:159], v[208:211], v[108:111]\n"
"v_exp_f32 v207, v207\n"
"v_mfma_f32_16x16x32_bf16 v[112:115], a[160:163], v[208:211], v[112:115]\n"
"v_cvt_pk_bf16_f32 v212, v200, v201\n"
"v_cvt_pk_bf16_f32 v213, v202, v203\n"
"v_mfma_f32_16x16x32_bf16 v[116:119], a[164:167], v[208:211], v[116:119]\n"
"v_cvt_pk_bf16_f32 v214, v204, v205\n"
"v_cvt_pk_bf16_f32 v215, v206, v207\n"
"v_mfma_f32_16x16x32_bf16 v[120:123], a[168:171], v[208:211], v[120:123]\n"
"v_mfma_f32_16x16x32_bf16 v[124:127], a[172:175], v[208:211], v[124:127]\n"
"v_mfma_f32_16x16x32_bf16 a[220:223], a[236:239], v[208:211], a[220:223]\n"
"s_waitcnt lgkmcnt(7)\n"
"v_mfma_f32_16x16x32_bf16 v[200:203], a[112:115], a[0:3], v[216:219]\n"
"s_waitcnt lgkmcnt(6)\n"
"v_exp_f32 v192, v192\n"
"v_mfma_f32_16x16x32_bf16 v[200:203], a[116:119], a[4:7], v[200:203]\n"
"s_waitcnt lgkmcnt(5)\n"
"v_exp_f32 v193, v193\n"
"v_mfma_f32_16x16x32_bf16 v[200:203], a[120:123], a[8:11], v[200:203]\n"
"s_waitcnt lgkmcnt(4)\n"
"v_exp_f32 v194, v194\n"
"v_mfma_f32_16x16x32_bf16 v[200:203], a[124:127], a[12:15], v[200:203]\n"
"s_waitcnt lgkmcnt(3)\n"
"v_exp_f32 v195, v195\n"
"v_mfma_f32_16x16x32_bf16 v[204:207], a[128:131], a[0:3], v[216:219]\n"
"s_waitcnt lgkmcnt(2)\n"
"v_exp_f32 v196, v196\n"
"v_mfma_f32_16x16x32_bf16 v[204:207], a[132:135], a[4:7], v[204:207]\n"
"s_waitcnt lgkmcnt(1)\n"
"v_exp_f32 v197, v197\n"
"v_mfma_f32_16x16x32_bf16 v[204:207], a[136:139], a[8:11], v[204:207]\n"
"s_waitcnt lgkmcnt(0)\n"
"v_exp_f32 v198, v198\n"
"v_mfma_f32_16x16x32_bf16 v[204:207], a[140:143], a[12:15], v[204:207]\n"
"v_exp_f32 v199, v199\n"
"v_mfma_f32_16x16x32_bf16 v[128:131], a[144:147], v[212:215], v[128:131]\n"
"v_mfma_f32_16x16x32_bf16 v[132:135], a[148:151], v[212:215], v[132:135]\n"
"s_add_u32 m0, s50, 34304\n"
"v_cvt_pk_bf16_f32 v208, v192, v193\n"
"v_mfma_f32_16x16x32_bf16 v[136:139], a[152:155], v[212:215], v[136:139]\n"
"buffer_load_dwordx4 v250, s[36:39], s53 offen nt lds\n"
"v_mfma_f32_16x16x32_bf16 v[140:143], a[156:159], v[212:215], v[140:143]\n"
"s_add_u32 m0, s50, 34368\n"
"v_cvt_pk_bf16_f32 v209, v194, v195\n"
"v_mfma_f32_16x16x32_bf16 v[144:147], a[160:163], v[212:215], v[144:147]\n"
"buffer_load_dwordx4 v250, s[36:39], s53 offen offset:1024 nt lds\n"
"v_mfma_f32_16x16x32_bf16 v[148:151], a[164:167], v[212:215], v[148:151]\n"
"s_add_u32 m0, s50, 34432\n"
"v_cvt_pk_bf16_f32 v210, v196, v197\n"
"v_mfma_f32_16x16x32_bf16 v[152:155], a[168:171], v[212:215], v[152:155]\n"
"buffer_load_dwordx4 v250, s[36:39], s53 offen offset:2048 nt lds\n"
"v_mfma_f32_16x16x32_bf16 v[156:159], a[172:175], v[212:215], v[156:159]\n"
"s_add_u32 m0, s50, 34496\n"
"v_cvt_pk_bf16_f32 v211, v198, v199\n"
"v_mfma_f32_16x16x32_bf16 a[224:227], a[236:239], v[212:215], a[224:227]\n"
"buffer_load_dwordx4 v250, s[36:39], s53 offen offset:3072 nt lds\n"
"v_mfma_f32_16x16x32_bf16 v[192:195], a[112:115], a[16:19], v[220:223]\n"
"v_exp_f32 v200, v200\n"
"v_mfma_f32_16x16x32_bf16 v[192:195], a[116:119], a[20:23], v[192:195]\n"
"v_exp_f32 v201, v201\n"
"v_mfma_f32_16x16x32_bf16 v[192:195], a[120:123], a[24:27], v[192:195]\n"
"v_exp_f32 v202, v202\n"
"v_mfma_f32_16x16x32_bf16 v[192:195], a[124:127], a[28:31], v[192:195]\n"
"v_exp_f32 v203, v203\n"
"v_mfma_f32_16x16x32_bf16 v[196:199], a[128:131], a[16:19], v[220:223]\n"
"v_exp_f32 v204, v204\n"
"v_mfma_f32_16x16x32_bf16 v[196:199], a[132:135], a[20:23], v[196:199]\n"
"v_exp_f32 v205, v205\n"
"v_mfma_f32_16x16x32_bf16 v[196:199], a[136:139], a[24:27], v[196:199]\n"
"v_exp_f32 v206, v206\n"
"v_mfma_f32_16x16x32_bf16 v[196:199], a[140:143], a[28:31], v[196:199]\n"
"v_exp_f32 v207, v207\n"
"v_mfma_f32_16x16x32_bf16 v[160:163], a[144:147], v[208:211], v[160:163]\n"
"ds_read_b64_tr_b16 a[144:145], v247\n"
"ds_read_b64_tr_b16 a[146:147], v247 offset:4224\n"
"v_mfma_f32_16x16x32_bf16 v[164:167], a[148:151], v[208:211], v[164:167]\n"
"ds_read_b64_tr_b16 a[148:149], v247 offset:32\n"
"v_cvt_pk_bf16_f32 v212, v200, v201\n"
"v_mfma_f32_16x16x32_bf16 v[168:171], a[152:155], v[208:211], v[168:171]\n"
"ds_read_b64_tr_b16 a[150:151], v247 offset:4256\n"
"ds_read_b64_tr_b16 a[152:153], v247 offset:64\n"
"v_mfma_f32_16x16x32_bf16 v[172:175], a[156:159], v[208:211], v[172:175]\n"
"ds_read_b64_tr_b16 a[154:155], v247 offset:4288\n"
"v_cvt_pk_bf16_f32 v213, v202, v203\n"
"v_mfma_f32_16x16x32_bf16 v[176:179], a[160:163], v[208:211], v[176:179]\n"
"ds_read_b64_tr_b16 a[156:157], v247 offset:96\n"
"ds_read_b64_tr_b16 a[158:159], v247 offset:4320\n"
"v_mfma_f32_16x16x32_bf16 v[180:183], a[164:167], v[208:211], v[180:183]\n"
"ds_read_b64_tr_b16 a[160:161], v249\n"
"v_cvt_pk_bf16_f32 v214, v204, v205\n"
"v_mfma_f32_16x16x32_bf16 v[184:187], a[168:171], v[208:211], v[184:187]\n"
"ds_read_b64_tr_b16 a[162:163], v249 offset:4224\n"
"ds_read_b64_tr_b16 a[164:165], v249 offset:32\n"
"v_mfma_f32_16x16x32_bf16 v[188:191], a[172:175], v[208:211], v[188:191]\n"
"ds_read_b64_tr_b16 a[166:167], v249 offset:4256\n"
"v_cvt_pk_bf16_f32 v215, v206, v207\n"
"v_mfma_f32_16x16x32_bf16 a[228:231], a[236:239], v[208:211], a[228:231]\n"
"ds_read_b64_tr_b16 a[168:169], v249 offset:64\n"
"ds_read_b64_tr_b16 a[170:171], v249 offset:4288\n"
"v_mfma_f32_16x16x32_bf16 v[200:203], a[112:115], a[32:35], v[224:227]\n"
"ds_read_b64_tr_b16 a[172:173], v249 offset:96\n"
"ds_read_b64_tr_b16 a[174:175], v249 offset:4320\n"
"v_mfma_f32_16x16x32_bf16 v[200:203], a[116:119], a[36:39], v[200:203]\n"
"v_exp_f32 v192, v192\n"
"v_mfma_f32_16x16x32_bf16 v[200:203], a[120:123], a[40:43], v[200:203]\n"
"s_memrealtime s[28:29]\n"
"v_exp_f32 v193, v193\n"
"v_mfma_f32_16x16x32_bf16 v[200:203], a[124:127], a[44:47], v[200:203]\n"
"v_exp_f32 v194, v194\n"
"v_mfma_f32_16x16x32_bf16 v[204:207], a[128:131], a[32:35], v[224:227]\n"
"v_exp_f32 v195, v195\n"
"v_mfma_f32_16x16x32_bf16 v[204:207], a[132:135], a[36:39], v[204:207]\n"
"v_exp_f32 v196, v196\n"
"v_mfma_f32_16x16x32_bf16 v[204:207], a[136:139], a[40:43], v[204:207]\n"
"v_exp_f32 v197, v197\n"
"v_mfma_f32_16x16x32_bf16 v[204:207], a[140:143], a[44:47], v[204:207]\n"
"s_waitcnt lgkmcnt(14)\n"
"v_exp_f32 v198, v198\n"
"v_mfma_f32_16x16x32_bf16 v[0:3], a[144:147], v[212:215], v[0:3]\n"
"s_waitcnt lgkmcnt(12)\n"
"v_exp_f32 v199, v199\n"
"v_mfma_f32_16x16x32_bf16 v[4:7], a[148:151], v[212:215], v[4:7]\n"
"s_waitcnt lgkmcnt(10)\n"
"s_add_u32 m0, s50, 38656\n"
"v_cvt_pk_bf16_f32 v208, v192, v193\n"
"v_mfma_f32_16x16x32_bf16 v[8:11], a[152:155], v[212:215], v[8:11]\n"
"s_waitcnt lgkmcnt(8)\n"
"buffer_load_dwordx4 v250, s[36:39], s54 offen nt lds\n"
"s_add_u32 s53, s53, 8192\n"
"v_mfma_f32_16x16x32_bf16 v[12:15], a[156:159], v[212:215], v[12:15]\n"
"s_waitcnt lgkmcnt(6)\n"
"s_add_u32 m0, s50, 38720\n"
"v_cvt_pk_bf16_f32 v209, v194, v195\n"
"v_mfma_f32_16x16x32_bf16 v[16:19], a[160:163], v[212:215], v[16:19]\n"
"s_waitcnt lgkmcnt(4)\n"
"buffer_load_dwordx4 v250, s[36:39], s54 offen offset:1024 nt lds\n"
"s_and_b32 s53, s53, 258047\n"
"v_mfma_f32_16x16x32_bf16 v[20:23], a[164:167], v[212:215], v[20:23]\n"
"s_waitcnt lgkmcnt(2)\n"
"s_add_u32 m0, s50, 38784\n"
"v_cvt_pk_bf16_f32 v210, v196, v197\n"
"v_mfma_f32_16x16x32_bf16 v[24:27], a[168:171], v[212:215], v[24:27]\n"
"s_waitcnt lgkmcnt(0)\n"
"buffer_load_dwordx4 v250, s[36:39], s54 offen offset:2048 nt lds\n"
"v_mfma_f32_16x16x32_bf16 v[28:31], a[172:175], v[212:215], v[28:31]\n"
"s_add_u32 m0, s50, 38848\n"
"v_cvt_pk_bf16_f32 v211, v198, v199\n"
"v_mfma_f32_16x16x32_bf16 a[208:211], a[236:239], v[212:215], a[208:211]\n"
"buffer_load_dwordx4 v250, s[36:39], s54 offen offset:3072 nt lds\n"
"s_lshl_b32 s38, s38, 1\n"
"s_add_u32 s54, s53, 4096\n"
"v_mfma_f32_16x16x32_bf16 v[192:195], a[112:115], a[48:51], v[228:231]\n"
"v_exp_f32 v200, v200\n"
"v_mfma_f32_16x16x32_bf16 v[192:195], a[116:119], a[52:55], v[192:195]\n"
"v_exp_f32 v201, v201\n"
"v_mfma_f32_16x16x32_bf16 v[192:195], a[120:123], a[56:59], v[192:195]\n"
"v_exp_f32 v202, v202\n"
"v_mfma_f32_16x16x32_bf16 v[192:195], a[124:127], a[60:63], v[192:195]\n"
"v_exp_f32 v203, v203\n"
"v_mfma_f32_16x16x32_bf16 v[196:199], a[128:131], a[48:51], v[228:231]\n"
"v_exp_f32 v204, v204\n"
"v_mfma_f32_16x16x32_bf16 v[196:199], a[132:135], a[52:55], v[196:199]\n"
"v_exp_f32 v205, v205\n"
"v_mfma_f32_16x16x32_bf16 v[196:199], a[136:139], a[56:59], v[196:199]\n"
"v_exp_f32 v206, v206\n"
"v_mfma_f32_16x16x32_bf16 v[196:199], a[140:143], a[60:63], v[196:199]\n"
"v_exp_f32 v207, v207\n"
"v_mfma_f32_16x16x32_bf16 v[32:35], a[144:147], v[208:211], v[32:35]\n"
"v_mfma_f32_16x16x32_bf16 v[36:39], a[148:151], v[208:211], v[36:39]\n"
"v_cvt_pk_bf16_f32 v212, v200, v201\n"
"v_cvt_pk_bf16_f32 v213, v202, v203\n"
"v_mfma_f32_16x16x32_bf16 v[40:43], a[152:155], v[208:211], v[40:43]\n"
"v_cvt_pk_bf16_f32 v214, v204, v205\n"
"v_cvt_pk_bf16_f32 v215, v206, v207\n"
"v_mfma_f32_16x16x32_bf16 v[44:47], a[156:159], v[208:211], v[44:47]\n"
"s_lshl_b32 s33, s28, 3\n"
"v_mfma_f32_16x16x32_bf16 v[48:51], a[160:163], v[208:211], v[48:51]\n"
"s_sub_i32 s32, s30, s33\n"
"v_mfma_f32_16x16x32_bf16 v[52:55], a[164:167], v[208:211], v[52:55]\n"
"s_max_i32 s30, s32, -400\n"
"v_mfma_f32_16x16x32_bf16 v[56:59], a[168:171], v[208:211], v[56:59]\n"
"s_add_i32 s30, s30, s33\n"
"v_mfma_f32_16x16x32_bf16 v[60:63], a[172:175], v[208:211], v[60:63]\n"
"s_add_i32 s30, s30, s66\n"
"v_mfma_f32_16x16x32_bf16 a[212:215], a[236:239], v[208:211], a[212:215]\n"
"s_cmp_lg_u32 s55, 0\n"
"s_cbranch_scc1 L1_npfb_h0\n"
"buffer_load_dwordx4 a[240:243], v251, s[60:63], 0 offen offset:1024\n"
"buffer_load_dwordx4 a[240:243], v251, s[60:63], 0 offen offset:3072\n"
"buffer_load_dwordx4 a[240:243], v254, s[60:63], 0 offen offset:1024\n"
"buffer_load_dwordx4 a[240:243], v254, s[60:63], 0 offen offset:3072\n"
"L1_npfb_h0:\n"
"v_mfma_f32_16x16x32_bf16 v[200:203], a[112:115], a[64:67], v[232:235]\n"
"v_exp_f32 v192, v192\n"
"v_mfma_f32_16x16x32_bf16 v[200:203], a[116:119], a[68:71], v[200:203]\n"
"v_exp_f32 v193, v193\n"
"v_mfma_f32_16x16x32_bf16 v[200:203], a[120:123], a[72:75], v[200:203]\n"
"v_exp_f32 v194, v194\n"
"v_mfma_f32_16x16x32_bf16 v[200:203], a[124:127], a[76:79], v[200:203]\n"
"v_exp_f32 v195, v195\n"
"v_mfma_f32_16x16x32_bf16 v[204:207], a[128:131], a[64:67], v[232:235]\n"
"v_exp_f32 v196, v196\n"
"v_mfma_f32_16x16x32_bf16 v[204:207], a[132:135], a[68:71], v[204:207]\n"
"v_exp_f32 v197, v197\n"
"v_mfma_f32_16x16x32_bf16 v[204:207], a[136:139], a[72:75], v[204:207]\n"
"v_exp_f32 v198, v198\n"
"v_mfma_f32_16x16x32_bf16 v[204:207], a[140:143], a[76:79], v[204:207]\n"
"v_exp_f32 v199, v199\n"
"v_mfma_f32_16x16x32_bf16 v[64:67], a[144:147], v[212:215], v[64:67]\n"
"v_mfma_f32_16x16x32_bf16 v[68:71], a[148:151], v[212:215], v[68:71]\n"
"v_cvt_pk_bf16_f32 v208, v192, v193\n"
"v_cvt_pk_bf16_f32 v209, v194, v195\n"
"v_mfma_f32_16x16x32_bf16 v[72:75], a[152:155], v[212:215], v[72:75]\n"
"v_cvt_pk_bf16_f32 v210, v196, v197\n"
"v_cvt_pk_bf16_f32 v211, v198, v199\n"
"v_mfma_f32_16x16x32_bf16 v[76:79], a[156:159], v[212:215], v[76:79]\n"
"s_sub_i32 s66, s66, 15\n"
"v_mfma_f32_16x16x32_bf16 v[80:83], a[160:163], v[212:215], v[80:83]\n"
"s_max_i32 s32, s32, 0\n"
"v_mfma_f32_16x16x32_bf16 v[84:87], a[164:167], v[212:215], v[84:87]\n"
"s_mul_i32 s32, s32, 13\n"
"v_mfma_f32_16x16x32_bf16 v[88:91], a[168:171], v[212:215], v[88:91]\n"
"s_lshr_b32 s32, s32, 8\n"
"v_mfma_f32_16x16x32_bf16 v[92:95], a[172:175], v[212:215], v[92:95]\n"
"s_min_u32 s31, s32, 24\n"
"v_mfma_f32_16x16x32_bf16 a[216:219], a[236:239], v[212:215], a[216:219]\n"
"1:\n"
"s_sub_u32 s31, s31, 1\n"
"s_cbranch_scc1 2f\n"
"s_nop 7\n"
"s_branch 1b\n"
"2:\n"
"s_waitcnt vmcnt(16)\n"
"s_barrier\n"
"v_mfma_f32_16x16x32_bf16 v[192:195], a[112:115], a[80:83], v[236:239]\n"
"v_exp_f32 v200, v200\n"
"v_mfma_f32_16x16x32_bf16 v[192:195], a[116:119], a[84:87], v[192:195]\n"
"v_exp_f32 v201, v201\n"
"v_mfma_f32_16x16x32_bf16 v[192:195], a[120:123], a[88:91], v[192:195]\n"
"v_exp_f32 v202, v202\n"
"v_mfma_f32_16x16x32_bf16 v[192:195], a[124:127], a[92:95], v[192:195]\n"
"v_exp_f32 v203, v203\n"
"v_mfma_f32_16x16x32_bf16 v[196:199], a[128:131], a[80:83], v[236:239]\n"
"v_exp_f32 v204, v204\n"
"v_mfma_f32_16x16x32_bf16 v[196:199], a[132:135], a[84:87], v[196:199]\n"
"v_exp_f32 v205, v205\n"
"v_mfma_f32_16x16x32_bf16 v[196:199], a[136:139], a[88:91], v[196:199]\n"
"v_exp_f32 v206, v206\n"
"v_mfma_f32_16x16x32_bf16 v[196:199], a[140:143], a[92:95], v[196:199]\n"
"v_exp_f32 v207, v207\n"
"v_mfma_f32_16x16x32_bf16 v[96:99], a[144:147], v[208:211], v[96:99]\n"
"v_mfma_f32_16x16x32_bf16 v[100:103], a[148:151], v[208:211], v[100:103]\n"
"s_add_u32 m0, s50, 68608\n"
"v_cvt_pk_bf16_f32 v212, v200, v201\n"
"v_mfma_f32_16x16x32_bf16 v[104:107], a[152:155], v[208:211], v[104:107]\n"
"buffer_load_dwordx4 v250, s[36:39], s53 offen nt lds\n"
"v_mfma_f32_16x16x32_bf16 v[108:111], a[156:159], v[208:211], v[108:111]\n"
"s_add_u32 m0, s50, 68672\n"
"v_cvt_pk_bf16_f32 v213, v202, v203\n"
"v_mfma_f32_16x16x32_bf16 v[112:115], a[160:163], v[208:211], v[112:115]\n"
"buffer_load_dwordx4 v250, s[36:39], s53 offen offset:1024 nt lds\n"
"v_mfma_f32_16x16x32_bf16 v[116:119], a[164:167], v[208:211], v[116:119]\n"
"s_add_u32 m0, s50, 68736\n"
"v_cvt_pk_bf16_f32 v214, v204, v205\n"
"v_mfma_f32_16x16x32_bf16 v[120:123], a[168:171], v[208:211], v[120:123]\n"
"buffer_load_dwordx4 v250, s[36:39], s53 offen offset:2048 nt lds\n"
"v_mfma_f32_16x16x32_bf16 v[124:127], a[172:175], v[208:211], v[124:127]\n"
"s_add_u32 m0, s50, 68800\n"
"v_cvt_pk_bf16_f32 v215, v206, v207\n"
"v_mfma_f32_16x16x32_bf16 a[220:223], a[236:239], v[208:211], a[220:223]\n"
"buffer_load_dwordx4 v250, s[36:39], s53 offen offset:3072 nt lds\n"
"v_mfma_f32_16x16x32_bf16 v[200:203], a[112:115], a[96:99], v[240:243]\n"
"v_exp_f32 v192, v192\n"
"v_mfma_f32_16x16x32_bf16 v[200:203], a[116:119], a[100:103], v[200:203]\n"
"ds_read_b128 a[112:115], v245 offset:34304\n"
"ds_read_b128 a[116:119], v245 offset:34368\n"
"v_mfma_f32_16x16x32_bf16 v[200:203], a[120:123], a[104:107], v[200:203]\n"
"v_exp_f32 v193, v193\n"
"v_mfma_f32_16x16x32_bf16 v[200:203], a[124:127], a[108:111], v[200:203]\n"
"ds_read_b128 a[120:123], v245 offset:34432\n"
"ds_read_b128 a[124:127], v245 offset:34496\n"
"v_mfma_f32_16x16x32_bf16 v[204:207], a[128:131], a[96:99], v[240:243]\n"
"v_exp_f32 v194, v194\n"
"v_mfma_f32_16x16x32_bf16 v[204:207], a[132:135], a[100:103], v[204:207]\n"
"ds_read_b128 a[128:131], v245 offset:38656\n"
"ds_read_b128 a[132:135], v245 offset:38720\n"
"v_mfma_f32_16x16x32_bf16 v[204:207], a[136:139], a[104:107], v[204:207]\n"
"v_exp_f32 v195, v195\n"
"v_mfma_f32_16x16x32_bf16 v[204:207], a[140:143], a[108:111], v[204:207]\n"
"ds_read_b128 a[136:139], v245 offset:38784\n"
"ds_read_b128 a[140:143], v245 offset:38848\n"
"v_mfma_f32_16x16x32_bf16 v[128:131], a[144:147], v[212:215], v[128:131]\n"
"v_exp_f32 v196, v196\n"
"v_mfma_f32_16x16x32_bf16 v[132:135], a[148:151], v[212:215], v[132:135]\n"
"v_exp_f32 v197, v197\n"
"v_mfma_f32_16x16x32_bf16 v[136:139], a[152:155], v[212:215], v[136:139]\n"
"v_exp_f32 v198, v198\n"
"v_mfma_f32_16x16x32_bf16 v[140:143], a[156:159], v[212:215], v[140:143]\n"
"v_exp_f32 v199, v199\n"
"v_mfma_f32_16x16x32_bf16 v[144:147], a[160:163], v[212:215], v[144:147]\n"
"v_cvt_pk_bf16_f32 v208, v192, v193\n"
"v_cvt_pk_bf16_f32 v209, v194, v195\n"
"v_mfma_f32_16x16x32_bf16 v[148:151], a[164:167], v[212:215], v[148:151]\n"
"v_cvt_pk_bf16_f32 v210, v196, v197\n"
"v_cvt_pk_bf16_f32 v211, v198, v199\n"
"v_mfma_f32_16x16x32_bf16 v[152:155], a[168:171], v[212:215], v[152:155]\n"
"v_mfma_f32_16x16x32_bf16 v[156:159], a[172:175], v[212:215], v[156:159]\n"
"v_mfma_f32_16x16x32_bf16 a[224:227], a[236:239], v[212:215], a[224:227]\n"
"s_waitcnt lgkmcnt(7)\n"
"v_mfma_f32_16x16x32_bf16 v[192:195], a[112:115], a[0:3], v[216:219]\n"
"s_waitcnt lgkmcnt(6)\n"
"v_exp_f32 v200, v200\n"
"v_mfma_f32_16x16x32_bf16 v[192:195], a[116:119], a[4:7], v[192:195]\n"
"s_waitcnt lgkmcnt(5)\n"
"v_exp_f32 v201, v201\n"
"v_mfma_f32_16x16x32_bf16 v[192:195], a[120:123], a[8:11], v[192:195]\n"
"s_waitcnt lgkmcnt(4)\n"
"v_exp_f32 v202, v202\n"
"v_mfma_f32_16x16x32_bf16 v[192:195], a[124:127], a[12:15], v[192:195]\n"
"s_waitcnt lgkmcnt(3)\n"
"v_exp_f32 v203, v203\n"
"v_mfma_f32_16x16x32_bf16 v[196:199], a[128:131], a[0:3], v[216:219]\n"
"s_waitcnt lgkmcnt(2)\n"
"v_exp_f32 v204, v204\n"
"v_mfma_f32_16x16x32_bf16 v[196:199], a[132:135], a[4:7], v[196:199]\n"
"s_waitcnt lgkmcnt(1)\n"
"v_exp_f32 v205, v205\n"
"v_mfma_f32_16x16x32_bf16 v[196:199], a[136:139], a[8:11], v[196:199]\n"
"s_waitcnt lgkmcnt(0)\n"
"v_exp_f32 v206, v206\n"
"v_mfma_f32_16x16x32_bf16 v[196:199], a[140:143], a[12:15], v[196:199]\n"
"v_exp_f32 v207, v207\n"
"v_mfma_f32_16x16x32_bf16 v[160:163], a[144:147], v[208:211], v[160:163]\n"
"s_add_u32 s53, s53, 8192\n"
"s_and_b32 s53, s53, 258047\n"
"v_mfma_f32_16x16x32_bf16 v[164:167], a[148:151], v[208:211], v[164:167]\n"
"s_add_u32 m0, s50, 72960\n"
"v_cvt_pk_bf16_f32 v212, v200, v201\n"
"v_mfma_f32_16x16x32_bf16 v[168:171], a[152:155], v[208:211], v[168:171]\n"
"buffer_load_dwordx4 v250, s[36:39], s54 offen nt lds\n"
"v_mfma_f32_16x16x32_bf16 v[172:175], a[156:159], v[208:211], v[172:175]\n"
"s_add_u32 m0, s50, 73024\n"
"v_cvt_pk_bf16_f32 v213, v202, v203\n"
"v_mfma_f32_16x16x32_bf16 v[176:179], a[160:163], v[208:211], v[176:179]\n"
"buffer_load_dwordx4 v250, s[36:39], s54 offen offset:1024 nt lds\n"
"v_mfma_f32_16x16x32_bf16 v[180:183], a[164:167], v[208:211], v[180:183]\n"
"s_add_u32 m0, s50, 73088\n"
"v_cvt_pk_bf16_f32 v214, v204, v205\n"
"v_mfma_f32_16x16x32_bf16 v[184:187], a[168:171], v[208:211], v[184:187]\n"
"buffer_load_dwordx4 v250, s[36:39], s54 offen offset:2048 nt lds\n"
"v_mfma_f32_16x16x32_bf16 v[188:191], a[172:175], v[208:211], v[188:191]\n"
"s_add_u32 m0, s50, 73152\n"
"v_cvt_pk_bf16_f32 v215, v206, v207\n"
"v_mfma_f32_16x16x32_bf16 a[228:231], a[236:239], v[208:211], a[228:231]\n"
"buffer_load_dwordx4 v250, s[36:39], s54 offen offset:3072 nt lds\n"
"s_lshl_b32 s38, s38, 1\n"
"s_add_u32 s54, s53, 4096\n"
"v_mfma_f32_16x16x32_bf16 v[200:203], a[112:115], a[16:19], v[220:223]\n"
"v_exp_f32 v192, v192\n"
"v_mfma_f32_16x16x32_bf16 v[200:203], a[116:119], a[20:23], v[200:203]\n"
"v_exp_f32 v193, v193\n"
"v_mfma_f32_16x16x32_bf16 v[200:203], a[120:123], a[24:27], v[200:203]\n"
"v_exp_f32 v194, v194\n"
"v_mfma_f32_16x16x32_bf16 v[200:203], a[124:127], a[28:31], v[200:203]\n"
"v_exp_f32 v195, v195\n"
"v_mfma_f32_16x16x32_bf16 v[204:207], a[128:131], a[16:19], v[220:223]\n"
"v_exp_f32 v196, v196\n"
"v_mfma_f32_16x16x32_bf16 v[204:207], a[132:135], a[20:23], v[204:207]\n"
"v_exp_f32 v197, v197\n"
"v_mfma_f32_16x16x32_bf16 v[204:207], a[136:139], a[24:27], v[204:207]\n"
"v_exp_f32 v198, v198\n"
"v_mfma_f32_16x16x32_bf16 v[204:207], a[140:143], a[28:31], v[204:207]\n"
"v_exp_f32 v199, v199\n"
"v_mfma_f32_16x16x32_bf16 a[176:179], a[144:147], v[212:215], a[176:179]\n"
"ds_read_b64_tr_b16 a[144:145], v247 offset:34304\n"
"ds_read_b64_tr_b16 a[146:147], v247 offset:38528\n"
"v_mfma_f32_16x16x32_bf16 a[180:183], a[148:151], v[212:215], a[180:183]\n"
"ds_read_b64_tr_b16 a[148:149], v247 offset:34336\n"
"v_cvt_pk_bf16_f32 v208, v192, v193\n"
"v_mfma_f32_16x16x32_bf16 a[184:187], a[152:155], v[212:215], a[184:187]\n"
"ds_read_b64_tr_b16 a[150:151], v247 offset:38560\n"
"ds_read_b64_tr_b16 a[152:153], v247 offset:34368\n"
"v_mfma_f32_16x16x32_bf16 a[188:191], a[156:159], v[212:215], a[188:191]\n"
"ds_read_b64_tr_b16 a[154:155], v247 offset:38592\n"
"v_cvt_pk_bf16_f32 v209, v194, v195\n"
"v_mfma_f32_16x16x32_bf16 a[192:195], a[160:163], v[212:215], a[192:195]\n"
"ds_read_b64_tr_b16 a[156:157], v247 offset:34400\n"
"ds_read_b64_tr_b16 a[158:159], v247 offset:38624\n"
"v_mfma_f32_16x16x32_bf16 a[196:199], a[164:167], v[212:215], a[196:199]\n"
"ds_read_b64_tr_b16 a[160:161], v249 offset:34304\n"
"v_cvt_pk_bf16_f32 v210, v196, v197\n"
"v_mfma_f32_16x16x32_bf16 a[200:203], a[168:171], v[212:215], a[200:203]\n"
"ds_read_b64_tr_b16 a[162:163], v249 offset:38528\n"
"ds_read_b64_tr_b16 a[164:165], v249 offset:34336\n"
"v_mfma_f32_16x16x32_bf16 a[204:207], a[172:175], v[212:215], a[204:207]\n"
"ds_read_b64_tr_b16 a[166:167], v249 offset:38560\n"
"v_cvt_pk_bf16_f32 v211, v198, v199\n"
"v_mfma_f32_16x16x32_bf16 a[232:235], a[236:239], v[212:215], a[232:235]\n"
"ds_read_b64_tr_b16 a[168:169], v249 offset:34368\n"
"ds_read_b64_tr_b16 a[170:171], v249 offset:38592\n"
"v_mfma_f32_16x16x32_bf16 v[192:195], a[112:115], a[32:35], v[224:227]\n"
"ds_read_b64_tr_b16 a[172:173], v249 offset:34400\n"
"ds_read_b64_tr_b16 a[174:175], v249 offset:38624\n"
"v_mfma_f32_16x16x32_bf16 v[192:195], a[116:119], a[36:39], v[192:195]\n"
"v_exp_f32 v200, v200\n"
"v_mfma_f32_16x16x32_bf16 v[192:195], a[120:123], a[40:43], v[192:195]\n"
"s_memrealtime s[28:29]\n"
"v_exp_f32 v201, v201\n"
"v_mfma_f32_16x16x32_bf16 v[192:195], a[124:127], a[44:47], v[192:195]\n"
"v_exp_f32 v202, v202\n"
"v_mfma_f32_16x16x32_bf16 v[196:199], a[128:131], a[32:35], v[224:227]\n"
"v_exp_f32 v203, v203\n"
"v_mfma_f32_16x16x32_bf16 v[196:199], a[132:135], a[36:39], v[196:199]\n"
"v_exp_f32 v204, v204\n"
"v_mfma_f32_16x16x32_bf16 v[196:199], a[136:139], a[40:43], v[196:199]\n"
"v_exp_f32 v205, v205\n"
"v_mfma_f32_16x16x32_bf16 v[196:199], a[140:143], a[44:47], v[196:199]\n"
"s_waitcnt lgkmcnt(14)\n"
"v_exp_f32 v206, v206\n"
"v_mfma_f32_16x16x32_bf16 v[0:3], a[144:147], v[208:211], v[0:3]\n"
"s_waitcnt lgkmcnt(12)\n"
"v_exp_f32 v207, v207\n"
"v_mfma_f32_16x16x32_bf16 v[4:7], a[148:151], v[208:211], v[4:7]\n"
"s_waitcnt lgkmcnt(10)\n"
"v_cvt_pk_bf16_f32 v212, v200, v201\n"
"v_cvt_pk_bf16_f32 v213, v202, v203\n"
"v_mfma_f32_16x16x32_bf16 v[8:11], a[152:155], v[208:211], v[8:11]\n"
"s_waitcnt lgkmcnt(8)\n"
"v_cvt_pk_bf16_f32 v214, v204, v205\n"
"v_cvt_pk_bf16_f32 v215, v206, v207\n"
"v_mfma_f32_16x16x32_bf16 v[12:15], a[156:159], v[208:211], v[12:15]\n"
"s_waitcnt lgkmcnt(6)\n"
"v_mfma_f32_16x16x32_bf16 v[16:19], a[160:163], v[208:211], v[16:19]\n"
"s_waitcnt lgkmcnt(4)\n"
"v_mfma_f32_16x16x32_bf16 v[20:23], a[164:167], v[208:211], v[20:23]\n"
"s_waitcnt lgkmcnt(2)\n"
"v_mfma_f32_16x16x32_bf16 v[24:27], a[168:171], v[208:211], v[24:27]\n"
"s_waitcnt lgkmcnt(0)\n"
"v_mfma_f32_16x16x32_bf16 v[28:31], a[172:175], v[208:211], v[28:31]\n"
"v_mfma_f32_16x16x32_bf16 a[208:211], a[236:239], v[208:211], a[208:211]\n"
"v_mfma_f32_16x16x32_bf16 v[200:203], a[112:115], a[48:51], v[228:231]\n"
"v_exp_f32 v192, v192\n"
"v_mfma_f32_16x16x32_bf16 v[200:203], a[116:119], a[52:55], v[200:203]\n"
"v_exp_f32 v193, v193\n"
"v_mfma_f32_16x16x32_bf16 v[200:203], a[120:123], a[56:59], v[200:203]\n"
"v_exp_f32 v194, v194\n"
"v_mfma_f32_16x16x32_bf16 v[200:203], a[124:127], a[60:63], v[200:203]\n"
"v_exp_f32 v195, v195\n"
"v_mfma_f32_16x16x32_bf16 v[204:207], a[128:131], a[48:51], v[228:231]\n"
"v_exp_f32 v196, v196\n"
"v_mfma_f32_16x16x32_bf16 v[204:207], a[132:135], a[52:55], v[204:207]\n"
"v_exp_f32 v197, v197\n"
"v_mfma_f32_16x16x32_bf16 v[204:207], a[136:139], a[56:59], v[204:207]\n"
"v_exp_f32 v198, v198\n"
"v_mfma_f32_16x16x32_bf16 v[204:207], a[140:143], a[60:63], v[204:207]\n"
"v_exp_f32 v199, v199\n"
"v_mfma_f32_16x16x32_bf16 v[32:35], a[144:147], v[212:215], v[32:35]\n"
"v_mfma_f32_16x16x32_bf16 v[36:39], a[148:151], v[212:215], v[36:39]\n"
"v_cvt_pk_bf16_f32 v208, v192, v193\n"
"v_cvt_pk_bf16_f32 v209, v194, v195\n"
"v_mfma_f32_16x16x32_bf16 v[40:43], a[152:155], v[212:215], v[40:43]\n"
"v_cvt_pk_bf16_f32 v210, v196, v197\n"
"v_cvt_pk_bf16_f32 v211, v198, v199\n"
"v_mfma_f32_16x16x32_bf16 v[44:47], a[156:159], v[212:215], v[44:47]\n"
"s_lshl_b32 s33, s28, 3\n"
"v_mfma_f32_16x16x32_bf16 v[48:51], a[160:163], v[212:215], v[48:51]\n"
"s_sub_i32 s32, s30, s33\n"
"v_mfma_f32_16x16x32_bf16 v[52:55], a[164:167], v[212:215], v[52:55]\n"
"s_max_i32 s30, s32, -400\n"
"v_mfma_f32_16x16x32_bf16 v[56:59], a[168:171], v[212:215], v[56:59]\n"
"s_add_i32 s30, s30, s33\n"
"v_mfma_f32_16x16x32_bf16 v[60:63], a[172:175], v[212:215], v[60:63]\n"
"s_add_i32 s30, s30, s66\n"
"v_mfma_f32_16x16x32_bf16 a[212:215], a[236:239], v[212:215], a[212:215]\n"
"v_mfma_f32_16x16x32_bf16 v[192:195], a[112:115], a[64:67], v[232:235]\n"
"v_exp_f32 v200, v200\n"
"v_mfma_f32_16x16x32_bf16 v[192:195], a[116:119], a[68:71], v[192:195]\n"
"v_exp_f32 v201, v201\n"
"v_mfma_f32_16x16x32_bf16 v[192:195], a[120:123], a[72:75], v[192:195]\n"
"v_exp_f32 v202, v202\n"
"v_mfma_f32_16x16x32_bf16 v[192:195], a[124:127], a[76:79], v[192:195]\n"
"v_exp_f32 v203, v203\n"
"v_mfma_f32_16x16x32_bf16 v[196:199], a[128:131], a[64:67], v[232:235]\n"
"v_exp_f32 v204, v204\n"
"v_mfma_f32_16x16x32_bf16 v[196:199], a[132:135], a[68:71], v[196:199]\n"
"v_exp_f32 v205, v205\n"
"v_mfma_f32_16x16x32_bf16 v[196:199], a[136:139], a[72:75], v[196:199]\n"
"v_exp_f32 v206, v206\n"
"v_mfma_f32_16x16x32_bf16 v[196:199], a[140:143], a[76:79], v[196:199]\n"
"v_exp_f32 v207, v207\n"
"v_mfma_f32_16x16x32_bf16 v[64:67], a[144:147], v[208:211], v[64:67]\n"
"v_mfma_f32_16x16x32_bf16 v[68:71], a[148:151], v[208:211], v[68:71]\n"
"v_cvt_pk_bf16_f32 v212, v200, v201\n"
"v_cvt_pk_bf16_f32 v213, v202, v203\n"
"v_mfma_f32_16x16x32_bf16 v[72:75], a[152:155], v[208:211], v[72:75]\n"
"v_cvt_pk_bf16_f32 v214, v204, v205\n"
"v_cvt_pk_bf16_f32 v215, v206, v207\n"
"v_mfma_f32_16x16x32_bf16 v[76:79], a[156:159], v[208:211], v[76:79]\n"
"s_sub_i32 s66, s66, 15\n"
"v_mfma_f32_16x16x32_bf16 v[80:83], a[160:163], v[208:211], v[80:83]\n"
"s_max_i32 s32, s32, 0\n"
"v_mfma_f32_16x16x32_bf16 v[84:87], a[164:167], v[208:211], v[84:87]\n"
"s_mul_i32 s32, s32, 13\n"
"v_mfma_f32_16x16x32_bf16 v[88:91], a[168:171], v[208:211], v[88:91]\n"
"s_lshr_b32 s32, s32, 8\n"
"v_mfma_f32_16x16x32_bf16 v[92:95], a[172:175], v[208:211], v[92:95]\n"
"s_min_u32 s31, s32, 24\n"
"v_mfma_f32_16x16x32_bf16 a[216:219], a[236:239], v[208:211], a[216:219]\n"
"1:\n"
"s_sub_u32 s31, s31, 1\n"
"s_cbranch_scc1 2f\n"
"s_nop 7\n"
"s_branch 1b\n"
"2:\n"
"s_waitcnt vmcnt(16)\n"
"s_barrier\n"
"v_mfma_f32_16x16x32_bf16 v[200:203], a[112:115], a[80:83], v[236:239]\n"
"v_exp_f32 v192, v192\n"
"v_mfma_f32_16x16x32_bf16 v[200:203], a[116:119], a[84:87], v[200:203]\n"
"ds_read_b128 a[112:115], v244\n"
"ds_read_b128 a[116:119], v244 offset:64\n"
"v_mfma_f32_16x16x32_bf16 v[200:203], a[120:123], a[88:91], v[200:203]\n"
"v_exp_f32 v193, v193\n"
"v_mfma_f32_16x16x32_bf16 v[200:203], a[124:127], a[92:95], v[200:203]\n"
"ds_read_b128 a[120:123], v244 offset:128\n"
"ds_read_b128 a[124:127], v244 offset:192\n"
"v_mfma_f32_16x16x32_bf16 v[204:207], a[128:131], a[80:83], v[236:239]\n"
"v_exp_f32 v194, v194\n"
"v_mfma_f32_16x16x32_bf16 v[204:207], a[132:135], a[84:87], v[204:207]\n"
"ds_read_b128 a[128:131], v244 offset:4352\n"
"ds_read_b128 a[132:135], v244 offset:4416\n"
"v_mfma_f32_16x16x32_bf16 v[204:207], a[136:139], a[88:91], v[204:207]\n"
"v_exp_f32 v195, v195\n"
"v_mfma_f32_16x16x32_bf16 v[204:207], a[140:143], a[92:95], v[204:207]\n"
"ds_read_b128 a[136:139], v244 offset:4480\n"
"ds_read_b128 a[140:143], v244 offset:4544\n"
"v_mfma_f32_16x16x32_bf16 v[96:99], a[144:147], v[212:215], v[96:99]\n"
"v_exp_f32 v196, v196\n"
"v_mfma_f32_16x16x32_bf16 v[100:103], a[148:151], v[212:215], v[100:103]\n"
"v_exp_f32 v197, v197\n"
"v_mfma_f32_16x16x32_bf16 v[104:107], a[152:155], v[212:215], v[104:107]\n"
"v_exp_f32 v198, v198\n"
"v_mfma_f32_16x16x32_bf16 v[108:111], a[156:159], v[212:215], v[108:111]\n"
"v_exp_f32 v199, v199\n"
"v_mfma_f32_16x16x32_bf16 v[112:115], a[160:163], v[212:215], v[112:115]\n"
"v_cvt_pk_bf16_f32 v208, v192, v193\n"
"v_cvt_pk_bf16_f32 v209, v194, v195\n"
"v_mfma_f32_16x16x32_bf16 v[116:119], a[164:167], v[212:215], v[116:119]\n"
"v_cvt_pk_bf16_f32 v210, v196, v197\n"
"v_cvt_pk_bf16_f32 v211, v198, v199\n"
"v_mfma_f32_16x16x32_bf16 v[120:123], a[168:171], v[212:215], v[120:123]\n"
"v_mfma_f32_16x16x32_bf16 v[124:127], a[172:175], v[212:215], v[124:127]\n"
"v_mfma_f32_16x16x32_bf16 a[220:223], a[236:239], v[212:215], a[220:223]\n"
"s_waitcnt lgkmcnt(7)\n"
"v_mfma_f32_16x16x32_bf16 v[192:195], a[112:115], a[0:3], v[216:219]\n"
"s_waitcnt lgkmcnt(6)\n"
"v_exp_f32 v200, v200\n"
"v_mfma_f32_16x16x32_bf16 v[192:195], a[116:119], a[4:7], v[192:195]\n"
"s_waitcnt lgkmcnt(5)\n"
"v_exp_f32 v201, v201\n"
"v_mfma_f32_16x16x32_bf16 v[192:195], a[120:123], a[8:11], v[192:195]\n"
"s_waitcnt lgkmcnt(4)\n"
"v_exp_f32 v202, v202\n"
"v_mfma_f32_16x16x32_bf16 v[192:195], a[124:127], a[12:15], v[192:195]\n"
"s_waitcnt lgkmcnt(3)\n"
"v_exp_f32 v203, v203\n"
"v_mfma_f32_16x16x32_bf16 v[196:199], a[128:131], a[0:3], v[216:219]\n"
"s_waitcnt lgkmcnt(2)\n"
"v_exp_f32 v204, v204\n"
"v_mfma_f32_16x16x32_bf16 v[196:199], a[132:135], a[4:7], v[196:199]\n"
"s_waitcnt lgkmcnt(1)\n"
"v_exp_f32 v205, v205\n"
"v_mfma_f32_16x16x32_bf16 v[196:199], a[136:139], a[8:11], v[196:199]\n"
"s_waitcnt lgkmcnt(0)\n"
"v_exp_f32 v206, v206\n"
"v_mfma_f32_16x16x32_bf16 v[196:199], a[140:143], a[12:15], v[196:199]\n"
"v_exp_f32 v207, v207\n"
"v_mfma_f32_16x16x32_bf16 v[128:131], a[144:147], v[208:211], v[128:131]\n"
"v_mfma_f32_16x16x32_bf16 v[132:135], a[148:151], v[208:211], v[132:135]\n"
"s_add_u32 m0, s50, 102912\n"
"v_cvt_pk_bf16_f32 v212, v200, v201\n"
"v_mfma_f32_16x16x32_bf16 v[136:139], a[152:155], v[208:211], v[136:139]\n"
"buffer_load_dwordx4 v250, s[36:39], s53 offen nt lds\n"
"v_mfma_f32_16x16x32_bf16 v[140:143], a[156:159], v[208:211], v[140:143]\n"
"s_add_u32 m0, s50, 102976\n"
"v_cvt_pk_bf16_f32 v213, v202, v203\n"
"v_mfma_f32_16x16x32_bf16 v[144:147], a[160:163], v[208:211], v[144:147]\n"
"buffer_load_dwordx4 v250, s[36:39], s53 offen offset:1024 nt lds\n"
"v_mfma_f32_16x16x32_bf16 v[148:151], a[164:167], v[208:211], v[148:151]\n"
"s_add_u32 m0, s50, 103040\n"
"v_cvt_pk_bf16_f32 v214, v204, v205\n"
"v_mfma_f32_16x16x32_bf16 v[152:155], a[168:171], v[208:211], v[152:155]\n"
"buffer_load_dwordx4 v250, s[36:39], s53 offen offset:2048 nt lds\n"
"v_mfma_f32_16x16x32_bf16 v[156:159], a[172:175], v[208:211], v[156:159]\n"
"s_add_u32 m0, s50, 103104\n"
"v_cvt_pk_bf16_f32 v215, v206, v207\n"
"v_mfma_f32_16x16x32_bf16 a[224:227], a[236:239], v[208:211], a[224:227]\n"
"s_sub_u32 s55, s55, 1\n"
"buffer_load_dwordx4 v250, s[36:39], s53 offen offset:3072 nt lds\n"
"s_cbranch_scc0 L1_loop_h0\n"
"s_nop 1\n"
"v_mfma_f32_16x16x32_bf16 v[160:163], a[144:147], v[212:215], v[160:163]\n"
"v_mfma_f32_16x16x32_bf16 v[164:167], a[148:151], v[212:215], v[164:167]\n"
"v_mfma_f32_16x16x32_bf16 v[168:171], a[152:155], v[212:215], v[168:171]\n"
"v_mfma_f32_16x16x32_bf16 v[172:175], a[156:159], v[212:215], v[172:175]\n"
"v_mfma_f32_16x16x32_bf16 v[176:179], a[160:163], v[212:215], v[176:179]\n"
"v_mfma_f32_16x16x32_bf16 v[180:183], a[164:167], v[212:215], v[180:183]\n"
"v_mfma_f32_16x16x32_bf16 v[184:187], a[168:171], v[212:215], v[184:187]\n"
"v_mfma_f32_16x16x32_bf16 v[188:191], a[172:175], v[212:215], v[188:191]\n"
"v_mfma_f32_16x16x32_bf16 a[228:231], a[236:239], v[212:215], a[228:231]\n"
"s_waitcnt lgkmcnt(0)\n"
"s_and_b32 s12, s24, 7\n"
"s_lshr_b32 s13, s24, 3\n"
"s_mul_i32 s48, s21, 34816\n"
"s_lshl_b32 s14, s21, 17\n"
"s_lshl_b32 s15, s12, 8\n"
"s_add_u32 s14, s14, s15\n"
"s_lshl_b32 s15, s13, 22\n"
"s_add_u32 s14, s14, s15\n"
"s_add_u32 s32, s16, s14\n"
"s_addc_u32 s33, s17, 0\n"
"s_and_b32 s33, s33, 65535\n"
"s_sub_u32 s34, 8388608, s14\n"
"s_mov_b32 s35, 131072\n"
"s_lshl_b32 s49, s12, 19\n"
"v_lshrrev_b32 v208, 4, v251\n"
"v_lshrrev_b32 v209, 4, v208\n"
"v_and_b32 v210, 15, v208\n"
"v_and_b32 v197, 1, v209\n"
"s_lshl_b32 s15, s13, 7\n"
"v_xor_b32 v198, v210, v197\n"
"v_lshlrev_b32 v198, 4, v198\n"
"v_xor_b32 v198, s15, v198\n"
"v_lshl_add_u32 v228, v209, 13, v198\n"
"v_add_u32 v229, 2048, v228\n"
"v_add_u32 v230, 4096, v228\n"
"v_add_u32 v231, 6144, v228\n"
"v_add_u32 v232, 32768, v228\n"
"v_add_u32 v233, 34816, v228\n"
"v_add_u32 v234, 36864, v228\n"
"v_add_u32 v235, 38912, v228\n"
"s_mul_i32 s25, s21, 4352\n"
"s_add_u32 s25, s25, 139264\n"
"v_add_u32 v252, s25, v251\n"
"s_xor_b32 s26, s21, 1\n"
"s_mul_i32 s26, s26, 4352\n"
"s_add_u32 s26, s26, 139264\n"
"v_add_u32 v253, s26, v251\n"
"v_lshrrev_b32 v204, 2, v251\n"
"v_add_u32 v205, s26, v204\n"
"v_add_u32 v204, s25, v204\n"
"v_mov_b32 v200, 2139095039\n"
"v_mov_b32 v201, 0\n"
"s_nop 3\n"
"ds_write_b128 v252, a[192:195]\n"
"ds_write_b128 v252, a[196:199] offset:1024\n"
"ds_write_b128 v252, a[200:203] offset:2048\n"
"ds_write_b128 v252, a[204:207] offset:3072\n"
"ds_write_b32 v204, a232 offset:4096\n"
"s_waitcnt vmcnt(0)\n"
"v_accvgpr_read_b32 v206, a208\n"
"v_min_f32 v200, v200, v206\n"
"v_max_f32 v201, v201, v206\n"
"v_rcp_f32 v202, v206\n"
"s_nop 0\n"
"v_pk_mul_f32 v[0:1], v[0:1], v[202:203] op_sel_hi:[1,0]\n"
"v_pk_mul_f32 v[2:3], v[2:3], v[202:203] op_sel_hi:[1,0]\n"
"v_pk_mul_f32 v[4:5], v[4:5], v[202:203] op_sel_hi:[1,0]\n"
"v_pk_mul_f32 v[6:7], v[6:7], v[202:203] op_sel_hi:[1,0]\n"
"v_cvt_pk_bf16_f32 v0, v0, v1\n"
"v_cvt_pk_bf16_f32 v1, v2, v3\n"
"v_cvt_pk_bf16_f32 v2, v4, v5\n"
"v_cvt_pk_bf16_f32 v3, v6, v7\n"
"s_mov_b32 s25, 0\n"
"buffer_store_dwordx4 v[0:3], v251, s[40:43], s25 offen sc1\n"
"v_mfma_f32_16x16x32_bf16 v[192:195], v[216:219], v[0:3], 0\n"
"v_pk_mul_f32 v[8:9], v[8:9], v[202:203] op_sel_hi:[1,0]\n"
"v_pk_mul_f32 v[10:11], v[10:11], v[202:203] op_sel_hi:[1,0]\n"
"v_pk_mul_f32 v[12:13], v[12:13], v[202:203] op_sel_hi:[1,0]\n"
"v_pk_mul_f32 v[14:15], v[14:15], v[202:203] op_sel_hi:[1,0]\n"
"v_cvt_pk_bf16_f32 v8, v8, v9\n"
"v_cvt_pk_bf16_f32 v9, v10, v11\n"
"v_cvt_pk_bf16_f32 v10, v12, v13\n"
"v_cvt_pk_bf16_f32 v11, v14, v15\n"
"s_mov_b32 s25, 13312\n"
"buffer_store_dwordx4 v[8:11], v251, s[40:43], s25 offen sc1\n"
"v_mfma_f32_16x16x32_bf16 v[192:195], v[216:219], v[8:11], v[192:195]\n"
"v_pk_mul_f32 v[16:17], v[16:17], v[202:203] op_sel_hi:[1,0]\n"
"v_pk_mul_f32 v[18:19], v[18:19], v[202:203] op_sel_hi:[1,0]\n"
"v_pk_mul_f32 v[20:21], v[20:21], v[202:203] op_sel_hi:[1,0]\n"
"v_pk_mul_f32 v[22:23], v[22:23], v[202:203] op_sel_hi:[1,0]\n"
"v_cvt_pk_bf16_f32 v16, v16, v17\n"
"v_cvt_pk_bf16_f32 v17, v18, v19\n"
"v_cvt_pk_bf16_f32 v18, v20, v21\n"
"v_cvt_pk_bf16_f32 v19, v22, v23\n"
"s_mov_b32 s25, 26624\n"
"buffer_store_dwordx4 v[16:19], v251, s[40:43], s25 offen sc1\n"
"v_mfma_f32_16x16x32_bf16 v[192:195], v[216:219], v[16:19], v[192:195]\n"
"v_pk_mul_f32 v[24:25], v[24:25], v[202:203] op_sel_hi:[1,0]\n"
"v_pk_mul_f32 v[26:27], v[26:27], v[202:203] op_sel_hi:[1,0]\n"
"v_pk_mul_f32 v[28:29], v[28:29], v[202:203] op_sel_hi:[1,0]\n"
"v_pk_mul_f32 v[30:31], v[30:31], v[202:203] op_sel_hi:[1,0]\n"
"v_cvt_pk_bf16_f32 v24, v24, v25\n"
"v_cvt_pk_bf16_f32 v25, v26, v27\n"
"v_cvt_pk_bf16_f32 v26, v28, v29\n"
"v_cvt_pk_bf16_f32 v27, v30, v31\n"
"s_mov_b32 s25, 39936\n"
"buffer_store_dwordx4 v[24:27], v251, s[40:43], s25 offen sc1\n"
"v_mfma_f32_16x16x32_bf16 v[192:195], v[216:219], v[24:27], v[192:195]\n"
"v_accvgpr_read_b32 v206, a212\n"
"v_min_f32 v200, v200, v206\n"
"v_max_f32 v201, v201, v206\n"
"v_rcp_f32 v202, v206\n"
"s_nop 0\n"
"v_pk_mul_f32 v[32:33], v[32:33], v[202:203] op_sel_hi:[1,0]\n"
"v_pk_mul_f32 v[34:35], v[34:35], v[202:203] op_sel_hi:[1,0]\n"
"v_pk_mul_f32 v[36:37], v[36:37], v[202:203] op_sel_hi:[1,0]\n"
"v_pk_mul_f32 v[38:39], v[38:39], v[202:203] op_sel_hi:[1,0]\n"
"v_cvt_pk_bf16_f32 v32, v32, v33\n"
"v_cvt_pk_bf16_f32 v33, v34, v35\n"
"v_cvt_pk_bf16_f32 v34, v36, v37\n"
"v_cvt_pk_bf16_f32 v35, v38, v39\n"
"s_mov_b32 s25, 1024\n"
"buffer_store_dwordx4 v[32:35], v251, s[40:43], s25 offen sc1\n"
"v_mfma_f32_16x16x32_bf16 v[192:195], v[216:219], v[32:35], v[192:195]\n"
"v_pk_mul_f32 v[40:41], v[40:41], v[202:203] op_sel_hi:[1,0]\n"
"v_pk_mul_f32 v[42:43], v[42:43], v[202:203] op_sel_hi:[1,0]\n"
"v_pk_mul_f32 v[44:45], v[44:45], v[202:203] op_sel_hi:[1,0]\n"
"v_pk_mul_f32 v[46:47], v[46:47], v[202:203] op_sel_hi:[1,0]\n"
"v_cvt_pk_bf16_f32 v40, v40, v41\n"
"v_cvt_pk_bf16_f32 v41, v42, v43\n"
"v_cvt_pk_bf16_f32 v42, v44, v45\n"
"v_cvt_pk_bf16_f32 v43, v46, v47\n"
"s_mov_b32 s25, 14336\n"
"buffer_store_dwordx4 v[40:43], v251, s[40:43], s25 offen sc1\n"
"v_mfma_f32_16x16x32_bf16 v[192:195], v[216:219], v[40:43], v[192:195]\n"
"v_pk_mul_f32 v[48:49], v[48:49], v[202:203] op_sel_hi:[1,0]\n"
"v_pk_mul_f32 v[50:51], v[50:51], v[202:203] op_sel_hi:[1,0]\n"
"v_pk_mul_f32 v[52:53], v[52:53], v[202:203] op_sel_hi:[1,0]\n"
"v_pk_mul_f32 v[54:55], v[54:55], v[202:203] op_sel_hi:[1,0]\n"
"v_cvt_pk_bf16_f32 v48, v48, v49\n"
"v_cvt_pk_bf16_f32 v49, v50, v51\n"
"v_cvt_pk_bf16_f32 v50, v52, v53\n"
"v_cvt_pk_bf16_f32 v51, v54, v55\n"
"s_mov_b32 s25, 27648\n"
"buffer_store_dwordx4 v[48:51], v251, s[40:43], s25 offen sc1\n"
"v_mfma_f32_16x16x32_bf16 v[192:195], v[216:219], v[48:51], v[192:195]\n"
"v_pk_mul_f32 v[56:57], v[56:57], v[202:203] op_sel_hi:[1,0]\n"
"v_pk_mul_f32 v[58:59], v[58:59], v[202:203] op_sel_hi:[1,0]\n"
"v_pk_mul_f32 v[60:61], v[60:61], v[202:203] op_sel_hi:[1,0]\n"
"v_pk_mul_f32 v[62:63], v[62:63], v[202:203] op_sel_hi:[1,0]\n"
"v_cvt_pk_bf16_f32 v56, v56, v57\n"
"v_cvt_pk_bf16_f32 v57, v58, v59\n"
"v_cvt_pk_bf16_f32 v58, v60, v61\n"
"v_cvt_pk_bf16_f32 v59, v62, v63\n"
"s_mov_b32 s25, 40960\n"
"buffer_store_dwordx4 v[56:59], v251, s[40:43], s25 offen sc1\n"
"v_mfma_f32_16x16x32_bf16 v[192:195], v[216:219], v[56:59], v[192:195]\n"
"v_lshrrev_b32 v211, 2, v210\n"
"v_and_b32 v196, 3, v208\n"
"v_xor_b32 v198, v196, v197\n"
"v_lshlrev_b32 v198, 4, v198\n"
"v_lshl_add_u32 v198, v209, 8, v198\n"
"v_mul_u32_u24 v199, 1088, v211\n"
"v_add3_u32 v198, v198, v199, s48\n"
"v_and_b32 v199, 1, v196\n"
"v_lshl_add_u32 v224, v199, 3, v198\n"
"v_xor_b32 v199, 1, v199\n"
"v_lshl_add_u32 v225, v199, 3, v198\n"
"v_add_u32 v226, 34816, v224\n"
"v_add_u32 v227, 34816, v225\n"
"s_mul_i32 s14, s22, 3072\n"
"s_sub_u32 s15, 3072, s14\n"
"s_add_u32 s14, s14, 0\n"
"s_add_u32 s15, s15, 0\n"
"v_add_u32 v236, s14, v251\n"
"v_add_u32 v237, s15, v251\n"
"v_add_u32 v238, 12288, v251\n"
"v_add_u32 v239, 26624, v236\n"
"v_add_u32 v240, 26624, v237\n"
"s_lshl_b32 s14, s23, 1\n"
"s_add_u32 s14, s14, s13\n"
"s_lshl_b32 s15, s14, 4\n"
"s_add_u32 s15, s15, s22\n"
"s_mul_i32 s15, s15, 53248\n"
"s_add_u32 s28, s18, s15\n"
"s_addc_u32 s29, s19, 0\n"
"s_and_b32 s29, s29, 65535\n"
"s_mov_b32 s30, 1650688\n"
"s_mov_b32 s31, 131072\n"
"s_lshl_b32 s14, s14, 11\n"
"s_lshl_b32 s15, s21, 5\n"
"s_add_u32 s14, s14, s15\n"
"v_and_b32 v198, 7, v208\n"
"v_lshlrev_b32 v198, 8, v198\n"
"v_add_u32 v241, s14, v198\n"
"s_cmp_eq_u32 s12, 0\n"
"s_cselect_b32 s59, 1, 0\n"
"s_mov_b32 s66, 0\n"
"s_add_u32 s70, s48, 17408\n"
"s_mov_b32 s72, 17408\n"
"s_waitcnt lgkmcnt(0)\n"
"s_barrier\n"
"ds_read_b32 v207, v205 offset:4096\n"
"ds_read_b128 v[196:199], v253\n"
"ds_read_b128 v[220:223], v253 offset:1024\n"
"s_add_u32 m0, s48, 0\n"
"v_accvgpr_read_b32 v206, a232\n"
"buffer_load_dwordx4 v228, s[32:35], s49 offen lds\n"
"v_accvgpr_read_b32 v208, a176\n"
"v_accvgpr_read_b32 v209, a177\n"
"v_accvgpr_read_b32 v210, a178\n"
"s_add_u32 m0, s48, 1088\n"
"v_accvgpr_read_b32 v211, a179\n"
"buffer_load_dwordx4 v229, s[32:35], s49 offen lds\n"
"v_accvgpr_read_b32 v212, a180\n"
"v_accvgpr_read_b32 v213, a181\n"
"v_accvgpr_read_b32 v214, a182\n"
"s_add_u32 m0, s48, 2176\n"
"v_accvgpr_read_b32 v215, a183\n"
"buffer_load_dwordx4 v230, s[32:35], s49 offen lds\n"
"s_add_u32 m0, s48, 3264\n"
"s_nop 0\n"
"buffer_load_dwordx4 v231, s[32:35], s49 offen lds\n"
"s_add_u32 m0, s48, 4352\n"
"s_nop 0\n"
"buffer_load_dwordx4 v232, s[32:35], s49 offen lds\n"
"s_add_u32 m0, s48, 5440\n"
"s_nop 0\n"
"buffer_load_dwordx4 v233, s[32:35], s49 offen lds\n"
"s_add_u32 m0, s48, 6528\n"
"s_nop 0\n"
"buffer_load_dwordx4 v234, s[32:35], s49 offen lds\n"
"s_waitcnt lgkmcnt(2)\n"
"v_add_f32 v206, v206, v207\n"
"s_waitcnt lgkmcnt(1)\n"
"v_pk_add_f32 v[208:209], v[208:209], v[196:197]\n"
"v_pk_add_f32 v[210:211], v[210:211], v[198:199]\n"
"s_add_u32 m0, s48, 7616\n"
"s_waitcnt lgkmcnt(0)\n"
"v_pk_add_f32 v[212:213], v[212:213], v[220:221]\n"
"buffer_load_dwordx4 v235, s[32:35], s49 offen lds\n"
"s_add_u32 s49, s49, 65536\n"
"v_pk_add_f32 v[214:215], v[214:215], v[222:223]\n"
"ds_read_b128 v[196:199], v253 offset:2048\n"
"ds_read_b128 v[220:223], v253 offset:3072\n"
"s_add_u32 m0, s48, 8704\n"
"v_min_f32 v200, v200, v206\n"
"buffer_load_dwordx4 v228, s[32:35], s49 offen lds\n"
"v_max_f32 v201, v201, v206\n"
"v_rcp_f32 v202, v206\n"
"s_nop 0\n"
"s_add_u32 m0, s48, 9792\n"
"v_pk_mul_f32 v[208:209], v[208:209], v[202:203] op_sel_hi:[1,0]\n"
"buffer_load_dwordx4 v229, s[32:35], s49 offen lds\n"
"v_pk_mul_f32 v[210:211], v[210:211], v[202:203] op_sel_hi:[1,0]\n"
"v_pk_mul_f32 v[212:213], v[212:213], v[202:203] op_sel_hi:[1,0]\n"
"v_pk_mul_f32 v[214:215], v[214:215], v[202:203] op_sel_hi:[1,0]\n"
"s_add_u32 m0, s48, 10880\n"
"v_cvt_pk_bf16_f32 v208, v208, v209\n"
"buffer_load_dwordx4 v230, s[32:35], s49 offen lds\n"
"v_cvt_pk_bf16_f32 v209, v210, v211\n"
"v_cvt_pk_bf16_f32 v210, v212, v213\n"
"v_cvt_pk_bf16_f32 v211, v214, v215\n"
"s_add_u32 m0, s48, 11968\n"
"s_mov_b32 s25, 12288\n"
"buffer_load_dwordx4 v231, s[32:35], s49 offen lds\n"
"buffer_store_dwordx4 v[208:211], v251, s[40:43], s25 offen sc1\n"
"v_mfma_f32_16x16x32_bf16 v[192:195], v[216:219], v[208:211], v[192:195]\n"
"s_waitcnt lgkmcnt(0)\n"
"s_waitcnt lgkmcnt(1)\n"
"v_accvgpr_write_b32 a136, v208\n"
"s_add_u32 m0, s48, 13056\n"
"v_accvgpr_write_b32 a137, v209\n"
"buffer_load_dwordx4 v232, s[32:35], s49 offen lds\n"
"v_accvgpr_write_b32 a138, v210\n"
"v_accvgpr_write_b32 a139, v211\n"
"v_accvgpr_read_b32 v208, a184\n"
"s_add_u32 m0, s48, 14144\n"
"v_accvgpr_read_b32 v209, a185\n"
"buffer_load_dwordx4 v233, s[32:35], s49 offen lds\n"
"v_accvgpr_read_b32 v210, a186\n"
"v_accvgpr_read_b32 v211, a187\n"
"v_accvgpr_read_b32 v212, a188\n"
"s_add_u32 m0, s48, 15232\n"
"v_accvgpr_read_b32 v213, a189\n"
"buffer_load_dwordx4 v234, s[32:35], s49 offen lds\n"
"v_accvgpr_read_b32 v214, a190\n"
"v_accvgpr_read_b32 v215, a191\n"
"v_pk_add_f32 v[208:209], v[208:209], v[196:197]\n"
"s_add_u32 m0, s48, 16320\n"
"v_pk_add_f32 v[210:211], v[210:211], v[198:199]\n"
"buffer_load_dwordx4 v235, s[32:35], s49 offen lds\n"
"s_lshl_b32 s49, s59, 19\n"
"v_pk_add_f32 v[212:213], v[212:213], v[220:221]\n"
"v_pk_add_f32 v[214:215], v[214:215], v[222:223]\n"
"v_pk_mul_f32 v[208:209], v[208:209], v[202:203] op_sel_hi:[1,0]\n"
"v_pk_mul_f32 v[210:211], v[210:211], v[202:203] op_sel_hi:[1,0]\n"
"v_pk_mul_f32 v[212:213], v[212:213], v[202:203] op_sel_hi:[1,0]\n"
"v_pk_mul_f32 v[214:215], v[214:215], v[202:203] op_sel_hi:[1,0]\n"
"v_cvt_pk_bf16_f32 v208, v208, v209\n"
"v_cvt_pk_bf16_f32 v209, v210, v211\n"
"v_cvt_pk_bf16_f32 v210, v212, v213\n"
"v_cvt_pk_bf16_f32 v211, v214, v215\n"
"s_mov_b32 s25, 25600\n"
"buffer_store_dwordx4 v[208:211], v251, s[40:43], s25 offen sc1\n"
"v_mfma_f32_16x16x32_bf16 v[192:195], v[216:219], v[208:211], v[192:195]\n"
"v_accvgpr_write_b32 a164, v208\n"
"v_accvgpr_write_b32 a165, v209\n"
"v_accvgpr_write_b32 a166, v210\n"
"v_accvgpr_write_b32 a167, v211\n"
"s_cmp_eq_u32 s22, 0\n"
"s_cbranch_scc1 L1_ko0_h0\n"
"v_accvgpr_write_b32 a124, v0\n"
"v_accvgpr_write_b32 a125, v1\n"
"v_accvgpr_write_b32 a126, v2\n"
"v_accvgpr_write_b32 a127, v3\n"
"v_accvgpr_write_b32 a152, v8\n"
"v_accvgpr_write_b32 a153, v9\n"
"v_accvgpr_write_b32 a154, v10\n"
"v_accvgpr_write_b32 a155, v11\n"
"v_accvgpr_write_b32 a180, v16\n"
"v_accvgpr_write_b32 a181, v17\n"
"v_accvgpr_write_b32 a182, v18\n"
"v_accvgpr_write_b32 a183, v19\n"
"v_accvgpr_write_b32 a208, v24\n"
"v_accvgpr_write_b32 a209, v25\n"
"v_accvgpr_write_b32 a210, v26\n"
"v_accvgpr_write_b32 a211, v27\n"
"s_branch L1_ko0_h0e\n"
"L1_ko0_h0:\n"
"v_accvgpr_write_b32 a112, v0\n"
"v_accvgpr_write_b32 a113, v1\n"
"v_accvgpr_write_b32 a114, v2\n"
"v_accvgpr_write_b32 a115, v3\n"
"v_accvgpr_write_b32 a140, v8\n"
"v_accvgpr_write_b32 a141, v9\n"
"v_accvgpr_write_b32 a142, v10\n"
"v_accvgpr_write_b32 a143, v11\n"
"v_accvgpr_write_b32 a168, v16\n"
"v_accvgpr_write_b32 a169, v17\n"
"v_accvgpr_write_b32 a170, v18\n"
"v_accvgpr_write_b32 a171, v19\n"
"v_accvgpr_write_b32 a196, v24\n"
"v_accvgpr_write_b32 a197, v25\n"
"v_accvgpr_write_b32 a198, v26\n"
"v_accvgpr_write_b32 a199, v27\n"
"L1_ko0_h0e:\n"
"s_cmp_eq_u32 s22, 0\n"
"s_cbranch_scc1 L1_ko1_h0\n"
"v_accvgpr_write_b32 a128, v32\n"
"v_accvgpr_write_b32 a129, v33\n"
"v_accvgpr_write_b32 a130, v34\n"
"v_accvgpr_write_b32 a131, v35\n"
"v_accvgpr_write_b32 a156, v40\n"
"v_accvgpr_write_b32 a157, v41\n"
"v_accvgpr_write_b32 a158, v42\n"
"v_accvgpr_write_b32 a159, v43\n"
"v_accvgpr_write_b32 a184, v48\n"
"v_accvgpr_write_b32 a185, v49\n"
"v_accvgpr_write_b32 a186, v50\n"
"v_accvgpr_write_b32 a187, v51\n"
"v_accvgpr_write_b32 a212, v56\n"
"v_accvgpr_write_b32 a213, v57\n"
"v_accvgpr_write_b32 a214, v58\n"
"v_accvgpr_write_b32 a215, v59\n"
"s_branch L1_ko1_h0e\n"
"L1_ko1_h0:\n"
"v_accvgpr_write_b32 a116, v32\n"
"v_accvgpr_write_b32 a117, v33\n"
"v_accvgpr_write_b32 a118, v34\n"
"v_accvgpr_write_b32 a119, v35\n"
"v_accvgpr_write_b32 a144, v40\n"
"v_accvgpr_write_b32 a145, v41\n"
"v_accvgpr_write_b32 a146, v42\n"
"v_accvgpr_write_b32 a147, v43\n"
"v_accvgpr_write_b32 a172, v48\n"
"v_accvgpr_write_b32 a173, v49\n"
"v_accvgpr_write_b32 a174, v50\n"
"v_accvgpr_write_b32 a175, v51\n"
"v_accvgpr_write_b32 a200, v56\n"
"v_accvgpr_write_b32 a201, v57\n"
"v_accvgpr_write_b32 a202, v58\n"
"v_accvgpr_write_b32 a203, v59\n"
"L1_ko1_h0e:\n"
"v_accvgpr_read_b32 v206, a216\n"
"v_min_f32 v200, v200, v206\n"
"v_max_f32 v201, v201, v206\n"
"v_rcp_f32 v202, v206\n"
"s_nop 0\n"
"v_pk_mul_f32 v[64:65], v[64:65], v[202:203] op_sel_hi:[1,0]\n"
"v_pk_mul_f32 v[66:67], v[66:67], v[202:203] op_sel_hi:[1,0]\n"
"v_pk_mul_f32 v[68:69], v[68:69], v[202:203] op_sel_hi:[1,0]\n"
"v_pk_mul_f32 v[70:71], v[70:71], v[202:203] op_sel_hi:[1,0]\n"
"v_cvt_pk_bf16_f32 v64, v64, v65\n"
"v_cvt_pk_bf16_f32 v65, v66, v67\n"
"v_cvt_pk_bf16_f32 v66, v68, v69\n"
"v_cvt_pk_bf16_f32 v67, v70, v71\n"
"s_mov_b32 s25, 2048\n"
"buffer_store_dwordx4 v[64:67], v251, s[40:43], s25 offen sc1\n"
"v_mfma_f32_16x16x32_bf16 v[192:195], v[216:219], v[64:67], v[192:195]\n"
"v_pk_mul_f32 v[72:73], v[72:73], v[202:203] op_sel_hi:[1,0]\n"
"v_pk_mul_f32 v[74:75], v[74:75], v[202:203] op_sel_hi:[1,0]\n"
"v_pk_mul_f32 v[76:77], v[76:77], v[202:203] op_sel_hi:[1,0]\n"
"v_pk_mul_f32 v[78:79], v[78:79], v[202:203] op_sel_hi:[1,0]\n"
"v_cvt_pk_bf16_f32 v72, v72, v73\n"
"v_cvt_pk_bf16_f32 v73, v74, v75\n"
"v_cvt_pk_bf16_f32 v74, v76, v77\n"
"v_cvt_pk_bf16_f32 v75, v78, v79\n"
"s_mov_b32 s25, 15360\n"
"buffer_store_dwordx4 v[72:75], v251, s[40:43], s25 offen sc1\n"
"v_mfma_f32_16x16x32_bf16 v[192:195], v[216:219], v[72:75], v[192:195]\n"
"v_pk_mul_f32 v[80:81], v[80:81], v[202:203] op_sel_hi:[1,0]\n"
"v_pk_mul_f32 v[82:83], v[82:83], v[202:203] op_sel_hi:[1,0]\n"
"v_pk_mul_f32 v[84:85], v[84:85], v[202:203] op_sel_hi:[1,0]\n"
"v_pk_mul_f32 v[86:87], v[86:87], v[202:203] op_sel_hi:[1,0]\n"
"v_cvt_pk_bf16_f32 v80, v80, v81\n"
"v_cvt_pk_bf16_f32 v81, v82, v83\n"
"v_cvt_pk_bf16_f32 v82, v84, v85\n"
"v_cvt_pk_bf16_f32 v83, v86, v87\n"
"s_mov_b32 s25, 28672\n"
"buffer_store_dwordx4 v[80:83], v251, s[40:43], s25 offen sc1\n"
"v_mfma_f32_16x16x32_bf16 v[192:195], v[216:219], v[80:83], v[192:195]\n"
"v_pk_mul_f32 v[88:89], v[88:89], v[202:203] op_sel_hi:[1,0]\n"
"v_pk_mul_f32 v[90:91], v[90:91], v[202:203] op_sel_hi:[1,0]\n"
"v_pk_mul_f32 v[92:93], v[92:93], v[202:203] op_sel_hi:[1,0]\n"
"v_pk_mul_f32 v[94:95], v[94:95], v[202:203] op_sel_hi:[1,0]\n"
"v_cvt_pk_bf16_f32 v88, v88, v89\n"
"v_cvt_pk_bf16_f32 v89, v90, v91\n"
"v_cvt_pk_bf16_f32 v90, v92, v93\n"
"v_cvt_pk_bf16_f32 v91, v94, v95\n"
"s_mov_b32 s25, 41984\n"
"buffer_store_dwordx4 v[88:91], v251, s[40:43], s25 offen sc1\n"
"v_mfma_f32_16x16x32_bf16 v[192:195], v[216:219], v[88:91], v[192:195]\n"
"s_cmp_eq_u32 s22, 0\n"
"s_cbranch_scc1 L1_ko2_h0\n"
"v_accvgpr_write_b32 a132, v64\n"
"v_accvgpr_write_b32 a133, v65\n"
"v_accvgpr_write_b32 a134, v66\n"
"v_accvgpr_write_b32 a135, v67\n"
"v_accvgpr_write_b32 a160, v72\n"
"v_accvgpr_write_b32 a161, v73\n"
"v_accvgpr_write_b32 a162, v74\n"
"v_accvgpr_write_b32 a163, v75\n"
"v_accvgpr_write_b32 a188, v80\n"
"v_accvgpr_write_b32 a189, v81\n"
"v_accvgpr_write_b32 a190, v82\n"
"v_accvgpr_write_b32 a191, v83\n"
"v_accvgpr_write_b32 a216, v88\n"
"v_accvgpr_write_b32 a217, v89\n"
"v_accvgpr_write_b32 a218, v90\n"
"v_accvgpr_write_b32 a219, v91\n"
"s_branch L1_ko2_h0e\n"
"L1_ko2_h0:\n"
"v_accvgpr_write_b32 a120, v64\n"
"v_accvgpr_write_b32 a121, v65\n"
"v_accvgpr_write_b32 a122, v66\n"
"v_accvgpr_write_b32 a123, v67\n"
"v_accvgpr_write_b32 a148, v72\n"
"v_accvgpr_write_b32 a149, v73\n"
"v_accvgpr_write_b32 a150, v74\n"
"v_accvgpr_write_b32 a151, v75\n"
"v_accvgpr_write_b32 a176, v80\n"
"v_accvgpr_write_b32 a177, v81\n"
"v_accvgpr_write_b32 a178, v82\n"
"v_accvgpr_write_b32 a179, v83\n"
"v_accvgpr_write_b32 a204, v88\n"
"v_accvgpr_write_b32 a205, v89\n"
"v_accvgpr_write_b32 a206, v90\n"
"v_accvgpr_write_b32 a207, v91\n"
"L1_ko2_h0e:\n"
"v_accvgpr_read_b32 v206, a220\n"
"v_min_f32 v200, v200, v206\n"
"v_max_f32 v201, v201, v206\n"
"v_rcp_f32 v202, v206\n"
"s_nop 0\n"
"v_pk_mul_f32 v[96:97], v[96:97], v[202:203] op_sel_hi:[1,0]\n"
"v_pk_mul_f32 v[98:99], v[98:99], v[202:203] op_sel_hi:[1,0]\n"
"v_pk_mul_f32 v[100:101], v[100:101], v[202:203] op_sel_hi:[1,0]\n"
"v_pk_mul_f32 v[102:103], v[102:103], v[202:203] op_sel_hi:[1,0]\n"
"v_cvt_pk_bf16_f32 v96, v96, v97\n"
"v_cvt_pk_bf16_f32 v97, v98, v99\n"
"v_cvt_pk_bf16_f32 v98, v100, v101\n"
"v_cvt_pk_bf16_f32 v99, v102, v103\n"
"s_mov_b32 s25, 3072\n"
"buffer_store_dwordx4 v[96:99], v251, s[40:43], s25 offen sc1\n"
"v_mfma_f32_16x16x32_bf16 v[192:195], v[216:219], v[96:99], v[192:195]\n"
"v_pk_mul_f32 v[104:105], v[104:105], v[202:203] op_sel_hi:[1,0]\n"
"v_pk_mul_f32 v[106:107], v[106:107], v[202:203] op_sel_hi:[1,0]\n"
"v_pk_mul_f32 v[108:109], v[108:109], v[202:203] op_sel_hi:[1,0]\n"
"v_pk_mul_f32 v[110:111], v[110:111], v[202:203] op_sel_hi:[1,0]\n"
"v_cvt_pk_bf16_f32 v104, v104, v105\n"
"v_cvt_pk_bf16_f32 v105, v106, v107\n"
"v_cvt_pk_bf16_f32 v106, v108, v109\n"
"v_cvt_pk_bf16_f32 v107, v110, v111\n"
"s_mov_b32 s25, 16384\n"
"buffer_store_dwordx4 v[104:107], v251, s[40:43], s25 offen sc1\n"
"v_mfma_f32_16x16x32_bf16 v[192:195], v[216:219], v[104:107], v[192:195]\n"
"v_pk_mul_f32 v[112:113], v[112:113], v[202:203] op_sel_hi:[1,0]\n"
"v_pk_mul_f32 v[114:115], v[114:115], v[202:203] op_sel_hi:[1,0]\n"
"v_pk_mul_f32 v[116:117], v[116:117], v[202:203] op_sel_hi:[1,0]\n"
"v_pk_mul_f32 v[118:119], v[118:119], v[202:203] op_sel_hi:[1,0]\n"
"v_cvt_pk_bf16_f32 v112, v112, v113\n"
"v_cvt_pk_bf16_f32 v113, v114, v115\n"
"v_cvt_pk_bf16_f32 v114, v116, v117\n"
"v_cvt_pk_bf16_f32 v115, v118, v119\n"
"s_mov_b32 s25, 29696\n"
"buffer_store_dwordx4 v[112:115], v251, s[40:43], s25 offen sc1\n"
"v_mfma_f32_16x16x32_bf16 v[192:195], v[216:219], v[112:115], v[192:195]\n"
"v_pk_mul_f32 v[120:121], v[120:121], v[202:203] op_sel_hi:[1,0]\n"
"v_pk_mul_f32 v[122:123], v[122:123], v[202:203] op_sel_hi:[1,0]\n"
"v_pk_mul_f32 v[124:125], v[124:125], v[202:203] op_sel_hi:[1,0]\n"
"v_pk_mul_f32 v[126:127], v[126:127], v[202:203] op_sel_hi:[1,0]\n"
"v_cvt_pk_bf16_f32 v120, v120, v121\n"
"v_cvt_pk_bf16_f32 v121, v122, v123\n"
"v_cvt_pk_bf16_f32 v122, v124, v125\n"
"v_cvt_pk_bf16_f32 v123, v126, v127\n"
"s_mov_b32 s25, 43008\n"
"buffer_store_dwordx4 v[120:123], v251, s[40:43], s25 offen sc1\n"
"v_mfma_f32_16x16x32_bf16 v[192:195], v[216:219], v[120:123], v[192:195]\n"
"s_cmp_eq_u32 s22, 0\n"
"s_cbranch_scc1 L1_ko3_h0\n"
"v_accvgpr_write_b32 a112, v96\n"
"v_accvgpr_write_b32 a113, v97\n"
"v_accvgpr_write_b32 a114, v98\n"
"v_accvgpr_write_b32 a115, v99\n"
"v_accvgpr_write_b32 a140, v104\n"
"v_accvgpr_write_b32 a141, v105\n"
"v_accvgpr_write_b32 a142, v106\n"
"v_accvgpr_write_b32 a143, v107\n"
"v_accvgpr_write_b32 a168, v112\n"
"v_accvgpr_write_b32 a169, v113\n"
"v_accvgpr_write_b32 a170, v114\n"
"v_accvgpr_write_b32 a171, v115\n"
"v_accvgpr_write_b32 a196, v120\n"
"v_accvgpr_write_b32 a197, v121\n"
"v_accvgpr_write_b32 a198, v122\n"
"v_accvgpr_write_b32 a199, v123\n"
"s_branch L1_ko3_h0e\n"
"L1_ko3_h0:\n"
"v_accvgpr_write_b32 a124, v96\n"
"v_accvgpr_write_b32 a125, v97\n"
"v_accvgpr_write_b32 a126, v98\n"
"v_accvgpr_write_b32 a127, v99\n"
"v_accvgpr_write_b32 a152, v104\n"
"v_accvgpr_write_b32 a153, v105\n"
"v_accvgpr_write_b32 a154, v106\n"
"v_accvgpr_write_b32 a155, v107\n"
"v_accvgpr_write_b32 a180, v112\n"
"v_accvgpr_write_b32 a181, v113\n"
"v_accvgpr_write_b32 a182, v114\n"
"v_accvgpr_write_b32 a183, v115\n"
"v_accvgpr_write_b32 a208, v120\n"
"v_accvgpr_write_b32 a209, v121\n"
"v_accvgpr_write_b32 a210, v122\n"
"v_accvgpr_write_b32 a211, v123\n"
"L1_ko3_h0e:\n"
"v_accvgpr_read_b32 v206, a224\n"
"v_min_f32 v200, v200, v206\n"
"v_max_f32 v201, v201, v206\n"
"v_rcp_f32 v202, v206\n"
"s_nop 0\n"
"v_pk_mul_f32 v[128:129], v[128:129], v[202:203] op_sel_hi:[1,0]\n"
"v_pk_mul_f32 v[130:131], v[130:131], v[202:203] op_sel_hi:[1,0]\n"
"v_pk_mul_f32 v[132:133], v[132:133], v[202:203] op_sel_hi:[1,0]\n"
"v_pk_mul_f32 v[134:135], v[134:135], v[202:203] op_sel_hi:[1,0]\n"
"v_cvt_pk_bf16_f32 v128, v128, v129\n"
"v_cvt_pk_bf16_f32 v129, v130, v131\n"
"v_cvt_pk_bf16_f32 v130, v132, v133\n"
"v_cvt_pk_bf16_f32 v131, v134, v135\n"
"s_mov_b32 s25, 4096\n"
"buffer_store_dwordx4 v[128:131], v251, s[40:43], s25 offen sc1\n"
"v_mfma_f32_16x16x32_bf16 v[192:195], v[216:219], v[128:131], v[192:195]\n"
"v_pk_mul_f32 v[136:137], v[136:137], v[202:203] op_sel_hi:[1,0]\n"
"v_pk_mul_f32 v[138:139], v[138:139], v[202:203] op_sel_hi:[1,0]\n"
"v_pk_mul_f32 v[140:141], v[140:141], v[202:203] op_sel_hi:[1,0]\n"
"v_pk_mul_f32 v[142:143], v[142:143], v[202:203] op_sel_hi:[1,0]\n"
"v_cvt_pk_bf16_f32 v136, v136, v137\n"
"v_cvt_pk_bf16_f32 v137, v138, v139\n"
"v_cvt_pk_bf16_f32 v138, v140, v141\n"
"v_cvt_pk_bf16_f32 v139, v142, v143\n"
"s_mov_b32 s25, 17408\n"
"buffer_store_dwordx4 v[136:139], v251, s[40:43], s25 offen sc1\n"
"v_mfma_f32_16x16x32_bf16 v[192:195], v[216:219], v[136:139], v[192:195]\n"
"v_pk_mul_f32 v[144:145], v[144:145], v[202:203] op_sel_hi:[1,0]\n"
"v_pk_mul_f32 v[146:147], v[146:147], v[202:203] op_sel_hi:[1,0]\n"
"v_pk_mul_f32 v[148:149], v[148:149], v[202:203] op_sel_hi:[1,0]\n"
"v_pk_mul_f32 v[150:151], v[150:151], v[202:203] op_sel_hi:[1,0]\n"
"v_cvt_pk_bf16_f32 v144, v144, v145\n"
"v_cvt_pk_bf16_f32 v145, v146, v147\n"
"v_cvt_pk_bf16_f32 v146, v148, v149\n"
"v_cvt_pk_bf16_f32 v147, v150, v151\n"
"s_mov_b32 s25, 30720\n"
"buffer_store_dwordx4 v[144:147], v251, s[40:43], s25 offen sc1\n"
"v_mfma_f32_16x16x32_bf16 v[192:195], v[216:219], v[144:147], v[192:195]\n"
"v_pk_mul_f32 v[152:153], v[152:153], v[202:203] op_sel_hi:[1,0]\n"
"v_pk_mul_f32 v[154:155], v[154:155], v[202:203] op_sel_hi:[1,0]\n"
"v_pk_mul_f32 v[156:157], v[156:157], v[202:203] op_sel_hi:[1,0]\n"
"v_pk_mul_f32 v[158:159], v[158:159], v[202:203] op_sel_hi:[1,0]\n"
"v_cvt_pk_bf16_f32 v152, v152, v153\n"
"v_cvt_pk_bf16_f32 v153, v154, v155\n"
"v_cvt_pk_bf16_f32 v154, v156, v157\n"
"v_cvt_pk_bf16_f32 v155, v158, v159\n"
"s_mov_b32 s25, 44032\n"
"buffer_store_dwordx4 v[152:155], v251, s[40:43], s25 offen sc1\n"
"v_mfma_f32_16x16x32_bf16 v[192:195], v[216:219], v[152:155], v[192:195]\n"
"s_cmp_eq_u32 s22, 0\n"
"s_cbranch_scc1 L1_ko4_h0\n"
"v_accvgpr_write_b32 a116, v128\n"
"v_accvgpr_write_b32 a117, v129\n"
"v_accvgpr_write_b32 a118, v130\n"
"v_accvgpr_write_b32 a119, v131\n"
"v_accvgpr_write_b32 a144, v136\n"
"v_accvgpr_write_b32 a145, v137\n"
"v_accvgpr_write_b32 a146, v138\n"
"v_accvgpr_write_b32 a147, v139\n"
"v_accvgpr_write_b32 a172, v144\n"
"v_accvgpr_write_b32 a173, v145\n"
"v_accvgpr_write_b32 a174, v146\n"
"v_accvgpr_write_b32 a175, v147\n"
"v_accvgpr_write_b32 a200, v152\n"
"v_accvgpr_write_b32 a201, v153\n"
"v_accvgpr_write_b32 a202, v154\n"
"v_accvgpr_write_b32 a203, v155\n"
"s_branch L1_ko4_h0e\n"
"L1_ko4_h0:\n"
"v_accvgpr_write_b32 a128, v128\n"
"v_accvgpr_write_b32 a129, v129\n"
"v_accvgpr_write_b32 a130, v130\n"
"v_accvgpr_write_b32 a131, v131\n"
"v_accvgpr_write_b32 a156, v136\n"
"v_accvgpr_write_b32 a157, v137\n"
"v_accvgpr_write_b32 a158, v138\n"
"v_accvgpr_write_b32 a159, v139\n"
"v_accvgpr_write_b32 a184, v144\n"
"v_accvgpr_write_b32 a185, v145\n"
"v_accvgpr_write_b32 a186, v146\n"
"v_accvgpr_write_b32 a187, v147\n"
"v_accvgpr_write_b32 a212, v152\n"
"v_accvgpr_write_b32 a213, v153\n"
"v_accvgpr_write_b32 a214, v154\n"
"v_accvgpr_write_b32 a215, v155\n"
"L1_ko4_h0e:\n"
"v_accvgpr_read_b32 v206, a228\n"
"v_min_f32 v200, v200, v206\n"
"v_max_f32 v201, v201, v206\n"
"v_rcp_f32 v202, v206\n"
"s_nop 0\n"
"v_pk_mul_f32 v[160:161], v[160:161], v[202:203] op_sel_hi:[1,0]\n"
"v_pk_mul_f32 v[162:163], v[162:163], v[202:203] op_sel_hi:[1,0]\n"
"v_pk_mul_f32 v[164:165], v[164:165], v[202:203] op_sel_hi:[1,0]\n"
"v_pk_mul_f32 v[166:167], v[166:167], v[202:203] op_sel_hi:[1,0]\n"
"v_cvt_pk_bf16_f32 v160, v160, v161\n"
"v_cvt_pk_bf16_f32 v161, v162, v163\n"
"v_cvt_pk_bf16_f32 v162, v164, v165\n"
"v_cvt_pk_bf16_f32 v163, v166, v167\n"
"s_mov_b32 s25, 5120\n"
"buffer_store_dwordx4 v[160:163], v251, s[40:43], s25 offen sc1\n"
"v_mfma_f32_16x16x32_bf16 v[192:195], v[216:219], v[160:163], v[192:195]\n"
"v_pk_mul_f32 v[168:169], v[168:169], v[202:203] op_sel_hi:[1,0]\n"
"v_pk_mul_f32 v[170:171], v[170:171], v[202:203] op_sel_hi:[1,0]\n"
"v_pk_mul_f32 v[172:173], v[172:173], v[202:203] op_sel_hi:[1,0]\n"
"v_pk_mul_f32 v[174:175], v[174:175], v[202:203] op_sel_hi:[1,0]\n"
"v_cvt_pk_bf16_f32 v168, v168, v169\n"
"v_cvt_pk_bf16_f32 v169, v170, v171\n"
"v_cvt_pk_bf16_f32 v170, v172, v173\n"
"v_cvt_pk_bf16_f32 v171, v174, v175\n"
"s_mov_b32 s25, 18432\n"
"buffer_store_dwordx4 v[168:171], v251, s[40:43], s25 offen sc1\n"
"v_mfma_f32_16x16x32_bf16 v[192:195], v[216:219], v[168:171], v[192:195]\n"
"v_pk_mul_f32 v[176:177], v[176:177], v[202:203] op_sel_hi:[1,0]\n"
"v_pk_mul_f32 v[178:179], v[178:179], v[202:203] op_sel_hi:[1,0]\n"
"v_pk_mul_f32 v[180:181], v[180:181], v[202:203] op_sel_hi:[1,0]\n"
"v_pk_mul_f32 v[182:183], v[182:183], v[202:203] op_sel_hi:[1,0]\n"
"v_cvt_pk_bf16_f32 v176, v176, v177\n"
"v_cvt_pk_bf16_f32 v177, v178, v179\n"
"v_cvt_pk_bf16_f32 v178, v180, v181\n"
"v_cvt_pk_bf16_f32 v179, v182, v183\n"
"s_mov_b32 s25, 31744\n"
"buffer_store_dwordx4 v[176:179], v251, s[40:43], s25 offen sc1\n"
"v_mfma_f32_16x16x32_bf16 v[192:195], v[216:219], v[176:179], v[192:195]\n"
"v_pk_mul_f32 v[184:185], v[184:185], v[202:203] op_sel_hi:[1,0]\n"
"v_pk_mul_f32 v[186:187], v[186:187], v[202:203] op_sel_hi:[1,0]\n"
"v_pk_mul_f32 v[188:189], v[188:189], v[202:203] op_sel_hi:[1,0]\n"
"v_pk_mul_f32 v[190:191], v[190:191], v[202:203] op_sel_hi:[1,0]\n"
"v_cvt_pk_bf16_f32 v184, v184, v185\n"
"v_cvt_pk_bf16_f32 v185, v186, v187\n"
"v_cvt_pk_bf16_f32 v186, v188, v189\n"
"v_cvt_pk_bf16_f32 v187, v190, v191\n"
"s_mov_b32 s25, 45056\n"
"buffer_store_dwordx4 v[184:187], v251, s[40:43], s25 offen sc1\n"
"v_mfma_f32_16x16x32_bf16 v[192:195], v[216:219], v[184:187], v[192:195]\n"
"s_cmp_eq_u32 s22, 0\n"
"s_cbranch_scc1 L1_ko5_h0\n"
"v_accvgpr_write_b32 a120, v160\n"
"v_accvgpr_write_b32 a121, v161\n"
"v_accvgpr_write_b32 a122, v162\n"
"v_accvgpr_write_b32 a123, v163\n"
"v_accvgpr_write_b32 a148, v168\n"
"v_accvgpr_write_b32 a149, v169\n"
"v_accvgpr_write_b32 a150, v170\n"
"v_accvgpr_write_b32 a151, v171\n"
"v_accvgpr_write_b32 a176, v176\n"
"v_accvgpr_write_b32 a177, v177\n"
"v_accvgpr_write_b32 a178, v178\n"
"v_accvgpr_write_b32 a179, v179\n"
"v_accvgpr_write_b32 a204, v184\n"
"v_accvgpr_write_b32 a205, v185\n"
"v_accvgpr_write_b32 a206, v186\n"
"v_accvgpr_write_b32 a207, v187\n"
"s_branch L1_ko5_h0e\n"
"L1_ko5_h0:\n"
"v_accvgpr_write_b32 a132, v160\n"
"v_accvgpr_write_b32 a133, v161\n"
"v_accvgpr_write_b32 a134, v162\n"
"v_accvgpr_write_b32 a135, v163\n"
"v_accvgpr_write_b32 a160, v168\n"
"v_accvgpr_write_b32 a161, v169\n"
"v_accvgpr_write_b32 a162, v170\n"
"v_accvgpr_write_b32 a163, v171\n"
"v_accvgpr_write_b32 a188, v176\n"
"v_accvgpr_write_b32 a189, v177\n"
"v_accvgpr_write_b32 a190, v178\n"
"v_accvgpr_write_b32 a191, v179\n"
"v_accvgpr_write_b32 a216, v184\n"
"v_accvgpr_write_b32 a217, v185\n"
"v_accvgpr_write_b32 a218, v186\n"
"v_accvgpr_write_b32 a219, v187\n"
"L1_ko5_h0e:\n"
"v_mov_b32 v204, 163832\n"
"s_cmp_lg_u32 s58, 0\n"
"s_cbranch_scc1 L1_sync_h0\n"
"v_cmp_gt_f32 vcc, 813694976, v200\n"
"s_nop 0\n"
"s_mov_b64 s[64:65], vcc\n"
"v_cmp_lt_f32 vcc, 1568669696, v201\n"
"s_nop 0\n"
"s_or_b64 s[64:65], s[64:65], vcc\n"
"s_nop 3\n"
"v_cmp_u_f32 vcc, v192, v192\n"
"s_nop 0\n"
"s_or_b64 s[64:65], s[64:65], vcc\n"
"v_mov_b32 v204, 163832\n"
"v_mov_b32 v205, 1\n"
"s_cbranch_scc0 L1_sync_h0\n"
"ds_write_b32 v204, v205\n"
"L1_sync_h0:\n"
"s_waitcnt lgkmcnt(0)\n"
"s_waitcnt vmcnt(17)\n"
"s_barrier\n"
"ds_read_b32 v205, v204\n"
"ds_read_b64_tr_b16 a[224:225], v224 offset:128\n"
"ds_read_b64_tr_b16 a[226:227], v224 offset:4480\n"
"ds_read_b64_tr_b16 a[228:229], v224 offset:8832\n"
"ds_read_b64_tr_b16 a[230:231], v224 offset:13184\n"
"ds_read_b64_tr_b16 a[232:233], v225 offset:128\n"
"ds_read_b64_tr_b16 a[234:235], v225 offset:4480\n"
"ds_read_b64_tr_b16 a[236:237], v225 offset:8832\n"
"ds_read_b64_tr_b16 a[238:239], v225 offset:13184\n"
"ds_read_b64_tr_b16 a[240:241], v224 offset:192\n"
"ds_read_b64_tr_b16 a[242:243], v224 offset:4544\n"
"ds_read_b64_tr_b16 a[244:245], v224 offset:8896\n"
"ds_read_b64_tr_b16 a[246:247], v224 offset:13248\n"
"ds_read_b64_tr_b16 a[248:249], v225 offset:192\n"
"ds_read_b64_tr_b16 a[250:251], v225 offset:4544\n"
"ds_read_b64_tr_b16 a[252:253], v225 offset:8896\n"
"ds_read_b64_tr_b16 a[254:255], v225 offset:13248\n"
"s_waitcnt lgkmcnt(15)\n"
"v_readfirstlane_b32 s25, v205\n"
"s_nop 0\n"
"s_andn2_b32 s25, s25, s58\n"
"s_cbranch_scc0 L2_go\n"
"v_mov_b32 v205, 1065369472\n"
"s_waitcnt lgkmcnt(0)\n"
"v_accvgpr_write_b32 a236, v205\n"
"v_accvgpr_write_b32 a237, v205\n"
"v_accvgpr_write_b32 a238, v205\n"
"v_accvgpr_write_b32 a239, v205\n"
"s_mov_b32 s58, 1\n"
"s_waitcnt vmcnt(0) lgkmcnt(0)\n"
"s_barrier\n"
"v_lshrrev_b32 v80, 4, v251\n"
"v_and_b32 v81, 15, v80\n"
"v_lshrrev_b32 v82, 4, v80\n"
"v_lshlrev_b32 v82, 4, v82\n"
"v_lshl_add_u32 v81, v81, 8, v82\n"
"v_xor_b32 v82, 16, v80\n"
"v_lshlrev_b32 v82, 2, v82\n"
"v_xor_b32 v83, 32, v80\n"
"v_lshlrev_b32 v83, 2, v83\n"
"s_mov_b32 s26, 0\n"
"v_mov_b32 v64, 4286578688\n"
"v_mov_b32 v65, 4286578688\n"
"v_mov_b32 v66, 4286578688\n"
"v_mov_b32 v67, 4286578688\n"
"v_mov_b32 v68, 4286578688\n"
"v_mov_b32 v69, 4286578688\n"
"v_mov_b32 v70, 4286578688\n"
"L1_fb_h0:\n"
"buffer_load_dwordx4 a[112:115], v81, s[44:47], s26 offen\n"
"buffer_load_dwordx4 a[116:119], v81, s[44:47], s26 offen offset:64\n"
"buffer_load_dwordx4 a[120:123], v81, s[44:47], s26 offen offset:128\n"
"buffer_load_dwordx4 a[124:127], v81, s[44:47], s26 offen offset:192\n"
"s_waitcnt vmcnt(0)\n"
"v_mfma_f32_16x16x32_bf16 v[0:3], a[112:115], a[0:3], 0\n"
"v_mfma_f32_16x16x32_bf16 v[0:3], a[116:119], a[4:7], v[0:3]\n"
"v_mfma_f32_16x16x32_bf16 v[0:3], a[120:123], a[8:11], v[0:3]\n"
"v_mfma_f32_16x16x32_bf16 v[0:3], a[124:127], a[12:15], v[0:3]\n"
"v_mfma_f32_16x16x32_bf16 v[4:7], a[112:115], a[16:19], 0\n"
"v_mfma_f32_16x16x32_bf16 v[4:7], a[116:119], a[20:23], v[4:7]\n"
"v_mfma_f32_16x16x32_bf16 v[4:7], a[120:123], a[24:27], v[4:7]\n"
"v_mfma_f32_16x16x32_bf16 v[4:7], a[124:127], a[28:31], v[4:7]\n"
"v_mfma_f32_16x16x32_bf16 v[8:11], a[112:115], a[32:35], 0\n"
"v_mfma_f32_16x16x32_bf16 v[8:11], a[116:119], a[36:39], v[8:11]\n"
"v_mfma_f32_16x16x32_bf16 v[8:11], a[120:123], a[40:43], v[8:11]\n"
"v_mfma_f32_16x16x32_bf16 v[8:11], a[124:127], a[44:47], v[8:11]\n"
"v_mfma_f32_16x16x32_bf16 v[12:15], a[112:115], a[48:51], 0\n"
"v_mfma_f32_16x16x32_bf16 v[12:15], a[116:119], a[52:55], v[12:15]\n"
"v_mfma_f32_16x16x32_bf16 v[12:15], a[120:123], a[56:59], v[12:15]\n"
"v_mfma_f32_16x16x32_bf16 v[12:15], a[124:127], a[60:63], v[12:15]\n"
"v_mfma_f32_16x16x32_bf16 v[16:19], a[112:115], a[64:67], 0\n"
"v_mfma_f32_16x16x32_bf16 v[16:19], a[116:119], a[68:71], v[16:19]\n"
"v_mfma_f32_16x16x32_bf16 v[16:19], a[120:123], a[72:75], v[16:19]\n"
"v_mfma_f32_16x16x32_bf16 v[16:19], a[124:127], a[76:79], v[16:19]\n"
"v_mfma_f32_16x16x32_bf16 v[20:23], a[112:115], a[80:83], 0\n"
"v_mfma_f32_16x16x32_bf16 v[20:23], a[116:119], a[84:87], v[20:23]\n"
"v_mfma_f32_16x16x32_bf16 v[20:23], a[120:123], a[88:91], v[20:23]\n"
"v_mfma_f32_16x16x32_bf16 v[20:23], a[124:127], a[92:95], v[20:23]\n"
"v_mfma_f32_16x16x32_bf16 v[24:27], a[112:115], a[96:99], 0\n"
"v_mfma_f32_16x16x32_bf16 v[24:27], a[116:119], a[100:103], v[24:27]\n"
"v_mfma_f32_16x16x32_bf16 v[24:27], a[120:123], a[104:107], v[24:27]\n"
"v_mfma_f32_16x16x32_bf16 v[24:27], a[124:127], a[108:111], v[24:27]\n"
"s_nop 7\n"
"v_max3_f32 v64, v64, v0, v1\n"
"v_max3_f32 v64, v64, v2, v3\n"
"v_max3_f32 v65, v65, v4, v5\n"
"v_max3_f32 v65, v65, v6, v7\n"
"v_max3_f32 v66, v66, v8, v9\n"
"v_max3_f32 v66, v66, v10, v11\n"
"v_max3_f32 v67, v67, v12, v13\n"
"v_max3_f32 v67, v67, v14, v15\n"
"v_max3_f32 v68, v68, v16, v17\n"
"v_max3_f32 v68, v68, v18, v19\n"
"v_max3_f32 v69, v69, v20, v21\n"
"v_max3_f32 v69, v69, v22, v23\n"
"v_max3_f32 v70, v70, v24, v25\n"
"v_max3_f32 v70, v70, v26, v27\n"
"s_add_u32 s26, s26, 4096\n"
"s_cmp_lt_u32 s26, 262144\n"
"s_cbranch_scc1 L1_fb_h0\n"
"ds_bpermute_b32 v72, v82, v64\n"
"ds_bpermute_b32 v73, v82, v65\n"
"ds_bpermute_b32 v74, v82, v66\n"
"ds_bpermute_b32 v75, v82, v67\n"
"ds_bpermute_b32 v76, v82, v68\n"
"ds_bpermute_b32 v77, v82, v69\n"
"ds_bpermute_b32 v78, v82, v70\n"
"s_waitcnt lgkmcnt(0)\n"
"v_max_f32 v64, v64, v72\n"
"v_max_f32 v65, v65, v73\n"
"v_max_f32 v66, v66, v74\n"
"v_max_f32 v67, v67, v75\n"
"v_max_f32 v68, v68, v76\n"
"v_max_f32 v69, v69, v77\n"
"v_max_f32 v70, v70, v78\n"
"ds_bpermute_b32 v72, v83, v64\n"
"ds_bpermute_b32 v73, v83, v65\n"
"ds_bpermute_b32 v74, v83, v66\n"
"ds_bpermute_b32 v75, v83, v67\n"
"ds_bpermute_b32 v76, v83, v68\n"
"ds_bpermute_b32 v77, v83, v69\n"
"ds_bpermute_b32 v78, v83, v70\n"
"s_waitcnt lgkmcnt(0)\n"
"v_max_f32 v64, v64, v72\n"
"v_max_f32 v65, v65, v73\n"
"v_max_f32 v66, v66, v74\n"
"v_max_f32 v67, v67, v75\n"
"v_max_f32 v68, v68, v76\n"
"v_max_f32 v69, v69, v77\n"
"v_max_f32 v70, v70, v78\n"
"v_xor_b32 v216, 2147483648, v64\n"
"v_xor_b32 v217, 2147483648, v64\n"
"v_xor_b32 v218, 2147483648, v64\n"
"v_xor_b32 v219, 2147483648, v64\n"
"v_xor_b32 v220, 2147483648, v65\n"
"v_xor_b32 v221, 2147483648, v65\n"
"v_xor_b32 v222, 2147483648, v65\n"
"v_xor_b32 v223, 2147483648, v65\n"
"v_xor_b32 v224, 2147483648, v66\n"
"v_xor_b32 v225, 2147483648, v66\n"
"v_xor_b32 v226, 2147483648, v66\n"
"v_xor_b32 v227, 2147483648, v66\n"
"v_xor_b32 v228, 2147483648, v67\n"
"v_xor_b32 v229, 2147483648, v67\n"
"v_xor_b32 v230, 2147483648, v67\n"
"v_xor_b32 v231, 2147483648, v67\n"
"v_xor_b32 v232, 2147483648, v68\n"
"v_xor_b32 v233, 2147483648, v68\n"
"v_xor_b32 v234, 2147483648, v68\n"
"v_xor_b32 v235, 2147483648, v68\n"
"v_xor_b32 v236, 2147483648, v69\n"
"v_xor_b32 v237, 2147483648, v69\n"
"v_xor_b32 v238, 2147483648, v69\n"
"v_xor_b32 v239, 2147483648, v69\n"
"v_xor_b32 v240, 2147483648, v70\n"
"v_xor_b32 v241, 2147483648, v70\n"
"v_xor_b32 v242, 2147483648, v70\n"
"v_xor_b32 v243, 2147483648, v70\n"
"s_mov_b32 s53, s67\n"
"s_add_u32 s54, s67, 4096\n"
"s_mov_b32 s38, -1\n"
"s_add_u32 m0, s50, 0\n"
"s_nop 0\n"
"buffer_load_dwordx4 v250, s[36:39], s53 offen nt lds\n"
"s_add_u32 m0, s50, 64\n"
"s_nop 0\n"
"buffer_load_dwordx4 v250, s[36:39], s53 offen offset:1024 nt lds\n"
"s_add_u32 m0, s50, 128\n"
"s_nop 0\n"
"buffer_load_dwordx4 v250, s[36:39], s53 offen offset:2048 nt lds\n"
"s_add_u32 m0, s50, 192\n"
"s_nop 0\n"
"buffer_load_dwordx4 v250, s[36:39], s53 offen offset:3072 nt lds\n"
"s_add_u32 m0, s50, 4352\n"
"s_nop 0\n"
"buffer_load_dwordx4 v250, s[36:39], s54 offen nt lds\n"
"s_add_u32 m0, s50, 4416\n"
"s_nop 0\n"
"buffer_load_dwordx4 v250, s[36:39], s54 offen offset:1024 nt lds\n"
"s_add_u32 m0, s50, 4480\n"
"s_nop 0\n"
"buffer_load_dwordx4 v250, s[36:39], s54 offen offset:2048 nt lds\n"
"s_add_u32 m0, s50, 4544\n"
"s_nop 0\n"
"buffer_load_dwordx4 v250, s[36:39], s54 offen offset:3072 nt lds\n"
"s_add_u32 s53, s53, 8192\n"
"s_and_b32 s53, s53, 258047\n"
"s_lshl_b32 s38, s38, 1\n"
"s_add_u32 s54, s53, 4096\n"
"s_add_u32 m0, s50, 34304\n"
"s_nop 0\n"
"buffer_load_dwordx4 v250, s[36:39], s53 offen nt lds\n"
"s_add_u32 m0, s50, 34368\n"
"s_nop 0\n"
"buffer_load_dwordx4 v250, s[36:39], s53 offen offset:1024 nt lds\n"
"s_add_u32 m0, s50, 34432\n"
"s_nop 0\n"
"buffer_load_dwordx4 v250, s[36:39], s53 offen offset:2048 nt lds\n"
"s_add_u32 m0, s50, 34496\n"
"s_nop 0\n"
"buffer_load_dwordx4 v250, s[36:39], s53 offen offset:3072 nt lds\n"
"s_add_u32 m0, s50, 38656\n"
"s_nop 0\n"
"buffer_load_dwordx4 v250, s[36:39], s54 offen nt lds\n"
"s_add_u32 m0, s50, 38720\n"
"s_nop 0\n"
"buffer_load_dwordx4 v250, s[36:39], s54 offen offset:1024 nt lds\n"
"s_add_u32 m0, s50, 38784\n"
"s_nop 0\n"
"buffer_load_dwordx4 v250, s[36:39], s54 offen offset:2048 nt lds\n"
"s_add_u32 m0, s50, 38848\n"
"s_nop 0\n"
"buffer_load_dwordx4 v250, s[36:39], s54 offen offset:3072 nt lds\n"
"s_add_u32 s53, s53, 8192\n"
"s_and_b32 s53, s53, 258047\n"
"s_lshl_b32 s38, s38, 1\n"
"s_add_u32 s54, s53, 4096\n"
"s_add_u32 m0, s50, 68608\n"
"s_nop 0\n"
"buffer_load_dwordx4 v250, s[36:39], s53 offen nt lds\n"
"s_add_u32 m0, s50, 68672\n"
"s_nop 0\n"
"buffer_load_dwordx4 v250, s[36:39], s53 offen offset:1024 nt lds\n"
"s_add_u32 m0, s50, 68736\n"
"s_nop 0\n"
"buffer_load_dwordx4 v250, s[36:39], s53 offen offset:2048 nt lds\n"
"s_add_u32 m0, s50, 68800\n"
"s_nop 0\n"
"buffer_load_dwordx4 v250, s[36:39], s53 offen offset:3072 nt lds\n"
"s_add_u32 m0, s50, 72960\n"
"s_nop 0\n"
"buffer_load_dwordx4 v250, s[36:39], s54 offen nt lds\n"
"s_add_u32 m0, s50, 73024\n"
"s_nop 0\n"
"buffer_load_dwordx4 v250, s[36:39], s54 offen offset:1024 nt lds\n"
"s_add_u32 m0, s50, 73088\n"
"s_nop 0\n"
"buffer_load_dwordx4 v250, s[36:39], s54 offen offset:2048 nt lds\n"
"s_add_u32 m0, s50, 73152\n"
"s_nop 0\n"
"buffer_load_dwordx4 v250, s[36:39], s54 offen offset:3072 nt lds\n"
"s_add_u32 s53, s53, 8192\n"
"s_and_b32 s53, s53, 258047\n"
"s_lshl_b32 s38, s38, 1\n"
"s_add_u32 s54, s53, 4096\n"
"s_add_u32 m0, s50, 102912\n"
"s_nop 0\n"
"buffer_load_dwordx4 v250, s[36:39], s53 offen nt lds\n"
"s_add_u32 m0, s50, 102976\n"
"s_nop 0\n"
"buffer_load_dwordx4 v250, s[36:39], s53 offen offset:1024 nt lds\n"
"s_add_u32 m0, s50, 103040\n"
"s_nop 0\n"
"buffer_load_dwordx4 v250, s[36:39], s53 offen offset:2048 nt lds\n"
"s_add_u32 m0, s50, 103104\n"
"s_nop 0\n"
"buffer_load_dwordx4 v250, s[36:39], s53 offen offset:3072 nt lds\n"
"s_add_u32 m0, s50, 107264\n"
"s_nop 0\n"
"buffer_load_dwordx4 v250, s[36:39], s54 offen nt lds\n"
"s_add_u32 m0, s50, 107328\n"
"s_nop 0\n"
"buffer_load_dwordx4 v250, s[36:39], s54 offen offset:1024 nt lds\n"
"s_add_u32 m0, s50, 107392\n"
"s_nop 0\n"
"buffer_load_dwordx4 v250, s[36:39], s54 offen offset:2048 nt lds\n"
"s_add_u32 m0, s50, 107456\n"
"s_nop 0\n"
"buffer_load_dwordx4 v250, s[36:39], s54 offen offset:3072 nt lds\n"
"s_add_u32 s53, s53, 8192\n"
"s_and_b32 s53, s53, 258047\n"
"s_lshl_b32 s38, s38, 1\n"
"s_add_u32 s54, s53, 4096\n"
"s_waitcnt vmcnt(0)\n"
"s_barrier\n"
"s_branch L1_attn_h0\n"
"L1_h1:\n"
"v_add_u32 v216, 114688, v216\n"
"v_add_u32 v217, 16384, v216\n"
"v_add_u32 v218, 32768, v216\n"
"v_add_u32 v219, 49152, v216\n"
"v_add_u32 v220, 65536, v216\n"
"v_add_u32 v221, 81920, v216\n"
"v_add_u32 v212, 32768, v212\n"
"v_add_u32 v213, 8192, v212\n"
"v_add_u32 v214, 16384, v212\n"
"v_add_u32 v215, 24576, v212\n"
"s_add_u32 m0, s48, 133824\n"
"s_nop 0\n"
"buffer_load_dwordx4 v216, s[28:31], s51 offen lds\n"
"s_add_u32 m0, s48, 134912\n"
"s_nop 0\n"
"buffer_load_dwordx4 v217, s[28:31], s51 offen lds\n"
"s_add_u32 m0, s48, 136000\n"
"s_nop 0\n"
"buffer_load_dwordx4 v218, s[28:31], s51 offen lds\n"
"s_add_u32 m0, s48, 137088\n"
"s_nop 0\n"
"buffer_load_dwordx4 v219, s[28:31], s51 offen lds\n"
"s_add_u32 m0, s48, 138176\n"
"s_nop 0\n"
"buffer_load_dwordx4 v220, s[28:31], s51 offen lds\n"
"s_add_u32 m0, s48, 139264\n"
"s_nop 0\n"
"buffer_load_dwordx4 v221, s[28:31], s51 offen lds\n"
"s_add_u32 s51, s51, 128\n"
"s_add_u32 m0, s49, 60928\n"
"s_nop 0\n"
"buffer_load_dwordx4 v212, s[32:35], s52 offen lds\n"
"s_add_u32 m0, s49, 62016\n"
"s_nop 0\n"
"buffer_load_dwordx4 v213, s[32:35], s52 offen lds\n"
"s_add_u32 m0, s49, 63104\n"
"s_nop 0\n"
"buffer_load_dwordx4 v214, s[32:35], s52 offen lds\n"
"s_add_u32 m0, s49, 64192\n"
"s_nop 0\n"
"buffer_load_dwordx4 v215, s[32:35], s52 offen lds\n"
"s_add_u32 s52, s52, 262144\n"
"s_add_u32 m0, s49, 78336\n"
"s_nop 0\n"
"buffer_load_dwordx4 v212, s[32:35], s52 offen lds\n"
"s_add_u32 m0, s49, 79424\n"
"s_nop 0\n"
"buffer_load_dwordx4 v213, s[32:35], s52 offen lds\n"
"s_add_u32 m0, s49, 80512\n"
"s_nop 0\n"
"buffer_load_dwordx4 v214, s[32:35], s52 offen lds\n"
"s_add_u32 m0, s49, 81600\n"
"s_nop 0\n"
"buffer_load_dwordx4 v215, s[32:35], s52 offen lds\n"
"s_add_u32 s52, s52, 262144\n"
"s_add_u32 m0, s48, 7616\n"
"s_nop 0\n"
"buffer_load_dwordx4 v216, s[28:31], s51 offen lds\n"
"s_add_u32 m0, s48, 8704\n"
"s_nop 0\n"
"buffer_load_dwordx4 v217, s[28:31], s51 offen lds\n"
"s_add_u32 m0, s48, 9792\n"
"s_nop 0\n"
"buffer_load_dwordx4 v218, s[28:31], s51 offen lds\n"
"s_add_u32 m0, s48, 10880\n"
"s_nop 0\n"
"buffer_load_dwordx4 v219, s[28:31], s51 offen lds\n"
"s_add_u32 m0, s48, 11968\n"
"s_nop 0\n"
"buffer_load_dwordx4 v220, s[28:31], s51 offen lds\n"
"s_add_u32 m0, s48, 13056\n"
"s_nop 0\n"
"buffer_load_dwordx4 v221, s[28:31], s51 offen lds\n"
"s_add_u32 s51, s51, 128\n"
"s_add_u32 m0, s49, 95744\n"
"s_nop 0\n"
"buffer_load_dwordx4 v212, s[32:35], s52 offen lds\n"
"s_add_u32 m0, s49, 96832\n"
"s_nop 0\n"
"buffer_load_dwordx4 v213, s[32:35], s52 offen lds\n"
"s_add_u32 m0, s49, 97920\n"
"s_nop 0\n"
"buffer_load_dwordx4 v214, s[32:35], s52 offen lds\n"
"s_add_u32 m0, s49, 99008\n"
"s_nop 0\n"
"buffer_load_dwordx4 v215, s[32:35], s52 offen lds\n"
"s_add_u32 s52, s52, 262144\n"
"s_add_u32 m0, s49, 113152\n"
"s_nop 0\n"
"buffer_load_dwordx4 v212, s[32:35], s52 offen lds\n"
"s_add_u32 m0, s49, 114240\n"
"s_nop 0\n"
"buffer_load_dwordx4 v213, s[32:35], s52 offen lds\n"
"s_add_u32 m0, s49, 115328\n"
"s_nop 0\n"
"buffer_load_dwordx4 v214, s[32:35], s52 offen lds\n"
"s_add_u32 m0, s49, 116416\n"
"s_nop 0\n"
"buffer_load_dwordx4 v215, s[32:35], s52 offen lds\n"
"s_add_u32 s52, s52, 262144\n"
"s_add_u32 m0, s48, 35904\n"
"s_nop 0\n"
"buffer_load_dwordx4 v216, s[28:31], s51 offen lds\n"
"s_add_u32 m0, s48, 36992\n"
"s_nop 0\n"
"buffer_load_dwordx4 v217, s[28:31], s51 offen lds\n"
"s_add_u32 m0, s48, 38080\n"
"s_nop 0\n"
"buffer_load_dwordx4 v218, s[28:31], s51 offen lds\n"
"s_add_u32 m0, s48, 39168\n"
"s_nop 0\n"
"buffer_load_dwordx4 v219, s[28:31], s51 offen lds\n"
"v_lshrrev_b32 v10, 1, v6\n"
"v_and_b32 v10, 3, v10\n"
"v_xor_b32 v10, v10, v5\n"
"v_lshlrev_b32 v10, 4, v10\n"
"v_and_b32 v11, 7, v6\n"
"v_lshl_add_u32 v10, v11, 7, v10\n"
"v_lshrrev_b32 v11, 3, v6\n"
"v_mul_u32_u24 v11, 1088, v11\n"
"v_add_u32 v208, v10, v11\n"
"v_add_u32 v209, 126208, v208\n"
"v_xor_b32 v10, v8, v9\n"
"v_lshlrev_b32 v10, 4, v10\n"
"v_lshl_add_u32 v10, v5, 8, v10\n"
"v_mul_u32_u24 v11, 1088, v7\n"
"v_add3_u32 v10, v10, v11, s49\n"
"v_add_u32 v10, 56576, v10\n"
"v_and_b32 v11, 1, v8\n"
"v_lshl_add_u32 v210, v11, 3, v10\n"
"v_xor_b32 v11, 1, v11\n"
"v_lshl_add_u32 v211, v11, 3, v10\n"
"v_xor_b32 v10, v5, v8\n"
"v_lshlrev_b32 v10, 4, v10\n"
"v_lshl_add_u32 v10, v8, 8, v10\n"
"v_mul_u32_u24 v11, 1088, v7\n"
"v_add3_u32 v244, v10, v11, s50\n"
"v_add_u32 v245, 68608, v244\n"
"v_lshlrev_b32 v10, 3, v8\n"
"v_lshl_add_u32 v10, v5, 8, v10\n"
"v_mul_u32_u24 v11, 1056, v7\n"
"v_add3_u32 v10, v10, v11, s50\n"
"v_add_u32 v10, 8704, v10\n"
"v_lshl_add_u32 v246, v9, 7, v10\n"
"v_xor_b32 v11, 1, v9\n"
"v_lshl_add_u32 v248, v11, 7, v10\n"
"v_add_u32 v247, 68608, v246\n"
"v_add_u32 v249, 68608, v248\n"
"v_lshlrev_b32 v251, 4, v4\n"
"v_lshlrev_b32 v10, 3, v9\n"
"v_xor_b32 v10, v10, v6\n"
"v_lshlrev_b32 v10, 4, v10\n"
"v_lshl_add_u32 v250, v5, 10, v10\n"
"s_lshl_b32 s25, s23, 5\n"
"s_add_u32 s25, s25, s59\n"
"s_lshl_b32 s26, s25, 18\n"
"s_add_u32 s36, s12, s26\n"
"s_addc_u32 s37, s13, 0\n"
"s_and_b32 s37, s37, 65535\n"
"s_mov_b32 s38, 262144\n"
"s_mov_b32 s39, 131072\n"
"s_add_u32 s44, s10, s26\n"
"s_addc_u32 s45, s11, 0\n"
"s_and_b32 s45, s45, 65535\n"
"s_mov_b32 s46, 262144\n"
"s_mov_b32 s47, 131072\n"
"s_lshl_b32 s67, s20, 1\n"
"s_add_u32 s67, s67, s22\n"
"s_and_b32 s67, s67, 31\n"
"s_and_b32 s53, s67, 0\n"
"s_lshl_b32 s53, s53, 12\n"
"s_lshr_b32 s67, s67, 0\n"
"s_lshl_b32 s67, s67, 13\n"
"s_or_b32 s67, s67, s53\n"
"s_mov_b32 s53, s67\n"
"s_add_u32 s54, s67, 4096\n"
"s_mov_b32 s38, -1\n"
"s_mul_i32 s26, s25, 53248\n"
"s_add_u32 s40, s18, s26\n"
"s_addc_u32 s41, s19, 0\n"
"s_and_b32 s41, s41, 65535\n"
"s_mov_b32 s42, 53248\n"
"s_mov_b32 s43, 131072\n"
"s_lshl_b32 s25, s20, 2\n"
"s_add_u32 s25, s25, s21\n"
"s_lshl_b32 s25, s25, 13\n"
"s_add_u32 s60, s16, s25\n"
"s_addc_u32 s61, s17, 0\n"
"s_and_b32 s61, s61, 65535\n"
"s_mov_b32 s62, 8192\n"
"s_mov_b32 s63, 131072\n"
"s_mov_b32 s56, 1040356334\n"
"s_mov_b32 s57, s56\n"
"v_mov_b32 v10, 1065369472\n"
"v_mov_b32 v11, 0\n"
"v_mov_b32 v12, 163832\n"
"v_accvgpr_write_b32 a236, v10\n"
"v_accvgpr_write_b32 a237, v10\n"
"v_accvgpr_write_b32 a238, v10\n"
"v_accvgpr_write_b32 a239, v10\n"
"ds_write_b32 v12, v11\n"
"s_add_u32 s76, s18, 33554432\n"
"s_addc_u32 s77, s19, 0\n"
"s_and_b32 s77, s77, 65535\n"
"s_mov_b32 s78, 131072\n"
"s_mov_b32 s79, 131072\n"
"s_lshl_b32 s25, s23, 8\n"
"s_add_u32 s25, s25, 65536\n"
"buffer_load_dwordx2 a[232:233], off, s[76:79], s25 sc1\n"
"v_add_u32 v254, 4096, v251\n"
"s_bfe_u32 s66, s20, 3\n"
"s_lshl_b32 s66, s66, 13\n"
"s_add_u32 s66, s66, s67\n"
"s_add_u32 s25, s50, 8704\n"
"v_add_u32 v252, s25, v251\n"
"v_add_u32 v253, 68608, v252\n"
"s_waitcnt vmcnt(19)\n"
"s_waitcnt lgkmcnt(0)\n"
"s_barrier\n"
"ds_read_b64_tr_b16 a[112:113], v210\n"
"ds_read_b64_tr_b16 a[114:115], v210 offset:4352\n"
"ds_read_b128 a[176:179], v209 offset:13056\n"
"ds_read_b64_tr_b16 a[116:117], v210 offset:17408\n"
"ds_read_b64_tr_b16 a[118:119], v210 offset:21760\n"
"ds_read_b128 a[180:183], v209 offset:13120\n"
"ds_read_b64_tr_b16 a[120:121], v211\n"
"ds_read_b64_tr_b16 a[122:123], v211 offset:4352\n"
"ds_read_b64_tr_b16 a[124:125], v211 offset:17408\n"
"ds_read_b64_tr_b16 a[126:127], v211 offset:21760\n"
"ds_read_b64_tr_b16 a[128:129], v210 offset:64\n"
"ds_read_b64_tr_b16 a[130:131], v210 offset:4416\n"
"ds_read_b64_tr_b16 a[132:133], v210 offset:17472\n"
"ds_read_b64_tr_b16 a[134:135], v210 offset:21824\n"
"ds_read_b64_tr_b16 a[136:137], v211 offset:64\n"
"ds_read_b64_tr_b16 a[138:139], v211 offset:4416\n"
"ds_read_b64_tr_b16 a[140:141], v211 offset:17472\n"
"ds_read_b64_tr_b16 a[142:143], v211 offset:21824\n"
"ds_read_b128 a[184:187], v209 offset:15232\n"
"ds_read_b128 a[188:191], v209 offset:15296\n"
"ds_read_b128 a[192:195], v209 offset:17408\n"
"ds_read_b128 a[196:199], v209 offset:17472\n"
"ds_read_b128 a[200:203], v209 offset:19584\n"
"ds_read_b128 a[204:207], v209 offset:19648\n"
"ds_read_b128 a[208:211], v209 offset:21760\n"
"ds_read_b128 a[212:215], v209 offset:21824\n"
"ds_read_b128 a[224:227], v209 offset:26112\n"
"ds_read_b128 a[228:231], v209 offset:26176\n"
"s_waitcnt lgkmcnt(15)\n"
"v_mfma_f32_16x16x32_bf16 v[0:3], a[112:115], a[176:179], 0\n"
"s_waitcnt lgkmcnt(15)\n"
"ds_read_b128 a[216:219], v209 offset:23936\n"
"v_mfma_f32_16x16x32_bf16 v[0:3], a[116:119], a[180:183], v[0:3]\n"
"s_waitcnt lgkmcnt(15)\n"
"ds_read_b128 a[220:223], v209 offset:24000\n"
"v_mfma_f32_16x16x32_bf16 v[4:7], a[120:123], a[176:179], 0\n"
"s_waitcnt lgkmcnt(15)\n"
"ds_read_b64_tr_b16 a[144:145], v210 offset:128\n"
"v_mfma_f32_16x16x32_bf16 v[4:7], a[124:127], a[180:183], v[4:7]\n"
"s_waitcnt lgkmcnt(15)\n"
"ds_read_b64_tr_b16 a[146:147], v210 offset:4480\n"
"v_mfma_f32_16x16x32_bf16 v[8:11], a[128:131], a[176:179], 0\n"
"s_waitcnt lgkmcnt(15)\n"
"ds_read_b64_tr_b16 a[148:149], v210 offset:17536\n"
"v_mfma_f32_16x16x32_bf16 v[8:11], a[132:135], a[180:183], v[8:11]\n"
"s_waitcnt lgkmcnt(15)\n"
"ds_read_b64_tr_b16 a[150:151], v210 offset:21888\n"
"v_mfma_f32_16x16x32_bf16 v[12:15], a[136:139], a[176:179], 0\n"
"s_waitcnt lgkmcnt(15)\n"
"ds_read_b64_tr_b16 a[152:153], v211 offset:128\n"
"v_mfma_f32_16x16x32_bf16 v[12:15], a[140:143], a[180:183], v[12:15]\n"
"s_waitcnt lgkmcnt(15)\n"
"ds_read_b64_tr_b16 a[154:155], v211 offset:4480\n"
"v_mfma_f32_16x16x32_bf16 v[32:35], a[112:115], a[184:187], 0\n"
"s_waitcnt lgkmcnt(15)\n"
"ds_read_b64_tr_b16 a[156:157], v211 offset:17536\n"
"v_mfma_f32_16x16x32_bf16 v[32:35], a[116:119], a[188:191], v[32:35]\n"
"ds_read_b64_tr_b16 a[158:159], v211 offset:21888\n"
"v_mfma_f32_16x16x32_bf16 v[36:39], a[120:123], a[184:187], 0\n"
"ds_read_b64_tr_b16 a[160:161], v210 offset:192\n"
"v_mfma_f32_16x16x32_bf16 v[36:39], a[124:127], a[188:191], v[36:39]\n"
"ds_read_b64_tr_b16 a[162:163], v210 offset:4544\n"
"v_mfma_f32_16x16x32_bf16 v[40:43], a[128:131], a[184:187], 0\n"
"ds_read_b64_tr_b16 a[164:165], v210 offset:17600\n"
"v_mfma_f32_16x16x32_bf16 v[40:43], a[132:135], a[188:191], v[40:43]\n"
"ds_read_b64_tr_b16 a[166:167], v210 offset:21952\n"
"v_mfma_f32_16x16x32_bf16 v[44:47], a[136:139], a[184:187], 0\n"
"ds_read_b64_tr_b16 a[168:169], v211 offset:192\n"
"v_mfma_f32_16x16x32_bf16 v[44:47], a[140:143], a[188:191], v[44:47]\n"
"s_waitcnt lgkmcnt(15)\n"
"ds_read_b64_tr_b16 a[170:171], v211 offset:4544\n"
"v_mfma_f32_16x16x32_bf16 v[64:67], a[112:115], a[192:195], 0\n"
"s_waitcnt lgkmcnt(15)\n"
"ds_read_b64_tr_b16 a[172:173], v211 offset:17600\n"
"v_mfma_f32_16x16x32_bf16 v[64:67], a[116:119], a[196:199], v[64:67]\n"
"ds_read_b64_tr_b16 a[174:175], v211 offset:21952\n"
"s_add_u32 m0, s48, 40256\n"
"v_mfma_f32_16x16x32_bf16 v[68:71], a[120:123], a[192:195], 0\n"
"buffer_load_dwordx4 v220, s[28:31], s51 offen lds\n"
"v_mfma_f32_16x16x32_bf16 v[68:71], a[124:127], a[196:199], v[68:71]\n"
"v_mfma_f32_16x16x32_bf16 v[72:75], a[128:131], a[192:195], 0\n"
"s_add_u32 m0, s48, 41344\n"
"v_mfma_f32_16x16x32_bf16 v[72:75], a[132:135], a[196:199], v[72:75]\n"
"buffer_load_dwordx4 v221, s[28:31], s51 offen lds\n"
"s_add_u32 s51, s51, 128\n"
"v_mfma_f32_16x16x32_bf16 v[76:79], a[136:139], a[192:195], 0\n"
"v_mfma_f32_16x16x32_bf16 v[76:79], a[140:143], a[196:199], v[76:79]\n"
"s_waitcnt lgkmcnt(15)\n"
"v_mfma_f32_16x16x32_bf16 v[96:99], a[112:115], a[200:203], 0\n"
"s_waitcnt lgkmcnt(15)\n"
"v_mfma_f32_16x16x32_bf16 v[96:99], a[116:119], a[204:207], v[96:99]\n"
"v_mfma_f32_16x16x32_bf16 v[100:103], a[120:123], a[200:203], 0\n"
"v_mfma_f32_16x16x32_bf16 v[100:103], a[124:127], a[204:207], v[100:103]\n"
"v_mfma_f32_16x16x32_bf16 v[104:107], a[128:131], a[200:203], 0\n"
"v_mfma_f32_16x16x32_bf16 v[104:107], a[132:135], a[204:207], v[104:107]\n"
"v_mfma_f32_16x16x32_bf16 v[108:111], a[136:139], a[200:203], 0\n"
"v_mfma_f32_16x16x32_bf16 v[108:111], a[140:143], a[204:207], v[108:111]\n"
"s_waitcnt lgkmcnt(15)\n"
"v_mfma_f32_16x16x32_bf16 v[128:131], a[112:115], a[208:211], 0\n"
"s_waitcnt lgkmcnt(15)\n"
"v_mfma_f32_16x16x32_bf16 v[128:131], a[116:119], a[212:215], v[128:131]\n"
"v_mfma_f32_16x16x32_bf16 v[132:135], a[120:123], a[208:211], 0\n"
"v_mfma_f32_16x16x32_bf16 v[132:135], a[124:127], a[212:215], v[132:135]\n"
"v_mfma_f32_16x16x32_bf16 v[136:139], a[128:131], a[208:211], 0\n"
"v_mfma_f32_16x16x32_bf16 v[136:139], a[132:135], a[212:215], v[136:139]\n"
"v_mfma_f32_16x16x32_bf16 v[140:143], a[136:139], a[208:211], 0\n"
"v_mfma_f32_16x16x32_bf16 v[140:143], a[140:143], a[212:215], v[140:143]\n"
"s_waitcnt lgkmcnt(15)\n"
"v_mfma_f32_16x16x32_bf16 v[160:163], a[112:115], a[216:219], 0\n"
"s_waitcnt lgkmcnt(0)\n"
"s_waitcnt vmcnt(15)\n"
"s_barrier\n"
"v_mfma_f32_16x16x32_bf16 v[160:163], a[116:119], a[220:223], v[160:163]\n"
"s_add_u32 m0, s49, 60928\n"
"v_mfma_f32_16x16x32_bf16 v[164:167], a[120:123], a[216:219], 0\n"
"buffer_load_dwordx4 v212, s[32:35], s52 offen lds\n"
"v_mfma_f32_16x16x32_bf16 v[164:167], a[124:127], a[220:223], v[164:167]\n"
"v_mfma_f32_16x16x32_bf16 v[168:171], a[128:131], a[216:219], 0\n"
"v_mfma_f32_16x16x32_bf16 v[168:171], a[132:135], a[220:223], v[168:171]\n"
"v_mfma_f32_16x16x32_bf16 v[172:175], a[136:139], a[216:219], 0\n"
"s_add_u32 m0, s49, 62016\n"
"v_mfma_f32_16x16x32_bf16 v[172:175], a[140:143], a[220:223], v[172:175]\n"
"buffer_load_dwordx4 v213, s[32:35], s52 offen lds\n"
"v_mfma_f32_16x16x32_bf16 v[192:195], a[144:147], a[224:227], 0\n"
"v_mfma_f32_16x16x32_bf16 v[192:195], a[148:151], a[228:231], v[192:195]\n"
"v_mfma_f32_16x16x32_bf16 v[196:199], a[152:155], a[224:227], 0\n"
"v_mfma_f32_16x16x32_bf16 v[196:199], a[156:159], a[228:231], v[196:199]\n"
"s_add_u32 m0, s49, 63104\n"
"v_mfma_f32_16x16x32_bf16 v[200:203], a[160:163], a[224:227], 0\n"
"buffer_load_dwordx4 v214, s[32:35], s52 offen lds\n"
"v_mfma_f32_16x16x32_bf16 v[200:203], a[164:167], a[228:231], v[200:203]\n"
"v_mfma_f32_16x16x32_bf16 v[204:207], a[168:171], a[224:227], 0\n"
"v_mfma_f32_16x16x32_bf16 v[204:207], a[172:175], a[228:231], v[204:207]\n"
"v_mfma_f32_16x16x32_bf16 v[16:19], a[144:147], a[176:179], 0\n"
"ds_read_b128 a[224:227], v208 offset:26112\n"
"v_mfma_f32_16x16x32_bf16 v[16:19], a[148:151], a[180:183], v[16:19]\n"
"ds_read_b128 a[228:231], v208 offset:26176\n"
"s_add_u32 m0, s49, 64192\n"
"v_mfma_f32_16x16x32_bf16 v[20:23], a[152:155], a[176:179], 0\n"
"buffer_load_dwordx4 v215, s[32:35], s52 offen lds\n"
"s_add_u32 s52, s52, 262144\n"
"v_mfma_f32_16x16x32_bf16 v[20:23], a[156:159], a[180:183], v[20:23]\n"
"v_mfma_f32_16x16x32_bf16 v[24:27], a[160:163], a[176:179], 0\n"
"v_mfma_f32_16x16x32_bf16 v[24:27], a[164:167], a[180:183], v[24:27]\n"
"v_mfma_f32_16x16x32_bf16 v[28:31], a[168:171], a[176:179], 0\n"
"s_add_u32 m0, s49, 78336\n"
"v_mfma_f32_16x16x32_bf16 v[28:31], a[172:175], a[180:183], v[28:31]\n"
"buffer_load_dwordx4 v212, s[32:35], s52 offen lds\n"
"v_mfma_f32_16x16x32_bf16 v[48:51], a[144:147], a[184:187], 0\n"
"ds_read_b128 a[176:179], v208 offset:13056\n"
"v_mfma_f32_16x16x32_bf16 v[48:51], a[148:151], a[188:191], v[48:51]\n"
"ds_read_b128 a[180:183], v208 offset:13120\n"
"v_mfma_f32_16x16x32_bf16 v[52:55], a[152:155], a[184:187], 0\n"
"v_mfma_f32_16x16x32_bf16 v[52:55], a[156:159], a[188:191], v[52:55]\n"
"s_add_u32 m0, s49, 79424\n"
"v_mfma_f32_16x16x32_bf16 v[56:59], a[160:163], a[184:187], 0\n"
"buffer_load_dwordx4 v213, s[32:35], s52 offen lds\n"
"v_mfma_f32_16x16x32_bf16 v[56:59], a[164:167], a[188:191], v[56:59]\n"
"v_mfma_f32_16x16x32_bf16 v[60:63], a[168:171], a[184:187], 0\n"
"v_mfma_f32_16x16x32_bf16 v[60:63], a[172:175], a[188:191], v[60:63]\n"
"v_mfma_f32_16x16x32_bf16 v[80:83], a[144:147], a[192:195], 0\n"
"ds_read_b128 a[184:187], v208 offset:15232\n"
"v_mfma_f32_16x16x32_bf16 v[80:83], a[148:151], a[196:199], v[80:83]\n"
"ds_read_b128 a[188:191], v208 offset:15296\n"
"s_add_u32 m0, s49, 80512\n"
"v_mfma_f32_16x16x32_bf16 v[84:87], a[152:155], a[192:195], 0\n"
"buffer_load_dwordx4 v214, s[32:35], s52 offen lds\n"
"v_mfma_f32_16x16x32_bf16 v[84:87], a[156:159], a[196:199], v[84:87]\n"
"v_mfma_f32_16x16x32_bf16 v[88:91], a[160:163], a[192:195], 0\n"
"v_mfma_f32_16x16x32_bf16 v[88:91], a[164:167], a[196:199], v[88:91]\n"
"s_waitcnt vmcnt(14)\n"
"s_barrier\n"
"v_mfma_f32_16x16x32_bf16 v[92:95], a[168:171], a[192:195], 0\n"
"s_add_u32 m0, s49, 81600\n"
"ds_read_b64_tr_b16 a[112:113], v210 offset:34816\n"
"v_mfma_f32_16x16x32_bf16 v[92:95], a[172:175], a[196:199], v[92:95]\n"
"buffer_load_dwordx4 v215, s[32:35], s52 offen lds\n"
"s_add_u32 s52, s52, 262144\n"
"ds_read_b64_tr_b16 a[114:115], v210 offset:39168\n"
"v_mfma_f32_16x16x32_bf16 v[112:115], a[144:147], a[200:203], 0\n"
"ds_read_b128 a[192:195], v208 offset:17408\n"
"ds_read_b64_tr_b16 a[116:117], v210 offset:52224\n"
"v_mfma_f32_16x16x32_bf16 v[112:115], a[148:151], a[204:207], v[112:115]\n"
"ds_read_b128 a[196:199], v208 offset:17472\n"
"ds_read_b64_tr_b16 a[118:119], v210 offset:56576\n"
"v_mfma_f32_16x16x32_bf16 v[116:119], a[152:155], a[200:203], 0\n"
"ds_read_b64_tr_b16 a[120:121], v211 offset:34816\n"
"v_mfma_f32_16x16x32_bf16 v[116:119], a[156:159], a[204:207], v[116:119]\n"
"ds_read_b64_tr_b16 a[122:123], v211 offset:39168\n"
"v_mfma_f32_16x16x32_bf16 v[120:123], a[160:163], a[200:203], 0\n"
"s_add_u32 m0, s48, 133824\n"
"ds_read_b64_tr_b16 a[124:125], v211 offset:52224\n"
"v_mfma_f32_16x16x32_bf16 v[120:123], a[164:167], a[204:207], v[120:123]\n"
"buffer_load_dwordx4 v216, s[28:31], s51 offen lds\n"
"ds_read_b64_tr_b16 a[126:127], v211 offset:56576\n"
"v_mfma_f32_16x16x32_bf16 v[124:127], a[168:171], a[200:203], 0\n"
"ds_read_b64_tr_b16 a[128:129], v210 offset:34880\n"
"v_mfma_f32_16x16x32_bf16 v[124:127], a[172:175], a[204:207], v[124:127]\n"
"ds_read_b64_tr_b16 a[130:131], v210 offset:39232\n"
"v_mfma_f32_16x16x32_bf16 v[144:147], a[144:147], a[208:211], 0\n"
"ds_read_b128 a[200:203], v208 offset:19584\n"
"ds_read_b64_tr_b16 a[132:133], v210 offset:52288\n"
"v_mfma_f32_16x16x32_bf16 v[144:147], a[148:151], a[212:215], v[144:147]\n"
"ds_read_b128 a[204:207], v208 offset:19648\n"
"s_add_u32 m0, s48, 134912\n"
"ds_read_b64_tr_b16 a[134:135], v210 offset:56640\n"
"v_mfma_f32_16x16x32_bf16 v[148:151], a[152:155], a[208:211], 0\n"
"buffer_load_dwordx4 v217, s[28:31], s51 offen lds\n"
"ds_read_b64_tr_b16 a[136:137], v211 offset:34880\n"
"v_mfma_f32_16x16x32_bf16 v[148:151], a[156:159], a[212:215], v[148:151]\n"
"ds_read_b64_tr_b16 a[138:139], v211 offset:39232\n"
"v_mfma_f32_16x16x32_bf16 v[152:155], a[160:163], a[208:211], 0\n"
"ds_read_b64_tr_b16 a[140:141], v211 offset:52288\n"
"v_mfma_f32_16x16x32_bf16 v[152:155], a[164:167], a[212:215], v[152:155]\n"
"ds_read_b64_tr_b16 a[142:143], v211 offset:56640\n"
"v_mfma_f32_16x16x32_bf16 v[156:159], a[168:171], a[208:211], 0\n"
"s_add_u32 m0, s48, 136000\n"
"v_mfma_f32_16x16x32_bf16 v[156:159], a[172:175], a[212:215], v[156:159]\n"
"buffer_load_dwordx4 v218, s[28:31], s51 offen lds\n"
"v_mfma_f32_16x16x32_bf16 v[176:179], a[144:147], a[216:219], 0\n"
"ds_read_b128 a[208:211], v208 offset:21760\n"
"v_mfma_f32_16x16x32_bf16 v[176:179], a[148:151], a[220:223], v[176:179]\n"
"ds_read_b128 a[212:215], v208 offset:21824\n"
"v_mfma_f32_16x16x32_bf16 v[180:183], a[152:155], a[216:219], 0\n"
"v_mfma_f32_16x16x32_bf16 v[180:183], a[156:159], a[220:223], v[180:183]\n"
"v_mfma_f32_16x16x32_bf16 v[184:187], a[160:163], a[216:219], 0\n"
"s_add_u32 m0, s48, 137088\n"
"v_mfma_f32_16x16x32_bf16 v[184:187], a[164:167], a[220:223], v[184:187]\n"
"buffer_load_dwordx4 v219, s[28:31], s51 offen lds\n"
"v_mfma_f32_16x16x32_bf16 v[188:191], a[168:171], a[216:219], 0\n"
"s_waitcnt lgkmcnt(2)\n"
"v_mfma_f32_16x16x32_bf16 v[188:191], a[172:175], a[220:223], v[188:191]\n"
"v_mfma_f32_16x16x32_bf16 v[0:3], a[112:115], a[176:179], v[0:3]\n"
"ds_read_b128 a[216:219], v208 offset:23936\n"
"v_mfma_f32_16x16x32_bf16 v[0:3], a[116:119], a[180:183], v[0:3]\n"
"ds_read_b128 a[220:223], v208 offset:24000\n"
"s_add_u32 m0, s48, 138176\n"
"v_mfma_f32_16x16x32_bf16 v[4:7], a[120:123], a[176:179], v[4:7]\n"
"ds_read_b64_tr_b16 a[144:145], v210 offset:34944\n"
"buffer_load_dwordx4 v220, s[28:31], s51 offen lds\n"
"v_mfma_f32_16x16x32_bf16 v[4:7], a[124:127], a[180:183], v[4:7]\n"
"ds_read_b64_tr_b16 a[146:147], v210 offset:39296\n"
"v_mfma_f32_16x16x32_bf16 v[8:11], a[128:131], a[176:179], v[8:11]\n"
"ds_read_b64_tr_b16 a[148:149], v210 offset:52352\n"
"v_mfma_f32_16x16x32_bf16 v[8:11], a[132:135], a[180:183], v[8:11]\n"
"ds_read_b64_tr_b16 a[150:151], v210 offset:56704\n"
"v_mfma_f32_16x16x32_bf16 v[12:15], a[136:139], a[176:179], v[12:15]\n"
"ds_read_b64_tr_b16 a[152:153], v211 offset:34944\n"
"s_add_u32 m0, s48, 139264\n"
"v_mfma_f32_16x16x32_bf16 v[12:15], a[140:143], a[180:183], v[12:15]\n"
"ds_read_b64_tr_b16 a[154:155], v211 offset:39296\n"
"buffer_load_dwordx4 v221, s[28:31], s51 offen lds\n"
"s_add_u32 s51, s51, 128\n"
"v_mfma_f32_16x16x32_bf16 v[32:35], a[112:115], a[184:187], v[32:35]\n"
"ds_read_b64_tr_b16 a[156:157], v211 offset:52352\n"
"v_mfma_f32_16x16x32_bf16 v[32:35], a[116:119], a[188:191], v[32:35]\n"
"ds_read_b64_tr_b16 a[158:159], v211 offset:56704\n"
"v_mfma_f32_16x16x32_bf16 v[36:39], a[120:123], a[184:187], v[36:39]\n"
"ds_read_b64_tr_b16 a[160:161], v210 offset:35008\n"
"v_mfma_f32_16x16x32_bf16 v[36:39], a[124:127], a[188:191], v[36:39]\n"
"ds_read_b64_tr_b16 a[162:163], v210 offset:39360\n"
"v_mfma_f32_16x16x32_bf16 v[40:43], a[128:131], a[184:187], v[40:43]\n"
"ds_read_b64_tr_b16 a[164:165], v210 offset:52416\n"
"v_mfma_f32_16x16x32_bf16 v[40:43], a[132:135], a[188:191], v[40:43]\n"
"ds_read_b64_tr_b16 a[166:167], v210 offset:56768\n"
"buffer_load_dwordx4 a[0:3], v250, s[36:39], s53 offen nt\n"
"v_mfma_f32_16x16x32_bf16 v[44:47], a[136:139], a[184:187], v[44:47]\n"
"ds_read_b64_tr_b16 a[168:169], v211 offset:35008\n"
"v_mfma_f32_16x16x32_bf16 v[44:47], a[140:143], a[188:191], v[44:47]\n"
"ds_read_b64_tr_b16 a[170:171], v211 offset:39360\n"
"v_mfma_f32_16x16x32_bf16 v[64:67], a[112:115], a[192:195], v[64:67]\n"
"ds_read_b64_tr_b16 a[172:173], v211 offset:52416\n"
"v_mfma_f32_16x16x32_bf16 v[64:67], a[116:119], a[196:199], v[64:67]\n"
"ds_read_b64_tr_b16 a[174:175], v211 offset:56768\n"
"v_mfma_f32_16x16x32_bf16 v[68:71], a[120:123], a[192:195], v[68:71]\n"
"buffer_load_dwordx4 a[4:7], v250, s[36:39], s53 offen offset:256 nt\n"
"v_mfma_f32_16x16x32_bf16 v[68:71], a[124:127], a[196:199], v[68:71]\n"
"v_mfma_f32_16x16x32_bf16 v[72:75], a[128:131], a[192:195], v[72:75]\n"
"v_mfma_f32_16x16x32_bf16 v[72:75], a[132:135], a[196:199], v[72:75]\n"
"v_mfma_f32_16x16x32_bf16 v[76:79], a[136:139], a[192:195], v[76:79]\n"
"v_mfma_f32_16x16x32_bf16 v[76:79], a[140:143], a[196:199], v[76:79]\n"
"v_mfma_f32_16x16x32_bf16 v[96:99], a[112:115], a[200:203], v[96:99]\n"
"s_waitcnt lgkmcnt(0)\n"
"s_waitcnt vmcnt(16)\n"
"s_barrier\n"
"v_mfma_f32_16x16x32_bf16 v[96:99], a[116:119], a[204:207], v[96:99]\n"
"s_add_u32 m0, s49, 95744\n"
"v_mfma_f32_16x16x32_bf16 v[100:103], a[120:123], a[200:203], v[100:103]\n"
"buffer_load_dwordx4 v212, s[32:35], s52 offen lds\n"
"v_mfma_f32_16x16x32_bf16 v[100:103], a[124:127], a[204:207], v[100:103]\n"
"v_mfma_f32_16x16x32_bf16 v[104:107], a[128:131], a[200:203], v[104:107]\n"
"v_mfma_f32_16x16x32_bf16 v[104:107], a[132:135], a[204:207], v[104:107]\n"
"v_mfma_f32_16x16x32_bf16 v[108:111], a[136:139], a[200:203], v[108:111]\n"
"v_mfma_f32_16x16x32_bf16 v[108:111], a[140:143], a[204:207], v[108:111]\n"
"s_add_u32 m0, s49, 96832\n"
"v_mfma_f32_16x16x32_bf16 v[128:131], a[112:115], a[208:211], v[128:131]\n"
"buffer_load_dwordx4 v213, s[32:35], s52 offen lds\n"
"v_mfma_f32_16x16x32_bf16 v[128:131], a[116:119], a[212:215], v[128:131]\n"
"v_mfma_f32_16x16x32_bf16 v[132:135], a[120:123], a[208:211], v[132:135]\n"
"v_mfma_f32_16x16x32_bf16 v[132:135], a[124:127], a[212:215], v[132:135]\n"
"v_mfma_f32_16x16x32_bf16 v[136:139], a[128:131], a[208:211], v[136:139]\n"
"v_mfma_f32_16x16x32_bf16 v[136:139], a[132:135], a[212:215], v[136:139]\n"
"s_add_u32 m0, s49, 97920\n"
"v_mfma_f32_16x16x32_bf16 v[140:143], a[136:139], a[208:211], v[140:143]\n"
"buffer_load_dwordx4 v214, s[32:35], s52 offen lds\n"
"v_mfma_f32_16x16x32_bf16 v[140:143], a[140:143], a[212:215], v[140:143]\n"
"v_mfma_f32_16x16x32_bf16 v[160:163], a[112:115], a[216:219], v[160:163]\n"
"v_mfma_f32_16x16x32_bf16 v[160:163], a[116:119], a[220:223], v[160:163]\n"
"v_mfma_f32_16x16x32_bf16 v[164:167], a[120:123], a[216:219], v[164:167]\n"
"v_mfma_f32_16x16x32_bf16 v[164:167], a[124:127], a[220:223], v[164:167]\n"
"s_add_u32 m0, s49, 99008\n"
"v_mfma_f32_16x16x32_bf16 v[168:171], a[128:131], a[216:219], v[168:171]\n"
"buffer_load_dwordx4 v215, s[32:35], s52 offen lds\n"
"s_add_u32 s52, s52, 262144\n"
"v_mfma_f32_16x16x32_bf16 v[168:171], a[132:135], a[220:223], v[168:171]\n"
"v_mfma_f32_16x16x32_bf16 v[172:175], a[136:139], a[216:219], v[172:175]\n"
"v_mfma_f32_16x16x32_bf16 v[172:175], a[140:143], a[220:223], v[172:175]\n"
"v_mfma_f32_16x16x32_bf16 v[192:195], a[144:147], a[224:227], v[192:195]\n"
"v_mfma_f32_16x16x32_bf16 v[192:195], a[148:151], a[228:231], v[192:195]\n"
"s_add_u32 m0, s49, 113152\n"
"v_mfma_f32_16x16x32_bf16 v[196:199], a[152:155], a[224:227], v[196:199]\n"
"buffer_load_dwordx4 v212, s[32:35], s52 offen lds\n"
"v_mfma_f32_16x16x32_bf16 v[196:199], a[156:159], a[228:231], v[196:199]\n"
"v_mfma_f32_16x16x32_bf16 v[200:203], a[160:163], a[224:227], v[200:203]\n"
"v_mfma_f32_16x16x32_bf16 v[200:203], a[164:167], a[228:231], v[200:203]\n"
"v_mfma_f32_16x16x32_bf16 v[204:207], a[168:171], a[224:227], v[204:207]\n"
"v_mfma_f32_16x16x32_bf16 v[204:207], a[172:175], a[228:231], v[204:207]\n"
"s_add_u32 m0, s49, 114240\n"
"v_mfma_f32_16x16x32_bf16 v[16:19], a[144:147], a[176:179], v[16:19]\n"
"ds_read_b128 a[224:227], v208 offset:54400\n"
"buffer_load_dwordx4 v213, s[32:35], s52 offen lds\n"
"v_mfma_f32_16x16x32_bf16 v[16:19], a[148:151], a[180:183], v[16:19]\n"
"ds_read_b128 a[228:231], v208 offset:54464\n"
"v_mfma_f32_16x16x32_bf16 v[20:23], a[152:155], a[176:179], v[20:23]\n"
"v_mfma_f32_16x16x32_bf16 v[20:23], a[156:159], a[180:183], v[20:23]\n"
"v_mfma_f32_16x16x32_bf16 v[24:27], a[160:163], a[176:179], v[24:27]\n"
"v_mfma_f32_16x16x32_bf16 v[24:27], a[164:167], a[180:183], v[24:27]\n"
"s_add_u32 m0, s49, 115328\n"
"v_mfma_f32_16x16x32_bf16 v[28:31], a[168:171], a[176:179], v[28:31]\n"
"buffer_load_dwordx4 v214, s[32:35], s52 offen lds\n"
"v_mfma_f32_16x16x32_bf16 v[28:31], a[172:175], a[180:183], v[28:31]\n"
"v_mfma_f32_16x16x32_bf16 v[48:51], a[144:147], a[184:187], v[48:51]\n"
"ds_read_b128 a[176:179], v208 offset:41344\n"
"v_mfma_f32_16x16x32_bf16 v[48:51], a[148:151], a[188:191], v[48:51]\n"
"ds_read_b128 a[180:183], v208 offset:41408\n"
"v_mfma_f32_16x16x32_bf16 v[52:55], a[152:155], a[184:187], v[52:55]\n"
"v_mfma_f32_16x16x32_bf16 v[52:55], a[156:159], a[188:191], v[52:55]\n"
"s_add_u32 m0, s49, 116416\n"
"v_mfma_f32_16x16x32_bf16 v[56:59], a[160:163], a[184:187], v[56:59]\n"
"buffer_load_dwordx4 v215, s[32:35], s52 offen lds\n"
"s_add_u32 s52, s52, 262144\n"
"v_mfma_f32_16x16x32_bf16 v[56:59], a[164:167], a[188:191], v[56:59]\n"
"v_mfma_f32_16x16x32_bf16 v[60:63], a[168:171], a[184:187], v[60:63]\n"
"v_mfma_f32_16x16x32_bf16 v[60:63], a[172:175], a[188:191], v[60:63]\n"
"v_mfma_f32_16x16x32_bf16 v[80:83], a[144:147], a[192:195], v[80:83]\n"
"ds_read_b128 a[184:187], v208 offset:43520\n"
"v_mfma_f32_16x16x32_bf16 v[80:83], a[148:151], a[196:199], v[80:83]\n"
"ds_read_b128 a[188:191], v208 offset:43584\n"
"s_add_u32 m0, s48, 7616\n"
"v_mfma_f32_16x16x32_bf16 v[84:87], a[152:155], a[192:195], v[84:87]\n"
"buffer_load_dwordx4 v216, s[28:31], s51 offen lds\n"
"v_mfma_f32_16x16x32_bf16 v[84:87], a[156:159], a[196:199], v[84:87]\n"
"v_mfma_f32_16x16x32_bf16 v[88:91], a[160:163], a[192:195], v[88:91]\n"
"v_mfma_f32_16x16x32_bf16 v[88:91], a[164:167], a[196:199], v[88:91]\n"
"s_waitcnt vmcnt(17)\n"
"s_barrier\n"
"v_mfma_f32_16x16x32_bf16 v[92:95], a[168:171], a[192:195], v[92:95]\n"
"ds_read_b64_tr_b16 a[112:113], v210\n"
"v_mfma_f32_16x16x32_bf16 v[92:95], a[172:175], a[196:199], v[92:95]\n"
"s_add_u32 m0, s48, 8704\n"
"ds_read_b64_tr_b16 a[114:115], v210 offset:4352\n"
"v_mfma_f32_16x16x32_bf16 v[112:115], a[144:147], a[200:203], v[112:115]\n"
"ds_read_b128 a[192:195], v208 offset:45696\n"
"buffer_load_dwordx4 v217, s[28:31], s51 offen lds\n"
"ds_read_b64_tr_b16 a[116:117], v210 offset:17408\n"
"v_mfma_f32_16x16x32_bf16 v[112:115], a[148:151], a[204:207], v[112:115]\n"
"ds_read_b128 a[196:199], v208 offset:45760\n"
"ds_read_b64_tr_b16 a[118:119], v210 offset:21760\n"
"v_mfma_f32_16x16x32_bf16 v[116:119], a[152:155], a[200:203], v[116:119]\n"
"ds_read_b64_tr_b16 a[120:121], v211\n"
"v_mfma_f32_16x16x32_bf16 v[116:119], a[156:159], a[204:207], v[116:119]\n"
"ds_read_b64_tr_b16 a[122:123], v211 offset:4352\n"
"v_mfma_f32_16x16x32_bf16 v[120:123], a[160:163], a[200:203], v[120:123]\n"
"ds_read_b64_tr_b16 a[124:125], v211 offset:17408\n"
"v_mfma_f32_16x16x32_bf16 v[120:123], a[164:167], a[204:207], v[120:123]\n"
"s_add_u32 m0, s48, 9792\n"
"ds_read_b64_tr_b16 a[126:127], v211 offset:21760\n"
"v_mfma_f32_16x16x32_bf16 v[124:127], a[168:171], a[200:203], v[124:127]\n"
"buffer_load_dwordx4 v218, s[28:31], s51 offen lds\n"
"ds_read_b64_tr_b16 a[128:129], v210 offset:64\n"
"v_mfma_f32_16x16x32_bf16 v[124:127], a[172:175], a[204:207], v[124:127]\n"
"ds_read_b64_tr_b16 a[130:131], v210 offset:4416\n"
"v_mfma_f32_16x16x32_bf16 v[144:147], a[144:147], a[208:211], v[144:147]\n"
"ds_read_b128 a[200:203], v208 offset:47872\n"
"ds_read_b64_tr_b16 a[132:133], v210 offset:17472\n"
"v_mfma_f32_16x16x32_bf16 v[144:147], a[148:151], a[212:215], v[144:147]\n"
"ds_read_b128 a[204:207], v208 offset:47936\n"
"ds_read_b64_tr_b16 a[134:135], v210 offset:21824\n"
"v_mfma_f32_16x16x32_bf16 v[148:151], a[152:155], a[208:211], v[148:151]\n"
"ds_read_b64_tr_b16 a[136:137], v211 offset:64\n"
"v_mfma_f32_16x16x32_bf16 v[148:151], a[156:159], a[212:215], v[148:151]\n"
"s_add_u32 m0, s48, 10880\n"
"ds_read_b64_tr_b16 a[138:139], v211 offset:4416\n"
"v_mfma_f32_16x16x32_bf16 v[152:155], a[160:163], a[208:211], v[152:155]\n"
"buffer_load_dwordx4 v219, s[28:31], s51 offen lds\n"
"ds_read_b64_tr_b16 a[140:141], v211 offset:17472\n"
"v_mfma_f32_16x16x32_bf16 v[152:155], a[164:167], a[212:215], v[152:155]\n"
"ds_read_b64_tr_b16 a[142:143], v211 offset:21824\n"
"v_mfma_f32_16x16x32_bf16 v[156:159], a[168:171], a[208:211], v[156:159]\n"
"v_mfma_f32_16x16x32_bf16 v[156:159], a[172:175], a[212:215], v[156:159]\n"
"v_mfma_f32_16x16x32_bf16 v[176:179], a[144:147], a[216:219], v[176:179]\n"
"ds_read_b128 a[208:211], v208 offset:50048\n"
"v_mfma_f32_16x16x32_bf16 v[176:179], a[148:151], a[220:223], v[176:179]\n"
"ds_read_b128 a[212:215], v208 offset:50112\n"
"s_add_u32 m0, s48, 11968\n"
"v_mfma_f32_16x16x32_bf16 v[180:183], a[152:155], a[216:219], v[180:183]\n"
"buffer_load_dwordx4 v220, s[28:31], s51 offen lds\n"
"v_mfma_f32_16x16x32_bf16 v[180:183], a[156:159], a[220:223], v[180:183]\n"
"v_mfma_f32_16x16x32_bf16 v[184:187], a[160:163], a[216:219], v[184:187]\n"
"v_mfma_f32_16x16x32_bf16 v[184:187], a[164:167], a[220:223], v[184:187]\n"
"v_mfma_f32_16x16x32_bf16 v[188:191], a[168:171], a[216:219], v[188:191]\n"
"s_waitcnt lgkmcnt(2)\n"
"v_mfma_f32_16x16x32_bf16 v[188:191], a[172:175], a[220:223], v[188:191]\n"
"v_mfma_f32_16x16x32_bf16 v[0:3], a[112:115], a[176:179], v[0:3]\n"
"ds_read_b128 a[216:219], v208 offset:52224\n"
"s_add_u32 m0, s48, 13056\n"
"v_mfma_f32_16x16x32_bf16 v[0:3], a[116:119], a[180:183], v[0:3]\n"
"ds_read_b128 a[220:223], v208 offset:52288\n"
"buffer_load_dwordx4 v221, s[28:31], s51 offen lds\n"
"s_add_u32 s51, s51, 128\n"
"v_mfma_f32_16x16x32_bf16 v[4:7], a[120:123], a[176:179], v[4:7]\n"
"ds_read_b64_tr_b16 a[144:145], v210 offset:128\n"
"v_mfma_f32_16x16x32_bf16 v[4:7], a[124:127], a[180:183], v[4:7]\n"
"ds_read_b64_tr_b16 a[146:147], v210 offset:4480\n"
"v_mfma_f32_16x16x32_bf16 v[8:11], a[128:131], a[176:179], v[8:11]\n"
"ds_read_b64_tr_b16 a[148:149], v210 offset:17536\n"
"v_mfma_f32_16x16x32_bf16 v[8:11], a[132:135], a[180:183], v[8:11]\n"
"ds_read_b64_tr_b16 a[150:151], v210 offset:21888\n"
"v_mfma_f32_16x16x32_bf16 v[12:15], a[136:139], a[176:179], v[12:15]\n"
"ds_read_b64_tr_b16 a[152:153], v211 offset:128\n"
"buffer_load_dwordx4 a[8:11], v250, s[36:39], s53 offen offset:512 nt\n"
"v_mfma_f32_16x16x32_bf16 v[12:15], a[140:143], a[180:183], v[12:15]\n"
"ds_read_b64_tr_b16 a[154:155], v211 offset:4480\n"
"v_mfma_f32_16x16x32_bf16 v[32:35], a[112:115], a[184:187], v[32:35]\n"
"ds_read_b64_tr_b16 a[156:157], v211 offset:17536\n"
"v_mfma_f32_16x16x32_bf16 v[32:35], a[116:119], a[188:191], v[32:35]\n"
"ds_read_b64_tr_b16 a[158:159], v211 offset:21888\n"
"v_mfma_f32_16x16x32_bf16 v[36:39], a[120:123], a[184:187], v[36:39]\n"
"ds_read_b64_tr_b16 a[160:161], v210 offset:192\n"
"v_mfma_f32_16x16x32_bf16 v[36:39], a[124:127], a[188:191], v[36:39]\n"
"ds_read_b64_tr_b16 a[162:163], v210 offset:4544\n"
"v_mfma_f32_16x16x32_bf16 v[40:43], a[128:131], a[184:187], v[40:43]\n"
"ds_read_b64_tr_b16 a[164:165], v210 offset:17600\n"
"buffer_load_dwordx4 a[12:15], v250, s[36:39], s53 offen offset:768 nt\n"
"v_mfma_f32_16x16x32_bf16 v[40:43], a[132:135], a[188:191], v[40:43]\n"
"ds_read_b64_tr_b16 a[166:167], v210 offset:21952\n"
"v_mfma_f32_16x16x32_bf16 v[44:47], a[136:139], a[184:187], v[44:47]\n"
"ds_read_b64_tr_b16 a[168:169], v211 offset:192\n"
"v_mfma_f32_16x16x32_bf16 v[44:47], a[140:143], a[188:191], v[44:47]\n"
"ds_read_b64_tr_b16 a[170:171], v211 offset:4544\n"
"v_mfma_f32_16x16x32_bf16 v[64:67], a[112:115], a[192:195], v[64:67]\n"
"ds_read_b64_tr_b16 a[172:173], v211 offset:17600\n"
"v_mfma_f32_16x16x32_bf16 v[64:67], a[116:119], a[196:199], v[64:67]\n"
"ds_read_b64_tr_b16 a[174:175], v211 offset:21952\n"
"v_mfma_f32_16x16x32_bf16 v[68:71], a[120:123], a[192:195], v[68:71]\n"
"v_mfma_f32_16x16x32_bf16 v[68:71], a[124:127], a[196:199], v[68:71]\n"
"v_mfma_f32_16x16x32_bf16 v[72:75], a[128:131], a[192:195], v[72:75]\n"
"v_mfma_f32_16x16x32_bf16 v[72:75], a[132:135], a[196:199], v[72:75]\n"
"v_mfma_f32_16x16x32_bf16 v[76:79], a[136:139], a[192:195], v[76:79]\n"
"v_mfma_f32_16x16x32_bf16 v[76:79], a[140:143], a[196:199], v[76:79]\n"
"v_mfma_f32_16x16x32_bf16 v[96:99], a[112:115], a[200:203], v[96:99]\n"
"s_waitcnt lgkmcnt(0)\n"
"s_waitcnt vmcnt(18)\n"
"s_barrier\n"
"v_mfma_f32_16x16x32_bf16 v[96:99], a[116:119], a[204:207], v[96:99]\n"
"s_add_u32 m0, s49, 60928\n"
"v_mfma_f32_16x16x32_bf16 v[100:103], a[120:123], a[200:203], v[100:103]\n"
"buffer_load_dwordx4 v212, s[32:35], s52 offen lds\n"
"v_mfma_f32_16x16x32_bf16 v[100:103], a[124:127], a[204:207], v[100:103]\n"
"v_mfma_f32_16x16x32_bf16 v[104:107], a[128:131], a[200:203], v[104:107]\n"
"v_mfma_f32_16x16x32_bf16 v[104:107], a[132:135], a[204:207], v[104:107]\n"
"v_mfma_f32_16x16x32_bf16 v[108:111], a[136:139], a[200:203], v[108:111]\n"
"v_mfma_f32_16x16x32_bf16 v[108:111], a[140:143], a[204:207], v[108:111]\n"
"s_add_u32 m0, s49, 62016\n"
"v_mfma_f32_16x16x32_bf16 v[128:131], a[112:115], a[208:211], v[128:131]\n"
"buffer_load_dwordx4 v213, s[32:35], s52 offen lds\n"
"v_mfma_f32_16x16x32_bf16 v[128:131], a[116:119], a[212:215], v[128:131]\n"
"v_mfma_f32_16x16x32_bf16 v[132:135], a[120:123], a[208:211], v[132:135]\n"
"v_mfma_f32_16x16x32_bf16 v[132:135], a[124:127], a[212:215], v[132:135]\n"
"v_mfma_f32_16x16x32_bf16 v[136:139], a[128:131], a[208:211], v[136:139]\n"
"v_mfma_f32_16x16x32_bf16 v[136:139], a[132:135], a[212:215], v[136:139]\n"
"s_add_u32 m0, s49, 63104\n"
"v_mfma_f32_16x16x32_bf16 v[140:143], a[136:139], a[208:211], v[140:143]\n"
"buffer_load_dwordx4 v214, s[32:35], s52 offen lds\n"
"v_mfma_f32_16x16x32_bf16 v[140:143], a[140:143], a[212:215], v[140:143]\n"
"v_mfma_f32_16x16x32_bf16 v[160:163], a[112:115], a[216:219], v[160:163]\n"
"v_mfma_f32_16x16x32_bf16 v[160:163], a[116:119], a[220:223], v[160:163]\n"
"v_mfma_f32_16x16x32_bf16 v[164:167], a[120:123], a[216:219], v[164:167]\n"
"v_mfma_f32_16x16x32_bf16 v[164:167], a[124:127], a[220:223], v[164:167]\n"
"s_add_u32 m0, s49, 64192\n"
"v_mfma_f32_16x16x32_bf16 v[168:171], a[128:131], a[216:219], v[168:171]\n"
"buffer_load_dwordx4 v215, s[32:35], s52 offen lds\n"
"s_add_u32 s52, s52, 262144\n"
"v_mfma_f32_16x16x32_bf16 v[168:171], a[132:135], a[220:223], v[168:171]\n"
"v_mfma_f32_16x16x32_bf16 v[172:175], a[136:139], a[216:219], v[172:175]\n"
"v_mfma_f32_16x16x32_bf16 v[172:175], a[140:143], a[220:223], v[172:175]\n"
"v_mfma_f32_16x16x32_bf16 v[192:195], a[144:147], a[224:227], v[192:195]\n"
"v_mfma_f32_16x16x32_bf16 v[192:195], a[148:151], a[228:231], v[192:195]\n"
"s_add_u32 m0, s49, 78336\n"
"v_mfma_f32_16x16x32_bf16 v[196:199], a[152:155], a[224:227], v[196:199]\n"
"buffer_load_dwordx4 v212, s[32:35], s52 offen lds\n"
"v_mfma_f32_16x16x32_bf16 v[196:199], a[156:159], a[228:231], v[196:199]\n"
"v_mfma_f32_16x16x32_bf16 v[200:203], a[160:163], a[224:227], v[200:203]\n"
"v_mfma_f32_16x16x32_bf16 v[200:203], a[164:167], a[228:231], v[200:203]\n"
"v_mfma_f32_16x16x32_bf16 v[204:207], a[168:171], a[224:227], v[204:207]\n"
"v_mfma_f32_16x16x32_bf16 v[204:207], a[172:175], a[228:231], v[204:207]\n"
"s_add_u32 m0, s49, 79424\n"
"v_mfma_f32_16x16x32_bf16 v[16:19], a[144:147], a[176:179], v[16:19]\n"
"ds_read_b128 a[224:227], v209 offset:26112\n"
"buffer_load_dwordx4 v213, s[32:35], s52 offen lds\n"
"v_mfma_f32_16x16x32_bf16 v[16:19], a[148:151], a[180:183], v[16:19]\n"
"ds_read_b128 a[228:231], v209 offset:26176\n"
"v_mfma_f32_16x16x32_bf16 v[20:23], a[152:155], a[176:179], v[20:23]\n"
"v_mfma_f32_16x16x32_bf16 v[20:23], a[156:159], a[180:183], v[20:23]\n"
"v_mfma_f32_16x16x32_bf16 v[24:27], a[160:163], a[176:179], v[24:27]\n"
"v_mfma_f32_16x16x32_bf16 v[24:27], a[164:167], a[180:183], v[24:27]\n"
"s_add_u32 m0, s49, 80512\n"
"v_mfma_f32_16x16x32_bf16 v[28:31], a[168:171], a[176:179], v[28:31]\n"
"buffer_load_dwordx4 v214, s[32:35], s52 offen lds\n"
"v_mfma_f32_16x16x32_bf16 v[28:31], a[172:175], a[180:183], v[28:31]\n"
"v_mfma_f32_16x16x32_bf16 v[48:51], a[144:147], a[184:187], v[48:51]\n"
"ds_read_b128 a[176:179], v209 offset:13056\n"
"v_mfma_f32_16x16x32_bf16 v[48:51], a[148:151], a[188:191], v[48:51]\n"
"ds_read_b128 a[180:183], v209 offset:13120\n"
"v_mfma_f32_16x16x32_bf16 v[52:55], a[152:155], a[184:187], v[52:55]\n"
"v_mfma_f32_16x16x32_bf16 v[52:55], a[156:159], a[188:191], v[52:55]\n"
"s_add_u32 m0, s49, 81600\n"
"v_mfma_f32_16x16x32_bf16 v[56:59], a[160:163], a[184:187], v[56:59]\n"
"buffer_load_dwordx4 v215, s[32:35], s52 offen lds\n"
"s_add_u32 s52, s52, 262144\n"
"v_mfma_f32_16x16x32_bf16 v[56:59], a[164:167], a[188:191], v[56:59]\n"
"v_mfma_f32_16x16x32_bf16 v[60:63], a[168:171], a[184:187], v[60:63]\n"
"v_mfma_f32_16x16x32_bf16 v[60:63], a[172:175], a[188:191], v[60:63]\n"
"v_mfma_f32_16x16x32_bf16 v[80:83], a[144:147], a[192:195], v[80:83]\n"
"ds_read_b128 a[184:187], v209 offset:15232\n"
"v_mfma_f32_16x16x32_bf16 v[80:83], a[148:151], a[196:199], v[80:83]\n"
"ds_read_b128 a[188:191], v209 offset:15296\n"
"s_add_u32 m0, s48, 35904\n"
"v_mfma_f32_16x16x32_bf16 v[84:87], a[152:155], a[192:195], v[84:87]\n"
"buffer_load_dwordx4 v216, s[28:31], s51 offen lds\n"
"v_mfma_f32_16x16x32_bf16 v[84:87], a[156:159], a[196:199], v[84:87]\n"
"v_mfma_f32_16x16x32_bf16 v[88:91], a[160:163], a[192:195], v[88:91]\n"
"v_mfma_f32_16x16x32_bf16 v[88:91], a[164:167], a[196:199], v[88:91]\n"
"s_waitcnt vmcnt(17)\n"
"s_barrier\n"
"v_mfma_f32_16x16x32_bf16 v[92:95], a[168:171], a[192:195], v[92:95]\n"
"ds_read_b64_tr_b16 a[112:113], v210 offset:34816\n"
"v_mfma_f32_16x16x32_bf16 v[92:95], a[172:175], a[196:199], v[92:95]\n"
"s_add_u32 m0, s48, 36992\n"
"ds_read_b64_tr_b16 a[114:115], v210 offset:39168\n"
"v_mfma_f32_16x16x32_bf16 v[112:115], a[144:147], a[200:203], v[112:115]\n"
"ds_read_b128 a[192:195], v209 offset:17408\n"
"buffer_load_dwordx4 v217, s[28:31], s51 offen lds\n"
"ds_read_b64_tr_b16 a[116:117], v210 offset:52224\n"
"v_mfma_f32_16x16x32_bf16 v[112:115], a[148:151], a[204:207], v[112:115]\n"
"ds_read_b128 a[196:199], v209 offset:17472\n"
"ds_read_b64_tr_b16 a[118:119], v210 offset:56576\n"
"v_mfma_f32_16x16x32_bf16 v[116:119], a[152:155], a[200:203], v[116:119]\n"
"ds_read_b64_tr_b16 a[120:121], v211 offset:34816\n"
"v_mfma_f32_16x16x32_bf16 v[116:119], a[156:159], a[204:207], v[116:119]\n"
"ds_read_b64_tr_b16 a[122:123], v211 offset:39168\n"
"v_mfma_f32_16x16x32_bf16 v[120:123], a[160:163], a[200:203], v[120:123]\n"
"ds_read_b64_tr_b16 a[124:125], v211 offset:52224\n"
"v_mfma_f32_16x16x32_bf16 v[120:123], a[164:167], a[204:207], v[120:123]\n"
"s_add_u32 m0, s48, 38080\n"
"ds_read_b64_tr_b16 a[126:127], v211 offset:56576\n"
"v_mfma_f32_16x16x32_bf16 v[124:127], a[168:171], a[200:203], v[124:127]\n"
"buffer_load_dwordx4 v218, s[28:31], s51 offen lds\n"
"ds_read_b64_tr_b16 a[128:129], v210 offset:34880\n"
"v_mfma_f32_16x16x32_bf16 v[124:127], a[172:175], a[204:207], v[124:127]\n"
"ds_read_b64_tr_b16 a[130:131], v210 offset:39232\n"
"v_mfma_f32_16x16x32_bf16 v[144:147], a[144:147], a[208:211], v[144:147]\n"
"ds_read_b128 a[200:203], v209 offset:19584\n"
"ds_read_b64_tr_b16 a[132:133], v210 offset:52288\n"
"v_mfma_f32_16x16x32_bf16 v[144:147], a[148:151], a[212:215], v[144:147]\n"
"ds_read_b128 a[204:207], v209 offset:19648\n"
"ds_read_b64_tr_b16 a[134:135], v210 offset:56640\n"
"v_mfma_f32_16x16x32_bf16 v[148:151], a[152:155], a[208:211], v[148:151]\n"
"ds_read_b64_tr_b16 a[136:137], v211 offset:34880\n"
"v_mfma_f32_16x16x32_bf16 v[148:151], a[156:159], a[212:215], v[148:151]\n"
"s_add_u32 m0, s48, 39168\n"
"ds_read_b64_tr_b16 a[138:139], v211 offset:39232\n"
"v_mfma_f32_16x16x32_bf16 v[152:155], a[160:163], a[208:211], v[152:155]\n"
"buffer_load_dwordx4 v219, s[28:31], s51 offen lds\n"
"ds_read_b64_tr_b16 a[140:141], v211 offset:52288\n"
"v_mfma_f32_16x16x32_bf16 v[152:155], a[164:167], a[212:215], v[152:155]\n"
"ds_read_b64_tr_b16 a[142:143], v211 offset:56640\n"
"v_mfma_f32_16x16x32_bf16 v[156:159], a[168:171], a[208:211], v[156:159]\n"
"v_mfma_f32_16x16x32_bf16 v[156:159], a[172:175], a[212:215], v[156:159]\n"
"v_mfma_f32_16x16x32_bf16 v[176:179], a[144:147], a[216:219], v[176:179]\n"
"ds_read_b128 a[208:211], v209 offset:21760\n"
"v_mfma_f32_16x16x32_bf16 v[176:179], a[148:151], a[220:223], v[176:179]\n"
"ds_read_b128 a[212:215], v209 offset:21824\n"
"s_add_u32 m0, s48, 40256\n"
"v_mfma_f32_16x16x32_bf16 v[180:183], a[152:155], a[216:219], v[180:183]\n"
"buffer_load_dwordx4 v220, s[28:31], s51 offen lds\n"
"v_mfma_f32_16x16x32_bf16 v[180:183], a[156:159], a[220:223], v[180:183]\n"
"v_mfma_f32_16x16x32_bf16 v[184:187], a[160:163], a[216:219], v[184:187]\n"
"v_mfma_f32_16x16x32_bf16 v[184:187], a[164:167], a[220:223], v[184:187]\n"
"v_mfma_f32_16x16x32_bf16 v[188:191], a[168:171], a[216:219], v[188:191]\n"
"s_waitcnt lgkmcnt(2)\n"
"v_mfma_f32_16x16x32_bf16 v[188:191], a[172:175], a[220:223], v[188:191]\n"
"v_mfma_f32_16x16x32_bf16 v[0:3], a[112:115], a[176:179], v[0:3]\n"
"ds_read_b128 a[216:219], v209 offset:23936\n"
"s_add_u32 m0, s48, 41344\n"
"v_mfma_f32_16x16x32_bf16 v[0:3], a[116:119], a[180:183], v[0:3]\n"
"ds_read_b128 a[220:223], v209 offset:24000\n"
"buffer_load_dwordx4 v221, s[28:31], s51 offen lds\n"
"s_add_u32 s51, s51, 128\n"
"v_mfma_f32_16x16x32_bf16 v[4:7], a[120:123], a[176:179], v[4:7]\n"
"ds_read_b64_tr_b16 a[144:145], v210 offset:34944\n"
"v_mfma_f32_16x16x32_bf16 v[4:7], a[124:127], a[180:183], v[4:7]\n"
"ds_read_b64_tr_b16 a[146:147], v210 offset:39296\n"
"v_mfma_f32_16x16x32_bf16 v[8:11], a[128:131], a[176:179], v[8:11]\n"
"ds_read_b64_tr_b16 a[148:149], v210 offset:52352\n"
"v_mfma_f32_16x16x32_bf16 v[8:11], a[132:135], a[180:183], v[8:11]\n"
"ds_read_b64_tr_b16 a[150:151], v210 offset:56704\n"
"v_mfma_f32_16x16x32_bf16 v[12:15], a[136:139], a[176:179], v[12:15]\n"
"ds_read_b64_tr_b16 a[152:153], v211 offset:34944\n"
"buffer_load_dwordx4 a[16:19], v250, s[36:39], s54 offen nt\n"
"v_mfma_f32_16x16x32_bf16 v[12:15], a[140:143], a[180:183], v[12:15]\n"
"ds_read_b64_tr_b16 a[154:155], v211 offset:39296\n"
"v_mfma_f32_16x16x32_bf16 v[32:35], a[112:115], a[184:187], v[32:35]\n"
"ds_read_b64_tr_b16 a[156:157], v211 offset:52352\n"
"v_mfma_f32_16x16x32_bf16 v[32:35], a[116:119], a[188:191], v[32:35]\n"
"ds_read_b64_tr_b16 a[158:159], v211 offset:56704\n"
"v_mfma_f32_16x16x32_bf16 v[36:39], a[120:123], a[184:187], v[36:39]\n"
"ds_read_b64_tr_b16 a[160:161], v210 offset:35008\n"
"v_mfma_f32_16x16x32_bf16 v[36:39], a[124:127], a[188:191], v[36:39]\n"
"ds_read_b64_tr_b16 a[162:163], v210 offset:39360\n"
"v_mfma_f32_16x16x32_bf16 v[40:43], a[128:131], a[184:187], v[40:43]\n"
"ds_read_b64_tr_b16 a[164:165], v210 offset:52416\n"
"buffer_load_dwordx4 a[20:23], v250, s[36:39], s54 offen offset:256 nt\n"
"v_mfma_f32_16x16x32_bf16 v[40:43], a[132:135], a[188:191], v[40:43]\n"
"ds_read_b64_tr_b16 a[166:167], v210 offset:56768\n"
"v_mfma_f32_16x16x32_bf16 v[44:47], a[136:139], a[184:187], v[44:47]\n"
"ds_read_b64_tr_b16 a[168:169], v211 offset:35008\n"
"v_mfma_f32_16x16x32_bf16 v[44:47], a[140:143], a[188:191], v[44:47]\n"
"ds_read_b64_tr_b16 a[170:171], v211 offset:39360\n"
"v_mfma_f32_16x16x32_bf16 v[64:67], a[112:115], a[192:195], v[64:67]\n"
"ds_read_b64_tr_b16 a[172:173], v211 offset:52416\n"
"v_mfma_f32_16x16x32_bf16 v[64:67], a[116:119], a[196:199], v[64:67]\n"
"ds_read_b64_tr_b16 a[174:175], v211 offset:56768\n"
"v_mfma_f32_16x16x32_bf16 v[68:71], a[120:123], a[192:195], v[68:71]\n"
"v_mfma_f32_16x16x32_bf16 v[68:71], a[124:127], a[196:199], v[68:71]\n"
"v_mfma_f32_16x16x32_bf16 v[72:75], a[128:131], a[192:195], v[72:75]\n"
"v_mfma_f32_16x16x32_bf16 v[72:75], a[132:135], a[196:199], v[72:75]\n"
"v_mfma_f32_16x16x32_bf16 v[76:79], a[136:139], a[192:195], v[76:79]\n"
"v_mfma_f32_16x16x32_bf16 v[76:79], a[140:143], a[196:199], v[76:79]\n"
"v_mfma_f32_16x16x32_bf16 v[96:99], a[112:115], a[200:203], v[96:99]\n"
"s_waitcnt lgkmcnt(0)\n"
"s_waitcnt vmcnt(18)\n"
"s_barrier\n"
"v_mfma_f32_16x16x32_bf16 v[96:99], a[116:119], a[204:207], v[96:99]\n"
"s_add_u32 m0, s49, 95744\n"
"v_mfma_f32_16x16x32_bf16 v[100:103], a[120:123], a[200:203], v[100:103]\n"
"buffer_load_dwordx4 v212, s[32:35], s52 offen lds\n"
"v_mfma_f32_16x16x32_bf16 v[100:103], a[124:127], a[204:207], v[100:103]\n"
"v_mfma_f32_16x16x32_bf16 v[104:107], a[128:131], a[200:203], v[104:107]\n"
"v_mfma_f32_16x16x32_bf16 v[104:107], a[132:135], a[204:207], v[104:107]\n"
"v_mfma_f32_16x16x32_bf16 v[108:111], a[136:139], a[200:203], v[108:111]\n"
"v_mfma_f32_16x16x32_bf16 v[108:111], a[140:143], a[204:207], v[108:111]\n"
"s_add_u32 m0, s49, 96832\n"
"v_mfma_f32_16x16x32_bf16 v[128:131], a[112:115], a[208:211], v[128:131]\n"
"buffer_load_dwordx4 v213, s[32:35], s52 offen lds\n"
"v_mfma_f32_16x16x32_bf16 v[128:131], a[116:119], a[212:215], v[128:131]\n"
"v_mfma_f32_16x16x32_bf16 v[132:135], a[120:123], a[208:211], v[132:135]\n"
"v_mfma_f32_16x16x32_bf16 v[132:135], a[124:127], a[212:215], v[132:135]\n"
"v_mfma_f32_16x16x32_bf16 v[136:139], a[128:131], a[208:211], v[136:139]\n"
"v_mfma_f32_16x16x32_bf16 v[136:139], a[132:135], a[212:215], v[136:139]\n"
"s_add_u32 m0, s49, 97920\n"
"v_mfma_f32_16x16x32_bf16 v[140:143], a[136:139], a[208:211], v[140:143]\n"
"buffer_load_dwordx4 v214, s[32:35], s52 offen lds\n"
"v_mfma_f32_16x16x32_bf16 v[140:143], a[140:143], a[212:215], v[140:143]\n"
"v_mfma_f32_16x16x32_bf16 v[160:163], a[112:115], a[216:219], v[160:163]\n"
"v_mfma_f32_16x16x32_bf16 v[160:163], a[116:119], a[220:223], v[160:163]\n"
"v_mfma_f32_16x16x32_bf16 v[164:167], a[120:123], a[216:219], v[164:167]\n"
"v_mfma_f32_16x16x32_bf16 v[164:167], a[124:127], a[220:223], v[164:167]\n"
"s_add_u32 m0, s49, 99008\n"
"v_mfma_f32_16x16x32_bf16 v[168:171], a[128:131], a[216:219], v[168:171]\n"
"buffer_load_dwordx4 v215, s[32:35], s52 offen lds\n"
"s_add_u32 s52, s52, 262144\n"
"v_mfma_f32_16x16x32_bf16 v[168:171], a[132:135], a[220:223], v[168:171]\n"
"v_mfma_f32_16x16x32_bf16 v[172:175], a[136:139], a[216:219], v[172:175]\n"
"v_mfma_f32_16x16x32_bf16 v[172:175], a[140:143], a[220:223], v[172:175]\n"
"v_mfma_f32_16x16x32_bf16 v[192:195], a[144:147], a[224:227], v[192:195]\n"
"v_mfma_f32_16x16x32_bf16 v[192:195], a[148:151], a[228:231], v[192:195]\n"
"s_add_u32 m0, s49, 113152\n"
"v_mfma_f32_16x16x32_bf16 v[196:199], a[152:155], a[224:227], v[196:199]\n"
"buffer_load_dwordx4 v212, s[32:35], s52 offen lds\n"
"v_mfma_f32_16x16x32_bf16 v[196:199], a[156:159], a[228:231], v[196:199]\n"
"v_mfma_f32_16x16x32_bf16 v[200:203], a[160:163], a[224:227], v[200:203]\n"
"v_mfma_f32_16x16x32_bf16 v[200:203], a[164:167], a[228:231], v[200:203]\n"
"v_mfma_f32_16x16x32_bf16 v[204:207], a[168:171], a[224:227], v[204:207]\n"
"v_mfma_f32_16x16x32_bf16 v[204:207], a[172:175], a[228:231], v[204:207]\n"
"s_add_u32 m0, s49, 114240\n"
"v_mfma_f32_16x16x32_bf16 v[16:19], a[144:147], a[176:179], v[16:19]\n"
"ds_read_b128 a[224:227], v208 offset:26112\n"
"buffer_load_dwordx4 v213, s[32:35], s52 offen lds\n"
"v_mfma_f32_16x16x32_bf16 v[16:19], a[148:151], a[180:183], v[16:19]\n"
"ds_read_b128 a[228:231], v208 offset:26176\n"
"v_mfma_f32_16x16x32_bf16 v[20:23], a[152:155], a[176:179], v[20:23]\n"
"v_mfma_f32_16x16x32_bf16 v[20:23], a[156:159], a[180:183], v[20:23]\n"
"v_mfma_f32_16x16x32_bf16 v[24:27], a[160:163], a[176:179], v[24:27]\n"
"v_mfma_f32_16x16x32_bf16 v[24:27], a[164:167], a[180:183], v[24:27]\n"
"s_add_u32 m0, s49, 115328\n"
"v_mfma_f32_16x16x32_bf16 v[28:31], a[168:171], a[176:179], v[28:31]\n"
"buffer_load_dwordx4 v214, s[32:35], s52 offen lds\n"
"v_mfma_f32_16x16x32_bf16 v[28:31], a[172:175], a[180:183], v[28:31]\n"
"v_mfma_f32_16x16x32_bf16 v[48:51], a[144:147], a[184:187], v[48:51]\n"
"ds_read_b128 a[176:179], v208 offset:13056\n"
"v_mfma_f32_16x16x32_bf16 v[48:51], a[148:151], a[188:191], v[48:51]\n"
"ds_read_b128 a[180:183], v208 offset:13120\n"
"v_mfma_f32_16x16x32_bf16 v[52:55], a[152:155], a[184:187], v[52:55]\n"
"v_mfma_f32_16x16x32_bf16 v[52:55], a[156:159], a[188:191], v[52:55]\n"
"s_add_u32 m0, s49, 116416\n"
"v_mfma_f32_16x16x32_bf16 v[56:59], a[160:163], a[184:187], v[56:59]\n"
"buffer_load_dwordx4 v215, s[32:35], s52 offen lds\n"
"s_add_u32 s52, s52, 262144\n"
"v_mfma_f32_16x16x32_bf16 v[56:59], a[164:167], a[188:191], v[56:59]\n"
"v_mfma_f32_16x16x32_bf16 v[60:63], a[168:171], a[184:187], v[60:63]\n"
"v_mfma_f32_16x16x32_bf16 v[60:63], a[172:175], a[188:191], v[60:63]\n"
"v_mfma_f32_16x16x32_bf16 v[80:83], a[144:147], a[192:195], v[80:83]\n"
"ds_read_b128 a[184:187], v208 offset:15232\n"
"v_mfma_f32_16x16x32_bf16 v[80:83], a[148:151], a[196:199], v[80:83]\n"
"ds_read_b128 a[188:191], v208 offset:15296\n"
"s_add_u32 m0, s48, 133824\n"
"v_mfma_f32_16x16x32_bf16 v[84:87], a[152:155], a[192:195], v[84:87]\n"
"buffer_load_dwordx4 v216, s[28:31], s51 offen lds\n"
"v_mfma_f32_16x16x32_bf16 v[84:87], a[156:159], a[196:199], v[84:87]\n"
"v_mfma_f32_16x16x32_bf16 v[88:91], a[160:163], a[192:195], v[88:91]\n"
"v_mfma_f32_16x16x32_bf16 v[88:91], a[164:167], a[196:199], v[88:91]\n"
"s_waitcnt vmcnt(17)\n"
"s_barrier\n"
"v_mfma_f32_16x16x32_bf16 v[92:95], a[168:171], a[192:195], v[92:95]\n"
"ds_read_b64_tr_b16 a[112:113], v210\n"
"v_mfma_f32_16x16x32_bf16 v[92:95], a[172:175], a[196:199], v[92:95]\n"
"s_add_u32 m0, s48, 134912\n"
"ds_read_b64_tr_b16 a[114:115], v210 offset:4352\n"
"v_mfma_f32_16x16x32_bf16 v[112:115], a[144:147], a[200:203], v[112:115]\n"
"ds_read_b128 a[192:195], v208 offset:17408\n"
"buffer_load_dwordx4 v217, s[28:31], s51 offen lds\n"
"ds_read_b64_tr_b16 a[116:117], v210 offset:17408\n"
"v_mfma_f32_16x16x32_bf16 v[112:115], a[148:151], a[204:207], v[112:115]\n"
"ds_read_b128 a[196:199], v208 offset:17472\n"
"ds_read_b64_tr_b16 a[118:119], v210 offset:21760\n"
"v_mfma_f32_16x16x32_bf16 v[116:119], a[152:155], a[200:203], v[116:119]\n"
"ds_read_b64_tr_b16 a[120:121], v211\n"
"v_mfma_f32_16x16x32_bf16 v[116:119], a[156:159], a[204:207], v[116:119]\n"
"ds_read_b64_tr_b16 a[122:123], v211 offset:4352\n"
"v_mfma_f32_16x16x32_bf16 v[120:123], a[160:163], a[200:203], v[120:123]\n"
"ds_read_b64_tr_b16 a[124:125], v211 offset:17408\n"
"v_mfma_f32_16x16x32_bf16 v[120:123], a[164:167], a[204:207], v[120:123]\n"
"s_add_u32 m0, s48, 136000\n"
"ds_read_b64_tr_b16 a[126:127], v211 offset:21760\n"
"v_mfma_f32_16x16x32_bf16 v[124:127], a[168:171], a[200:203], v[124:127]\n"
"buffer_load_dwordx4 v218, s[28:31], s51 offen lds\n"
"ds_read_b64_tr_b16 a[128:129], v210 offset:64\n"
"v_mfma_f32_16x16x32_bf16 v[124:127], a[172:175], a[204:207], v[124:127]\n"
"ds_read_b64_tr_b16 a[130:131], v210 offset:4416\n"
"v_mfma_f32_16x16x32_bf16 v[144:147], a[144:147], a[208:211], v[144:147]\n"
"ds_read_b128 a[200:203], v208 offset:19584\n"
"ds_read_b64_tr_b16 a[132:133], v210 offset:17472\n"
"v_mfma_f32_16x16x32_bf16 v[144:147], a[148:151], a[212:215], v[144:147]\n"
"ds_read_b128 a[204:207], v208 offset:19648\n"
"ds_read_b64_tr_b16 a[134:135], v210 offset:21824\n"
"v_mfma_f32_16x16x32_bf16 v[148:151], a[152:155], a[208:211], v[148:151]\n"
"ds_read_b64_tr_b16 a[136:137], v211 offset:64\n"
"v_mfma_f32_16x16x32_bf16 v[148:151], a[156:159], a[212:215], v[148:151]\n"
"s_add_u32 m0, s48, 137088\n"
"ds_read_b64_tr_b16 a[138:139], v211 offset:4416\n"
"v_mfma_f32_16x16x32_bf16 v[152:155], a[160:163], a[208:211], v[152:155]\n"
"buffer_load_dwordx4 v219, s[28:31], s51 offen lds\n"
"ds_read_b64_tr_b16 a[140:141], v211 offset:17472\n"
"v_mfma_f32_16x16x32_bf16 v[152:155], a[164:167], a[212:215], v[152:155]\n"
"ds_read_b64_tr_b16 a[142:143], v211 offset:21824\n"
"v_mfma_f32_16x16x32_bf16 v[156:159], a[168:171], a[208:211], v[156:159]\n"
"v_mfma_f32_16x16x32_bf16 v[156:159], a[172:175], a[212:215], v[156:159]\n"
"v_mfma_f32_16x16x32_bf16 v[176:179], a[144:147], a[216:219], v[176:179]\n"
"ds_read_b128 a[208:211], v208 offset:21760\n"
"v_mfma_f32_16x16x32_bf16 v[176:179], a[148:151], a[220:223], v[176:179]\n"
"ds_read_b128 a[212:215], v208 offset:21824\n"
"s_add_u32 m0, s48, 138176\n"
"v_mfma_f32_16x16x32_bf16 v[180:183], a[152:155], a[216:219], v[180:183]\n"
"buffer_load_dwordx4 v220, s[28:31], s51 offen lds\n"
"v_mfma_f32_16x16x32_bf16 v[180:183], a[156:159], a[220:223], v[180:183]\n"
"v_mfma_f32_16x16x32_bf16 v[184:187], a[160:163], a[216:219], v[184:187]\n"
"v_mfma_f32_16x16x32_bf16 v[184:187], a[164:167], a[220:223], v[184:187]\n"
"v_mfma_f32_16x16x32_bf16 v[188:191], a[168:171], a[216:219], v[188:191]\n"
"s_waitcnt lgkmcnt(2)\n"
"v_mfma_f32_16x16x32_bf16 v[188:191], a[172:175], a[220:223], v[188:191]\n"
"v_mfma_f32_16x16x32_bf16 v[0:3], a[112:115], a[176:179], v[0:3]\n"
"ds_read_b128 a[216:219], v208 offset:23936\n"
"s_add_u32 m0, s48, 139264\n"
"v_mfma_f32_16x16x32_bf16 v[0:3], a[116:119], a[180:183], v[0:3]\n"
"ds_read_b128 a[220:223], v208 offset:24000\n"
"buffer_load_dwordx4 v221, s[28:31], s51 offen lds\n"
"s_add_u32 s51, s51, 128\n"
"v_mfma_f32_16x16x32_bf16 v[4:7], a[120:123], a[176:179], v[4:7]\n"
"ds_read_b64_tr_b16 a[144:145], v210 offset:128\n"
"v_mfma_f32_16x16x32_bf16 v[4:7], a[124:127], a[180:183], v[4:7]\n"
"ds_read_b64_tr_b16 a[146:147], v210 offset:4480\n"
"v_mfma_f32_16x16x32_bf16 v[8:11], a[128:131], a[176:179], v[8:11]\n"
"ds_read_b64_tr_b16 a[148:149], v210 offset:17536\n"
"v_mfma_f32_16x16x32_bf16 v[8:11], a[132:135], a[180:183], v[8:11]\n"
"ds_read_b64_tr_b16 a[150:151], v210 offset:21888\n"
"v_mfma_f32_16x16x32_bf16 v[12:15], a[136:139], a[176:179], v[12:15]\n"
"ds_read_b64_tr_b16 a[152:153], v211 offset:128\n"
"buffer_load_dwordx4 a[24:27], v250, s[36:39], s54 offen offset:512 nt\n"
"v_mfma_f32_16x16x32_bf16 v[12:15], a[140:143], a[180:183], v[12:15]\n"
"ds_read_b64_tr_b16 a[154:155], v211 offset:4480\n"
"v_mfma_f32_16x16x32_bf16 v[32:35], a[112:115], a[184:187], v[32:35]\n"
"ds_read_b64_tr_b16 a[156:157], v211 offset:17536\n"
"v_mfma_f32_16x16x32_bf16 v[32:35], a[116:119], a[188:191], v[32:35]\n"
"ds_read_b64_tr_b16 a[158:159], v211 offset:21888\n"
"v_mfma_f32_16x16x32_bf16 v[36:39], a[120:123], a[184:187], v[36:39]\n"
"ds_read_b64_tr_b16 a[160:161], v210 offset:192\n"
"v_mfma_f32_16x16x32_bf16 v[36:39], a[124:127], a[188:191], v[36:39]\n"
"ds_read_b64_tr_b16 a[162:163], v210 offset:4544\n"
"v_mfma_f32_16x16x32_bf16 v[40:43], a[128:131], a[184:187], v[40:43]\n"
"ds_read_b64_tr_b16 a[164:165], v210 offset:17600\n"
"buffer_load_dwordx4 a[28:31], v250, s[36:39], s54 offen offset:768 nt\n"
"s_add_u32 s53, s53, 8192\n"
"s_and_b32 s53, s53, 258047\n"
"s_lshl_b32 s38, s38, 1\n"
"s_add_u32 s54, s53, 4096\n"
"v_mfma_f32_16x16x32_bf16 v[40:43], a[132:135], a[188:191], v[40:43]\n"
"ds_read_b64_tr_b16 a[166:167], v210 offset:21952\n"
"v_mfma_f32_16x16x32_bf16 v[44:47], a[136:139], a[184:187], v[44:47]\n"
"ds_read_b64_tr_b16 a[168:169], v211 offset:192\n"
"v_mfma_f32_16x16x32_bf16 v[44:47], a[140:143], a[188:191], v[44:47]\n"
"ds_read_b64_tr_b16 a[170:171], v211 offset:4544\n"
"v_mfma_f32_16x16x32_bf16 v[64:67], a[112:115], a[192:195], v[64:67]\n"
"ds_read_b64_tr_b16 a[172:173], v211 offset:17600\n"
"v_mfma_f32_16x16x32_bf16 v[64:67], a[116:119], a[196:199], v[64:67]\n"
"ds_read_b64_tr_b16 a[174:175], v211 offset:21952\n"
"v_mfma_f32_16x16x32_bf16 v[68:71], a[120:123], a[192:195], v[68:71]\n"
"v_mfma_f32_16x16x32_bf16 v[68:71], a[124:127], a[196:199], v[68:71]\n"
"v_mfma_f32_16x16x32_bf16 v[72:75], a[128:131], a[192:195], v[72:75]\n"
"v_mfma_f32_16x16x32_bf16 v[72:75], a[132:135], a[196:199], v[72:75]\n"
"v_mfma_f32_16x16x32_bf16 v[76:79], a[136:139], a[192:195], v[76:79]\n"
"v_mfma_f32_16x16x32_bf16 v[76:79], a[140:143], a[196:199], v[76:79]\n"
"v_mfma_f32_16x16x32_bf16 v[96:99], a[112:115], a[200:203], v[96:99]\n"
"s_waitcnt lgkmcnt(0)\n"
"s_waitcnt vmcnt(18)\n"
"s_barrier\n"
"v_mfma_f32_16x16x32_bf16 v[96:99], a[116:119], a[204:207], v[96:99]\n"
"s_add_u32 m0, s49, 60928\n"
"v_mfma_f32_16x16x32_bf16 v[100:103], a[120:123], a[200:203], v[100:103]\n"
"buffer_load_dwordx4 v212, s[32:35], s52 offen lds\n"
"v_mfma_f32_16x16x32_bf16 v[100:103], a[124:127], a[204:207], v[100:103]\n"
"v_mfma_f32_16x16x32_bf16 v[104:107], a[128:131], a[200:203], v[104:107]\n"
"v_mfma_f32_16x16x32_bf16 v[104:107], a[132:135], a[204:207], v[104:107]\n"
"v_mfma_f32_16x16x32_bf16 v[108:111], a[136:139], a[200:203], v[108:111]\n"
"v_mfma_f32_16x16x32_bf16 v[108:111], a[140:143], a[204:207], v[108:111]\n"
"s_add_u32 m0, s49, 62016\n"
"v_mfma_f32_16x16x32_bf16 v[128:131], a[112:115], a[208:211], v[128:131]\n"
"buffer_load_dwordx4 v213, s[32:35], s52 offen lds\n"
"v_mfma_f32_16x16x32_bf16 v[128:131], a[116:119], a[212:215], v[128:131]\n"
"v_mfma_f32_16x16x32_bf16 v[132:135], a[120:123], a[208:211], v[132:135]\n"
"v_mfma_f32_16x16x32_bf16 v[132:135], a[124:127], a[212:215], v[132:135]\n"
"v_mfma_f32_16x16x32_bf16 v[136:139], a[128:131], a[208:211], v[136:139]\n"
"v_mfma_f32_16x16x32_bf16 v[136:139], a[132:135], a[212:215], v[136:139]\n"
"s_add_u32 m0, s49, 63104\n"
"v_mfma_f32_16x16x32_bf16 v[140:143], a[136:139], a[208:211], v[140:143]\n"
"buffer_load_dwordx4 v214, s[32:35], s52 offen lds\n"
"v_mfma_f32_16x16x32_bf16 v[140:143], a[140:143], a[212:215], v[140:143]\n"
"v_mfma_f32_16x16x32_bf16 v[160:163], a[112:115], a[216:219], v[160:163]\n"
"v_mfma_f32_16x16x32_bf16 v[160:163], a[116:119], a[220:223], v[160:163]\n"
"v_mfma_f32_16x16x32_bf16 v[164:167], a[120:123], a[216:219], v[164:167]\n"
"v_mfma_f32_16x16x32_bf16 v[164:167], a[124:127], a[220:223], v[164:167]\n"
"s_add_u32 m0, s49, 64192\n"
"v_mfma_f32_16x16x32_bf16 v[168:171], a[128:131], a[216:219], v[168:171]\n"
"buffer_load_dwordx4 v215, s[32:35], s52 offen lds\n"
"s_add_u32 s52, s52, 262144\n"
"v_mfma_f32_16x16x32_bf16 v[168:171], a[132:135], a[220:223], v[168:171]\n"
"v_mfma_f32_16x16x32_bf16 v[172:175], a[136:139], a[216:219], v[172:175]\n"
"v_mfma_f32_16x16x32_bf16 v[172:175], a[140:143], a[220:223], v[172:175]\n"
"v_mfma_f32_16x16x32_bf16 v[192:195], a[144:147], a[224:227], v[192:195]\n"
"v_mfma_f32_16x16x32_bf16 v[192:195], a[148:151], a[228:231], v[192:195]\n"
"s_add_u32 m0, s49, 78336\n"
"v_mfma_f32_16x16x32_bf16 v[196:199], a[152:155], a[224:227], v[196:199]\n"
"buffer_load_dwordx4 v212, s[32:35], s52 offen lds\n"
"v_mfma_f32_16x16x32_bf16 v[196:199], a[156:159], a[228:231], v[196:199]\n"
"v_mfma_f32_16x16x32_bf16 v[200:203], a[160:163], a[224:227], v[200:203]\n"
"v_mfma_f32_16x16x32_bf16 v[200:203], a[164:167], a[228:231], v[200:203]\n"
"v_mfma_f32_16x16x32_bf16 v[204:207], a[168:171], a[224:227], v[204:207]\n"
"v_mfma_f32_16x16x32_bf16 v[204:207], a[172:175], a[228:231], v[204:207]\n"
"s_add_u32 m0, s49, 79424\n"
"v_mfma_f32_16x16x32_bf16 v[16:19], a[144:147], a[176:179], v[16:19]\n"
"ds_read_b128 a[224:227], v208 offset:54400\n"
"buffer_load_dwordx4 v213, s[32:35], s52 offen lds\n"
"v_mfma_f32_16x16x32_bf16 v[16:19], a[148:151], a[180:183], v[16:19]\n"
"ds_read_b128 a[228:231], v208 offset:54464\n"
"v_mfma_f32_16x16x32_bf16 v[20:23], a[152:155], a[176:179], v[20:23]\n"
"v_mfma_f32_16x16x32_bf16 v[20:23], a[156:159], a[180:183], v[20:23]\n"
"v_mfma_f32_16x16x32_bf16 v[24:27], a[160:163], a[176:179], v[24:27]\n"
"v_mfma_f32_16x16x32_bf16 v[24:27], a[164:167], a[180:183], v[24:27]\n"
"s_add_u32 m0, s49, 80512\n"
"v_mfma_f32_16x16x32_bf16 v[28:31], a[168:171], a[176:179], v[28:31]\n"
"buffer_load_dwordx4 v214, s[32:35], s52 offen lds\n"
"v_mfma_f32_16x16x32_bf16 v[28:31], a[172:175], a[180:183], v[28:31]\n"
"v_mfma_f32_16x16x32_bf16 v[48:51], a[144:147], a[184:187], v[48:51]\n"
"ds_read_b128 a[176:179], v208 offset:41344\n"
"v_mfma_f32_16x16x32_bf16 v[48:51], a[148:151], a[188:191], v[48:51]\n"
"ds_read_b128 a[180:183], v208 offset:41408\n"
"v_mfma_f32_16x16x32_bf16 v[52:55], a[152:155], a[184:187], v[52:55]\n"
"v_mfma_f32_16x16x32_bf16 v[52:55], a[156:159], a[188:191], v[52:55]\n"
"s_add_u32 m0, s49, 81600\n"
"v_mfma_f32_16x16x32_bf16 v[56:59], a[160:163], a[184:187], v[56:59]\n"
"buffer_load_dwordx4 v215, s[32:35], s52 offen lds\n"
"s_add_u32 s52, s52, 262144\n"
"v_mfma_f32_16x16x32_bf16 v[56:59], a[164:167], a[188:191], v[56:59]\n"
"v_mfma_f32_16x16x32_bf16 v[60:63], a[168:171], a[184:187], v[60:63]\n"
"v_mfma_f32_16x16x32_bf16 v[60:63], a[172:175], a[188:191], v[60:63]\n"
"v_mfma_f32_16x16x32_bf16 v[80:83], a[144:147], a[192:195], v[80:83]\n"
"ds_read_b128 a[184:187], v208 offset:43520\n"
"v_mfma_f32_16x16x32_bf16 v[80:83], a[148:151], a[196:199], v[80:83]\n"
"ds_read_b128 a[188:191], v208 offset:43584\n"
"s_add_u32 m0, s48, 7616\n"
"v_mfma_f32_16x16x32_bf16 v[84:87], a[152:155], a[192:195], v[84:87]\n"
"buffer_load_dwordx4 v216, s[28:31], s51 offen lds\n"
"v_mfma_f32_16x16x32_bf16 v[84:87], a[156:159], a[196:199], v[84:87]\n"
"v_mfma_f32_16x16x32_bf16 v[88:91], a[160:163], a[192:195], v[88:91]\n"
"v_mfma_f32_16x16x32_bf16 v[88:91], a[164:167], a[196:199], v[88:91]\n"
"s_waitcnt vmcnt(17)\n"
"s_barrier\n"
"v_mfma_f32_16x16x32_bf16 v[92:95], a[168:171], a[192:195], v[92:95]\n"
"ds_read_b64_tr_b16 a[112:113], v210 offset:34816\n"
"v_mfma_f32_16x16x32_bf16 v[92:95], a[172:175], a[196:199], v[92:95]\n"
"s_add_u32 m0, s48, 8704\n"
"ds_read_b64_tr_b16 a[114:115], v210 offset:39168\n"
"v_mfma_f32_16x16x32_bf16 v[112:115], a[144:147], a[200:203], v[112:115]\n"
"ds_read_b128 a[192:195], v208 offset:45696\n"
"buffer_load_dwordx4 v217, s[28:31], s51 offen lds\n"
"ds_read_b64_tr_b16 a[116:117], v210 offset:52224\n"
"v_mfma_f32_16x16x32_bf16 v[112:115], a[148:151], a[204:207], v[112:115]\n"
"ds_read_b128 a[196:199], v208 offset:45760\n"
"ds_read_b64_tr_b16 a[118:119], v210 offset:56576\n"
"v_mfma_f32_16x16x32_bf16 v[116:119], a[152:155], a[200:203], v[116:119]\n"
"ds_read_b64_tr_b16 a[120:121], v211 offset:34816\n"
"v_mfma_f32_16x16x32_bf16 v[116:119], a[156:159], a[204:207], v[116:119]\n"
"ds_read_b64_tr_b16 a[122:123], v211 offset:39168\n"
"v_mfma_f32_16x16x32_bf16 v[120:123], a[160:163], a[200:203], v[120:123]\n"
"ds_read_b64_tr_b16 a[124:125], v211 offset:52224\n"
"v_mfma_f32_16x16x32_bf16 v[120:123], a[164:167], a[204:207], v[120:123]\n"
"s_add_u32 m0, s48, 9792\n"
"ds_read_b64_tr_b16 a[126:127], v211 offset:56576\n"
"v_mfma_f32_16x16x32_bf16 v[124:127], a[168:171], a[200:203], v[124:127]\n"
"buffer_load_dwordx4 v218, s[28:31], s51 offen lds\n"
"ds_read_b64_tr_b16 a[128:129], v210 offset:34880\n"
"v_mfma_f32_16x16x32_bf16 v[124:127], a[172:175], a[204:207], v[124:127]\n"
"ds_read_b64_tr_b16 a[130:131], v210 offset:39232\n"
"v_mfma_f32_16x16x32_bf16 v[144:147], a[144:147], a[208:211], v[144:147]\n"
"ds_read_b128 a[200:203], v208 offset:47872\n"
"ds_read_b64_tr_b16 a[132:133], v210 offset:52288\n"
"v_mfma_f32_16x16x32_bf16 v[144:147], a[148:151], a[212:215], v[144:147]\n"
"ds_read_b128 a[204:207], v208 offset:47936\n"
"ds_read_b64_tr_b16 a[134:135], v210 offset:56640\n"
"v_mfma_f32_16x16x32_bf16 v[148:151], a[152:155], a[208:211], v[148:151]\n"
"ds_read_b64_tr_b16 a[136:137], v211 offset:34880\n"
"v_mfma_f32_16x16x32_bf16 v[148:151], a[156:159], a[212:215], v[148:151]\n"
"s_add_u32 m0, s48, 10880\n"
"ds_read_b64_tr_b16 a[138:139], v211 offset:39232\n"
"v_mfma_f32_16x16x32_bf16 v[152:155], a[160:163], a[208:211], v[152:155]\n"
"buffer_load_dwordx4 v219, s[28:31], s51 offen lds\n"
"ds_read_b64_tr_b16 a[140:141], v211 offset:52288\n"
"v_mfma_f32_16x16x32_bf16 v[152:155], a[164:167], a[212:215], v[152:155]\n"
"ds_read_b64_tr_b16 a[142:143], v211 offset:56640\n"
"v_mfma_f32_16x16x32_bf16 v[156:159], a[168:171], a[208:211], v[156:159]\n"
"v_mfma_f32_16x16x32_bf16 v[156:159], a[172:175], a[212:215], v[156:159]\n"
"v_mfma_f32_16x16x32_bf16 v[176:179], a[144:147], a[216:219], v[176:179]\n"
"ds_read_b128 a[208:211], v208 offset:50048\n"
"v_mfma_f32_16x16x32_bf16 v[176:179], a[148:151], a[220:223], v[176:179]\n"
"ds_read_b128 a[212:215], v208 offset:50112\n"
"s_add_u32 m0, s48, 11968\n"
"v_mfma_f32_16x16x32_bf16 v[180:183], a[152:155], a[216:219], v[180:183]\n"
"buffer_load_dwordx4 v220, s[28:31], s51 offen lds\n"
"v_mfma_f32_16x16x32_bf16 v[180:183], a[156:159], a[220:223], v[180:183]\n"
"v_mfma_f32_16x16x32_bf16 v[184:187], a[160:163], a[216:219], v[184:187]\n"
"v_mfma_f32_16x16x32_bf16 v[184:187], a[164:167], a[220:223], v[184:187]\n"
"v_mfma_f32_16x16x32_bf16 v[188:191], a[168:171], a[216:219], v[188:191]\n"
"s_waitcnt lgkmcnt(2)\n"
"v_mfma_f32_16x16x32_bf16 v[188:191], a[172:175], a[220:223], v[188:191]\n"
"v_mfma_f32_16x16x32_bf16 v[0:3], a[112:115], a[176:179], v[0:3]\n"
"ds_read_b128 a[216:219], v208 offset:52224\n"
"s_add_u32 m0, s48, 13056\n"
"v_mfma_f32_16x16x32_bf16 v[0:3], a[116:119], a[180:183], v[0:3]\n"
"ds_read_b128 a[220:223], v208 offset:52288\n"
"buffer_load_dwordx4 v221, s[28:31], s51 offen lds\n"
"s_add_u32 s51, s51, 128\n"
"v_mfma_f32_16x16x32_bf16 v[4:7], a[120:123], a[176:179], v[4:7]\n"
"ds_read_b64_tr_b16 a[144:145], v210 offset:34944\n"
"v_mfma_f32_16x16x32_bf16 v[4:7], a[124:127], a[180:183], v[4:7]\n"
"ds_read_b64_tr_b16 a[146:147], v210 offset:39296\n"
"v_mfma_f32_16x16x32_bf16 v[8:11], a[128:131], a[176:179], v[8:11]\n"
"ds_read_b64_tr_b16 a[148:149], v210 offset:52352\n"
"v_mfma_f32_16x16x32_bf16 v[8:11], a[132:135], a[180:183], v[8:11]\n"
"ds_read_b64_tr_b16 a[150:151], v210 offset:56704\n"
"v_mfma_f32_16x16x32_bf16 v[12:15], a[136:139], a[176:179], v[12:15]\n"
"ds_read_b64_tr_b16 a[152:153], v211 offset:34944\n"
"buffer_load_dwordx4 a[32:35], v250, s[36:39], s53 offen nt\n"
"v_mfma_f32_16x16x32_bf16 v[12:15], a[140:143], a[180:183], v[12:15]\n"
"ds_read_b64_tr_b16 a[154:155], v211 offset:39296\n"
"v_mfma_f32_16x16x32_bf16 v[32:35], a[112:115], a[184:187], v[32:35]\n"
"ds_read_b64_tr_b16 a[156:157], v211 offset:52352\n"
"v_mfma_f32_16x16x32_bf16 v[32:35], a[116:119], a[188:191], v[32:35]\n"
"ds_read_b64_tr_b16 a[158:159], v211 offset:56704\n"
"v_mfma_f32_16x16x32_bf16 v[36:39], a[120:123], a[184:187], v[36:39]\n"
"ds_read_b64_tr_b16 a[160:161], v210 offset:35008\n"
"v_mfma_f32_16x16x32_bf16 v[36:39], a[124:127], a[188:191], v[36:39]\n"
"ds_read_b64_tr_b16 a[162:163], v210 offset:39360\n"
"v_mfma_f32_16x16x32_bf16 v[40:43], a[128:131], a[184:187], v[40:43]\n"
"ds_read_b64_tr_b16 a[164:165], v210 offset:52416\n"
"buffer_load_dwordx4 a[36:39], v250, s[36:39], s53 offen offset:256 nt\n"
"v_mfma_f32_16x16x32_bf16 v[40:43], a[132:135], a[188:191], v[40:43]\n"
"ds_read_b64_tr_b16 a[166:167], v210 offset:56768\n"
"v_mfma_f32_16x16x32_bf16 v[44:47], a[136:139], a[184:187], v[44:47]\n"
"ds_read_b64_tr_b16 a[168:169], v211 offset:35008\n"
"v_mfma_f32_16x16x32_bf16 v[44:47], a[140:143], a[188:191], v[44:47]\n"
"ds_read_b64_tr_b16 a[170:171], v211 offset:39360\n"
"v_mfma_f32_16x16x32_bf16 v[64:67], a[112:115], a[192:195], v[64:67]\n"
"ds_read_b64_tr_b16 a[172:173], v211 offset:52416\n"
"v_mfma_f32_16x16x32_bf16 v[64:67], a[116:119], a[196:199], v[64:67]\n"
"ds_read_b64_tr_b16 a[174:175], v211 offset:56768\n"
"v_mfma_f32_16x16x32_bf16 v[68:71], a[120:123], a[192:195], v[68:71]\n"
"v_mfma_f32_16x16x32_bf16 v[68:71], a[124:127], a[196:199], v[68:71]\n"
"v_mfma_f32_16x16x32_bf16 v[72:75], a[128:131], a[192:195], v[72:75]\n"
"v_mfma_f32_16x16x32_bf16 v[72:75], a[132:135], a[196:199], v[72:75]\n"
"v_mfma_f32_16x16x32_bf16 v[76:79], a[136:139], a[192:195], v[76:79]\n"
"v_mfma_f32_16x16x32_bf16 v[76:79], a[140:143], a[196:199], v[76:79]\n"
"v_mfma_f32_16x16x32_bf16 v[96:99], a[112:115], a[200:203], v[96:99]\n"
"s_waitcnt lgkmcnt(0)\n"
"s_waitcnt vmcnt(18)\n"
"s_barrier\n"
"v_mfma_f32_16x16x32_bf16 v[96:99], a[116:119], a[204:207], v[96:99]\n"
"s_add_u32 m0, s49, 95744\n"
"v_mfma_f32_16x16x32_bf16 v[100:103], a[120:123], a[200:203], v[100:103]\n"
"buffer_load_dwordx4 v212, s[32:35], s52 offen lds\n"
"v_mfma_f32_16x16x32_bf16 v[100:103], a[124:127], a[204:207], v[100:103]\n"
"v_mfma_f32_16x16x32_bf16 v[104:107], a[128:131], a[200:203], v[104:107]\n"
"v_mfma_f32_16x16x32_bf16 v[104:107], a[132:135], a[204:207], v[104:107]\n"
"v_mfma_f32_16x16x32_bf16 v[108:111], a[136:139], a[200:203], v[108:111]\n"
"v_mfma_f32_16x16x32_bf16 v[108:111], a[140:143], a[204:207], v[108:111]\n"
"s_add_u32 m0, s49, 96832\n"
"v_mfma_f32_16x16x32_bf16 v[128:131], a[112:115], a[208:211], v[128:131]\n"
"buffer_load_dwordx4 v213, s[32:35], s52 offen lds\n"
"v_mfma_f32_16x16x32_bf16 v[128:131], a[116:119], a[212:215], v[128:131]\n"
"v_mfma_f32_16x16x32_bf16 v[132:135], a[120:123], a[208:211], v[132:135]\n"
"v_mfma_f32_16x16x32_bf16 v[132:135], a[124:127], a[212:215], v[132:135]\n"
"v_mfma_f32_16x16x32_bf16 v[136:139], a[128:131], a[208:211], v[136:139]\n"
"v_mfma_f32_16x16x32_bf16 v[136:139], a[132:135], a[212:215], v[136:139]\n"
"s_add_u32 m0, s49, 97920\n"
"v_mfma_f32_16x16x32_bf16 v[140:143], a[136:139], a[208:211], v[140:143]\n"
"buffer_load_dwordx4 v214, s[32:35], s52 offen lds\n"
"v_mfma_f32_16x16x32_bf16 v[140:143], a[140:143], a[212:215], v[140:143]\n"
"v_mfma_f32_16x16x32_bf16 v[160:163], a[112:115], a[216:219], v[160:163]\n"
"v_mfma_f32_16x16x32_bf16 v[160:163], a[116:119], a[220:223], v[160:163]\n"
"v_mfma_f32_16x16x32_bf16 v[164:167], a[120:123], a[216:219], v[164:167]\n"
"v_mfma_f32_16x16x32_bf16 v[164:167], a[124:127], a[220:223], v[164:167]\n"
"s_add_u32 m0, s49, 99008\n"
"v_mfma_f32_16x16x32_bf16 v[168:171], a[128:131], a[216:219], v[168:171]\n"
"buffer_load_dwordx4 v215, s[32:35], s52 offen lds\n"
"s_add_u32 s52, s52, 262144\n"
"v_mfma_f32_16x16x32_bf16 v[168:171], a[132:135], a[220:223], v[168:171]\n"
"v_mfma_f32_16x16x32_bf16 v[172:175], a[136:139], a[216:219], v[172:175]\n"
"v_mfma_f32_16x16x32_bf16 v[172:175], a[140:143], a[220:223], v[172:175]\n"
"v_mfma_f32_16x16x32_bf16 v[192:195], a[144:147], a[224:227], v[192:195]\n"
"v_mfma_f32_16x16x32_bf16 v[192:195], a[148:151], a[228:231], v[192:195]\n"
"s_add_u32 m0, s49, 113152\n"
"v_mfma_f32_16x16x32_bf16 v[196:199], a[152:155], a[224:227], v[196:199]\n"
"buffer_load_dwordx4 v212, s[32:35], s52 offen lds\n"
"v_mfma_f32_16x16x32_bf16 v[196:199], a[156:159], a[228:231], v[196:199]\n"
"v_mfma_f32_16x16x32_bf16 v[200:203], a[160:163], a[224:227], v[200:203]\n"
"v_mfma_f32_16x16x32_bf16 v[200:203], a[164:167], a[228:231], v[200:203]\n"
"v_mfma_f32_16x16x32_bf16 v[204:207], a[168:171], a[224:227], v[204:207]\n"
"v_mfma_f32_16x16x32_bf16 v[204:207], a[172:175], a[228:231], v[204:207]\n"
"s_add_u32 m0, s49, 114240\n"
"v_mfma_f32_16x16x32_bf16 v[16:19], a[144:147], a[176:179], v[16:19]\n"
"ds_read_b128 a[224:227], v209 offset:26112\n"
"buffer_load_dwordx4 v213, s[32:35], s52 offen lds\n"
"v_mfma_f32_16x16x32_bf16 v[16:19], a[148:151], a[180:183], v[16:19]\n"
"ds_read_b128 a[228:231], v209 offset:26176\n"
"v_mfma_f32_16x16x32_bf16 v[20:23], a[152:155], a[176:179], v[20:23]\n"
"v_mfma_f32_16x16x32_bf16 v[20:23], a[156:159], a[180:183], v[20:23]\n"
"v_mfma_f32_16x16x32_bf16 v[24:27], a[160:163], a[176:179], v[24:27]\n"
"v_mfma_f32_16x16x32_bf16 v[24:27], a[164:167], a[180:183], v[24:27]\n"
"s_add_u32 m0, s49, 115328\n"
"v_mfma_f32_16x16x32_bf16 v[28:31], a[168:171], a[176:179], v[28:31]\n"
"buffer_load_dwordx4 v214, s[32:35], s52 offen lds\n"
"v_mfma_f32_16x16x32_bf16 v[28:31], a[172:175], a[180:183], v[28:31]\n"
"v_mfma_f32_16x16x32_bf16 v[48:51], a[144:147], a[184:187], v[48:51]\n"
"ds_read_b128 a[176:179], v209 offset:13056\n"
"v_mfma_f32_16x16x32_bf16 v[48:51], a[148:151], a[188:191], v[48:51]\n"
"ds_read_b128 a[180:183], v209 offset:13120\n"
"v_mfma_f32_16x16x32_bf16 v[52:55], a[152:155], a[184:187], v[52:55]\n"
"v_mfma_f32_16x16x32_bf16 v[52:55], a[156:159], a[188:191], v[52:55]\n"
"s_add_u32 m0, s49, 116416\n"
"v_mfma_f32_16x16x32_bf16 v[56:59], a[160:163], a[184:187], v[56:59]\n"
"buffer_load_dwordx4 v215, s[32:35], s52 offen lds\n"
"s_add_u32 s52, s52, 262144\n"
"v_mfma_f32_16x16x32_bf16 v[56:59], a[164:167], a[188:191], v[56:59]\n"
"v_mfma_f32_16x16x32_bf16 v[60:63], a[168:171], a[184:187], v[60:63]\n"
"v_mfma_f32_16x16x32_bf16 v[60:63], a[172:175], a[188:191], v[60:63]\n"
"v_mfma_f32_16x16x32_bf16 v[80:83], a[144:147], a[192:195], v[80:83]\n"
"ds_read_b128 a[184:187], v209 offset:15232\n"
"v_mfma_f32_16x16x32_bf16 v[80:83], a[148:151], a[196:199], v[80:83]\n"
"ds_read_b128 a[188:191], v209 offset:15296\n"
"s_add_u32 m0, s48, 35904\n"
"v_mfma_f32_16x16x32_bf16 v[84:87], a[152:155], a[192:195], v[84:87]\n"
"buffer_load_dwordx4 v216, s[28:31], s51 offen lds\n"
"v_mfma_f32_16x16x32_bf16 v[84:87], a[156:159], a[196:199], v[84:87]\n"
"v_mfma_f32_16x16x32_bf16 v[88:91], a[160:163], a[192:195], v[88:91]\n"
"v_mfma_f32_16x16x32_bf16 v[88:91], a[164:167], a[196:199], v[88:91]\n"
"s_waitcnt vmcnt(17)\n"
"s_barrier\n"
"v_mfma_f32_16x16x32_bf16 v[92:95], a[168:171], a[192:195], v[92:95]\n"
"ds_read_b64_tr_b16 a[112:113], v210\n"
"v_mfma_f32_16x16x32_bf16 v[92:95], a[172:175], a[196:199], v[92:95]\n"
"s_add_u32 m0, s48, 36992\n"
"ds_read_b64_tr_b16 a[114:115], v210 offset:4352\n"
"v_mfma_f32_16x16x32_bf16 v[112:115], a[144:147], a[200:203], v[112:115]\n"
"ds_read_b128 a[192:195], v209 offset:17408\n"
"buffer_load_dwordx4 v217, s[28:31], s51 offen lds\n"
"ds_read_b64_tr_b16 a[116:117], v210 offset:17408\n"
"v_mfma_f32_16x16x32_bf16 v[112:115], a[148:151], a[204:207], v[112:115]\n"
"ds_read_b128 a[196:199], v209 offset:17472\n"
"ds_read_b64_tr_b16 a[118:119], v210 offset:21760\n"
"v_mfma_f32_16x16x32_bf16 v[116:119], a[152:155], a[200:203], v[116:119]\n"
"ds_read_b64_tr_b16 a[120:121], v211\n"
"v_mfma_f32_16x16x32_bf16 v[116:119], a[156:159], a[204:207], v[116:119]\n"
"ds_read_b64_tr_b16 a[122:123], v211 offset:4352\n"
"v_mfma_f32_16x16x32_bf16 v[120:123], a[160:163], a[200:203], v[120:123]\n"
"ds_read_b64_tr_b16 a[124:125], v211 offset:17408\n"
"v_mfma_f32_16x16x32_bf16 v[120:123], a[164:167], a[204:207], v[120:123]\n"
"s_add_u32 m0, s48, 38080\n"
"ds_read_b64_tr_b16 a[126:127], v211 offset:21760\n"
"v_mfma_f32_16x16x32_bf16 v[124:127], a[168:171], a[200:203], v[124:127]\n"
"buffer_load_dwordx4 v218, s[28:31], s51 offen lds\n"
"ds_read_b64_tr_b16 a[128:129], v210 offset:64\n"
"v_mfma_f32_16x16x32_bf16 v[124:127], a[172:175], a[204:207], v[124:127]\n"
"ds_read_b64_tr_b16 a[130:131], v210 offset:4416\n"
"v_mfma_f32_16x16x32_bf16 v[144:147], a[144:147], a[208:211], v[144:147]\n"
"ds_read_b128 a[200:203], v209 offset:19584\n"
"ds_read_b64_tr_b16 a[132:133], v210 offset:17472\n"
"v_mfma_f32_16x16x32_bf16 v[144:147], a[148:151], a[212:215], v[144:147]\n"
"ds_read_b128 a[204:207], v209 offset:19648\n"
"ds_read_b64_tr_b16 a[134:135], v210 offset:21824\n"
"v_mfma_f32_16x16x32_bf16 v[148:151], a[152:155], a[208:211], v[148:151]\n"
"ds_read_b64_tr_b16 a[136:137], v211 offset:64\n"
"v_mfma_f32_16x16x32_bf16 v[148:151], a[156:159], a[212:215], v[148:151]\n"
"s_add_u32 m0, s48, 39168\n"
"ds_read_b64_tr_b16 a[138:139], v211 offset:4416\n"
"v_mfma_f32_16x16x32_bf16 v[152:155], a[160:163], a[208:211], v[152:155]\n"
"buffer_load_dwordx4 v219, s[28:31], s51 offen lds\n"
"ds_read_b64_tr_b16 a[140:141], v211 offset:17472\n"
"v_mfma_f32_16x16x32_bf16 v[152:155], a[164:167], a[212:215], v[152:155]\n"
"ds_read_b64_tr_b16 a[142:143], v211 offset:21824\n"
"v_mfma_f32_16x16x32_bf16 v[156:159], a[168:171], a[208:211], v[156:159]\n"
"v_mfma_f32_16x16x32_bf16 v[156:159], a[172:175], a[212:215], v[156:159]\n"
"v_mfma_f32_16x16x32_bf16 v[176:179], a[144:147], a[216:219], v[176:179]\n"
"ds_read_b128 a[208:211], v209 offset:21760\n"
"v_mfma_f32_16x16x32_bf16 v[176:179], a[148:151], a[220:223], v[176:179]\n"
"ds_read_b128 a[212:215], v209 offset:21824\n"
"s_add_u32 m0, s48, 40256\n"
"v_mfma_f32_16x16x32_bf16 v[180:183], a[152:155], a[216:219], v[180:183]\n"
"buffer_load_dwordx4 v220, s[28:31], s51 offen lds\n"
"v_mfma_f32_16x16x32_bf16 v[180:183], a[156:159], a[220:223], v[180:183]\n"
"v_mfma_f32_16x16x32_bf16 v[184:187], a[160:163], a[216:219], v[184:187]\n"
"v_mfma_f32_16x16x32_bf16 v[184:187], a[164:167], a[220:223], v[184:187]\n"
"v_mfma_f32_16x16x32_bf16 v[188:191], a[168:171], a[216:219], v[188:191]\n"
"s_waitcnt lgkmcnt(2)\n"
"v_mfma_f32_16x16x32_bf16 v[188:191], a[172:175], a[220:223], v[188:191]\n"
"v_mfma_f32_16x16x32_bf16 v[0:3], a[112:115], a[176:179], v[0:3]\n"
"ds_read_b128 a[216:219], v209 offset:23936\n"
"s_add_u32 m0, s48, 41344\n"
"v_mfma_f32_16x16x32_bf16 v[0:3], a[116:119], a[180:183], v[0:3]\n"
"ds_read_b128 a[220:223], v209 offset:24000\n"
"buffer_load_dwordx4 v221, s[28:31], s51 offen lds\n"
"s_add_u32 s51, s51, 128\n"
"v_mfma_f32_16x16x32_bf16 v[4:7], a[120:123], a[176:179], v[4:7]\n"
"ds_read_b64_tr_b16 a[144:145], v210 offset:128\n"
"v_mfma_f32_16x16x32_bf16 v[4:7], a[124:127], a[180:183], v[4:7]\n"
"ds_read_b64_tr_b16 a[146:147], v210 offset:4480\n"
"v_mfma_f32_16x16x32_bf16 v[8:11], a[128:131], a[176:179], v[8:11]\n"
"ds_read_b64_tr_b16 a[148:149], v210 offset:17536\n"
"v_mfma_f32_16x16x32_bf16 v[8:11], a[132:135], a[180:183], v[8:11]\n"
"ds_read_b64_tr_b16 a[150:151], v210 offset:21888\n"
"v_mfma_f32_16x16x32_bf16 v[12:15], a[136:139], a[176:179], v[12:15]\n"
"ds_read_b64_tr_b16 a[152:153], v211 offset:128\n"
"buffer_load_dwordx4 a[40:43], v250, s[36:39], s53 offen offset:512 nt\n"
"v_mfma_f32_16x16x32_bf16 v[12:15], a[140:143], a[180:183], v[12:15]\n"
"ds_read_b64_tr_b16 a[154:155], v211 offset:4480\n"
"v_mfma_f32_16x16x32_bf16 v[32:35], a[112:115], a[184:187], v[32:35]\n"
"ds_read_b64_tr_b16 a[156:157], v211 offset:17536\n"
"v_mfma_f32_16x16x32_bf16 v[32:35], a[116:119], a[188:191], v[32:35]\n"
"ds_read_b64_tr_b16 a[158:159], v211 offset:21888\n"
"v_mfma_f32_16x16x32_bf16 v[36:39], a[120:123], a[184:187], v[36:39]\n"
"ds_read_b64_tr_b16 a[160:161], v210 offset:192\n"
"v_mfma_f32_16x16x32_bf16 v[36:39], a[124:127], a[188:191], v[36:39]\n"
"ds_read_b64_tr_b16 a[162:163], v210 offset:4544\n"
"v_mfma_f32_16x16x32_bf16 v[40:43], a[128:131], a[184:187], v[40:43]\n"
"ds_read_b64_tr_b16 a[164:165], v210 offset:17600\n"
"buffer_load_dwordx4 a[44:47], v250, s[36:39], s53 offen offset:768 nt\n"
"v_mfma_f32_16x16x32_bf16 v[40:43], a[132:135], a[188:191], v[40:43]\n"
"ds_read_b64_tr_b16 a[166:167], v210 offset:21952\n"
"v_mfma_f32_16x16x32_bf16 v[44:47], a[136:139], a[184:187], v[44:47]\n"
"ds_read_b64_tr_b16 a[168:169], v211 offset:192\n"
"v_mfma_f32_16x16x32_bf16 v[44:47], a[140:143], a[188:191], v[44:47]\n"
"ds_read_b64_tr_b16 a[170:171], v211 offset:4544\n"
"v_mfma_f32_16x16x32_bf16 v[64:67], a[112:115], a[192:195], v[64:67]\n"
"ds_read_b64_tr_b16 a[172:173], v211 offset:17600\n"
"v_mfma_f32_16x16x32_bf16 v[64:67], a[116:119], a[196:199], v[64:67]\n"
"ds_read_b64_tr_b16 a[174:175], v211 offset:21952\n"
"v_mfma_f32_16x16x32_bf16 v[68:71], a[120:123], a[192:195], v[68:71]\n"
"v_mfma_f32_16x16x32_bf16 v[68:71], a[124:127], a[196:199], v[68:71]\n"
"v_mfma_f32_16x16x32_bf16 v[72:75], a[128:131], a[192:195], v[72:75]\n"
"v_mfma_f32_16x16x32_bf16 v[72:75], a[132:135], a[196:199], v[72:75]\n"
"v_mfma_f32_16x16x32_bf16 v[76:79], a[136:139], a[192:195], v[76:79]\n"
"v_mfma_f32_16x16x32_bf16 v[76:79], a[140:143], a[196:199], v[76:79]\n"
"v_mfma_f32_16x16x32_bf16 v[96:99], a[112:115], a[200:203], v[96:99]\n"
"s_waitcnt lgkmcnt(0)\n"
"s_waitcnt vmcnt(18)\n"
"s_barrier\n"
"v_mfma_f32_16x16x32_bf16 v[96:99], a[116:119], a[204:207], v[96:99]\n"
"s_add_u32 m0, s49, 60928\n"
"v_mfma_f32_16x16x32_bf16 v[100:103], a[120:123], a[200:203], v[100:103]\n"
"buffer_load_dwordx4 v212, s[32:35], s52 offen lds\n"
"v_mfma_f32_16x16x32_bf16 v[100:103], a[124:127], a[204:207], v[100:103]\n"
"v_mfma_f32_16x16x32_bf16 v[104:107], a[128:131], a[200:203], v[104:107]\n"
"v_mfma_f32_16x16x32_bf16 v[104:107], a[132:135], a[204:207], v[104:107]\n"
"v_mfma_f32_16x16x32_bf16 v[108:111], a[136:139], a[200:203], v[108:111]\n"
"v_mfma_f32_16x16x32_bf16 v[108:111], a[140:143], a[204:207], v[108:111]\n"
"s_add_u32 m0, s49, 62016\n"
"v_mfma_f32_16x16x32_bf16 v[128:131], a[112:115], a[208:211], v[128:131]\n"
"buffer_load_dwordx4 v213, s[32:35], s52 offen lds\n"
"v_mfma_f32_16x16x32_bf16 v[128:131], a[116:119], a[212:215], v[128:131]\n"
"v_mfma_f32_16x16x32_bf16 v[132:135], a[120:123], a[208:211], v[132:135]\n"
"v_mfma_f32_16x16x32_bf16 v[132:135], a[124:127], a[212:215], v[132:135]\n"
"v_mfma_f32_16x16x32_bf16 v[136:139], a[128:131], a[208:211], v[136:139]\n"
"v_mfma_f32_16x16x32_bf16 v[136:139], a[132:135], a[212:215], v[136:139]\n"
"s_add_u32 m0, s49, 63104\n"
"v_mfma_f32_16x16x32_bf16 v[140:143], a[136:139], a[208:211], v[140:143]\n"
"buffer_load_dwordx4 v214, s[32:35], s52 offen lds\n"
"v_mfma_f32_16x16x32_bf16 v[140:143], a[140:143], a[212:215], v[140:143]\n"
"v_mfma_f32_16x16x32_bf16 v[160:163], a[112:115], a[216:219], v[160:163]\n"
"v_mfma_f32_16x16x32_bf16 v[160:163], a[116:119], a[220:223], v[160:163]\n"
"v_mfma_f32_16x16x32_bf16 v[164:167], a[120:123], a[216:219], v[164:167]\n"
"v_mfma_f32_16x16x32_bf16 v[164:167], a[124:127], a[220:223], v[164:167]\n"
"s_add_u32 m0, s49, 64192\n"
"v_mfma_f32_16x16x32_bf16 v[168:171], a[128:131], a[216:219], v[168:171]\n"
"buffer_load_dwordx4 v215, s[32:35], s52 offen lds\n"
"s_add_u32 s52, s52, 262144\n"
"v_mfma_f32_16x16x32_bf16 v[168:171], a[132:135], a[220:223], v[168:171]\n"
"v_mfma_f32_16x16x32_bf16 v[172:175], a[136:139], a[216:219], v[172:175]\n"
"v_mfma_f32_16x16x32_bf16 v[172:175], a[140:143], a[220:223], v[172:175]\n"
"v_mfma_f32_16x16x32_bf16 v[192:195], a[144:147], a[224:227], v[192:195]\n"
"v_mfma_f32_16x16x32_bf16 v[192:195], a[148:151], a[228:231], v[192:195]\n"
"s_add_u32 m0, s49, 78336\n"
"v_mfma_f32_16x16x32_bf16 v[196:199], a[152:155], a[224:227], v[196:199]\n"
"buffer_load_dwordx4 v212, s[32:35], s52 offen lds\n"
"v_mfma_f32_16x16x32_bf16 v[196:199], a[156:159], a[228:231], v[196:199]\n"
"v_mfma_f32_16x16x32_bf16 v[200:203], a[160:163], a[224:227], v[200:203]\n"
"v_mfma_f32_16x16x32_bf16 v[200:203], a[164:167], a[228:231], v[200:203]\n"
"v_mfma_f32_16x16x32_bf16 v[204:207], a[168:171], a[224:227], v[204:207]\n"
"v_mfma_f32_16x16x32_bf16 v[204:207], a[172:175], a[228:231], v[204:207]\n"
"s_add_u32 m0, s49, 79424\n"
"v_mfma_f32_16x16x32_bf16 v[16:19], a[144:147], a[176:179], v[16:19]\n"
"ds_read_b128 a[224:227], v208 offset:26112\n"
"buffer_load_dwordx4 v213, s[32:35], s52 offen lds\n"
"v_mfma_f32_16x16x32_bf16 v[16:19], a[148:151], a[180:183], v[16:19]\n"
"ds_read_b128 a[228:231], v208 offset:26176\n"
"v_mfma_f32_16x16x32_bf16 v[20:23], a[152:155], a[176:179], v[20:23]\n"
"v_mfma_f32_16x16x32_bf16 v[20:23], a[156:159], a[180:183], v[20:23]\n"
"v_mfma_f32_16x16x32_bf16 v[24:27], a[160:163], a[176:179], v[24:27]\n"
"v_mfma_f32_16x16x32_bf16 v[24:27], a[164:167], a[180:183], v[24:27]\n"
"s_add_u32 m0, s49, 80512\n"
"v_mfma_f32_16x16x32_bf16 v[28:31], a[168:171], a[176:179], v[28:31]\n"
"buffer_load_dwordx4 v214, s[32:35], s52 offen lds\n"
"v_mfma_f32_16x16x32_bf16 v[28:31], a[172:175], a[180:183], v[28:31]\n"
"v_mfma_f32_16x16x32_bf16 v[48:51], a[144:147], a[184:187], v[48:51]\n"
"ds_read_b128 a[176:179], v208 offset:13056\n"
"v_mfma_f32_16x16x32_bf16 v[48:51], a[148:151], a[188:191], v[48:51]\n"
"ds_read_b128 a[180:183], v208 offset:13120\n"
"v_mfma_f32_16x16x32_bf16 v[52:55], a[152:155], a[184:187], v[52:55]\n"
"v_mfma_f32_16x16x32_bf16 v[52:55], a[156:159], a[188:191], v[52:55]\n"
"s_add_u32 m0, s49, 81600\n"
"v_mfma_f32_16x16x32_bf16 v[56:59], a[160:163], a[184:187], v[56:59]\n"
"buffer_load_dwordx4 v215, s[32:35], s52 offen lds\n"
"s_add_u32 s52, s52, 262144\n"
"v_mfma_f32_16x16x32_bf16 v[56:59], a[164:167], a[188:191], v[56:59]\n"
"v_mfma_f32_16x16x32_bf16 v[60:63], a[168:171], a[184:187], v[60:63]\n"
"v_mfma_f32_16x16x32_bf16 v[60:63], a[172:175], a[188:191], v[60:63]\n"
"v_mfma_f32_16x16x32_bf16 v[80:83], a[144:147], a[192:195], v[80:83]\n"
"ds_read_b128 a[184:187], v208 offset:15232\n"
"v_mfma_f32_16x16x32_bf16 v[80:83], a[148:151], a[196:199], v[80:83]\n"
"ds_read_b128 a[188:191], v208 offset:15296\n"
"s_add_u32 m0, s48, 133824\n"
"v_mfma_f32_16x16x32_bf16 v[84:87], a[152:155], a[192:195], v[84:87]\n"
"buffer_load_dwordx4 v216, s[28:31], s51 offen lds\n"
"v_mfma_f32_16x16x32_bf16 v[84:87], a[156:159], a[196:199], v[84:87]\n"
"v_mfma_f32_16x16x32_bf16 v[88:91], a[160:163], a[192:195], v[88:91]\n"
"v_mfma_f32_16x16x32_bf16 v[88:91], a[164:167], a[196:199], v[88:91]\n"
"s_waitcnt vmcnt(17)\n"
"s_barrier\n"
"v_mfma_f32_16x16x32_bf16 v[92:95], a[168:171], a[192:195], v[92:95]\n"
"ds_read_b64_tr_b16 a[112:113], v210 offset:34816\n"
"v_mfma_f32_16x16x32_bf16 v[92:95], a[172:175], a[196:199], v[92:95]\n"
"s_add_u32 m0, s48, 134912\n"
"ds_read_b64_tr_b16 a[114:115], v210 offset:39168\n"
"v_mfma_f32_16x16x32_bf16 v[112:115], a[144:147], a[200:203], v[112:115]\n"
"ds_read_b128 a[192:195], v208 offset:17408\n"
"buffer_load_dwordx4 v217, s[28:31], s51 offen lds\n"
"ds_read_b64_tr_b16 a[116:117], v210 offset:52224\n"
"v_mfma_f32_16x16x32_bf16 v[112:115], a[148:151], a[204:207], v[112:115]\n"
"ds_read_b128 a[196:199], v208 offset:17472\n"
"ds_read_b64_tr_b16 a[118:119], v210 offset:56576\n"
"v_mfma_f32_16x16x32_bf16 v[116:119], a[152:155], a[200:203], v[116:119]\n"
"ds_read_b64_tr_b16 a[120:121], v211 offset:34816\n"
"v_mfma_f32_16x16x32_bf16 v[116:119], a[156:159], a[204:207], v[116:119]\n"
"ds_read_b64_tr_b16 a[122:123], v211 offset:39168\n"
"v_mfma_f32_16x16x32_bf16 v[120:123], a[160:163], a[200:203], v[120:123]\n"
"ds_read_b64_tr_b16 a[124:125], v211 offset:52224\n"
"v_mfma_f32_16x16x32_bf16 v[120:123], a[164:167], a[204:207], v[120:123]\n"
"s_add_u32 m0, s48, 136000\n"
"ds_read_b64_tr_b16 a[126:127], v211 offset:56576\n"
"v_mfma_f32_16x16x32_bf16 v[124:127], a[168:171], a[200:203], v[124:127]\n"
"buffer_load_dwordx4 v218, s[28:31], s51 offen lds\n"
"ds_read_b64_tr_b16 a[128:129], v210 offset:34880\n"
"v_mfma_f32_16x16x32_bf16 v[124:127], a[172:175], a[204:207], v[124:127]\n"
"ds_read_b64_tr_b16 a[130:131], v210 offset:39232\n"
"v_mfma_f32_16x16x32_bf16 v[144:147], a[144:147], a[208:211], v[144:147]\n"
"ds_read_b128 a[200:203], v208 offset:19584\n"
"ds_read_b64_tr_b16 a[132:133], v210 offset:52288\n"
"v_mfma_f32_16x16x32_bf16 v[144:147], a[148:151], a[212:215], v[144:147]\n"
"ds_read_b128 a[204:207], v208 offset:19648\n"
"ds_read_b64_tr_b16 a[134:135], v210 offset:56640\n"
"v_mfma_f32_16x16x32_bf16 v[148:151], a[152:155], a[208:211], v[148:151]\n"
"ds_read_b64_tr_b16 a[136:137], v211 offset:34880\n"
"v_mfma_f32_16x16x32_bf16 v[148:151], a[156:159], a[212:215], v[148:151]\n"
"s_add_u32 m0, s48, 137088\n"
"ds_read_b64_tr_b16 a[138:139], v211 offset:39232\n"
"v_mfma_f32_16x16x32_bf16 v[152:155], a[160:163], a[208:211], v[152:155]\n"
"buffer_load_dwordx4 v219, s[28:31], s51 offen lds\n"
"ds_read_b64_tr_b16 a[140:141], v211 offset:52288\n"
"v_mfma_f32_16x16x32_bf16 v[152:155], a[164:167], a[212:215], v[152:155]\n"
"ds_read_b64_tr_b16 a[142:143], v211 offset:56640\n"
"v_mfma_f32_16x16x32_bf16 v[156:159], a[168:171], a[208:211], v[156:159]\n"
"v_mfma_f32_16x16x32_bf16 v[156:159], a[172:175], a[212:215], v[156:159]\n"
"v_mfma_f32_16x16x32_bf16 v[176:179], a[144:147], a[216:219], v[176:179]\n"
"ds_read_b128 a[208:211], v208 offset:21760\n"
"v_mfma_f32_16x16x32_bf16 v[176:179], a[148:151], a[220:223], v[176:179]\n"
"ds_read_b128 a[212:215], v208 offset:21824\n"
"s_add_u32 m0, s48, 138176\n"
"v_mfma_f32_16x16x32_bf16 v[180:183], a[152:155], a[216:219], v[180:183]\n"
"buffer_load_dwordx4 v220, s[28:31], s51 offen lds\n"
"v_mfma_f32_16x16x32_bf16 v[180:183], a[156:159], a[220:223], v[180:183]\n"
"v_mfma_f32_16x16x32_bf16 v[184:187], a[160:163], a[216:219], v[184:187]\n"
"v_mfma_f32_16x16x32_bf16 v[184:187], a[164:167], a[220:223], v[184:187]\n"
"v_mfma_f32_16x16x32_bf16 v[188:191], a[168:171], a[216:219], v[188:191]\n"
"s_waitcnt lgkmcnt(2)\n"
"v_mfma_f32_16x16x32_bf16 v[188:191], a[172:175], a[220:223], v[188:191]\n"
"v_mfma_f32_16x16x32_bf16 v[0:3], a[112:115], a[176:179], v[0:3]\n"
"ds_read_b128 a[216:219], v208 offset:23936\n"
"s_add_u32 m0, s48, 139264\n"
"v_mfma_f32_16x16x32_bf16 v[0:3], a[116:119], a[180:183], v[0:3]\n"
"ds_read_b128 a[220:223], v208 offset:24000\n"
"buffer_load_dwordx4 v221, s[28:31], s51 offen lds\n"
"s_add_u32 s51, s51, 128\n"
"v_mfma_f32_16x16x32_bf16 v[4:7], a[120:123], a[176:179], v[4:7]\n"
"ds_read_b64_tr_b16 a[144:145], v210 offset:34944\n"
"v_mfma_f32_16x16x32_bf16 v[4:7], a[124:127], a[180:183], v[4:7]\n"
"ds_read_b64_tr_b16 a[146:147], v210 offset:39296\n"
"v_mfma_f32_16x16x32_bf16 v[8:11], a[128:131], a[176:179], v[8:11]\n"
"ds_read_b64_tr_b16 a[148:149], v210 offset:52352\n"
"v_mfma_f32_16x16x32_bf16 v[8:11], a[132:135], a[180:183], v[8:11]\n"
"ds_read_b64_tr_b16 a[150:151], v210 offset:56704\n"
"v_mfma_f32_16x16x32_bf16 v[12:15], a[136:139], a[176:179], v[12:15]\n"
"ds_read_b64_tr_b16 a[152:153], v211 offset:34944\n"
"buffer_load_dwordx4 a[48:51], v250, s[36:39], s54 offen nt\n"
"v_mfma_f32_16x16x32_bf16 v[12:15], a[140:143], a[180:183], v[12:15]\n"
"ds_read_b64_tr_b16 a[154:155], v211 offset:39296\n"
"v_mfma_f32_16x16x32_bf16 v[32:35], a[112:115], a[184:187], v[32:35]\n"
"ds_read_b64_tr_b16 a[156:157], v211 offset:52352\n"
"v_mfma_f32_16x16x32_bf16 v[32:35], a[116:119], a[188:191], v[32:35]\n"
"ds_read_b64_tr_b16 a[158:159], v211 offset:56704\n"
"v_mfma_f32_16x16x32_bf16 v[36:39], a[120:123], a[184:187], v[36:39]\n"
"ds_read_b64_tr_b16 a[160:161], v210 offset:35008\n"
"v_mfma_f32_16x16x32_bf16 v[36:39], a[124:127], a[188:191], v[36:39]\n"
"ds_read_b64_tr_b16 a[162:163], v210 offset:39360\n"
"v_mfma_f32_16x16x32_bf16 v[40:43], a[128:131], a[184:187], v[40:43]\n"
"ds_read_b64_tr_b16 a[164:165], v210 offset:52416\n"
"buffer_load_dwordx4 a[52:55], v250, s[36:39], s54 offen offset:256 nt\n"
"v_mfma_f32_16x16x32_bf16 v[40:43], a[132:135], a[188:191], v[40:43]\n"
"ds_read_b64_tr_b16 a[166:167], v210 offset:56768\n"
"v_mfma_f32_16x16x32_bf16 v[44:47], a[136:139], a[184:187], v[44:47]\n"
"ds_read_b64_tr_b16 a[168:169], v211 offset:35008\n"
"v_mfma_f32_16x16x32_bf16 v[44:47], a[140:143], a[188:191], v[44:47]\n"
"ds_read_b64_tr_b16 a[170:171], v211 offset:39360\n"
"v_mfma_f32_16x16x32_bf16 v[64:67], a[112:115], a[192:195], v[64:67]\n"
"ds_read_b64_tr_b16 a[172:173], v211 offset:52416\n"
"v_mfma_f32_16x16x32_bf16 v[64:67], a[116:119], a[196:199], v[64:67]\n"
"ds_read_b64_tr_b16 a[174:175], v211 offset:56768\n"
"v_mfma_f32_16x16x32_bf16 v[68:71], a[120:123], a[192:195], v[68:71]\n"
"v_mfma_f32_16x16x32_bf16 v[68:71], a[124:127], a[196:199], v[68:71]\n"
"v_mfma_f32_16x16x32_bf16 v[72:75], a[128:131], a[192:195], v[72:75]\n"
"v_mfma_f32_16x16x32_bf16 v[72:75], a[132:135], a[196:199], v[72:75]\n"
"v_mfma_f32_16x16x32_bf16 v[76:79], a[136:139], a[192:195], v[76:79]\n"
"v_mfma_f32_16x16x32_bf16 v[76:79], a[140:143], a[196:199], v[76:79]\n"
"v_mfma_f32_16x16x32_bf16 v[96:99], a[112:115], a[200:203], v[96:99]\n"
"s_waitcnt lgkmcnt(0)\n"
"s_waitcnt vmcnt(18)\n"
"s_barrier\n"
"v_mfma_f32_16x16x32_bf16 v[96:99], a[116:119], a[204:207], v[96:99]\n"
"s_add_u32 m0, s49, 95744\n"
"v_mfma_f32_16x16x32_bf16 v[100:103], a[120:123], a[200:203], v[100:103]\n"
"buffer_load_dwordx4 v212, s[32:35], s52 offen lds\n"
"v_mfma_f32_16x16x32_bf16 v[100:103], a[124:127], a[204:207], v[100:103]\n"
"v_mfma_f32_16x16x32_bf16 v[104:107], a[128:131], a[200:203], v[104:107]\n"
"v_mfma_f32_16x16x32_bf16 v[104:107], a[132:135], a[204:207], v[104:107]\n"
"v_mfma_f32_16x16x32_bf16 v[108:111], a[136:139], a[200:203], v[108:111]\n"
"v_mfma_f32_16x16x32_bf16 v[108:111], a[140:143], a[204:207], v[108:111]\n"
"s_add_u32 m0, s49, 96832\n"
"v_mfma_f32_16x16x32_bf16 v[128:131], a[112:115], a[208:211], v[128:131]\n"
"buffer_load_dwordx4 v213, s[32:35], s52 offen lds\n"
"v_mfma_f32_16x16x32_bf16 v[128:131], a[116:119], a[212:215], v[128:131]\n"
"v_mfma_f32_16x16x32_bf16 v[132:135], a[120:123], a[208:211], v[132:135]\n"
"v_mfma_f32_16x16x32_bf16 v[132:135], a[124:127], a[212:215], v[132:135]\n"
"v_mfma_f32_16x16x32_bf16 v[136:139], a[128:131], a[208:211], v[136:139]\n"
"v_mfma_f32_16x16x32_bf16 v[136:139], a[132:135], a[212:215], v[136:139]\n"
"s_add_u32 m0, s49, 97920\n"
"v_mfma_f32_16x16x32_bf16 v[140:143], a[136:139], a[208:211], v[140:143]\n"
"buffer_load_dwordx4 v214, s[32:35], s52 offen lds\n"
"v_mfma_f32_16x16x32_bf16 v[140:143], a[140:143], a[212:215], v[140:143]\n"
"v_mfma_f32_16x16x32_bf16 v[160:163], a[112:115], a[216:219], v[160:163]\n"
"v_mfma_f32_16x16x32_bf16 v[160:163], a[116:119], a[220:223], v[160:163]\n"
"v_mfma_f32_16x16x32_bf16 v[164:167], a[120:123], a[216:219], v[164:167]\n"
"v_mfma_f32_16x16x32_bf16 v[164:167], a[124:127], a[220:223], v[164:167]\n"
"s_add_u32 m0, s49, 99008\n"
"v_mfma_f32_16x16x32_bf16 v[168:171], a[128:131], a[216:219], v[168:171]\n"
"buffer_load_dwordx4 v215, s[32:35], s52 offen lds\n"
"s_add_u32 s52, s52, 262144\n"
"v_mfma_f32_16x16x32_bf16 v[168:171], a[132:135], a[220:223], v[168:171]\n"
"v_mfma_f32_16x16x32_bf16 v[172:175], a[136:139], a[216:219], v[172:175]\n"
"v_mfma_f32_16x16x32_bf16 v[172:175], a[140:143], a[220:223], v[172:175]\n"
"v_mfma_f32_16x16x32_bf16 v[192:195], a[144:147], a[224:227], v[192:195]\n"
"v_mfma_f32_16x16x32_bf16 v[192:195], a[148:151], a[228:231], v[192:195]\n"
"s_add_u32 m0, s49, 113152\n"
"v_mfma_f32_16x16x32_bf16 v[196:199], a[152:155], a[224:227], v[196:199]\n"
"buffer_load_dwordx4 v212, s[32:35], s52 offen lds\n"
"v_mfma_f32_16x16x32_bf16 v[196:199], a[156:159], a[228:231], v[196:199]\n"
"v_mfma_f32_16x16x32_bf16 v[200:203], a[160:163], a[224:227], v[200:203]\n"
"v_mfma_f32_16x16x32_bf16 v[200:203], a[164:167], a[228:231], v[200:203]\n"
"v_mfma_f32_16x16x32_bf16 v[204:207], a[168:171], a[224:227], v[204:207]\n"
"v_mfma_f32_16x16x32_bf16 v[204:207], a[172:175], a[228:231], v[204:207]\n"
"s_add_u32 m0, s49, 114240\n"
"v_mfma_f32_16x16x32_bf16 v[16:19], a[144:147], a[176:179], v[16:19]\n"
"ds_read_b128 a[224:227], v208 offset:54400\n"
"buffer_load_dwordx4 v213, s[32:35], s52 offen lds\n"
"v_mfma_f32_16x16x32_bf16 v[16:19], a[148:151], a[180:183], v[16:19]\n"
"ds_read_b128 a[228:231], v208 offset:54464\n"
"v_mfma_f32_16x16x32_bf16 v[20:23], a[152:155], a[176:179], v[20:23]\n"
"v_mfma_f32_16x16x32_bf16 v[20:23], a[156:159], a[180:183], v[20:23]\n"
"v_mfma_f32_16x16x32_bf16 v[24:27], a[160:163], a[176:179], v[24:27]\n"
"v_mfma_f32_16x16x32_bf16 v[24:27], a[164:167], a[180:183], v[24:27]\n"
"s_add_u32 m0, s49, 115328\n"
"v_mfma_f32_16x16x32_bf16 v[28:31], a[168:171], a[176:179], v[28:31]\n"
"buffer_load_dwordx4 v214, s[32:35], s52 offen lds\n"
"v_mfma_f32_16x16x32_bf16 v[28:31], a[172:175], a[180:183], v[28:31]\n"
"v_mfma_f32_16x16x32_bf16 v[48:51], a[144:147], a[184:187], v[48:51]\n"
"ds_read_b128 a[176:179], v208 offset:41344\n"
"v_mfma_f32_16x16x32_bf16 v[48:51], a[148:151], a[188:191], v[48:51]\n"
"ds_read_b128 a[180:183], v208 offset:41408\n"
"v_mfma_f32_16x16x32_bf16 v[52:55], a[152:155], a[184:187], v[52:55]\n"
"v_mfma_f32_16x16x32_bf16 v[52:55], a[156:159], a[188:191], v[52:55]\n"
"s_add_u32 m0, s49, 116416\n"
"v_mfma_f32_16x16x32_bf16 v[56:59], a[160:163], a[184:187], v[56:59]\n"
"buffer_load_dwordx4 v215, s[32:35], s52 offen lds\n"
"s_add_u32 s52, s52, 262144\n"
"v_mfma_f32_16x16x32_bf16 v[56:59], a[164:167], a[188:191], v[56:59]\n"
"v_mfma_f32_16x16x32_bf16 v[60:63], a[168:171], a[184:187], v[60:63]\n"
"v_mfma_f32_16x16x32_bf16 v[60:63], a[172:175], a[188:191], v[60:63]\n"
"v_mfma_f32_16x16x32_bf16 v[80:83], a[144:147], a[192:195], v[80:83]\n"
"ds_read_b128 a[184:187], v208 offset:43520\n"
"v_mfma_f32_16x16x32_bf16 v[80:83], a[148:151], a[196:199], v[80:83]\n"
"ds_read_b128 a[188:191], v208 offset:43584\n"
"s_add_u32 m0, s48, 7616\n"
"v_mfma_f32_16x16x32_bf16 v[84:87], a[152:155], a[192:195], v[84:87]\n"
"buffer_load_dwordx4 v216, s[28:31], s51 offen lds\n"
"v_mfma_f32_16x16x32_bf16 v[84:87], a[156:159], a[196:199], v[84:87]\n"
"v_mfma_f32_16x16x32_bf16 v[88:91], a[160:163], a[192:195], v[88:91]\n"
"v_mfma_f32_16x16x32_bf16 v[88:91], a[164:167], a[196:199], v[88:91]\n"
"s_waitcnt vmcnt(17)\n"
"s_barrier\n"
"v_mfma_f32_16x16x32_bf16 v[92:95], a[168:171], a[192:195], v[92:95]\n"
"ds_read_b64_tr_b16 a[112:113], v210\n"
"v_mfma_f32_16x16x32_bf16 v[92:95], a[172:175], a[196:199], v[92:95]\n"
"s_add_u32 m0, s48, 8704\n"
"ds_read_b64_tr_b16 a[114:115], v210 offset:4352\n"
"v_mfma_f32_16x16x32_bf16 v[112:115], a[144:147], a[200:203], v[112:115]\n"
"ds_read_b128 a[192:195], v208 offset:45696\n"
"buffer_load_dwordx4 v217, s[28:31], s51 offen lds\n"
"ds_read_b64_tr_b16 a[116:117], v210 offset:17408\n"
"v_mfma_f32_16x16x32_bf16 v[112:115], a[148:151], a[204:207], v[112:115]\n"
"ds_read_b128 a[196:199], v208 offset:45760\n"
"ds_read_b64_tr_b16 a[118:119], v210 offset:21760\n"
"v_mfma_f32_16x16x32_bf16 v[116:119], a[152:155], a[200:203], v[116:119]\n"
"ds_read_b64_tr_b16 a[120:121], v211\n"
"v_mfma_f32_16x16x32_bf16 v[116:119], a[156:159], a[204:207], v[116:119]\n"
"ds_read_b64_tr_b16 a[122:123], v211 offset:4352\n"
"v_mfma_f32_16x16x32_bf16 v[120:123], a[160:163], a[200:203], v[120:123]\n"
"ds_read_b64_tr_b16 a[124:125], v211 offset:17408\n"
"v_mfma_f32_16x16x32_bf16 v[120:123], a[164:167], a[204:207], v[120:123]\n"
"s_add_u32 m0, s48, 9792\n"
"ds_read_b64_tr_b16 a[126:127], v211 offset:21760\n"
"v_mfma_f32_16x16x32_bf16 v[124:127], a[168:171], a[200:203], v[124:127]\n"
"buffer_load_dwordx4 v218, s[28:31], s51 offen lds\n"
"ds_read_b64_tr_b16 a[128:129], v210 offset:64\n"
"v_mfma_f32_16x16x32_bf16 v[124:127], a[172:175], a[204:207], v[124:127]\n"
"ds_read_b64_tr_b16 a[130:131], v210 offset:4416\n"
"v_mfma_f32_16x16x32_bf16 v[144:147], a[144:147], a[208:211], v[144:147]\n"
"ds_read_b128 a[200:203], v208 offset:47872\n"
"ds_read_b64_tr_b16 a[132:133], v210 offset:17472\n"
"v_mfma_f32_16x16x32_bf16 v[144:147], a[148:151], a[212:215], v[144:147]\n"
"ds_read_b128 a[204:207], v208 offset:47936\n"
"ds_read_b64_tr_b16 a[134:135], v210 offset:21824\n"
"v_mfma_f32_16x16x32_bf16 v[148:151], a[152:155], a[208:211], v[148:151]\n"
"ds_read_b64_tr_b16 a[136:137], v211 offset:64\n"
"v_mfma_f32_16x16x32_bf16 v[148:151], a[156:159], a[212:215], v[148:151]\n"
"s_add_u32 m0, s48, 10880\n"
"ds_read_b64_tr_b16 a[138:139], v211 offset:4416\n"
"v_mfma_f32_16x16x32_bf16 v[152:155], a[160:163], a[208:211], v[152:155]\n"
"buffer_load_dwordx4 v219, s[28:31], s51 offen lds\n"
"ds_read_b64_tr_b16 a[140:141], v211 offset:17472\n"
"v_mfma_f32_16x16x32_bf16 v[152:155], a[164:167], a[212:215], v[152:155]\n"
"ds_read_b64_tr_b16 a[142:143], v211 offset:21824\n"
"v_mfma_f32_16x16x32_bf16 v[156:159], a[168:171], a[208:211], v[156:159]\n"
"v_mfma_f32_16x16x32_bf16 v[156:159], a[172:175], a[212:215], v[156:159]\n"
"v_mfma_f32_16x16x32_bf16 v[176:179], a[144:147], a[216:219], v[176:179]\n"
"ds_read_b128 a[208:211], v208 offset:50048\n"
"v_mfma_f32_16x16x32_bf16 v[176:179], a[148:151], a[220:223], v[176:179]\n"
"ds_read_b128 a[212:215], v208 offset:50112\n"
"s_add_u32 m0, s48, 11968\n"
"v_mfma_f32_16x16x32_bf16 v[180:183], a[152:155], a[216:219], v[180:183]\n"
"buffer_load_dwordx4 v220, s[28:31], s51 offen lds\n"
"v_mfma_f32_16x16x32_bf16 v[180:183], a[156:159], a[220:223], v[180:183]\n"
"v_mfma_f32_16x16x32_bf16 v[184:187], a[160:163], a[216:219], v[184:187]\n"
"v_mfma_f32_16x16x32_bf16 v[184:187], a[164:167], a[220:223], v[184:187]\n"
"v_mfma_f32_16x16x32_bf16 v[188:191], a[168:171], a[216:219], v[188:191]\n"
"s_waitcnt lgkmcnt(2)\n"
"v_mfma_f32_16x16x32_bf16 v[188:191], a[172:175], a[220:223], v[188:191]\n"
"v_mfma_f32_16x16x32_bf16 v[0:3], a[112:115], a[176:179], v[0:3]\n"
"ds_read_b128 a[216:219], v208 offset:52224\n"
"s_add_u32 m0, s48, 13056\n"
"v_mfma_f32_16x16x32_bf16 v[0:3], a[116:119], a[180:183], v[0:3]\n"
"ds_read_b128 a[220:223], v208 offset:52288\n"
"buffer_load_dwordx4 v221, s[28:31], s51 offen lds\n"
"s_add_u32 s51, s51, 128\n"
"v_mfma_f32_16x16x32_bf16 v[4:7], a[120:123], a[176:179], v[4:7]\n"
"ds_read_b64_tr_b16 a[144:145], v210 offset:128\n"
"v_mfma_f32_16x16x32_bf16 v[4:7], a[124:127], a[180:183], v[4:7]\n"
"ds_read_b64_tr_b16 a[146:147], v210 offset:4480\n"
"v_mfma_f32_16x16x32_bf16 v[8:11], a[128:131], a[176:179], v[8:11]\n"
"ds_read_b64_tr_b16 a[148:149], v210 offset:17536\n"
"v_mfma_f32_16x16x32_bf16 v[8:11], a[132:135], a[180:183], v[8:11]\n"
"ds_read_b64_tr_b16 a[150:151], v210 offset:21888\n"
"v_mfma_f32_16x16x32_bf16 v[12:15], a[136:139], a[176:179], v[12:15]\n"
"ds_read_b64_tr_b16 a[152:153], v211 offset:128\n"
"buffer_load_dwordx4 a[56:59], v250, s[36:39], s54 offen offset:512 nt\n"
"v_mfma_f32_16x16x32_bf16 v[12:15], a[140:143], a[180:183], v[12:15]\n"
"ds_read_b64_tr_b16 a[154:155], v211 offset:4480\n"
"v_mfma_f32_16x16x32_bf16 v[32:35], a[112:115], a[184:187], v[32:35]\n"
"ds_read_b64_tr_b16 a[156:157], v211 offset:17536\n"
"v_mfma_f32_16x16x32_bf16 v[32:35], a[116:119], a[188:191], v[32:35]\n"
"ds_read_b64_tr_b16 a[158:159], v211 offset:21888\n"
"v_mfma_f32_16x16x32_bf16 v[36:39], a[120:123], a[184:187], v[36:39]\n"
"ds_read_b64_tr_b16 a[160:161], v210 offset:192\n"
"v_mfma_f32_16x16x32_bf16 v[36:39], a[124:127], a[188:191], v[36:39]\n"
"ds_read_b64_tr_b16 a[162:163], v210 offset:4544\n"
"v_mfma_f32_16x16x32_bf16 v[40:43], a[128:131], a[184:187], v[40:43]\n"
"ds_read_b64_tr_b16 a[164:165], v210 offset:17600\n"
"buffer_load_dwordx4 a[60:63], v250, s[36:39], s54 offen offset:768 nt\n"
"s_add_u32 s53, s53, 8192\n"
"s_and_b32 s53, s53, 258047\n"
"s_lshl_b32 s38, s38, 1\n"
"s_add_u32 s54, s53, 4096\n"
"v_mfma_f32_16x16x32_bf16 v[40:43], a[132:135], a[188:191], v[40:43]\n"
"ds_read_b64_tr_b16 a[166:167], v210 offset:21952\n"
"v_mfma_f32_16x16x32_bf16 v[44:47], a[136:139], a[184:187], v[44:47]\n"
"ds_read_b64_tr_b16 a[168:169], v211 offset:192\n"
"v_mfma_f32_16x16x32_bf16 v[44:47], a[140:143], a[188:191], v[44:47]\n"
"ds_read_b64_tr_b16 a[170:171], v211 offset:4544\n"
"v_mfma_f32_16x16x32_bf16 v[64:67], a[112:115], a[192:195], v[64:67]\n"
"ds_read_b64_tr_b16 a[172:173], v211 offset:17600\n"
"v_mfma_f32_16x16x32_bf16 v[64:67], a[116:119], a[196:199], v[64:67]\n"
"ds_read_b64_tr_b16 a[174:175], v211 offset:21952\n"
"v_mfma_f32_16x16x32_bf16 v[68:71], a[120:123], a[192:195], v[68:71]\n"
"v_mfma_f32_16x16x32_bf16 v[68:71], a[124:127], a[196:199], v[68:71]\n"
"v_mfma_f32_16x16x32_bf16 v[72:75], a[128:131], a[192:195], v[72:75]\n"
"v_mfma_f32_16x16x32_bf16 v[72:75], a[132:135], a[196:199], v[72:75]\n"
"v_mfma_f32_16x16x32_bf16 v[76:79], a[136:139], a[192:195], v[76:79]\n"
"v_mfma_f32_16x16x32_bf16 v[76:79], a[140:143], a[196:199], v[76:79]\n"
"v_mfma_f32_16x16x32_bf16 v[96:99], a[112:115], a[200:203], v[96:99]\n"
"s_waitcnt lgkmcnt(0)\n"
"s_waitcnt vmcnt(18)\n"
"s_barrier\n"
"v_mfma_f32_16x16x32_bf16 v[96:99], a[116:119], a[204:207], v[96:99]\n"
"s_add_u32 m0, s49, 60928\n"
"v_mfma_f32_16x16x32_bf16 v[100:103], a[120:123], a[200:203], v[100:103]\n"
"buffer_load_dwordx4 v212, s[32:35], s52 offen lds\n"
"v_mfma_f32_16x16x32_bf16 v[100:103], a[124:127], a[204:207], v[100:103]\n"
"v_mfma_f32_16x16x32_bf16 v[104:107], a[128:131], a[200:203], v[104:107]\n"
"v_mfma_f32_16x16x32_bf16 v[104:107], a[132:135], a[204:207], v[104:107]\n"
"v_mfma_f32_16x16x32_bf16 v[108:111], a[136:139], a[200:203], v[108:111]\n"
"v_mfma_f32_16x16x32_bf16 v[108:111], a[140:143], a[204:207], v[108:111]\n"
"s_add_u32 m0, s49, 62016\n"
"v_mfma_f32_16x16x32_bf16 v[128:131], a[112:115], a[208:211], v[128:131]\n"
"buffer_load_dwordx4 v213, s[32:35], s52 offen lds\n"
"v_mfma_f32_16x16x32_bf16 v[128:131], a[116:119], a[212:215], v[128:131]\n"
"v_mfma_f32_16x16x32_bf16 v[132:135], a[120:123], a[208:211], v[132:135]\n"
"v_mfma_f32_16x16x32_bf16 v[132:135], a[124:127], a[212:215], v[132:135]\n"
"v_mfma_f32_16x16x32_bf16 v[136:139], a[128:131], a[208:211], v[136:139]\n"
"v_mfma_f32_16x16x32_bf16 v[136:139], a[132:135], a[212:215], v[136:139]\n"
"s_add_u32 m0, s49, 63104\n"
"v_mfma_f32_16x16x32_bf16 v[140:143], a[136:139], a[208:211], v[140:143]\n"
"buffer_load_dwordx4 v214, s[32:35], s52 offen lds\n"
"v_mfma_f32_16x16x32_bf16 v[140:143], a[140:143], a[212:215], v[140:143]\n"
"v_mfma_f32_16x16x32_bf16 v[160:163], a[112:115], a[216:219], v[160:163]\n"
"v_mfma_f32_16x16x32_bf16 v[160:163], a[116:119], a[220:223], v[160:163]\n"
"v_mfma_f32_16x16x32_bf16 v[164:167], a[120:123], a[216:219], v[164:167]\n"
"v_mfma_f32_16x16x32_bf16 v[164:167], a[124:127], a[220:223], v[164:167]\n"
"s_add_u32 m0, s49, 64192\n"
"v_mfma_f32_16x16x32_bf16 v[168:171], a[128:131], a[216:219], v[168:171]\n"
"buffer_load_dwordx4 v215, s[32:35], s52 offen lds\n"
"s_add_u32 s52, s52, 262144\n"
"v_mfma_f32_16x16x32_bf16 v[168:171], a[132:135], a[220:223], v[168:171]\n"
"v_mfma_f32_16x16x32_bf16 v[172:175], a[136:139], a[216:219], v[172:175]\n"
"v_mfma_f32_16x16x32_bf16 v[172:175], a[140:143], a[220:223], v[172:175]\n"
"v_mfma_f32_16x16x32_bf16 v[192:195], a[144:147], a[224:227], v[192:195]\n"
"v_mfma_f32_16x16x32_bf16 v[192:195], a[148:151], a[228:231], v[192:195]\n"
"s_add_u32 m0, s49, 78336\n"
"v_mfma_f32_16x16x32_bf16 v[196:199], a[152:155], a[224:227], v[196:199]\n"
"buffer_load_dwordx4 v212, s[32:35], s52 offen lds\n"
"v_mfma_f32_16x16x32_bf16 v[196:199], a[156:159], a[228:231], v[196:199]\n"
"v_mfma_f32_16x16x32_bf16 v[200:203], a[160:163], a[224:227], v[200:203]\n"
"v_mfma_f32_16x16x32_bf16 v[200:203], a[164:167], a[228:231], v[200:203]\n"
"v_mfma_f32_16x16x32_bf16 v[204:207], a[168:171], a[224:227], v[204:207]\n"
"v_mfma_f32_16x16x32_bf16 v[204:207], a[172:175], a[228:231], v[204:207]\n"
"s_add_u32 m0, s49, 79424\n"
"v_mfma_f32_16x16x32_bf16 v[16:19], a[144:147], a[176:179], v[16:19]\n"
"ds_read_b128 a[224:227], v209 offset:26112\n"
"buffer_load_dwordx4 v213, s[32:35], s52 offen lds\n"
"v_mfma_f32_16x16x32_bf16 v[16:19], a[148:151], a[180:183], v[16:19]\n"
"ds_read_b128 a[228:231], v209 offset:26176\n"
"v_mfma_f32_16x16x32_bf16 v[20:23], a[152:155], a[176:179], v[20:23]\n"
"v_mfma_f32_16x16x32_bf16 v[20:23], a[156:159], a[180:183], v[20:23]\n"
"v_mfma_f32_16x16x32_bf16 v[24:27], a[160:163], a[176:179], v[24:27]\n"
"v_mfma_f32_16x16x32_bf16 v[24:27], a[164:167], a[180:183], v[24:27]\n"
"s_add_u32 m0, s49, 80512\n"
"v_mfma_f32_16x16x32_bf16 v[28:31], a[168:171], a[176:179], v[28:31]\n"
"buffer_load_dwordx4 v214, s[32:35], s52 offen lds\n"
"v_mfma_f32_16x16x32_bf16 v[28:31], a[172:175], a[180:183], v[28:31]\n"
"v_mfma_f32_16x16x32_bf16 v[48:51], a[144:147], a[184:187], v[48:51]\n"
"ds_read_b128 a[176:179], v209 offset:13056\n"
"v_mfma_f32_16x16x32_bf16 v[48:51], a[148:151], a[188:191], v[48:51]\n"
"ds_read_b128 a[180:183], v209 offset:13120\n"
"v_mfma_f32_16x16x32_bf16 v[52:55], a[152:155], a[184:187], v[52:55]\n"
"v_mfma_f32_16x16x32_bf16 v[52:55], a[156:159], a[188:191], v[52:55]\n"
"s_add_u32 m0, s49, 81600\n"
"v_mfma_f32_16x16x32_bf16 v[56:59], a[160:163], a[184:187], v[56:59]\n"
"buffer_load_dwordx4 v215, s[32:35], s52 offen lds\n"
"s_add_u32 s52, s52, 262144\n"
"v_mfma_f32_16x16x32_bf16 v[56:59], a[164:167], a[188:191], v[56:59]\n"
"v_mfma_f32_16x16x32_bf16 v[60:63], a[168:171], a[184:187], v[60:63]\n"
"v_mfma_f32_16x16x32_bf16 v[60:63], a[172:175], a[188:191], v[60:63]\n"
"v_mfma_f32_16x16x32_bf16 v[80:83], a[144:147], a[192:195], v[80:83]\n"
"ds_read_b128 a[184:187], v209 offset:15232\n"
"v_mfma_f32_16x16x32_bf16 v[80:83], a[148:151], a[196:199], v[80:83]\n"
"ds_read_b128 a[188:191], v209 offset:15296\n"
"s_add_u32 m0, s48, 35904\n"
"v_mfma_f32_16x16x32_bf16 v[84:87], a[152:155], a[192:195], v[84:87]\n"
"buffer_load_dwordx4 v216, s[28:31], s51 offen lds\n"
"v_mfma_f32_16x16x32_bf16 v[84:87], a[156:159], a[196:199], v[84:87]\n"
"v_mfma_f32_16x16x32_bf16 v[88:91], a[160:163], a[192:195], v[88:91]\n"
"v_mfma_f32_16x16x32_bf16 v[88:91], a[164:167], a[196:199], v[88:91]\n"
"s_waitcnt vmcnt(17)\n"
"s_barrier\n"
"v_mfma_f32_16x16x32_bf16 v[92:95], a[168:171], a[192:195], v[92:95]\n"
"ds_read_b64_tr_b16 a[112:113], v210 offset:34816\n"
"v_mfma_f32_16x16x32_bf16 v[92:95], a[172:175], a[196:199], v[92:95]\n"
"s_add_u32 m0, s48, 36992\n"
"ds_read_b64_tr_b16 a[114:115], v210 offset:39168\n"
"v_mfma_f32_16x16x32_bf16 v[112:115], a[144:147], a[200:203], v[112:115]\n"
"ds_read_b128 a[192:195], v209 offset:17408\n"
"buffer_load_dwordx4 v217, s[28:31], s51 offen lds\n"
"ds_read_b64_tr_b16 a[116:117], v210 offset:52224\n"
"v_mfma_f32_16x16x32_bf16 v[112:115], a[148:151], a[204:207], v[112:115]\n"
"ds_read_b128 a[196:199], v209 offset:17472\n"
"ds_read_b64_tr_b16 a[118:119], v210 offset:56576\n"
"v_mfma_f32_16x16x32_bf16 v[116:119], a[152:155], a[200:203], v[116:119]\n"
"ds_read_b64_tr_b16 a[120:121], v211 offset:34816\n"
"v_mfma_f32_16x16x32_bf16 v[116:119], a[156:159], a[204:207], v[116:119]\n"
"ds_read_b64_tr_b16 a[122:123], v211 offset:39168\n"
"v_mfma_f32_16x16x32_bf16 v[120:123], a[160:163], a[200:203], v[120:123]\n"
"ds_read_b64_tr_b16 a[124:125], v211 offset:52224\n"
"v_mfma_f32_16x16x32_bf16 v[120:123], a[164:167], a[204:207], v[120:123]\n"
"s_add_u32 m0, s48, 38080\n"
"ds_read_b64_tr_b16 a[126:127], v211 offset:56576\n"
"v_mfma_f32_16x16x32_bf16 v[124:127], a[168:171], a[200:203], v[124:127]\n"
"buffer_load_dwordx4 v218, s[28:31], s51 offen lds\n"
"ds_read_b64_tr_b16 a[128:129], v210 offset:34880\n"
"v_mfma_f32_16x16x32_bf16 v[124:127], a[172:175], a[204:207], v[124:127]\n"
"ds_read_b64_tr_b16 a[130:131], v210 offset:39232\n"
"v_mfma_f32_16x16x32_bf16 v[144:147], a[144:147], a[208:211], v[144:147]\n"
"ds_read_b128 a[200:203], v209 offset:19584\n"
"ds_read_b64_tr_b16 a[132:133], v210 offset:52288\n"
"v_mfma_f32_16x16x32_bf16 v[144:147], a[148:151], a[212:215], v[144:147]\n"
"ds_read_b128 a[204:207], v209 offset:19648\n"
"ds_read_b64_tr_b16 a[134:135], v210 offset:56640\n"
"v_mfma_f32_16x16x32_bf16 v[148:151], a[152:155], a[208:211], v[148:151]\n"
"ds_read_b64_tr_b16 a[136:137], v211 offset:34880\n"
"v_mfma_f32_16x16x32_bf16 v[148:151], a[156:159], a[212:215], v[148:151]\n"
"s_add_u32 m0, s48, 39168\n"
"ds_read_b64_tr_b16 a[138:139], v211 offset:39232\n"
"v_mfma_f32_16x16x32_bf16 v[152:155], a[160:163], a[208:211], v[152:155]\n"
"buffer_load_dwordx4 v219, s[28:31], s51 offen lds\n"
"ds_read_b64_tr_b16 a[140:141], v211 offset:52288\n"
"v_mfma_f32_16x16x32_bf16 v[152:155], a[164:167], a[212:215], v[152:155]\n"
"ds_read_b64_tr_b16 a[142:143], v211 offset:56640\n"
"v_mfma_f32_16x16x32_bf16 v[156:159], a[168:171], a[208:211], v[156:159]\n"
"v_mfma_f32_16x16x32_bf16 v[156:159], a[172:175], a[212:215], v[156:159]\n"
"v_mfma_f32_16x16x32_bf16 v[176:179], a[144:147], a[216:219], v[176:179]\n"
"ds_read_b128 a[208:211], v209 offset:21760\n"
"v_mfma_f32_16x16x32_bf16 v[176:179], a[148:151], a[220:223], v[176:179]\n"
"ds_read_b128 a[212:215], v209 offset:21824\n"
"s_add_u32 m0, s48, 40256\n"
"v_mfma_f32_16x16x32_bf16 v[180:183], a[152:155], a[216:219], v[180:183]\n"
"buffer_load_dwordx4 v220, s[28:31], s51 offen lds\n"
"v_mfma_f32_16x16x32_bf16 v[180:183], a[156:159], a[220:223], v[180:183]\n"
"v_mfma_f32_16x16x32_bf16 v[184:187], a[160:163], a[216:219], v[184:187]\n"
"v_mfma_f32_16x16x32_bf16 v[184:187], a[164:167], a[220:223], v[184:187]\n"
"v_mfma_f32_16x16x32_bf16 v[188:191], a[168:171], a[216:219], v[188:191]\n"
"s_waitcnt lgkmcnt(2)\n"
"v_mfma_f32_16x16x32_bf16 v[188:191], a[172:175], a[220:223], v[188:191]\n"
"v_mfma_f32_16x16x32_bf16 v[0:3], a[112:115], a[176:179], v[0:3]\n"
"ds_read_b128 a[216:219], v209 offset:23936\n"
"s_add_u32 m0, s48, 41344\n"
"v_mfma_f32_16x16x32_bf16 v[0:3], a[116:119], a[180:183], v[0:3]\n"
"ds_read_b128 a[220:223], v209 offset:24000\n"
"buffer_load_dwordx4 v221, s[28:31], s51 offen lds\n"
"s_add_u32 s51, s51, 128\n"
"v_mfma_f32_16x16x32_bf16 v[4:7], a[120:123], a[176:179], v[4:7]\n"
"ds_read_b64_tr_b16 a[144:145], v210 offset:34944\n"
"v_mfma_f32_16x16x32_bf16 v[4:7], a[124:127], a[180:183], v[4:7]\n"
"ds_read_b64_tr_b16 a[146:147], v210 offset:39296\n"
"v_mfma_f32_16x16x32_bf16 v[8:11], a[128:131], a[176:179], v[8:11]\n"
"ds_read_b64_tr_b16 a[148:149], v210 offset:52352\n"
"v_mfma_f32_16x16x32_bf16 v[8:11], a[132:135], a[180:183], v[8:11]\n"
"ds_read_b64_tr_b16 a[150:151], v210 offset:56704\n"
"v_mfma_f32_16x16x32_bf16 v[12:15], a[136:139], a[176:179], v[12:15]\n"
"ds_read_b64_tr_b16 a[152:153], v211 offset:34944\n"
"buffer_load_dwordx4 a[64:67], v250, s[36:39], s53 offen nt\n"
"v_mfma_f32_16x16x32_bf16 v[12:15], a[140:143], a[180:183], v[12:15]\n"
"ds_read_b64_tr_b16 a[154:155], v211 offset:39296\n"
"v_mfma_f32_16x16x32_bf16 v[32:35], a[112:115], a[184:187], v[32:35]\n"
"ds_read_b64_tr_b16 a[156:157], v211 offset:52352\n"
"v_mfma_f32_16x16x32_bf16 v[32:35], a[116:119], a[188:191], v[32:35]\n"
"ds_read_b64_tr_b16 a[158:159], v211 offset:56704\n"
"v_mfma_f32_16x16x32_bf16 v[36:39], a[120:123], a[184:187], v[36:39]\n"
"ds_read_b64_tr_b16 a[160:161], v210 offset:35008\n"
"v_mfma_f32_16x16x32_bf16 v[36:39], a[124:127], a[188:191], v[36:39]\n"
"ds_read_b64_tr_b16 a[162:163], v210 offset:39360\n"
"v_mfma_f32_16x16x32_bf16 v[40:43], a[128:131], a[184:187], v[40:43]\n"
"ds_read_b64_tr_b16 a[164:165], v210 offset:52416\n"
"buffer_load_dwordx4 a[68:71], v250, s[36:39], s53 offen offset:256 nt\n"
"v_mfma_f32_16x16x32_bf16 v[40:43], a[132:135], a[188:191], v[40:43]\n"
"ds_read_b64_tr_b16 a[166:167], v210 offset:56768\n"
"v_mfma_f32_16x16x32_bf16 v[44:47], a[136:139], a[184:187], v[44:47]\n"
"ds_read_b64_tr_b16 a[168:169], v211 offset:35008\n"
"v_mfma_f32_16x16x32_bf16 v[44:47], a[140:143], a[188:191], v[44:47]\n"
"ds_read_b64_tr_b16 a[170:171], v211 offset:39360\n"
"v_mfma_f32_16x16x32_bf16 v[64:67], a[112:115], a[192:195], v[64:67]\n"
"ds_read_b64_tr_b16 a[172:173], v211 offset:52416\n"
"v_mfma_f32_16x16x32_bf16 v[64:67], a[116:119], a[196:199], v[64:67]\n"
"ds_read_b64_tr_b16 a[174:175], v211 offset:56768\n"
"v_mfma_f32_16x16x32_bf16 v[68:71], a[120:123], a[192:195], v[68:71]\n"
"v_mfma_f32_16x16x32_bf16 v[68:71], a[124:127], a[196:199], v[68:71]\n"
"v_mfma_f32_16x16x32_bf16 v[72:75], a[128:131], a[192:195], v[72:75]\n"
"v_mfma_f32_16x16x32_bf16 v[72:75], a[132:135], a[196:199], v[72:75]\n"
"v_mfma_f32_16x16x32_bf16 v[76:79], a[136:139], a[192:195], v[76:79]\n"
"v_mfma_f32_16x16x32_bf16 v[76:79], a[140:143], a[196:199], v[76:79]\n"
"v_mfma_f32_16x16x32_bf16 v[96:99], a[112:115], a[200:203], v[96:99]\n"
"s_waitcnt lgkmcnt(0)\n"
"s_waitcnt vmcnt(18)\n"
"s_barrier\n"
"v_mfma_f32_16x16x32_bf16 v[96:99], a[116:119], a[204:207], v[96:99]\n"
"s_add_u32 m0, s49, 95744\n"
"v_mfma_f32_16x16x32_bf16 v[100:103], a[120:123], a[200:203], v[100:103]\n"
"buffer_load_dwordx4 v212, s[32:35], s52 offen lds\n"
"v_mfma_f32_16x16x32_bf16 v[100:103], a[124:127], a[204:207], v[100:103]\n"
"v_mfma_f32_16x16x32_bf16 v[104:107], a[128:131], a[200:203], v[104:107]\n"
"v_mfma_f32_16x16x32_bf16 v[104:107], a[132:135], a[204:207], v[104:107]\n"
"v_mfma_f32_16x16x32_bf16 v[108:111], a[136:139], a[200:203], v[108:111]\n"
"v_mfma_f32_16x16x32_bf16 v[108:111], a[140:143], a[204:207], v[108:111]\n"
"s_add_u32 m0, s49, 96832\n"
"v_mfma_f32_16x16x32_bf16 v[128:131], a[112:115], a[208:211], v[128:131]\n"
"buffer_load_dwordx4 v213, s[32:35], s52 offen lds\n"
"v_mfma_f32_16x16x32_bf16 v[128:131], a[116:119], a[212:215], v[128:131]\n"
"v_mfma_f32_16x16x32_bf16 v[132:135], a[120:123], a[208:211], v[132:135]\n"
"v_mfma_f32_16x16x32_bf16 v[132:135], a[124:127], a[212:215], v[132:135]\n"
"v_mfma_f32_16x16x32_bf16 v[136:139], a[128:131], a[208:211], v[136:139]\n"
"v_mfma_f32_16x16x32_bf16 v[136:139], a[132:135], a[212:215], v[136:139]\n"
"s_add_u32 m0, s49, 97920\n"
"v_mfma_f32_16x16x32_bf16 v[140:143], a[136:139], a[208:211], v[140:143]\n"
"buffer_load_dwordx4 v214, s[32:35], s52 offen lds\n"
"v_mfma_f32_16x16x32_bf16 v[140:143], a[140:143], a[212:215], v[140:143]\n"
"v_mfma_f32_16x16x32_bf16 v[160:163], a[112:115], a[216:219], v[160:163]\n"
"v_mfma_f32_16x16x32_bf16 v[160:163], a[116:119], a[220:223], v[160:163]\n"
"v_mfma_f32_16x16x32_bf16 v[164:167], a[120:123], a[216:219], v[164:167]\n"
"v_mfma_f32_16x16x32_bf16 v[164:167], a[124:127], a[220:223], v[164:167]\n"
"s_add_u32 m0, s49, 99008\n"
"v_mfma_f32_16x16x32_bf16 v[168:171], a[128:131], a[216:219], v[168:171]\n"
"buffer_load_dwordx4 v215, s[32:35], s52 offen lds\n"
"s_add_u32 s52, s52, 262144\n"
"v_mfma_f32_16x16x32_bf16 v[168:171], a[132:135], a[220:223], v[168:171]\n"
"v_mfma_f32_16x16x32_bf16 v[172:175], a[136:139], a[216:219], v[172:175]\n"
"v_mfma_f32_16x16x32_bf16 v[172:175], a[140:143], a[220:223], v[172:175]\n"
"v_mfma_f32_16x16x32_bf16 v[192:195], a[144:147], a[224:227], v[192:195]\n"
"v_mfma_f32_16x16x32_bf16 v[192:195], a[148:151], a[228:231], v[192:195]\n"
"s_add_u32 m0, s49, 113152\n"
"v_mfma_f32_16x16x32_bf16 v[196:199], a[152:155], a[224:227], v[196:199]\n"
"buffer_load_dwordx4 v212, s[32:35], s52 offen lds\n"
"v_mfma_f32_16x16x32_bf16 v[196:199], a[156:159], a[228:231], v[196:199]\n"
"v_mfma_f32_16x16x32_bf16 v[200:203], a[160:163], a[224:227], v[200:203]\n"
"v_mfma_f32_16x16x32_bf16 v[200:203], a[164:167], a[228:231], v[200:203]\n"
"v_mfma_f32_16x16x32_bf16 v[204:207], a[168:171], a[224:227], v[204:207]\n"
"v_mfma_f32_16x16x32_bf16 v[204:207], a[172:175], a[228:231], v[204:207]\n"
"s_add_u32 m0, s49, 114240\n"
"v_mfma_f32_16x16x32_bf16 v[16:19], a[144:147], a[176:179], v[16:19]\n"
"ds_read_b128 a[224:227], v208 offset:26112\n"
"buffer_load_dwordx4 v213, s[32:35], s52 offen lds\n"
"v_mfma_f32_16x16x32_bf16 v[16:19], a[148:151], a[180:183], v[16:19]\n"
"ds_read_b128 a[228:231], v208 offset:26176\n"
"v_mfma_f32_16x16x32_bf16 v[20:23], a[152:155], a[176:179], v[20:23]\n"
"v_mfma_f32_16x16x32_bf16 v[20:23], a[156:159], a[180:183], v[20:23]\n"
"v_mfma_f32_16x16x32_bf16 v[24:27], a[160:163], a[176:179], v[24:27]\n"
"v_mfma_f32_16x16x32_bf16 v[24:27], a[164:167], a[180:183], v[24:27]\n"
"s_add_u32 m0, s49, 115328\n"
"v_mfma_f32_16x16x32_bf16 v[28:31], a[168:171], a[176:179], v[28:31]\n"
"buffer_load_dwordx4 v214, s[32:35], s52 offen lds\n"
"v_mfma_f32_16x16x32_bf16 v[28:31], a[172:175], a[180:183], v[28:31]\n"
"v_mfma_f32_16x16x32_bf16 v[48:51], a[144:147], a[184:187], v[48:51]\n"
"ds_read_b128 a[176:179], v208 offset:13056\n"
"v_mfma_f32_16x16x32_bf16 v[48:51], a[148:151], a[188:191], v[48:51]\n"
"ds_read_b128 a[180:183], v208 offset:13120\n"
"v_mfma_f32_16x16x32_bf16 v[52:55], a[152:155], a[184:187], v[52:55]\n"
"v_mfma_f32_16x16x32_bf16 v[52:55], a[156:159], a[188:191], v[52:55]\n"
"s_add_u32 m0, s49, 116416\n"
"v_mfma_f32_16x16x32_bf16 v[56:59], a[160:163], a[184:187], v[56:59]\n"
"buffer_load_dwordx4 v215, s[32:35], s52 offen lds\n"
"s_add_u32 s52, s52, 262144\n"
"v_mfma_f32_16x16x32_bf16 v[56:59], a[164:167], a[188:191], v[56:59]\n"
"v_mfma_f32_16x16x32_bf16 v[60:63], a[168:171], a[184:187], v[60:63]\n"
"v_mfma_f32_16x16x32_bf16 v[60:63], a[172:175], a[188:191], v[60:63]\n"
"v_mfma_f32_16x16x32_bf16 v[80:83], a[144:147], a[192:195], v[80:83]\n"
"ds_read_b128 a[184:187], v208 offset:15232\n"
"v_mfma_f32_16x16x32_bf16 v[80:83], a[148:151], a[196:199], v[80:83]\n"
"ds_read_b128 a[188:191], v208 offset:15296\n"
"s_add_u32 m0, s48, 133824\n"
"v_mfma_f32_16x16x32_bf16 v[84:87], a[152:155], a[192:195], v[84:87]\n"
"buffer_load_dwordx4 v216, s[28:31], s51 offen lds\n"
"v_mfma_f32_16x16x32_bf16 v[84:87], a[156:159], a[196:199], v[84:87]\n"
"v_mfma_f32_16x16x32_bf16 v[88:91], a[160:163], a[192:195], v[88:91]\n"
"v_mfma_f32_16x16x32_bf16 v[88:91], a[164:167], a[196:199], v[88:91]\n"
"s_waitcnt vmcnt(17)\n"
"s_barrier\n"
"v_mfma_f32_16x16x32_bf16 v[92:95], a[168:171], a[192:195], v[92:95]\n"
"ds_read_b64_tr_b16 a[112:113], v210\n"
"v_mfma_f32_16x16x32_bf16 v[92:95], a[172:175], a[196:199], v[92:95]\n"
"s_add_u32 m0, s48, 134912\n"
"ds_read_b64_tr_b16 a[114:115], v210 offset:4352\n"
"v_mfma_f32_16x16x32_bf16 v[112:115], a[144:147], a[200:203], v[112:115]\n"
"ds_read_b128 a[192:195], v208 offset:17408\n"
"buffer_load_dwordx4 v217, s[28:31], s51 offen lds\n"
"ds_read_b64_tr_b16 a[116:117], v210 offset:17408\n"
"v_mfma_f32_16x16x32_bf16 v[112:115], a[148:151], a[204:207], v[112:115]\n"
"ds_read_b128 a[196:199], v208 offset:17472\n"
"ds_read_b64_tr_b16 a[118:119], v210 offset:21760\n"
"v_mfma_f32_16x16x32_bf16 v[116:119], a[152:155], a[200:203], v[116:119]\n"
"ds_read_b64_tr_b16 a[120:121], v211\n"
"v_mfma_f32_16x16x32_bf16 v[116:119], a[156:159], a[204:207], v[116:119]\n"
"ds_read_b64_tr_b16 a[122:123], v211 offset:4352\n"
"v_mfma_f32_16x16x32_bf16 v[120:123], a[160:163], a[200:203], v[120:123]\n"
"ds_read_b64_tr_b16 a[124:125], v211 offset:17408\n"
"v_mfma_f32_16x16x32_bf16 v[120:123], a[164:167], a[204:207], v[120:123]\n"
"s_add_u32 m0, s48, 136000\n"
"ds_read_b64_tr_b16 a[126:127], v211 offset:21760\n"
"v_mfma_f32_16x16x32_bf16 v[124:127], a[168:171], a[200:203], v[124:127]\n"
"buffer_load_dwordx4 v218, s[28:31], s51 offen lds\n"
"ds_read_b64_tr_b16 a[128:129], v210 offset:64\n"
"v_mfma_f32_16x16x32_bf16 v[124:127], a[172:175], a[204:207], v[124:127]\n"
"ds_read_b64_tr_b16 a[130:131], v210 offset:4416\n"
"v_mfma_f32_16x16x32_bf16 v[144:147], a[144:147], a[208:211], v[144:147]\n"
"ds_read_b128 a[200:203], v208 offset:19584\n"
"ds_read_b64_tr_b16 a[132:133], v210 offset:17472\n"
"v_mfma_f32_16x16x32_bf16 v[144:147], a[148:151], a[212:215], v[144:147]\n"
"ds_read_b128 a[204:207], v208 offset:19648\n"
"ds_read_b64_tr_b16 a[134:135], v210 offset:21824\n"
"v_mfma_f32_16x16x32_bf16 v[148:151], a[152:155], a[208:211], v[148:151]\n"
"ds_read_b64_tr_b16 a[136:137], v211 offset:64\n"
"v_mfma_f32_16x16x32_bf16 v[148:151], a[156:159], a[212:215], v[148:151]\n"
"s_add_u32 m0, s48, 137088\n"
"ds_read_b64_tr_b16 a[138:139], v211 offset:4416\n"
"v_mfma_f32_16x16x32_bf16 v[152:155], a[160:163], a[208:211], v[152:155]\n"
"buffer_load_dwordx4 v219, s[28:31], s51 offen lds\n"
"ds_read_b64_tr_b16 a[140:141], v211 offset:17472\n"
"v_mfma_f32_16x16x32_bf16 v[152:155], a[164:167], a[212:215], v[152:155]\n"
"ds_read_b64_tr_b16 a[142:143], v211 offset:21824\n"
"v_mfma_f32_16x16x32_bf16 v[156:159], a[168:171], a[208:211], v[156:159]\n"
"v_mfma_f32_16x16x32_bf16 v[156:159], a[172:175], a[212:215], v[156:159]\n"
"v_mfma_f32_16x16x32_bf16 v[176:179], a[144:147], a[216:219], v[176:179]\n"
"ds_read_b128 a[208:211], v208 offset:21760\n"
"v_mfma_f32_16x16x32_bf16 v[176:179], a[148:151], a[220:223], v[176:179]\n"
"ds_read_b128 a[212:215], v208 offset:21824\n"
"s_add_u32 m0, s48, 138176\n"
"v_mfma_f32_16x16x32_bf16 v[180:183], a[152:155], a[216:219], v[180:183]\n"
"buffer_load_dwordx4 v220, s[28:31], s51 offen lds\n"
"v_mfma_f32_16x16x32_bf16 v[180:183], a[156:159], a[220:223], v[180:183]\n"
"v_mfma_f32_16x16x32_bf16 v[184:187], a[160:163], a[216:219], v[184:187]\n"
"v_mfma_f32_16x16x32_bf16 v[184:187], a[164:167], a[220:223], v[184:187]\n"
"v_mfma_f32_16x16x32_bf16 v[188:191], a[168:171], a[216:219], v[188:191]\n"
"s_waitcnt lgkmcnt(2)\n"
"v_mfma_f32_16x16x32_bf16 v[188:191], a[172:175], a[220:223], v[188:191]\n"
"v_mfma_f32_16x16x32_bf16 v[0:3], a[112:115], a[176:179], v[0:3]\n"
"ds_read_b128 a[216:219], v208 offset:23936\n"
"s_add_u32 m0, s48, 139264\n"
"v_mfma_f32_16x16x32_bf16 v[0:3], a[116:119], a[180:183], v[0:3]\n"
"ds_read_b128 a[220:223], v208 offset:24000\n"
"buffer_load_dwordx4 v221, s[28:31], s51 offen lds\n"
"s_add_u32 s51, s51, 128\n"
"v_mfma_f32_16x16x32_bf16 v[4:7], a[120:123], a[176:179], v[4:7]\n"
"ds_read_b64_tr_b16 a[144:145], v210 offset:128\n"
"v_mfma_f32_16x16x32_bf16 v[4:7], a[124:127], a[180:183], v[4:7]\n"
"ds_read_b64_tr_b16 a[146:147], v210 offset:4480\n"
"v_mfma_f32_16x16x32_bf16 v[8:11], a[128:131], a[176:179], v[8:11]\n"
"ds_read_b64_tr_b16 a[148:149], v210 offset:17536\n"
"v_mfma_f32_16x16x32_bf16 v[8:11], a[132:135], a[180:183], v[8:11]\n"
"ds_read_b64_tr_b16 a[150:151], v210 offset:21888\n"
"v_mfma_f32_16x16x32_bf16 v[12:15], a[136:139], a[176:179], v[12:15]\n"
"ds_read_b64_tr_b16 a[152:153], v211 offset:128\n"
"buffer_load_dwordx4 a[72:75], v250, s[36:39], s53 offen offset:512 nt\n"
"v_mfma_f32_16x16x32_bf16 v[12:15], a[140:143], a[180:183], v[12:15]\n"
"ds_read_b64_tr_b16 a[154:155], v211 offset:4480\n"
"v_mfma_f32_16x16x32_bf16 v[32:35], a[112:115], a[184:187], v[32:35]\n"
"ds_read_b64_tr_b16 a[156:157], v211 offset:17536\n"
"v_mfma_f32_16x16x32_bf16 v[32:35], a[116:119], a[188:191], v[32:35]\n"
"ds_read_b64_tr_b16 a[158:159], v211 offset:21888\n"
"v_mfma_f32_16x16x32_bf16 v[36:39], a[120:123], a[184:187], v[36:39]\n"
"ds_read_b64_tr_b16 a[160:161], v210 offset:192\n"
"v_mfma_f32_16x16x32_bf16 v[36:39], a[124:127], a[188:191], v[36:39]\n"
"ds_read_b64_tr_b16 a[162:163], v210 offset:4544\n"
"v_mfma_f32_16x16x32_bf16 v[40:43], a[128:131], a[184:187], v[40:43]\n"
"ds_read_b64_tr_b16 a[164:165], v210 offset:17600\n"
"buffer_load_dwordx4 a[76:79], v250, s[36:39], s53 offen offset:768 nt\n"
"v_mfma_f32_16x16x32_bf16 v[40:43], a[132:135], a[188:191], v[40:43]\n"
"ds_read_b64_tr_b16 a[166:167], v210 offset:21952\n"
"v_mfma_f32_16x16x32_bf16 v[44:47], a[136:139], a[184:187], v[44:47]\n"
"ds_read_b64_tr_b16 a[168:169], v211 offset:192\n"
"v_mfma_f32_16x16x32_bf16 v[44:47], a[140:143], a[188:191], v[44:47]\n"
"ds_read_b64_tr_b16 a[170:171], v211 offset:4544\n"
"v_mfma_f32_16x16x32_bf16 v[64:67], a[112:115], a[192:195], v[64:67]\n"
"ds_read_b64_tr_b16 a[172:173], v211 offset:17600\n"
"v_mfma_f32_16x16x32_bf16 v[64:67], a[116:119], a[196:199], v[64:67]\n"
"ds_read_b64_tr_b16 a[174:175], v211 offset:21952\n"
"v_mfma_f32_16x16x32_bf16 v[68:71], a[120:123], a[192:195], v[68:71]\n"
"v_mfma_f32_16x16x32_bf16 v[68:71], a[124:127], a[196:199], v[68:71]\n"
"v_mfma_f32_16x16x32_bf16 v[72:75], a[128:131], a[192:195], v[72:75]\n"
"v_mfma_f32_16x16x32_bf16 v[72:75], a[132:135], a[196:199], v[72:75]\n"
"v_mfma_f32_16x16x32_bf16 v[76:79], a[136:139], a[192:195], v[76:79]\n"
"v_mfma_f32_16x16x32_bf16 v[76:79], a[140:143], a[196:199], v[76:79]\n"
"v_mfma_f32_16x16x32_bf16 v[96:99], a[112:115], a[200:203], v[96:99]\n"
"s_waitcnt lgkmcnt(0)\n"
"s_waitcnt vmcnt(18)\n"
"s_barrier\n"
"v_mfma_f32_16x16x32_bf16 v[96:99], a[116:119], a[204:207], v[96:99]\n"
"s_add_u32 m0, s49, 60928\n"
"v_mfma_f32_16x16x32_bf16 v[100:103], a[120:123], a[200:203], v[100:103]\n"
"buffer_load_dwordx4 v212, s[32:35], s52 offen lds\n"
"v_mfma_f32_16x16x32_bf16 v[100:103], a[124:127], a[204:207], v[100:103]\n"
"v_mfma_f32_16x16x32_bf16 v[104:107], a[128:131], a[200:203], v[104:107]\n"
"v_mfma_f32_16x16x32_bf16 v[104:107], a[132:135], a[204:207], v[104:107]\n"
"v_mfma_f32_16x16x32_bf16 v[108:111], a[136:139], a[200:203], v[108:111]\n"
"v_mfma_f32_16x16x32_bf16 v[108:111], a[140:143], a[204:207], v[108:111]\n"
"s_add_u32 m0, s49, 62016\n"
"v_mfma_f32_16x16x32_bf16 v[128:131], a[112:115], a[208:211], v[128:131]\n"
"buffer_load_dwordx4 v213, s[32:35], s52 offen lds\n"
"v_mfma_f32_16x16x32_bf16 v[128:131], a[116:119], a[212:215], v[128:131]\n"
"v_mfma_f32_16x16x32_bf16 v[132:135], a[120:123], a[208:211], v[132:135]\n"
"v_mfma_f32_16x16x32_bf16 v[132:135], a[124:127], a[212:215], v[132:135]\n"
"v_mfma_f32_16x16x32_bf16 v[136:139], a[128:131], a[208:211], v[136:139]\n"
"v_mfma_f32_16x16x32_bf16 v[136:139], a[132:135], a[212:215], v[136:139]\n"
"s_add_u32 m0, s49, 63104\n"
"v_mfma_f32_16x16x32_bf16 v[140:143], a[136:139], a[208:211], v[140:143]\n"
"buffer_load_dwordx4 v214, s[32:35], s52 offen lds\n"
"v_mfma_f32_16x16x32_bf16 v[140:143], a[140:143], a[212:215], v[140:143]\n"
"v_mfma_f32_16x16x32_bf16 v[160:163], a[112:115], a[216:219], v[160:163]\n"
"v_mfma_f32_16x16x32_bf16 v[160:163], a[116:119], a[220:223], v[160:163]\n"
"v_mfma_f32_16x16x32_bf16 v[164:167], a[120:123], a[216:219], v[164:167]\n"
"v_mfma_f32_16x16x32_bf16 v[164:167], a[124:127], a[220:223], v[164:167]\n"
"s_add_u32 m0, s49, 64192\n"
"v_mfma_f32_16x16x32_bf16 v[168:171], a[128:131], a[216:219], v[168:171]\n"
"buffer_load_dwordx4 v215, s[32:35], s52 offen lds\n"
"s_add_u32 s52, s52, 262144\n"
"v_mfma_f32_16x16x32_bf16 v[168:171], a[132:135], a[220:223], v[168:171]\n"
"v_mfma_f32_16x16x32_bf16 v[172:175], a[136:139], a[216:219], v[172:175]\n"
"v_mfma_f32_16x16x32_bf16 v[172:175], a[140:143], a[220:223], v[172:175]\n"
"v_mfma_f32_16x16x32_bf16 v[192:195], a[144:147], a[224:227], v[192:195]\n"
"v_mfma_f32_16x16x32_bf16 v[192:195], a[148:151], a[228:231], v[192:195]\n"
"s_add_u32 m0, s49, 78336\n"
"v_mfma_f32_16x16x32_bf16 v[196:199], a[152:155], a[224:227], v[196:199]\n"
"buffer_load_dwordx4 v212, s[32:35], s52 offen lds\n"
"v_mfma_f32_16x16x32_bf16 v[196:199], a[156:159], a[228:231], v[196:199]\n"
"v_mfma_f32_16x16x32_bf16 v[200:203], a[160:163], a[224:227], v[200:203]\n"
"v_mfma_f32_16x16x32_bf16 v[200:203], a[164:167], a[228:231], v[200:203]\n"
"v_mfma_f32_16x16x32_bf16 v[204:207], a[168:171], a[224:227], v[204:207]\n"
"v_mfma_f32_16x16x32_bf16 v[204:207], a[172:175], a[228:231], v[204:207]\n"
"s_add_u32 m0, s49, 79424\n"
"v_mfma_f32_16x16x32_bf16 v[16:19], a[144:147], a[176:179], v[16:19]\n"
"ds_read_b128 a[224:227], v208 offset:54400\n"
"buffer_load_dwordx4 v213, s[32:35], s52 offen lds\n"
"v_mfma_f32_16x16x32_bf16 v[16:19], a[148:151], a[180:183], v[16:19]\n"
"ds_read_b128 a[228:231], v208 offset:54464\n"
"v_mfma_f32_16x16x32_bf16 v[20:23], a[152:155], a[176:179], v[20:23]\n"
"v_mfma_f32_16x16x32_bf16 v[20:23], a[156:159], a[180:183], v[20:23]\n"
"v_mfma_f32_16x16x32_bf16 v[24:27], a[160:163], a[176:179], v[24:27]\n"
"v_mfma_f32_16x16x32_bf16 v[24:27], a[164:167], a[180:183], v[24:27]\n"
"s_add_u32 m0, s49, 80512\n"
"v_mfma_f32_16x16x32_bf16 v[28:31], a[168:171], a[176:179], v[28:31]\n"
"buffer_load_dwordx4 v214, s[32:35], s52 offen lds\n"
"v_mfma_f32_16x16x32_bf16 v[28:31], a[172:175], a[180:183], v[28:31]\n"
"v_mfma_f32_16x16x32_bf16 v[48:51], a[144:147], a[184:187], v[48:51]\n"
"ds_read_b128 a[176:179], v208 offset:41344\n"
"v_mfma_f32_16x16x32_bf16 v[48:51], a[148:151], a[188:191], v[48:51]\n"
"ds_read_b128 a[180:183], v208 offset:41408\n"
"v_mfma_f32_16x16x32_bf16 v[52:55], a[152:155], a[184:187], v[52:55]\n"
"v_mfma_f32_16x16x32_bf16 v[52:55], a[156:159], a[188:191], v[52:55]\n"
"s_add_u32 m0, s49, 81600\n"
"v_mfma_f32_16x16x32_bf16 v[56:59], a[160:163], a[184:187], v[56:59]\n"
"buffer_load_dwordx4 v215, s[32:35], s52 offen lds\n"
"s_add_u32 s52, s52, 262144\n"
"v_mfma_f32_16x16x32_bf16 v[56:59], a[164:167], a[188:191], v[56:59]\n"
"v_mfma_f32_16x16x32_bf16 v[60:63], a[168:171], a[184:187], v[60:63]\n"
"v_mfma_f32_16x16x32_bf16 v[60:63], a[172:175], a[188:191], v[60:63]\n"
"v_mfma_f32_16x16x32_bf16 v[80:83], a[144:147], a[192:195], v[80:83]\n"
"ds_read_b128 a[184:187], v208 offset:43520\n"
"v_mfma_f32_16x16x32_bf16 v[80:83], a[148:151], a[196:199], v[80:83]\n"
"ds_read_b128 a[188:191], v208 offset:43584\n"
"s_add_u32 m0, s48, 7616\n"
"v_mfma_f32_16x16x32_bf16 v[84:87], a[152:155], a[192:195], v[84:87]\n"
"buffer_load_dwordx4 v216, s[28:31], s51 offen lds\n"
"v_mfma_f32_16x16x32_bf16 v[84:87], a[156:159], a[196:199], v[84:87]\n"
"v_mfma_f32_16x16x32_bf16 v[88:91], a[160:163], a[192:195], v[88:91]\n"
"v_mfma_f32_16x16x32_bf16 v[88:91], a[164:167], a[196:199], v[88:91]\n"
"s_waitcnt vmcnt(17)\n"
"s_barrier\n"
"v_mfma_f32_16x16x32_bf16 v[92:95], a[168:171], a[192:195], v[92:95]\n"
"ds_read_b64_tr_b16 a[112:113], v210 offset:34816\n"
"v_mfma_f32_16x16x32_bf16 v[92:95], a[172:175], a[196:199], v[92:95]\n"
"s_add_u32 m0, s48, 8704\n"
"ds_read_b64_tr_b16 a[114:115], v210 offset:39168\n"
"v_mfma_f32_16x16x32_bf16 v[112:115], a[144:147], a[200:203], v[112:115]\n"
"ds_read_b128 a[192:195], v208 offset:45696\n"
"buffer_load_dwordx4 v217, s[28:31], s51 offen lds\n"
"ds_read_b64_tr_b16 a[116:117], v210 offset:52224\n"
"v_mfma_f32_16x16x32_bf16 v[112:115], a[148:151], a[204:207], v[112:115]\n"
"ds_read_b128 a[196:199], v208 offset:45760\n"
"ds_read_b64_tr_b16 a[118:119], v210 offset:56576\n"
"v_mfma_f32_16x16x32_bf16 v[116:119], a[152:155], a[200:203], v[116:119]\n"
"ds_read_b64_tr_b16 a[120:121], v211 offset:34816\n"
"v_mfma_f32_16x16x32_bf16 v[116:119], a[156:159], a[204:207], v[116:119]\n"
"ds_read_b64_tr_b16 a[122:123], v211 offset:39168\n"
"v_mfma_f32_16x16x32_bf16 v[120:123], a[160:163], a[200:203], v[120:123]\n"
"ds_read_b64_tr_b16 a[124:125], v211 offset:52224\n"
"v_mfma_f32_16x16x32_bf16 v[120:123], a[164:167], a[204:207], v[120:123]\n"
"s_add_u32 m0, s48, 9792\n"
"ds_read_b64_tr_b16 a[126:127], v211 offset:56576\n"
"v_mfma_f32_16x16x32_bf16 v[124:127], a[168:171], a[200:203], v[124:127]\n"
"buffer_load_dwordx4 v218, s[28:31], s51 offen lds\n"
"ds_read_b64_tr_b16 a[128:129], v210 offset:34880\n"
"v_mfma_f32_16x16x32_bf16 v[124:127], a[172:175], a[204:207], v[124:127]\n"
"ds_read_b64_tr_b16 a[130:131], v210 offset:39232\n"
"v_mfma_f32_16x16x32_bf16 v[144:147], a[144:147], a[208:211], v[144:147]\n"
"ds_read_b128 a[200:203], v208 offset:47872\n"
"ds_read_b64_tr_b16 a[132:133], v210 offset:52288\n"
"v_mfma_f32_16x16x32_bf16 v[144:147], a[148:151], a[212:215], v[144:147]\n"
"ds_read_b128 a[204:207], v208 offset:47936\n"
"ds_read_b64_tr_b16 a[134:135], v210 offset:56640\n"
"v_mfma_f32_16x16x32_bf16 v[148:151], a[152:155], a[208:211], v[148:151]\n"
"ds_read_b64_tr_b16 a[136:137], v211 offset:34880\n"
"v_mfma_f32_16x16x32_bf16 v[148:151], a[156:159], a[212:215], v[148:151]\n"
"s_add_u32 m0, s48, 10880\n"
"ds_read_b64_tr_b16 a[138:139], v211 offset:39232\n"
"v_mfma_f32_16x16x32_bf16 v[152:155], a[160:163], a[208:211], v[152:155]\n"
"buffer_load_dwordx4 v219, s[28:31], s51 offen lds\n"
"ds_read_b64_tr_b16 a[140:141], v211 offset:52288\n"
"v_mfma_f32_16x16x32_bf16 v[152:155], a[164:167], a[212:215], v[152:155]\n"
"ds_read_b64_tr_b16 a[142:143], v211 offset:56640\n"
"v_mfma_f32_16x16x32_bf16 v[156:159], a[168:171], a[208:211], v[156:159]\n"
"v_mfma_f32_16x16x32_bf16 v[156:159], a[172:175], a[212:215], v[156:159]\n"
"v_mfma_f32_16x16x32_bf16 v[176:179], a[144:147], a[216:219], v[176:179]\n"
"ds_read_b128 a[208:211], v208 offset:50048\n"
"v_mfma_f32_16x16x32_bf16 v[176:179], a[148:151], a[220:223], v[176:179]\n"
"ds_read_b128 a[212:215], v208 offset:50112\n"
"s_add_u32 m0, s48, 11968\n"
"v_mfma_f32_16x16x32_bf16 v[180:183], a[152:155], a[216:219], v[180:183]\n"
"buffer_load_dwordx4 v220, s[28:31], s51 offen lds\n"
"v_mfma_f32_16x16x32_bf16 v[180:183], a[156:159], a[220:223], v[180:183]\n"
"v_mfma_f32_16x16x32_bf16 v[184:187], a[160:163], a[216:219], v[184:187]\n"
"v_mfma_f32_16x16x32_bf16 v[184:187], a[164:167], a[220:223], v[184:187]\n"
"v_mfma_f32_16x16x32_bf16 v[188:191], a[168:171], a[216:219], v[188:191]\n"
"s_waitcnt lgkmcnt(2)\n"
"v_mfma_f32_16x16x32_bf16 v[188:191], a[172:175], a[220:223], v[188:191]\n"
"v_mfma_f32_16x16x32_bf16 v[0:3], a[112:115], a[176:179], v[0:3]\n"
"ds_read_b128 a[216:219], v208 offset:52224\n"
"s_add_u32 m0, s48, 13056\n"
"v_mfma_f32_16x16x32_bf16 v[0:3], a[116:119], a[180:183], v[0:3]\n"
"ds_read_b128 a[220:223], v208 offset:52288\n"
"buffer_load_dwordx4 v221, s[28:31], s51 offen lds\n"
"s_add_u32 s51, s51, 128\n"
"v_mfma_f32_16x16x32_bf16 v[4:7], a[120:123], a[176:179], v[4:7]\n"
"ds_read_b64_tr_b16 a[144:145], v210 offset:34944\n"
"v_mfma_f32_16x16x32_bf16 v[4:7], a[124:127], a[180:183], v[4:7]\n"
"ds_read_b64_tr_b16 a[146:147], v210 offset:39296\n"
"v_mfma_f32_16x16x32_bf16 v[8:11], a[128:131], a[176:179], v[8:11]\n"
"ds_read_b64_tr_b16 a[148:149], v210 offset:52352\n"
"v_mfma_f32_16x16x32_bf16 v[8:11], a[132:135], a[180:183], v[8:11]\n"
"ds_read_b64_tr_b16 a[150:151], v210 offset:56704\n"
"v_mfma_f32_16x16x32_bf16 v[12:15], a[136:139], a[176:179], v[12:15]\n"
"ds_read_b64_tr_b16 a[152:153], v211 offset:34944\n"
"buffer_load_dwordx4 a[80:83], v250, s[36:39], s54 offen nt\n"
"v_mfma_f32_16x16x32_bf16 v[12:15], a[140:143], a[180:183], v[12:15]\n"
"ds_read_b64_tr_b16 a[154:155], v211 offset:39296\n"
"v_mfma_f32_16x16x32_bf16 v[32:35], a[112:115], a[184:187], v[32:35]\n"
"ds_read_b64_tr_b16 a[156:157], v211 offset:52352\n"
"v_mfma_f32_16x16x32_bf16 v[32:35], a[116:119], a[188:191], v[32:35]\n"
"ds_read_b64_tr_b16 a[158:159], v211 offset:56704\n"
"v_mfma_f32_16x16x32_bf16 v[36:39], a[120:123], a[184:187], v[36:39]\n"
"ds_read_b64_tr_b16 a[160:161], v210 offset:35008\n"
"v_mfma_f32_16x16x32_bf16 v[36:39], a[124:127], a[188:191], v[36:39]\n"
"ds_read_b64_tr_b16 a[162:163], v210 offset:39360\n"
"v_mfma_f32_16x16x32_bf16 v[40:43], a[128:131], a[184:187], v[40:43]\n"
"ds_read_b64_tr_b16 a[164:165], v210 offset:52416\n"
"buffer_load_dwordx4 a[84:87], v250, s[36:39], s54 offen offset:256 nt\n"
"v_mfma_f32_16x16x32_bf16 v[40:43], a[132:135], a[188:191], v[40:43]\n"
"ds_read_b64_tr_b16 a[166:167], v210 offset:56768\n"
"v_mfma_f32_16x16x32_bf16 v[44:47], a[136:139], a[184:187], v[44:47]\n"
"ds_read_b64_tr_b16 a[168:169], v211 offset:35008\n"
"v_mfma_f32_16x16x32_bf16 v[44:47], a[140:143], a[188:191], v[44:47]\n"
"ds_read_b64_tr_b16 a[170:171], v211 offset:39360\n"
"v_mfma_f32_16x16x32_bf16 v[64:67], a[112:115], a[192:195], v[64:67]\n"
"ds_read_b64_tr_b16 a[172:173], v211 offset:52416\n"
"v_mfma_f32_16x16x32_bf16 v[64:67], a[116:119], a[196:199], v[64:67]\n"
"ds_read_b64_tr_b16 a[174:175], v211 offset:56768\n"
"v_mfma_f32_16x16x32_bf16 v[68:71], a[120:123], a[192:195], v[68:71]\n"
"buffer_load_dwordx4 a[88:91], v250, s[36:39], s54 offen offset:512 nt\n"
"v_mfma_f32_16x16x32_bf16 v[68:71], a[124:127], a[196:199], v[68:71]\n"
"v_mfma_f32_16x16x32_bf16 v[72:75], a[128:131], a[192:195], v[72:75]\n"
"v_mfma_f32_16x16x32_bf16 v[72:75], a[132:135], a[196:199], v[72:75]\n"
"v_mfma_f32_16x16x32_bf16 v[76:79], a[136:139], a[192:195], v[76:79]\n"
"v_mfma_f32_16x16x32_bf16 v[76:79], a[140:143], a[196:199], v[76:79]\n"
"v_mfma_f32_16x16x32_bf16 v[96:99], a[112:115], a[200:203], v[96:99]\n"
"s_waitcnt lgkmcnt(0)\n"
"s_waitcnt vmcnt(19)\n"
"s_barrier\n"
"v_mfma_f32_16x16x32_bf16 v[96:99], a[116:119], a[204:207], v[96:99]\n"
"s_add_u32 m0, s49, 95744\n"
"v_mfma_f32_16x16x32_bf16 v[100:103], a[120:123], a[200:203], v[100:103]\n"
"buffer_load_dwordx4 v212, s[32:35], s52 offen lds\n"
"v_mfma_f32_16x16x32_bf16 v[100:103], a[124:127], a[204:207], v[100:103]\n"
"v_mfma_f32_16x16x32_bf16 v[104:107], a[128:131], a[200:203], v[104:107]\n"
"v_mfma_f32_16x16x32_bf16 v[104:107], a[132:135], a[204:207], v[104:107]\n"
"v_mfma_f32_16x16x32_bf16 v[108:111], a[136:139], a[200:203], v[108:111]\n"
"v_mfma_f32_16x16x32_bf16 v[108:111], a[140:143], a[204:207], v[108:111]\n"
"s_add_u32 m0, s49, 96832\n"
"v_mfma_f32_16x16x32_bf16 v[128:131], a[112:115], a[208:211], v[128:131]\n"
"buffer_load_dwordx4 v213, s[32:35], s52 offen lds\n"
"v_mfma_f32_16x16x32_bf16 v[128:131], a[116:119], a[212:215], v[128:131]\n"
"v_mfma_f32_16x16x32_bf16 v[132:135], a[120:123], a[208:211], v[132:135]\n"
"v_mfma_f32_16x16x32_bf16 v[132:135], a[124:127], a[212:215], v[132:135]\n"
"v_mfma_f32_16x16x32_bf16 v[136:139], a[128:131], a[208:211], v[136:139]\n"
"v_mfma_f32_16x16x32_bf16 v[136:139], a[132:135], a[212:215], v[136:139]\n"
"s_add_u32 m0, s49, 97920\n"
"v_mfma_f32_16x16x32_bf16 v[140:143], a[136:139], a[208:211], v[140:143]\n"
"buffer_load_dwordx4 v214, s[32:35], s52 offen lds\n"
"v_mfma_f32_16x16x32_bf16 v[140:143], a[140:143], a[212:215], v[140:143]\n"
"v_mfma_f32_16x16x32_bf16 v[160:163], a[112:115], a[216:219], v[160:163]\n"
"v_mfma_f32_16x16x32_bf16 v[160:163], a[116:119], a[220:223], v[160:163]\n"
"v_mfma_f32_16x16x32_bf16 v[164:167], a[120:123], a[216:219], v[164:167]\n"
"v_mfma_f32_16x16x32_bf16 v[164:167], a[124:127], a[220:223], v[164:167]\n"
"s_add_u32 m0, s49, 99008\n"
"v_mfma_f32_16x16x32_bf16 v[168:171], a[128:131], a[216:219], v[168:171]\n"
"buffer_load_dwordx4 v215, s[32:35], s52 offen lds\n"
"s_add_u32 s52, s52, 262144\n"
"v_mfma_f32_16x16x32_bf16 v[168:171], a[132:135], a[220:223], v[168:171]\n"
"v_mfma_f32_16x16x32_bf16 v[172:175], a[136:139], a[216:219], v[172:175]\n"
"v_mfma_f32_16x16x32_bf16 v[172:175], a[140:143], a[220:223], v[172:175]\n"
"v_mfma_f32_16x16x32_bf16 v[192:195], a[144:147], a[224:227], v[192:195]\n"
"v_mfma_f32_16x16x32_bf16 v[192:195], a[148:151], a[228:231], v[192:195]\n"
"s_add_u32 m0, s49, 113152\n"
"v_mfma_f32_16x16x32_bf16 v[196:199], a[152:155], a[224:227], v[196:199]\n"
"buffer_load_dwordx4 v212, s[32:35], s52 offen lds\n"
"v_mfma_f32_16x16x32_bf16 v[196:199], a[156:159], a[228:231], v[196:199]\n"
"v_mfma_f32_16x16x32_bf16 v[200:203], a[160:163], a[224:227], v[200:203]\n"
"v_mfma_f32_16x16x32_bf16 v[200:203], a[164:167], a[228:231], v[200:203]\n"
"v_mfma_f32_16x16x32_bf16 v[204:207], a[168:171], a[224:227], v[204:207]\n"
"v_mfma_f32_16x16x32_bf16 v[204:207], a[172:175], a[228:231], v[204:207]\n"
"s_add_u32 m0, s49, 114240\n"
"v_mfma_f32_16x16x32_bf16 v[16:19], a[144:147], a[176:179], v[16:19]\n"
"ds_read_b128 a[224:227], v209 offset:26112\n"
"buffer_load_dwordx4 v213, s[32:35], s52 offen lds\n"
"v_mfma_f32_16x16x32_bf16 v[16:19], a[148:151], a[180:183], v[16:19]\n"
"ds_read_b128 a[228:231], v209 offset:26176\n"
"v_mfma_f32_16x16x32_bf16 v[20:23], a[152:155], a[176:179], v[20:23]\n"
"v_mfma_f32_16x16x32_bf16 v[20:23], a[156:159], a[180:183], v[20:23]\n"
"v_mfma_f32_16x16x32_bf16 v[24:27], a[160:163], a[176:179], v[24:27]\n"
"v_mfma_f32_16x16x32_bf16 v[24:27], a[164:167], a[180:183], v[24:27]\n"
"s_add_u32 m0, s49, 115328\n"
"v_mfma_f32_16x16x32_bf16 v[28:31], a[168:171], a[176:179], v[28:31]\n"
"buffer_load_dwordx4 v214, s[32:35], s52 offen lds\n"
"v_mfma_f32_16x16x32_bf16 v[28:31], a[172:175], a[180:183], v[28:31]\n"
"v_mfma_f32_16x16x32_bf16 v[48:51], a[144:147], a[184:187], v[48:51]\n"
"ds_read_b128 a[176:179], v209 offset:13056\n"
"v_mfma_f32_16x16x32_bf16 v[48:51], a[148:151], a[188:191], v[48:51]\n"
"ds_read_b128 a[180:183], v209 offset:13120\n"
"v_mfma_f32_16x16x32_bf16 v[52:55], a[152:155], a[184:187], v[52:55]\n"
"v_mfma_f32_16x16x32_bf16 v[52:55], a[156:159], a[188:191], v[52:55]\n"
"s_add_u32 m0, s49, 116416\n"
"v_mfma_f32_16x16x32_bf16 v[56:59], a[160:163], a[184:187], v[56:59]\n"
"buffer_load_dwordx4 v215, s[32:35], s52 offen lds\n"
"s_add_u32 s52, s52, 262144\n"
"v_mfma_f32_16x16x32_bf16 v[56:59], a[164:167], a[188:191], v[56:59]\n"
"v_mfma_f32_16x16x32_bf16 v[60:63], a[168:171], a[184:187], v[60:63]\n"
"v_mfma_f32_16x16x32_bf16 v[60:63], a[172:175], a[188:191], v[60:63]\n"
"v_mfma_f32_16x16x32_bf16 v[80:83], a[144:147], a[192:195], v[80:83]\n"
"ds_read_b128 a[184:187], v209 offset:15232\n"
"v_mfma_f32_16x16x32_bf16 v[80:83], a[148:151], a[196:199], v[80:83]\n"
"ds_read_b128 a[188:191], v209 offset:15296\n"
"s_add_u32 m0, s48, 35904\n"
"v_mfma_f32_16x16x32_bf16 v[84:87], a[152:155], a[192:195], v[84:87]\n"
"buffer_load_dwordx4 v216, s[28:31], s51 offen lds\n"
"v_mfma_f32_16x16x32_bf16 v[84:87], a[156:159], a[196:199], v[84:87]\n"
"v_mfma_f32_16x16x32_bf16 v[88:91], a[160:163], a[192:195], v[88:91]\n"
"v_mfma_f32_16x16x32_bf16 v[88:91], a[164:167], a[196:199], v[88:91]\n"
"s_waitcnt vmcnt(18)\n"
"s_barrier\n"
"v_mfma_f32_16x16x32_bf16 v[92:95], a[168:171], a[192:195], v[92:95]\n"
"ds_read_b64_tr_b16 a[112:113], v210\n"
"v_mfma_f32_16x16x32_bf16 v[92:95], a[172:175], a[196:199], v[92:95]\n"
"s_add_u32 m0, s48, 36992\n"
"ds_read_b64_tr_b16 a[114:115], v210 offset:4352\n"
"v_mfma_f32_16x16x32_bf16 v[112:115], a[144:147], a[200:203], v[112:115]\n"
"ds_read_b128 a[192:195], v209 offset:17408\n"
"buffer_load_dwordx4 v217, s[28:31], s51 offen lds\n"
"ds_read_b64_tr_b16 a[116:117], v210 offset:17408\n"
"v_mfma_f32_16x16x32_bf16 v[112:115], a[148:151], a[204:207], v[112:115]\n"
"ds_read_b128 a[196:199], v209 offset:17472\n"
"ds_read_b64_tr_b16 a[118:119], v210 offset:21760\n"
"v_mfma_f32_16x16x32_bf16 v[116:119], a[152:155], a[200:203], v[116:119]\n"
"ds_read_b64_tr_b16 a[120:121], v211\n"
"v_mfma_f32_16x16x32_bf16 v[116:119], a[156:159], a[204:207], v[116:119]\n"
"ds_read_b64_tr_b16 a[122:123], v211 offset:4352\n"
"v_mfma_f32_16x16x32_bf16 v[120:123], a[160:163], a[200:203], v[120:123]\n"
"ds_read_b64_tr_b16 a[124:125], v211 offset:17408\n"
"v_mfma_f32_16x16x32_bf16 v[120:123], a[164:167], a[204:207], v[120:123]\n"
"s_add_u32 m0, s48, 38080\n"
"ds_read_b64_tr_b16 a[126:127], v211 offset:21760\n"
"v_mfma_f32_16x16x32_bf16 v[124:127], a[168:171], a[200:203], v[124:127]\n"
"buffer_load_dwordx4 v218, s[28:31], s51 offen lds\n"
"ds_read_b64_tr_b16 a[128:129], v210 offset:64\n"
"v_mfma_f32_16x16x32_bf16 v[124:127], a[172:175], a[204:207], v[124:127]\n"
"ds_read_b64_tr_b16 a[130:131], v210 offset:4416\n"
"v_mfma_f32_16x16x32_bf16 v[144:147], a[144:147], a[208:211], v[144:147]\n"
"ds_read_b128 a[200:203], v209 offset:19584\n"
"ds_read_b64_tr_b16 a[132:133], v210 offset:17472\n"
"v_mfma_f32_16x16x32_bf16 v[144:147], a[148:151], a[212:215], v[144:147]\n"
"ds_read_b128 a[204:207], v209 offset:19648\n"
"ds_read_b64_tr_b16 a[134:135], v210 offset:21824\n"
"v_mfma_f32_16x16x32_bf16 v[148:151], a[152:155], a[208:211], v[148:151]\n"
"ds_read_b64_tr_b16 a[136:137], v211 offset:64\n"
"v_mfma_f32_16x16x32_bf16 v[148:151], a[156:159], a[212:215], v[148:151]\n"
"s_add_u32 m0, s48, 39168\n"
"ds_read_b64_tr_b16 a[138:139], v211 offset:4416\n"
"v_mfma_f32_16x16x32_bf16 v[152:155], a[160:163], a[208:211], v[152:155]\n"
"buffer_load_dwordx4 v219, s[28:31], s51 offen lds\n"
"ds_read_b64_tr_b16 a[140:141], v211 offset:17472\n"
"v_mfma_f32_16x16x32_bf16 v[152:155], a[164:167], a[212:215], v[152:155]\n"
"ds_read_b64_tr_b16 a[142:143], v211 offset:21824\n"
"v_mfma_f32_16x16x32_bf16 v[156:159], a[168:171], a[208:211], v[156:159]\n"
"v_mfma_f32_16x16x32_bf16 v[156:159], a[172:175], a[212:215], v[156:159]\n"
"v_mfma_f32_16x16x32_bf16 v[176:179], a[144:147], a[216:219], v[176:179]\n"
"ds_read_b128 a[208:211], v209 offset:21760\n"
"v_mfma_f32_16x16x32_bf16 v[176:179], a[148:151], a[220:223], v[176:179]\n"
"ds_read_b128 a[212:215], v209 offset:21824\n"
"s_add_u32 m0, s48, 40256\n"
"v_mfma_f32_16x16x32_bf16 v[180:183], a[152:155], a[216:219], v[180:183]\n"
"buffer_load_dwordx4 v220, s[28:31], s51 offen lds\n"
"v_mfma_f32_16x16x32_bf16 v[180:183], a[156:159], a[220:223], v[180:183]\n"
"v_mfma_f32_16x16x32_bf16 v[184:187], a[160:163], a[216:219], v[184:187]\n"
"v_mfma_f32_16x16x32_bf16 v[184:187], a[164:167], a[220:223], v[184:187]\n"
"v_mfma_f32_16x16x32_bf16 v[188:191], a[168:171], a[216:219], v[188:191]\n"
"s_waitcnt lgkmcnt(2)\n"
"v_mfma_f32_16x16x32_bf16 v[188:191], a[172:175], a[220:223], v[188:191]\n"
"v_mfma_f32_16x16x32_bf16 v[0:3], a[112:115], a[176:179], v[0:3]\n"
"ds_read_b128 a[216:219], v209 offset:23936\n"
"s_add_u32 m0, s48, 41344\n"
"v_mfma_f32_16x16x32_bf16 v[0:3], a[116:119], a[180:183], v[0:3]\n"
"ds_read_b128 a[220:223], v209 offset:24000\n"
"buffer_load_dwordx4 v221, s[28:31], s51 offen lds\n"
"s_add_u32 s51, s51, 128\n"
"v_mfma_f32_16x16x32_bf16 v[4:7], a[120:123], a[176:179], v[4:7]\n"
"ds_read_b64_tr_b16 a[144:145], v210 offset:128\n"
"v_mfma_f32_16x16x32_bf16 v[4:7], a[124:127], a[180:183], v[4:7]\n"
"ds_read_b64_tr_b16 a[146:147], v210 offset:4480\n"
"v_mfma_f32_16x16x32_bf16 v[8:11], a[128:131], a[176:179], v[8:11]\n"
"ds_read_b64_tr_b16 a[148:149], v210 offset:17536\n"
"v_mfma_f32_16x16x32_bf16 v[8:11], a[132:135], a[180:183], v[8:11]\n"
"ds_read_b64_tr_b16 a[150:151], v210 offset:21888\n"
"v_mfma_f32_16x16x32_bf16 v[12:15], a[136:139], a[176:179], v[12:15]\n"
"ds_read_b64_tr_b16 a[152:153], v211 offset:128\n"
"buffer_load_dwordx4 a[92:95], v250, s[36:39], s54 offen offset:768 nt\n"
"s_add_u32 s53, s53, 8192\n"
"s_and_b32 s53, s53, 258047\n"
"s_lshl_b32 s38, s38, 1\n"
"s_add_u32 s54, s53, 4096\n"
"v_mfma_f32_16x16x32_bf16 v[12:15], a[140:143], a[180:183], v[12:15]\n"
"ds_read_b64_tr_b16 a[154:155], v211 offset:4480\n"
"v_mfma_f32_16x16x32_bf16 v[32:35], a[112:115], a[184:187], v[32:35]\n"
"ds_read_b64_tr_b16 a[156:157], v211 offset:17536\n"
"v_mfma_f32_16x16x32_bf16 v[32:35], a[116:119], a[188:191], v[32:35]\n"
"ds_read_b64_tr_b16 a[158:159], v211 offset:21888\n"
"v_mfma_f32_16x16x32_bf16 v[36:39], a[120:123], a[184:187], v[36:39]\n"
"ds_read_b64_tr_b16 a[160:161], v210 offset:192\n"
"v_mfma_f32_16x16x32_bf16 v[36:39], a[124:127], a[188:191], v[36:39]\n"
"ds_read_b64_tr_b16 a[162:163], v210 offset:4544\n"
"v_mfma_f32_16x16x32_bf16 v[40:43], a[128:131], a[184:187], v[40:43]\n"
"ds_read_b64_tr_b16 a[164:165], v210 offset:17600\n"
"buffer_load_dwordx4 a[96:99], v250, s[36:39], s53 offen nt\n"
"v_mfma_f32_16x16x32_bf16 v[40:43], a[132:135], a[188:191], v[40:43]\n"
"ds_read_b64_tr_b16 a[166:167], v210 offset:21952\n"
"v_mfma_f32_16x16x32_bf16 v[44:47], a[136:139], a[184:187], v[44:47]\n"
"ds_read_b64_tr_b16 a[168:169], v211 offset:192\n"
"v_mfma_f32_16x16x32_bf16 v[44:47], a[140:143], a[188:191], v[44:47]\n"
"ds_read_b64_tr_b16 a[170:171], v211 offset:4544\n"
"v_mfma_f32_16x16x32_bf16 v[64:67], a[112:115], a[192:195], v[64:67]\n"
"ds_read_b64_tr_b16 a[172:173], v211 offset:17600\n"
"v_mfma_f32_16x16x32_bf16 v[64:67], a[116:119], a[196:199], v[64:67]\n"
"ds_read_b64_tr_b16 a[174:175], v211 offset:21952\n"
"v_mfma_f32_16x16x32_bf16 v[68:71], a[120:123], a[192:195], v[68:71]\n"
"buffer_load_dwordx4 a[100:103], v250, s[36:39], s53 offen offset:256 nt\n"
"v_mfma_f32_16x16x32_bf16 v[68:71], a[124:127], a[196:199], v[68:71]\n"
"v_mfma_f32_16x16x32_bf16 v[72:75], a[128:131], a[192:195], v[72:75]\n"
"v_mfma_f32_16x16x32_bf16 v[72:75], a[132:135], a[196:199], v[72:75]\n"
"v_mfma_f32_16x16x32_bf16 v[76:79], a[136:139], a[192:195], v[76:79]\n"
"v_mfma_f32_16x16x32_bf16 v[76:79], a[140:143], a[196:199], v[76:79]\n"
"v_mfma_f32_16x16x32_bf16 v[96:99], a[112:115], a[200:203], v[96:99]\n"
"s_waitcnt lgkmcnt(0)\n"
"s_waitcnt vmcnt(20)\n"
"s_barrier\n"
"v_mfma_f32_16x16x32_bf16 v[96:99], a[116:119], a[204:207], v[96:99]\n"
"s_add_u32 m0, s49, 60928\n"
"v_mfma_f32_16x16x32_bf16 v[100:103], a[120:123], a[200:203], v[100:103]\n"
"buffer_load_dwordx4 v212, s[32:35], s52 offen lds\n"
"v_mfma_f32_16x16x32_bf16 v[100:103], a[124:127], a[204:207], v[100:103]\n"
"v_mfma_f32_16x16x32_bf16 v[104:107], a[128:131], a[200:203], v[104:107]\n"
"v_mfma_f32_16x16x32_bf16 v[104:107], a[132:135], a[204:207], v[104:107]\n"
"v_mfma_f32_16x16x32_bf16 v[108:111], a[136:139], a[200:203], v[108:111]\n"
"v_mfma_f32_16x16x32_bf16 v[108:111], a[140:143], a[204:207], v[108:111]\n"
"s_add_u32 m0, s49, 62016\n"
"v_mfma_f32_16x16x32_bf16 v[128:131], a[112:115], a[208:211], v[128:131]\n"
"buffer_load_dwordx4 v213, s[32:35], s52 offen lds\n"
"v_mfma_f32_16x16x32_bf16 v[128:131], a[116:119], a[212:215], v[128:131]\n"
"v_mfma_f32_16x16x32_bf16 v[132:135], a[120:123], a[208:211], v[132:135]\n"
"v_mfma_f32_16x16x32_bf16 v[132:135], a[124:127], a[212:215], v[132:135]\n"
"v_mfma_f32_16x16x32_bf16 v[136:139], a[128:131], a[208:211], v[136:139]\n"
"v_mfma_f32_16x16x32_bf16 v[136:139], a[132:135], a[212:215], v[136:139]\n"
"s_add_u32 m0, s49, 63104\n"
"v_mfma_f32_16x16x32_bf16 v[140:143], a[136:139], a[208:211], v[140:143]\n"
"buffer_load_dwordx4 v214, s[32:35], s52 offen lds\n"
"v_mfma_f32_16x16x32_bf16 v[140:143], a[140:143], a[212:215], v[140:143]\n"
"v_mfma_f32_16x16x32_bf16 v[160:163], a[112:115], a[216:219], v[160:163]\n"
"v_mfma_f32_16x16x32_bf16 v[160:163], a[116:119], a[220:223], v[160:163]\n"
"v_mfma_f32_16x16x32_bf16 v[164:167], a[120:123], a[216:219], v[164:167]\n"
"v_mfma_f32_16x16x32_bf16 v[164:167], a[124:127], a[220:223], v[164:167]\n"
"s_add_u32 m0, s49, 64192\n"
"v_mfma_f32_16x16x32_bf16 v[168:171], a[128:131], a[216:219], v[168:171]\n"
"buffer_load_dwordx4 v215, s[32:35], s52 offen lds\n"
"s_add_u32 s52, s52, 262144\n"
"v_mfma_f32_16x16x32_bf16 v[168:171], a[132:135], a[220:223], v[168:171]\n"
"v_mfma_f32_16x16x32_bf16 v[172:175], a[136:139], a[216:219], v[172:175]\n"
"v_mfma_f32_16x16x32_bf16 v[172:175], a[140:143], a[220:223], v[172:175]\n"
"v_mfma_f32_16x16x32_bf16 v[192:195], a[144:147], a[224:227], v[192:195]\n"
"v_mfma_f32_16x16x32_bf16 v[192:195], a[148:151], a[228:231], v[192:195]\n"
"s_add_u32 m0, s49, 78336\n"
"v_mfma_f32_16x16x32_bf16 v[196:199], a[152:155], a[224:227], v[196:199]\n"
"buffer_load_dwordx4 v212, s[32:35], s52 offen lds\n"
"v_mfma_f32_16x16x32_bf16 v[196:199], a[156:159], a[228:231], v[196:199]\n"
"v_mfma_f32_16x16x32_bf16 v[200:203], a[160:163], a[224:227], v[200:203]\n"
"v_mfma_f32_16x16x32_bf16 v[200:203], a[164:167], a[228:231], v[200:203]\n"
"v_mfma_f32_16x16x32_bf16 v[204:207], a[168:171], a[224:227], v[204:207]\n"
"v_mfma_f32_16x16x32_bf16 v[204:207], a[172:175], a[228:231], v[204:207]\n"
"s_add_u32 m0, s49, 79424\n"
"v_mfma_f32_16x16x32_bf16 v[16:19], a[144:147], a[176:179], v[16:19]\n"
"ds_read_b128 a[224:227], v208 offset:26112\n"
"buffer_load_dwordx4 v213, s[32:35], s52 offen lds\n"
"v_mfma_f32_16x16x32_bf16 v[16:19], a[148:151], a[180:183], v[16:19]\n"
"ds_read_b128 a[228:231], v208 offset:26176\n"
"v_mfma_f32_16x16x32_bf16 v[20:23], a[152:155], a[176:179], v[20:23]\n"
"v_mfma_f32_16x16x32_bf16 v[20:23], a[156:159], a[180:183], v[20:23]\n"
"v_mfma_f32_16x16x32_bf16 v[24:27], a[160:163], a[176:179], v[24:27]\n"
"v_mfma_f32_16x16x32_bf16 v[24:27], a[164:167], a[180:183], v[24:27]\n"
"s_add_u32 m0, s49, 80512\n"
"v_mfma_f32_16x16x32_bf16 v[28:31], a[168:171], a[176:179], v[28:31]\n"
"buffer_load_dwordx4 v214, s[32:35], s52 offen lds\n"
"v_mfma_f32_16x16x32_bf16 v[28:31], a[172:175], a[180:183], v[28:31]\n"
"v_mfma_f32_16x16x32_bf16 v[48:51], a[144:147], a[184:187], v[48:51]\n"
"ds_read_b128 a[176:179], v208 offset:13056\n"
"v_mfma_f32_16x16x32_bf16 v[48:51], a[148:151], a[188:191], v[48:51]\n"
"ds_read_b128 a[180:183], v208 offset:13120\n"
"v_mfma_f32_16x16x32_bf16 v[52:55], a[152:155], a[184:187], v[52:55]\n"
"v_mfma_f32_16x16x32_bf16 v[52:55], a[156:159], a[188:191], v[52:55]\n"
"s_add_u32 m0, s49, 81600\n"
"v_mfma_f32_16x16x32_bf16 v[56:59], a[160:163], a[184:187], v[56:59]\n"
"buffer_load_dwordx4 v215, s[32:35], s52 offen lds\n"
"s_add_u32 s52, s52, 262144\n"
"v_mfma_f32_16x16x32_bf16 v[56:59], a[164:167], a[188:191], v[56:59]\n"
"v_mfma_f32_16x16x32_bf16 v[60:63], a[168:171], a[184:187], v[60:63]\n"
"v_mfma_f32_16x16x32_bf16 v[60:63], a[172:175], a[188:191], v[60:63]\n"
"v_mfma_f32_16x16x32_bf16 v[80:83], a[144:147], a[192:195], v[80:83]\n"
"ds_read_b128 a[184:187], v208 offset:15232\n"
"v_mfma_f32_16x16x32_bf16 v[80:83], a[148:151], a[196:199], v[80:83]\n"
"ds_read_b128 a[188:191], v208 offset:15296\n"
"s_add_u32 m0, s48, 133824\n"
"v_mfma_f32_16x16x32_bf16 v[84:87], a[152:155], a[192:195], v[84:87]\n"
"buffer_load_dwordx4 v216, s[28:31], s51 offen lds\n"
"v_mfma_f32_16x16x32_bf16 v[84:87], a[156:159], a[196:199], v[84:87]\n"
"v_mfma_f32_16x16x32_bf16 v[88:91], a[160:163], a[192:195], v[88:91]\n"
"v_mfma_f32_16x16x32_bf16 v[88:91], a[164:167], a[196:199], v[88:91]\n"
"s_waitcnt vmcnt(18)\n"
"s_barrier\n"
"v_mfma_f32_16x16x32_bf16 v[92:95], a[168:171], a[192:195], v[92:95]\n"
"ds_read_b64_tr_b16 a[112:113], v210 offset:34816\n"
"v_mfma_f32_16x16x32_bf16 v[92:95], a[172:175], a[196:199], v[92:95]\n"
"s_add_u32 m0, s48, 134912\n"
"ds_read_b64_tr_b16 a[114:115], v210 offset:39168\n"
"v_mfma_f32_16x16x32_bf16 v[112:115], a[144:147], a[200:203], v[112:115]\n"
"ds_read_b128 a[192:195], v208 offset:17408\n"
"buffer_load_dwordx4 v217, s[28:31], s51 offen lds\n"
"ds_read_b64_tr_b16 a[116:117], v210 offset:52224\n"
"v_mfma_f32_16x16x32_bf16 v[112:115], a[148:151], a[204:207], v[112:115]\n"
"ds_read_b128 a[196:199], v208 offset:17472\n"
"ds_read_b64_tr_b16 a[118:119], v210 offset:56576\n"
"v_mfma_f32_16x16x32_bf16 v[116:119], a[152:155], a[200:203], v[116:119]\n"
"ds_read_b64_tr_b16 a[120:121], v211 offset:34816\n"
"v_mfma_f32_16x16x32_bf16 v[116:119], a[156:159], a[204:207], v[116:119]\n"
"ds_read_b64_tr_b16 a[122:123], v211 offset:39168\n"
"v_mfma_f32_16x16x32_bf16 v[120:123], a[160:163], a[200:203], v[120:123]\n"
"ds_read_b64_tr_b16 a[124:125], v211 offset:52224\n"
"v_mfma_f32_16x16x32_bf16 v[120:123], a[164:167], a[204:207], v[120:123]\n"
"s_add_u32 m0, s48, 136000\n"
"ds_read_b64_tr_b16 a[126:127], v211 offset:56576\n"
"v_mfma_f32_16x16x32_bf16 v[124:127], a[168:171], a[200:203], v[124:127]\n"
"buffer_load_dwordx4 v218, s[28:31], s51 offen lds\n"
"ds_read_b64_tr_b16 a[128:129], v210 offset:34880\n"
"v_mfma_f32_16x16x32_bf16 v[124:127], a[172:175], a[204:207], v[124:127]\n"
"ds_read_b64_tr_b16 a[130:131], v210 offset:39232\n"
"v_mfma_f32_16x16x32_bf16 v[144:147], a[144:147], a[208:211], v[144:147]\n"
"ds_read_b128 a[200:203], v208 offset:19584\n"
"ds_read_b64_tr_b16 a[132:133], v210 offset:52288\n"
"v_mfma_f32_16x16x32_bf16 v[144:147], a[148:151], a[212:215], v[144:147]\n"
"ds_read_b128 a[204:207], v208 offset:19648\n"
"ds_read_b64_tr_b16 a[134:135], v210 offset:56640\n"
"v_mfma_f32_16x16x32_bf16 v[148:151], a[152:155], a[208:211], v[148:151]\n"
"ds_read_b64_tr_b16 a[136:137], v211 offset:34880\n"
"v_mfma_f32_16x16x32_bf16 v[148:151], a[156:159], a[212:215], v[148:151]\n"
"s_add_u32 m0, s48, 137088\n"
"ds_read_b64_tr_b16 a[138:139], v211 offset:39232\n"
"v_mfma_f32_16x16x32_bf16 v[152:155], a[160:163], a[208:211], v[152:155]\n"
"buffer_load_dwordx4 v219, s[28:31], s51 offen lds\n"
"ds_read_b64_tr_b16 a[140:141], v211 offset:52288\n"
"v_mfma_f32_16x16x32_bf16 v[152:155], a[164:167], a[212:215], v[152:155]\n"
"ds_read_b64_tr_b16 a[142:143], v211 offset:56640\n"
"v_mfma_f32_16x16x32_bf16 v[156:159], a[168:171], a[208:211], v[156:159]\n"
"v_mfma_f32_16x16x32_bf16 v[156:159], a[172:175], a[212:215], v[156:159]\n"
"v_mfma_f32_16x16x32_bf16 v[176:179], a[144:147], a[216:219], v[176:179]\n"
"ds_read_b128 a[208:211], v208 offset:21760\n"
"v_mfma_f32_16x16x32_bf16 v[176:179], a[148:151], a[220:223], v[176:179]\n"
"ds_read_b128 a[212:215], v208 offset:21824\n"
"s_add_u32 m0, s48, 138176\n"
"v_mfma_f32_16x16x32_bf16 v[180:183], a[152:155], a[216:219], v[180:183]\n"
"buffer_load_dwordx4 v220, s[28:31], s51 offen lds\n"
"v_mfma_f32_16x16x32_bf16 v[180:183], a[156:159], a[220:223], v[180:183]\n"
"global_load_dword v223, v255, s[100:101]\n"
"v_mfma_f32_16x16x32_bf16 v[184:187], a[160:163], a[216:219], v[184:187]\n"
"v_mfma_f32_16x16x32_bf16 v[184:187], a[164:167], a[220:223], v[184:187]\n"
"v_mfma_f32_16x16x32_bf16 v[188:191], a[168:171], a[216:219], v[188:191]\n"
"s_waitcnt lgkmcnt(2)\n"
"v_mfma_f32_16x16x32_bf16 v[188:191], a[172:175], a[220:223], v[188:191]\n"
"v_mfma_f32_16x16x32_bf16 v[0:3], a[112:115], a[176:179], v[0:3]\n"
"ds_read_b128 a[216:219], v208 offset:23936\n"
"s_add_u32 m0, s48, 139264\n"
"v_mfma_f32_16x16x32_bf16 v[0:3], a[116:119], a[180:183], v[0:3]\n"
"ds_read_b128 a[220:223], v208 offset:24000\n"
"buffer_load_dwordx4 v221, s[28:31], s51 offen lds\n"
"s_add_u32 s51, s51, 128\n"
"v_mfma_f32_16x16x32_bf16 v[4:7], a[120:123], a[176:179], v[4:7]\n"
"ds_read_b64_tr_b16 a[144:145], v210 offset:34944\n"
"v_mfma_f32_16x16x32_bf16 v[4:7], a[124:127], a[180:183], v[4:7]\n"
"ds_read_b64_tr_b16 a[146:147], v210 offset:39296\n"
"v_mfma_f32_16x16x32_bf16 v[8:11], a[128:131], a[176:179], v[8:11]\n"
"ds_read_b64_tr_b16 a[148:149], v210 offset:52352\n"
"v_mfma_f32_16x16x32_bf16 v[8:11], a[132:135], a[180:183], v[8:11]\n"
"ds_read_b64_tr_b16 a[150:151], v210 offset:56704\n"
"v_mfma_f32_16x16x32_bf16 v[12:15], a[136:139], a[176:179], v[12:15]\n"
"ds_read_b64_tr_b16 a[152:153], v211 offset:34944\n"
"buffer_load_dwordx4 a[104:107], v250, s[36:39], s53 offen offset:512 nt\n"
"v_mfma_f32_16x16x32_bf16 v[12:15], a[140:143], a[180:183], v[12:15]\n"
"ds_read_b64_tr_b16 a[154:155], v211 offset:39296\n"
"v_mfma_f32_16x16x32_bf16 v[32:35], a[112:115], a[184:187], v[32:35]\n"
"ds_read_b64_tr_b16 a[156:157], v211 offset:52352\n"
"v_mfma_f32_16x16x32_bf16 v[32:35], a[116:119], a[188:191], v[32:35]\n"
"ds_read_b64_tr_b16 a[158:159], v211 offset:56704\n"
"v_mfma_f32_16x16x32_bf16 v[36:39], a[120:123], a[184:187], v[36:39]\n"
"ds_read_b64_tr_b16 a[160:161], v210 offset:35008\n"
"v_mfma_f32_16x16x32_bf16 v[36:39], a[124:127], a[188:191], v[36:39]\n"
"ds_read_b64_tr_b16 a[162:163], v210 offset:39360\n"
"v_mfma_f32_16x16x32_bf16 v[40:43], a[128:131], a[184:187], v[40:43]\n"
"ds_read_b64_tr_b16 a[164:165], v210 offset:52416\n"
"buffer_load_dwordx4 a[108:111], v250, s[36:39], s53 offen offset:768 nt\n"
"v_mfma_f32_16x16x32_bf16 v[40:43], a[132:135], a[188:191], v[40:43]\n"
"ds_read_b64_tr_b16 a[166:167], v210 offset:56768\n"
"v_mfma_f32_16x16x32_bf16 v[44:47], a[136:139], a[184:187], v[44:47]\n"
"ds_read_b64_tr_b16 a[168:169], v211 offset:35008\n"
"v_mfma_f32_16x16x32_bf16 v[44:47], a[140:143], a[188:191], v[44:47]\n"
"ds_read_b64_tr_b16 a[170:171], v211 offset:39360\n"
"v_mfma_f32_16x16x32_bf16 v[64:67], a[112:115], a[192:195], v[64:67]\n"
"ds_read_b64_tr_b16 a[172:173], v211 offset:52416\n"
"v_mfma_f32_16x16x32_bf16 v[64:67], a[116:119], a[196:199], v[64:67]\n"
"ds_read_b64_tr_b16 a[174:175], v211 offset:56768\n"
"v_mfma_f32_16x16x32_bf16 v[68:71], a[120:123], a[192:195], v[68:71]\n"
"buffer_load_dwordx4 a[240:243], v250, s[36:39], s54 offen nt\n"
"v_mfma_f32_16x16x32_bf16 v[68:71], a[124:127], a[196:199], v[68:71]\n"
"v_mfma_f32_16x16x32_bf16 v[72:75], a[128:131], a[192:195], v[72:75]\n"
"v_mfma_f32_16x16x32_bf16 v[72:75], a[132:135], a[196:199], v[72:75]\n"
"v_mfma_f32_16x16x32_bf16 v[76:79], a[136:139], a[192:195], v[76:79]\n"
"v_mfma_f32_16x16x32_bf16 v[76:79], a[140:143], a[196:199], v[76:79]\n"
"v_mfma_f32_16x16x32_bf16 v[96:99], a[112:115], a[200:203], v[96:99]\n"
"s_waitcnt lgkmcnt(0)\n"
"s_waitcnt vmcnt(21)\n"
"s_barrier\n"
"v_mfma_f32_16x16x32_bf16 v[96:99], a[116:119], a[204:207], v[96:99]\n"
"s_add_u32 m0, s49, 95744\n"
"v_mfma_f32_16x16x32_bf16 v[100:103], a[120:123], a[200:203], v[100:103]\n"
"buffer_load_dwordx4 v212, s[32:35], s52 offen lds\n"
"v_mfma_f32_16x16x32_bf16 v[100:103], a[124:127], a[204:207], v[100:103]\n"
"v_mfma_f32_16x16x32_bf16 v[104:107], a[128:131], a[200:203], v[104:107]\n"
"v_mfma_f32_16x16x32_bf16 v[104:107], a[132:135], a[204:207], v[104:107]\n"
"v_mfma_f32_16x16x32_bf16 v[108:111], a[136:139], a[200:203], v[108:111]\n"
"v_mfma_f32_16x16x32_bf16 v[108:111], a[140:143], a[204:207], v[108:111]\n"
"s_add_u32 m0, s49, 96832\n"
"v_mfma_f32_16x16x32_bf16 v[128:131], a[112:115], a[208:211], v[128:131]\n"
"buffer_load_dwordx4 v213, s[32:35], s52 offen lds\n"
"v_mfma_f32_16x16x32_bf16 v[128:131], a[116:119], a[212:215], v[128:131]\n"
"v_mfma_f32_16x16x32_bf16 v[132:135], a[120:123], a[208:211], v[132:135]\n"
"v_mfma_f32_16x16x32_bf16 v[132:135], a[124:127], a[212:215], v[132:135]\n"
"v_mfma_f32_16x16x32_bf16 v[136:139], a[128:131], a[208:211], v[136:139]\n"
"v_mfma_f32_16x16x32_bf16 v[136:139], a[132:135], a[212:215], v[136:139]\n"
"s_add_u32 m0, s49, 97920\n"
"v_mfma_f32_16x16x32_bf16 v[140:143], a[136:139], a[208:211], v[140:143]\n"
"buffer_load_dwordx4 v214, s[32:35], s52 offen lds\n"
"v_mfma_f32_16x16x32_bf16 v[140:143], a[140:143], a[212:215], v[140:143]\n"
"v_mfma_f32_16x16x32_bf16 v[160:163], a[112:115], a[216:219], v[160:163]\n"
"v_mfma_f32_16x16x32_bf16 v[160:163], a[116:119], a[220:223], v[160:163]\n"
"v_mfma_f32_16x16x32_bf16 v[164:167], a[120:123], a[216:219], v[164:167]\n"
"v_mfma_f32_16x16x32_bf16 v[164:167], a[124:127], a[220:223], v[164:167]\n"
"s_add_u32 m0, s49, 99008\n"
"v_mfma_f32_16x16x32_bf16 v[168:171], a[128:131], a[216:219], v[168:171]\n"
"buffer_load_dwordx4 v215, s[32:35], s52 offen lds\n"
"s_add_u32 s52, s52, 262144\n"
"v_mfma_f32_16x16x32_bf16 v[168:171], a[132:135], a[220:223], v[168:171]\n"
"v_mfma_f32_16x16x32_bf16 v[172:175], a[136:139], a[216:219], v[172:175]\n"
"v_mfma_f32_16x16x32_bf16 v[172:175], a[140:143], a[220:223], v[172:175]\n"
"v_mfma_f32_16x16x32_bf16 v[192:195], a[144:147], a[224:227], v[192:195]\n"
"v_mfma_f32_16x16x32_bf16 v[192:195], a[148:151], a[228:231], v[192:195]\n"
"s_add_u32 m0, s49, 113152\n"
"v_mfma_f32_16x16x32_bf16 v[196:199], a[152:155], a[224:227], v[196:199]\n"
"buffer_load_dwordx4 v212, s[32:35], s52 offen lds\n"
"v_mfma_f32_16x16x32_bf16 v[196:199], a[156:159], a[228:231], v[196:199]\n"
"v_mfma_f32_16x16x32_bf16 v[200:203], a[160:163], a[224:227], v[200:203]\n"
"v_mfma_f32_16x16x32_bf16 v[200:203], a[164:167], a[228:231], v[200:203]\n"
"v_mfma_f32_16x16x32_bf16 v[204:207], a[168:171], a[224:227], v[204:207]\n"
"v_mfma_f32_16x16x32_bf16 v[204:207], a[172:175], a[228:231], v[204:207]\n"
"s_add_u32 m0, s49, 114240\n"
"v_mfma_f32_16x16x32_bf16 v[16:19], a[144:147], a[176:179], v[16:19]\n"
"ds_read_b128 a[224:227], v208 offset:54400\n"
"buffer_load_dwordx4 v213, s[32:35], s52 offen lds\n"
"v_mfma_f32_16x16x32_bf16 v[16:19], a[148:151], a[180:183], v[16:19]\n"
"ds_read_b128 a[228:231], v208 offset:54464\n"
"v_mfma_f32_16x16x32_bf16 v[20:23], a[152:155], a[176:179], v[20:23]\n"
"v_mfma_f32_16x16x32_bf16 v[20:23], a[156:159], a[180:183], v[20:23]\n"
"v_mfma_f32_16x16x32_bf16 v[24:27], a[160:163], a[176:179], v[24:27]\n"
"v_mfma_f32_16x16x32_bf16 v[24:27], a[164:167], a[180:183], v[24:27]\n"
"s_add_u32 m0, s49, 115328\n"
"v_mfma_f32_16x16x32_bf16 v[28:31], a[168:171], a[176:179], v[28:31]\n"
"buffer_load_dwordx4 v214, s[32:35], s52 offen lds\n"
"v_mfma_f32_16x16x32_bf16 v[28:31], a[172:175], a[180:183], v[28:31]\n"
"v_mfma_f32_16x16x32_bf16 v[48:51], a[144:147], a[184:187], v[48:51]\n"
"ds_read_b128 a[176:179], v208 offset:41344\n"
"v_mfma_f32_16x16x32_bf16 v[48:51], a[148:151], a[188:191], v[48:51]\n"
"ds_read_b128 a[180:183], v208 offset:41408\n"
"v_mfma_f32_16x16x32_bf16 v[52:55], a[152:155], a[184:187], v[52:55]\n"
"v_mfma_f32_16x16x32_bf16 v[52:55], a[156:159], a[188:191], v[52:55]\n"
"s_add_u32 m0, s49, 116416\n"
"v_mfma_f32_16x16x32_bf16 v[56:59], a[160:163], a[184:187], v[56:59]\n"
"buffer_load_dwordx4 v215, s[32:35], s52 offen lds\n"
"s_add_u32 s52, s52, 262144\n"
"v_mfma_f32_16x16x32_bf16 v[56:59], a[164:167], a[188:191], v[56:59]\n"
"v_mfma_f32_16x16x32_bf16 v[60:63], a[168:171], a[184:187], v[60:63]\n"
"v_mfma_f32_16x16x32_bf16 v[60:63], a[172:175], a[188:191], v[60:63]\n"
"v_mfma_f32_16x16x32_bf16 v[80:83], a[144:147], a[192:195], v[80:83]\n"
"ds_read_b128 a[184:187], v208 offset:43520\n"
"v_mfma_f32_16x16x32_bf16 v[80:83], a[148:151], a[196:199], v[80:83]\n"
"ds_read_b128 a[188:191], v208 offset:43584\n"
"v_mfma_f32_16x16x32_bf16 v[84:87], a[152:155], a[192:195], v[84:87]\n"
"buffer_load_dwordx4 a[244:247], v250, s[36:39], s54 offen offset:256 nt\n"
"v_mfma_f32_16x16x32_bf16 v[84:87], a[156:159], a[196:199], v[84:87]\n"
"v_mfma_f32_16x16x32_bf16 v[88:91], a[160:163], a[192:195], v[88:91]\n"
"v_mfma_f32_16x16x32_bf16 v[88:91], a[164:167], a[196:199], v[88:91]\n"
"s_waitcnt vmcnt(19)\n"
"s_barrier\n"
"v_mfma_f32_16x16x32_bf16 v[92:95], a[168:171], a[192:195], v[92:95]\n"
"ds_read_b64_tr_b16 a[112:113], v210\n"
"v_mfma_f32_16x16x32_bf16 v[92:95], a[172:175], a[196:199], v[92:95]\n"
"ds_read_b64_tr_b16 a[114:115], v210 offset:4352\n"
"v_mfma_f32_16x16x32_bf16 v[112:115], a[144:147], a[200:203], v[112:115]\n"
"ds_read_b128 a[192:195], v208 offset:45696\n"
"buffer_load_dwordx4 a[248:251], v250, s[36:39], s54 offen offset:512 nt\n"
"ds_read_b64_tr_b16 a[116:117], v210 offset:17408\n"
"v_mfma_f32_16x16x32_bf16 v[112:115], a[148:151], a[204:207], v[112:115]\n"
"ds_read_b128 a[196:199], v208 offset:45760\n"
"ds_read_b64_tr_b16 a[118:119], v210 offset:21760\n"
"v_mfma_f32_16x16x32_bf16 v[116:119], a[152:155], a[200:203], v[116:119]\n"
"ds_read_b64_tr_b16 a[120:121], v211\n"
"v_mfma_f32_16x16x32_bf16 v[116:119], a[156:159], a[204:207], v[116:119]\n"
"ds_read_b64_tr_b16 a[122:123], v211 offset:4352\n"
"v_mfma_f32_16x16x32_bf16 v[120:123], a[160:163], a[200:203], v[120:123]\n"
"ds_read_b64_tr_b16 a[124:125], v211 offset:17408\n"
"v_mfma_f32_16x16x32_bf16 v[120:123], a[164:167], a[204:207], v[120:123]\n"
"ds_read_b64_tr_b16 a[126:127], v211 offset:21760\n"
"v_mfma_f32_16x16x32_bf16 v[124:127], a[168:171], a[200:203], v[124:127]\n"
"buffer_load_dwordx4 a[252:255], v250, s[36:39], s54 offen offset:768 nt\n"
"s_add_u32 s53, s53, 8192\n"
"s_and_b32 s53, s53, 258047\n"
"s_lshl_b32 s38, s38, 1\n"
"s_add_u32 s54, s53, 4096\n"
"ds_read_b64_tr_b16 a[128:129], v210 offset:64\n"
"v_mfma_f32_16x16x32_bf16 v[124:127], a[172:175], a[204:207], v[124:127]\n"
"ds_read_b64_tr_b16 a[130:131], v210 offset:4416\n"
"v_mfma_f32_16x16x32_bf16 v[144:147], a[144:147], a[208:211], v[144:147]\n"
"ds_read_b128 a[200:203], v208 offset:47872\n"
"ds_read_b64_tr_b16 a[132:133], v210 offset:17472\n"
"v_mfma_f32_16x16x32_bf16 v[144:147], a[148:151], a[212:215], v[144:147]\n"
"ds_read_b128 a[204:207], v208 offset:47936\n"
"ds_read_b64_tr_b16 a[134:135], v210 offset:21824\n"
"v_mfma_f32_16x16x32_bf16 v[148:151], a[152:155], a[208:211], v[148:151]\n"
"ds_read_b64_tr_b16 a[136:137], v211 offset:64\n"
"v_mfma_f32_16x16x32_bf16 v[148:151], a[156:159], a[212:215], v[148:151]\n"
"ds_read_b64_tr_b16 a[138:139], v211 offset:4416\n"
"v_mfma_f32_16x16x32_bf16 v[152:155], a[160:163], a[208:211], v[152:155]\n"
"ds_read_b64_tr_b16 a[140:141], v211 offset:17472\n"
"v_mfma_f32_16x16x32_bf16 v[152:155], a[164:167], a[212:215], v[152:155]\n"
"ds_read_b64_tr_b16 a[142:143], v211 offset:21824\n"
"v_mfma_f32_16x16x32_bf16 v[156:159], a[168:171], a[208:211], v[156:159]\n"
"v_mfma_f32_16x16x32_bf16 v[156:159], a[172:175], a[212:215], v[156:159]\n"
"v_mfma_f32_16x16x32_bf16 v[176:179], a[144:147], a[216:219], v[176:179]\n"
"ds_read_b128 a[208:211], v208 offset:50048\n"
"v_mfma_f32_16x16x32_bf16 v[176:179], a[148:151], a[220:223], v[176:179]\n"
"ds_read_b128 a[212:215], v208 offset:50112\n"
"v_mfma_f32_16x16x32_bf16 v[180:183], a[152:155], a[216:219], v[180:183]\n"
"v_mfma_f32_16x16x32_bf16 v[180:183], a[156:159], a[220:223], v[180:183]\n"
"v_mfma_f32_16x16x32_bf16 v[184:187], a[160:163], a[216:219], v[184:187]\n"
"v_mfma_f32_16x16x32_bf16 v[184:187], a[164:167], a[220:223], v[184:187]\n"
"v_mfma_f32_16x16x32_bf16 v[188:191], a[168:171], a[216:219], v[188:191]\n"
"s_waitcnt lgkmcnt(2)\n"
"v_mfma_f32_16x16x32_bf16 v[188:191], a[172:175], a[220:223], v[188:191]\n"
"v_mfma_f32_16x16x32_bf16 v[0:3], a[112:115], a[176:179], v[0:3]\n"
"ds_read_b128 a[216:219], v208 offset:52224\n"
"v_mfma_f32_16x16x32_bf16 v[0:3], a[116:119], a[180:183], v[0:3]\n"
"ds_read_b128 a[220:223], v208 offset:52288\n"
"v_mfma_f32_16x16x32_bf16 v[4:7], a[120:123], a[176:179], v[4:7]\n"
"ds_read_b64_tr_b16 a[144:145], v210 offset:128\n"
"v_mfma_f32_16x16x32_bf16 v[4:7], a[124:127], a[180:183], v[4:7]\n"
"ds_read_b64_tr_b16 a[146:147], v210 offset:4480\n"
"v_mfma_f32_16x16x32_bf16 v[8:11], a[128:131], a[176:179], v[8:11]\n"
"ds_read_b64_tr_b16 a[148:149], v210 offset:17536\n"
"v_mfma_f32_16x16x32_bf16 v[8:11], a[132:135], a[180:183], v[8:11]\n"
"ds_read_b64_tr_b16 a[150:151], v210 offset:21888\n"
"v_mfma_f32_16x16x32_bf16 v[12:15], a[136:139], a[176:179], v[12:15]\n"
"ds_read_b64_tr_b16 a[152:153], v211 offset:128\n"
"v_mfma_f32_16x16x32_bf16 v[12:15], a[140:143], a[180:183], v[12:15]\n"
"ds_read_b64_tr_b16 a[154:155], v211 offset:4480\n"
"v_mfma_f32_16x16x32_bf16 v[32:35], a[112:115], a[184:187], v[32:35]\n"
"ds_read_b64_tr_b16 a[156:157], v211 offset:17536\n"
"v_mfma_f32_16x16x32_bf16 v[32:35], a[116:119], a[188:191], v[32:35]\n"
"ds_read_b64_tr_b16 a[158:159], v211 offset:21888\n"
"v_mfma_f32_16x16x32_bf16 v[36:39], a[120:123], a[184:187], v[36:39]\n"
"ds_read_b64_tr_b16 a[160:161], v210 offset:192\n"
"v_mfma_f32_16x16x32_bf16 v[36:39], a[124:127], a[188:191], v[36:39]\n"
"ds_read_b64_tr_b16 a[162:163], v210 offset:4544\n"
"v_mfma_f32_16x16x32_bf16 v[40:43], a[128:131], a[184:187], v[40:43]\n"
"ds_read_b64_tr_b16 a[164:165], v210 offset:17600\n"
"v_mfma_f32_16x16x32_bf16 v[40:43], a[132:135], a[188:191], v[40:43]\n"
"ds_read_b64_tr_b16 a[166:167], v210 offset:21952\n"
"v_mfma_f32_16x16x32_bf16 v[44:47], a[136:139], a[184:187], v[44:47]\n"
"ds_read_b64_tr_b16 a[168:169], v211 offset:192\n"
"v_mfma_f32_16x16x32_bf16 v[44:47], a[140:143], a[188:191], v[44:47]\n"
"ds_read_b64_tr_b16 a[170:171], v211 offset:4544\n"
"v_mfma_f32_16x16x32_bf16 v[64:67], a[112:115], a[192:195], v[64:67]\n"
"ds_read_b64_tr_b16 a[172:173], v211 offset:17600\n"
"v_mfma_f32_16x16x32_bf16 v[64:67], a[116:119], a[196:199], v[64:67]\n"
"ds_read_b64_tr_b16 a[174:175], v211 offset:21952\n"
"v_mfma_f32_16x16x32_bf16 v[68:71], a[120:123], a[192:195], v[68:71]\n"
"v_mfma_f32_16x16x32_bf16 v[68:71], a[124:127], a[196:199], v[68:71]\n"
"v_mfma_f32_16x16x32_bf16 v[72:75], a[128:131], a[192:195], v[72:75]\n"
"v_mfma_f32_16x16x32_bf16 v[72:75], a[132:135], a[196:199], v[72:75]\n"
"v_mfma_f32_16x16x32_bf16 v[76:79], a[136:139], a[192:195], v[76:79]\n"
"v_mfma_f32_16x16x32_bf16 v[76:79], a[140:143], a[196:199], v[76:79]\n"
"v_mfma_f32_16x16x32_bf16 v[96:99], a[112:115], a[200:203], v[96:99]\n"
"s_waitcnt lgkmcnt(0)\n"
"s_waitcnt vmcnt(14)\n"
"s_barrier\n"
"v_mfma_f32_16x16x32_bf16 v[96:99], a[116:119], a[204:207], v[96:99]\n"
"v_mfma_f32_16x16x32_bf16 v[100:103], a[120:123], a[200:203], v[100:103]\n"
"ds_write_b128 v252, a[0:3]\n"
"v_mfma_f32_16x16x32_bf16 v[100:103], a[124:127], a[204:207], v[100:103]\n"
"v_mfma_f32_16x16x32_bf16 v[104:107], a[128:131], a[200:203], v[104:107]\n"
"v_mfma_f32_16x16x32_bf16 v[104:107], a[132:135], a[204:207], v[104:107]\n"
"ds_write_b128 v252, a[4:7] offset:1056\n"
"v_mfma_f32_16x16x32_bf16 v[108:111], a[136:139], a[200:203], v[108:111]\n"
"v_mfma_f32_16x16x32_bf16 v[108:111], a[140:143], a[204:207], v[108:111]\n"
"v_mfma_f32_16x16x32_bf16 v[128:131], a[112:115], a[208:211], v[128:131]\n"
"ds_write_b128 v252, a[8:11] offset:2112\n"
"v_mfma_f32_16x16x32_bf16 v[128:131], a[116:119], a[212:215], v[128:131]\n"
"v_mfma_f32_16x16x32_bf16 v[132:135], a[120:123], a[208:211], v[132:135]\n"
"v_mfma_f32_16x16x32_bf16 v[132:135], a[124:127], a[212:215], v[132:135]\n"
"ds_write_b128 v252, a[12:15] offset:3168\n"
"v_mfma_f32_16x16x32_bf16 v[136:139], a[128:131], a[208:211], v[136:139]\n"
"v_mfma_f32_16x16x32_bf16 v[136:139], a[132:135], a[212:215], v[136:139]\n"
"v_mfma_f32_16x16x32_bf16 v[140:143], a[136:139], a[208:211], v[140:143]\n"
"ds_write_b128 v252, a[16:19] offset:4224\n"
"v_mfma_f32_16x16x32_bf16 v[140:143], a[140:143], a[212:215], v[140:143]\n"
"v_mfma_f32_16x16x32_bf16 v[160:163], a[112:115], a[216:219], v[160:163]\n"
"v_mfma_f32_16x16x32_bf16 v[160:163], a[116:119], a[220:223], v[160:163]\n"
"ds_write_b128 v252, a[20:23] offset:5280\n"
"v_mfma_f32_16x16x32_bf16 v[164:167], a[120:123], a[216:219], v[164:167]\n"
"v_mfma_f32_16x16x32_bf16 v[164:167], a[124:127], a[220:223], v[164:167]\n"
"v_mfma_f32_16x16x32_bf16 v[168:171], a[128:131], a[216:219], v[168:171]\n"
"ds_write_b128 v252, a[24:27] offset:6336\n"
"v_mfma_f32_16x16x32_bf16 v[168:171], a[132:135], a[220:223], v[168:171]\n"
"v_mfma_f32_16x16x32_bf16 v[172:175], a[136:139], a[216:219], v[172:175]\n"
"v_mfma_f32_16x16x32_bf16 v[172:175], a[140:143], a[220:223], v[172:175]\n"
"ds_write_b128 v252, a[28:31] offset:7392\n"
"v_mfma_f32_16x16x32_bf16 v[192:195], a[144:147], a[224:227], v[192:195]\n"
"v_mfma_f32_16x16x32_bf16 v[192:195], a[148:151], a[228:231], v[192:195]\n"
"v_mfma_f32_16x16x32_bf16 v[196:199], a[152:155], a[224:227], v[196:199]\n"
"ds_write_b128 v252, a[32:35] offset:34304\n"
"v_mfma_f32_16x16x32_bf16 v[196:199], a[156:159], a[228:231], v[196:199]\n"
"v_mfma_f32_16x16x32_bf16 v[200:203], a[160:163], a[224:227], v[200:203]\n"
"v_mfma_f32_16x16x32_bf16 v[200:203], a[164:167], a[228:231], v[200:203]\n"
"ds_write_b128 v252, a[36:39] offset:35360\n"
"v_mfma_f32_16x16x32_bf16 v[204:207], a[168:171], a[224:227], v[204:207]\n"
"v_mfma_f32_16x16x32_bf16 v[204:207], a[172:175], a[228:231], v[204:207]\n"
"v_mfma_f32_16x16x32_bf16 v[16:19], a[144:147], a[176:179], v[16:19]\n"
"ds_read_b128 a[224:227], v209 offset:26112\n"
"ds_write_b128 v252, a[40:43] offset:36416\n"
"v_mfma_f32_16x16x32_bf16 v[16:19], a[148:151], a[180:183], v[16:19]\n"
"ds_read_b128 a[228:231], v209 offset:26176\n"
"v_mfma_f32_16x16x32_bf16 v[20:23], a[152:155], a[176:179], v[20:23]\n"
"v_mfma_f32_16x16x32_bf16 v[20:23], a[156:159], a[180:183], v[20:23]\n"
"ds_write_b128 v252, a[44:47] offset:37472\n"
"v_mfma_f32_16x16x32_bf16 v[24:27], a[160:163], a[176:179], v[24:27]\n"
"v_mfma_f32_16x16x32_bf16 v[24:27], a[164:167], a[180:183], v[24:27]\n"
"v_mfma_f32_16x16x32_bf16 v[28:31], a[168:171], a[176:179], v[28:31]\n"
"ds_write_b128 v252, a[48:51] offset:38528\n"
"v_mfma_f32_16x16x32_bf16 v[28:31], a[172:175], a[180:183], v[28:31]\n"
"v_mfma_f32_16x16x32_bf16 v[48:51], a[144:147], a[184:187], v[48:51]\n"
"ds_read_b128 a[176:179], v209 offset:13056\n"
"v_mfma_f32_16x16x32_bf16 v[48:51], a[148:151], a[188:191], v[48:51]\n"
"ds_read_b128 a[180:183], v209 offset:13120\n"
"ds_write_b128 v252, a[52:55] offset:39584\n"
"v_mfma_f32_16x16x32_bf16 v[52:55], a[152:155], a[184:187], v[52:55]\n"
"v_mfma_f32_16x16x32_bf16 v[52:55], a[156:159], a[188:191], v[52:55]\n"
"v_mfma_f32_16x16x32_bf16 v[56:59], a[160:163], a[184:187], v[56:59]\n"
"ds_write_b128 v252, a[56:59] offset:40640\n"
"v_mfma_f32_16x16x32_bf16 v[56:59], a[164:167], a[188:191], v[56:59]\n"
"v_mfma_f32_16x16x32_bf16 v[60:63], a[168:171], a[184:187], v[60:63]\n"
"v_mfma_f32_16x16x32_bf16 v[60:63], a[172:175], a[188:191], v[60:63]\n"
"ds_write_b128 v252, a[60:63] offset:41696\n"
"v_mfma_f32_16x16x32_bf16 v[80:83], a[144:147], a[192:195], v[80:83]\n"
"ds_read_b128 a[184:187], v209 offset:15232\n"
"v_mfma_f32_16x16x32_bf16 v[80:83], a[148:151], a[196:199], v[80:83]\n"
"ds_read_b128 a[188:191], v209 offset:15296\n"
"v_mfma_f32_16x16x32_bf16 v[84:87], a[152:155], a[192:195], v[84:87]\n"
"v_mfma_f32_16x16x32_bf16 v[84:87], a[156:159], a[196:199], v[84:87]\n"
"v_mfma_f32_16x16x32_bf16 v[88:91], a[160:163], a[192:195], v[88:91]\n"
"v_mfma_f32_16x16x32_bf16 v[88:91], a[164:167], a[196:199], v[88:91]\n"
"s_waitcnt vmcnt(3)\n"
"s_barrier\n"
"v_mfma_f32_16x16x32_bf16 v[92:95], a[168:171], a[192:195], v[92:95]\n"
"ds_read_b64_tr_b16 a[112:113], v210 offset:34816\n"
"v_mfma_f32_16x16x32_bf16 v[92:95], a[172:175], a[196:199], v[92:95]\n"
"ds_read_b64_tr_b16 a[114:115], v210 offset:39168\n"
"v_mfma_f32_16x16x32_bf16 v[112:115], a[144:147], a[200:203], v[112:115]\n"
"ds_read_b128 a[192:195], v209 offset:17408\n"
"ds_read_b64_tr_b16 a[116:117], v210 offset:52224\n"
"v_mfma_f32_16x16x32_bf16 v[112:115], a[148:151], a[204:207], v[112:115]\n"
"ds_read_b128 a[196:199], v209 offset:17472\n"
"ds_read_b64_tr_b16 a[118:119], v210 offset:56576\n"
"v_mfma_f32_16x16x32_bf16 v[116:119], a[152:155], a[200:203], v[116:119]\n"
"ds_read_b64_tr_b16 a[120:121], v211 offset:34816\n"
"v_mfma_f32_16x16x32_bf16 v[116:119], a[156:159], a[204:207], v[116:119]\n"
"ds_read_b64_tr_b16 a[122:123], v211 offset:39168\n"
"v_mfma_f32_16x16x32_bf16 v[120:123], a[160:163], a[200:203], v[120:123]\n"
"ds_read_b64_tr_b16 a[124:125], v211 offset:52224\n"
"v_mfma_f32_16x16x32_bf16 v[120:123], a[164:167], a[204:207], v[120:123]\n"
"ds_read_b64_tr_b16 a[126:127], v211 offset:56576\n"
"v_mfma_f32_16x16x32_bf16 v[124:127], a[168:171], a[200:203], v[124:127]\n"
"ds_read_b64_tr_b16 a[128:129], v210 offset:34880\n"
"v_mfma_f32_16x16x32_bf16 v[124:127], a[172:175], a[204:207], v[124:127]\n"
"ds_read_b64_tr_b16 a[130:131], v210 offset:39232\n"
"v_mfma_f32_16x16x32_bf16 v[144:147], a[144:147], a[208:211], v[144:147]\n"
"ds_read_b128 a[200:203], v209 offset:19584\n"
"ds_read_b64_tr_b16 a[132:133], v210 offset:52288\n"
"v_mfma_f32_16x16x32_bf16 v[144:147], a[148:151], a[212:215], v[144:147]\n"
"ds_read_b128 a[204:207], v209 offset:19648\n"
"ds_read_b64_tr_b16 a[134:135], v210 offset:56640\n"
"v_mfma_f32_16x16x32_bf16 v[148:151], a[152:155], a[208:211], v[148:151]\n"
"ds_read_b64_tr_b16 a[136:137], v211 offset:34880\n"
"v_mfma_f32_16x16x32_bf16 v[148:151], a[156:159], a[212:215], v[148:151]\n"
"ds_read_b64_tr_b16 a[138:139], v211 offset:39232\n"
"v_mfma_f32_16x16x32_bf16 v[152:155], a[160:163], a[208:211], v[152:155]\n"
"ds_read_b64_tr_b16 a[140:141], v211 offset:52288\n"
"v_mfma_f32_16x16x32_bf16 v[152:155], a[164:167], a[212:215], v[152:155]\n"
"ds_read_b64_tr_b16 a[142:143], v211 offset:56640\n"
"v_mfma_f32_16x16x32_bf16 v[156:159], a[168:171], a[208:211], v[156:159]\n"
"v_mfma_f32_16x16x32_bf16 v[156:159], a[172:175], a[212:215], v[156:159]\n"
"v_mfma_f32_16x16x32_bf16 v[176:179], a[144:147], a[216:219], v[176:179]\n"
"ds_read_b128 a[208:211], v209 offset:21760\n"
"v_mfma_f32_16x16x32_bf16 v[176:179], a[148:151], a[220:223], v[176:179]\n"
"ds_read_b128 a[212:215], v209 offset:21824\n"
"v_mfma_f32_16x16x32_bf16 v[180:183], a[152:155], a[216:219], v[180:183]\n"
"v_mfma_f32_16x16x32_bf16 v[180:183], a[156:159], a[220:223], v[180:183]\n"
"v_mfma_f32_16x16x32_bf16 v[184:187], a[160:163], a[216:219], v[184:187]\n"
"v_mfma_f32_16x16x32_bf16 v[184:187], a[164:167], a[220:223], v[184:187]\n"
"v_mfma_f32_16x16x32_bf16 v[188:191], a[168:171], a[216:219], v[188:191]\n"
"s_waitcnt lgkmcnt(2)\n"
"v_mfma_f32_16x16x32_bf16 v[188:191], a[172:175], a[220:223], v[188:191]\n"
"v_mfma_f32_16x16x32_bf16 v[0:3], a[112:115], a[176:179], v[0:3]\n"
"ds_read_b128 a[216:219], v209 offset:23936\n"
"v_mfma_f32_16x16x32_bf16 v[0:3], a[116:119], a[180:183], v[0:3]\n"
"ds_read_b128 a[220:223], v209 offset:24000\n"
"v_mfma_f32_16x16x32_bf16 v[4:7], a[120:123], a[176:179], v[4:7]\n"
"ds_read_b64_tr_b16 a[144:145], v210 offset:34944\n"
"v_mfma_f32_16x16x32_bf16 v[4:7], a[124:127], a[180:183], v[4:7]\n"
"ds_read_b64_tr_b16 a[146:147], v210 offset:39296\n"
"v_mfma_f32_16x16x32_bf16 v[8:11], a[128:131], a[176:179], v[8:11]\n"
"ds_read_b64_tr_b16 a[148:149], v210 offset:52352\n"
"v_mfma_f32_16x16x32_bf16 v[8:11], a[132:135], a[180:183], v[8:11]\n"
"ds_read_b64_tr_b16 a[150:151], v210 offset:56704\n"
"v_mfma_f32_16x16x32_bf16 v[12:15], a[136:139], a[176:179], v[12:15]\n"
"ds_read_b64_tr_b16 a[152:153], v211 offset:34944\n"
"v_mfma_f32_16x16x32_bf16 v[12:15], a[140:143], a[180:183], v[12:15]\n"
"ds_read_b64_tr_b16 a[154:155], v211 offset:39296\n"
"v_mfma_f32_16x16x32_bf16 v[32:35], a[112:115], a[184:187], v[32:35]\n"
"ds_read_b64_tr_b16 a[156:157], v211 offset:52352\n"
"v_mfma_f32_16x16x32_bf16 v[32:35], a[116:119], a[188:191], v[32:35]\n"
"ds_read_b64_tr_b16 a[158:159], v211 offset:56704\n"
"v_mfma_f32_16x16x32_bf16 v[36:39], a[120:123], a[184:187], v[36:39]\n"
"ds_read_b64_tr_b16 a[160:161], v210 offset:35008\n"
"v_mfma_f32_16x16x32_bf16 v[36:39], a[124:127], a[188:191], v[36:39]\n"
"ds_read_b64_tr_b16 a[162:163], v210 offset:39360\n"
"v_mfma_f32_16x16x32_bf16 v[40:43], a[128:131], a[184:187], v[40:43]\n"
"ds_read_b64_tr_b16 a[164:165], v210 offset:52416\n"
"v_mfma_f32_16x16x32_bf16 v[40:43], a[132:135], a[188:191], v[40:43]\n"
"ds_read_b64_tr_b16 a[166:167], v210 offset:56768\n"
"v_mfma_f32_16x16x32_bf16 v[44:47], a[136:139], a[184:187], v[44:47]\n"
"ds_read_b64_tr_b16 a[168:169], v211 offset:35008\n"
"v_mfma_f32_16x16x32_bf16 v[44:47], a[140:143], a[188:191], v[44:47]\n"
"ds_read_b64_tr_b16 a[170:171], v211 offset:39360\n"
"v_mfma_f32_16x16x32_bf16 v[64:67], a[112:115], a[192:195], v[64:67]\n"
"ds_read_b64_tr_b16 a[172:173], v211 offset:52416\n"
"v_mfma_f32_16x16x32_bf16 v[64:67], a[116:119], a[196:199], v[64:67]\n"
"ds_read_b64_tr_b16 a[174:175], v211 offset:56768\n"
"v_mfma_f32_16x16x32_bf16 v[68:71], a[120:123], a[192:195], v[68:71]\n"
"v_mfma_f32_16x16x32_bf16 v[68:71], a[124:127], a[196:199], v[68:71]\n"
"v_mfma_f32_16x16x32_bf16 v[72:75], a[128:131], a[192:195], v[72:75]\n"
"v_mfma_f32_16x16x32_bf16 v[72:75], a[132:135], a[196:199], v[72:75]\n"
"v_mfma_f32_16x16x32_bf16 v[76:79], a[136:139], a[192:195], v[76:79]\n"
"v_mov_b32 v216, 0\n"
"v_mfma_f32_16x16x32_bf16 v[76:79], a[140:143], a[196:199], v[76:79]\n"
"v_mfma_f32_16x16x32_bf16 v[96:99], a[112:115], a[200:203], v[96:99]\n"
"s_waitcnt lgkmcnt(0)\n"
"s_barrier\n"
"v_mfma_f32_16x16x32_bf16 v[96:99], a[116:119], a[204:207], v[96:99]\n"
"v_mov_b32 v217, 0\n"
"v_mfma_f32_16x16x32_bf16 v[100:103], a[120:123], a[200:203], v[100:103]\n"
"ds_write_b128 v253, a[64:67]\n"
"v_mfma_f32_16x16x32_bf16 v[100:103], a[124:127], a[204:207], v[100:103]\n"
"v_mfma_f32_16x16x32_bf16 v[104:107], a[128:131], a[200:203], v[104:107]\n"
"v_mov_b32 v218, 0\n"
"v_mfma_f32_16x16x32_bf16 v[104:107], a[132:135], a[204:207], v[104:107]\n"
"v_mfma_f32_16x16x32_bf16 v[108:111], a[136:139], a[200:203], v[108:111]\n"
"v_mov_b32 v219, 0\n"
"v_mfma_f32_16x16x32_bf16 v[108:111], a[140:143], a[204:207], v[108:111]\n"
"ds_write_b128 v253, a[68:71] offset:1056\n"
"v_mfma_f32_16x16x32_bf16 v[128:131], a[112:115], a[208:211], v[128:131]\n"
"v_mfma_f32_16x16x32_bf16 v[128:131], a[116:119], a[212:215], v[128:131]\n"
"v_mov_b32 v220, 0\n"
"v_mfma_f32_16x16x32_bf16 v[132:135], a[120:123], a[208:211], v[132:135]\n"
"v_mfma_f32_16x16x32_bf16 v[132:135], a[124:127], a[212:215], v[132:135]\n"
"v_mov_b32 v221, 0\n"
"v_mfma_f32_16x16x32_bf16 v[136:139], a[128:131], a[208:211], v[136:139]\n"
"ds_write_b128 v253, a[72:75] offset:2112\n"
"v_mfma_f32_16x16x32_bf16 v[136:139], a[132:135], a[212:215], v[136:139]\n"
"v_mfma_f32_16x16x32_bf16 v[140:143], a[136:139], a[208:211], v[140:143]\n"
"v_mov_b32 v222, 0\n"
"v_mfma_f32_16x16x32_bf16 v[140:143], a[140:143], a[212:215], v[140:143]\n"
"v_mfma_f32_16x16x32_bf16 v[160:163], a[112:115], a[216:219], v[160:163]\n"
"v_mov_b32 v223, 0\n"
"v_mfma_f32_16x16x32_bf16 v[160:163], a[116:119], a[220:223], v[160:163]\n"
"ds_write_b128 v253, a[76:79] offset:3168\n"
"v_mfma_f32_16x16x32_bf16 v[164:167], a[120:123], a[216:219], v[164:167]\n"
"v_mfma_f32_16x16x32_bf16 v[164:167], a[124:127], a[220:223], v[164:167]\n"
"v_mov_b32 v224, 0\n"
"v_mfma_f32_16x16x32_bf16 v[168:171], a[128:131], a[216:219], v[168:171]\n"
"v_mfma_f32_16x16x32_bf16 v[168:171], a[132:135], a[220:223], v[168:171]\n"
"v_mov_b32 v225, 0\n"
"v_mfma_f32_16x16x32_bf16 v[172:175], a[136:139], a[216:219], v[172:175]\n"
"ds_write_b128 v253, a[80:83] offset:4224\n"
"v_mfma_f32_16x16x32_bf16 v[172:175], a[140:143], a[220:223], v[172:175]\n"
"v_mfma_f32_16x16x32_bf16 v[192:195], a[144:147], a[224:227], v[192:195]\n"
"v_mov_b32 v226, 0\n"
"v_mfma_f32_16x16x32_bf16 v[192:195], a[148:151], a[228:231], v[192:195]\n"
"v_mfma_f32_16x16x32_bf16 v[196:199], a[152:155], a[224:227], v[196:199]\n"
"v_mov_b32 v227, 0\n"
"v_mfma_f32_16x16x32_bf16 v[196:199], a[156:159], a[228:231], v[196:199]\n"
"ds_write_b128 v253, a[84:87] offset:5280\n"
"v_mfma_f32_16x16x32_bf16 v[200:203], a[160:163], a[224:227], v[200:203]\n"
"v_mfma_f32_16x16x32_bf16 v[200:203], a[164:167], a[228:231], v[200:203]\n"
"v_mov_b32 v228, 0\n"
"v_mfma_f32_16x16x32_bf16 v[204:207], a[168:171], a[224:227], v[204:207]\n"
"v_mfma_f32_16x16x32_bf16 v[204:207], a[172:175], a[228:231], v[204:207]\n"
"v_mov_b32 v229, 0\n"
"v_mfma_f32_16x16x32_bf16 v[16:19], a[144:147], a[176:179], v[16:19]\n"
"ds_write_b128 v253, a[88:91] offset:6336\n"
"v_mfma_f32_16x16x32_bf16 v[16:19], a[148:151], a[180:183], v[16:19]\n"
"v_mfma_f32_16x16x32_bf16 v[20:23], a[152:155], a[176:179], v[20:23]\n"
"v_mov_b32 v230, 0\n"
"v_mfma_f32_16x16x32_bf16 v[20:23], a[156:159], a[180:183], v[20:23]\n"
"v_mfma_f32_16x16x32_bf16 v[24:27], a[160:163], a[176:179], v[24:27]\n"
"v_mov_b32 v231, 0\n"
"v_mfma_f32_16x16x32_bf16 v[24:27], a[164:167], a[180:183], v[24:27]\n"
"ds_write_b128 v253, a[92:95] offset:7392\n"
"v_mfma_f32_16x16x32_bf16 v[28:31], a[168:171], a[176:179], v[28:31]\n"
"v_mfma_f32_16x16x32_bf16 v[28:31], a[172:175], a[180:183], v[28:31]\n"
"v_mov_b32 v232, 0\n"
"v_mfma_f32_16x16x32_bf16 v[48:51], a[144:147], a[184:187], v[48:51]\n"
"v_mfma_f32_16x16x32_bf16 v[48:51], a[148:151], a[188:191], v[48:51]\n"
"v_mov_b32 v233, 0\n"
"v_mfma_f32_16x16x32_bf16 v[52:55], a[152:155], a[184:187], v[52:55]\n"
"ds_write_b128 v253, a[96:99] offset:34304\n"
"v_mfma_f32_16x16x32_bf16 v[52:55], a[156:159], a[188:191], v[52:55]\n"
"v_mfma_f32_16x16x32_bf16 v[56:59], a[160:163], a[184:187], v[56:59]\n"
"v_mov_b32 v234, 0\n"
"v_mfma_f32_16x16x32_bf16 v[56:59], a[164:167], a[188:191], v[56:59]\n"
"v_mfma_f32_16x16x32_bf16 v[60:63], a[168:171], a[184:187], v[60:63]\n"
"v_mov_b32 v235, 0\n"
"v_mfma_f32_16x16x32_bf16 v[60:63], a[172:175], a[188:191], v[60:63]\n"
"ds_write_b128 v253, a[100:103] offset:35360\n"
"v_mfma_f32_16x16x32_bf16 v[80:83], a[144:147], a[192:195], v[80:83]\n"
"v_mfma_f32_16x16x32_bf16 v[80:83], a[148:151], a[196:199], v[80:83]\n"
"v_mov_b32 v236, 0\n"
"v_mfma_f32_16x16x32_bf16 v[84:87], a[152:155], a[192:195], v[84:87]\n"
"v_mfma_f32_16x16x32_bf16 v[84:87], a[156:159], a[196:199], v[84:87]\n"
"v_mov_b32 v237, 0\n"
"v_mfma_f32_16x16x32_bf16 v[88:91], a[160:163], a[192:195], v[88:91]\n"
"ds_write_b128 v253, a[104:107] offset:36416\n"
"v_mfma_f32_16x16x32_bf16 v[88:91], a[164:167], a[196:199], v[88:91]\n"
"v_mfma_f32_16x16x32_bf16 v[92:95], a[168:171], a[192:195], v[92:95]\n"
"v_mov_b32 v238, 0\n"
"v_mfma_f32_16x16x32_bf16 v[92:95], a[172:175], a[196:199], v[92:95]\n"
"v_mfma_f32_16x16x32_bf16 v[112:115], a[144:147], a[200:203], v[112:115]\n"
"v_mov_b32 v239, 0\n"
"v_mfma_f32_16x16x32_bf16 v[112:115], a[148:151], a[204:207], v[112:115]\n"
"ds_write_b128 v253, a[108:111] offset:37472\n"
"v_mfma_f32_16x16x32_bf16 v[116:119], a[152:155], a[200:203], v[116:119]\n"
"v_mfma_f32_16x16x32_bf16 v[116:119], a[156:159], a[204:207], v[116:119]\n"
"v_mov_b32 v240, 0\n"
"v_mfma_f32_16x16x32_bf16 v[120:123], a[160:163], a[200:203], v[120:123]\n"
"v_mfma_f32_16x16x32_bf16 v[120:123], a[164:167], a[204:207], v[120:123]\n"
"v_mov_b32 v241, 0\n"
"v_mfma_f32_16x16x32_bf16 v[124:127], a[168:171], a[200:203], v[124:127]\n"
"ds_write_b128 v253, a[240:243] offset:38528\n"
"v_mfma_f32_16x16x32_bf16 v[124:127], a[172:175], a[204:207], v[124:127]\n"
"v_mfma_f32_16x16x32_bf16 v[144:147], a[144:147], a[208:211], v[144:147]\n"
"v_mov_b32 v242, 0\n"
"v_mfma_f32_16x16x32_bf16 v[144:147], a[148:151], a[212:215], v[144:147]\n"
"v_mfma_f32_16x16x32_bf16 v[148:151], a[152:155], a[208:211], v[148:151]\n"
"v_mov_b32 v243, 0\n"
"v_mfma_f32_16x16x32_bf16 v[148:151], a[156:159], a[212:215], v[148:151]\n"
"s_waitcnt vmcnt(2)\n"
"ds_write_b128 v253, a[244:247] offset:39584\n"
"v_mfma_f32_16x16x32_bf16 v[152:155], a[160:163], a[208:211], v[152:155]\n"
"v_mfma_f32_16x16x32_bf16 v[152:155], a[164:167], a[212:215], v[152:155]\n"
"v_mfma_f32_16x16x32_bf16 v[156:159], a[168:171], a[208:211], v[156:159]\n"
"v_mfma_f32_16x16x32_bf16 v[156:159], a[172:175], a[212:215], v[156:159]\n"
"v_mfma_f32_16x16x32_bf16 v[176:179], a[144:147], a[216:219], v[176:179]\n"
"s_waitcnt vmcnt(1)\n"
"ds_write_b128 v253, a[248:251] offset:40640\n"
"v_mfma_f32_16x16x32_bf16 v[176:179], a[148:151], a[220:223], v[176:179]\n"
"v_mfma_f32_16x16x32_bf16 v[180:183], a[152:155], a[216:219], v[180:183]\n"
"v_mfma_f32_16x16x32_bf16 v[180:183], a[156:159], a[220:223], v[180:183]\n"
"v_mfma_f32_16x16x32_bf16 v[184:187], a[160:163], a[216:219], v[184:187]\n"
"v_mfma_f32_16x16x32_bf16 v[184:187], a[164:167], a[220:223], v[184:187]\n"
"s_waitcnt vmcnt(0)\n"
"ds_write_b128 v253, a[252:255] offset:41696\n"
"v_mfma_f32_16x16x32_bf16 v[188:191], a[168:171], a[216:219], v[188:191]\n"
"v_mfma_f32_16x16x32_bf16 v[188:191], a[172:175], a[220:223], v[188:191]\n"
"s_nop 3\n"
"v_accvgpr_read_b32 v252, a232\n"
"v_accvgpr_read_b32 v253, a233\n"
"s_nop 0\n"
"v_readfirstlane_b32 s74, v252\n"
"v_readfirstlane_b32 s75, v253\n"
"s_nop 0\n"
"s_add_u32 s74, s74, 2135587861\n"
"s_addc_u32 s75, s75, 2654435769\n"
"s_lshl_b32 s25, s21, 11\n"
"s_add_u32 s25, s25, 154496\n"
"v_add_u32 v252, s25, v251\n"
"v_pk_mul_f32 v[0:1], v[0:1], s[56:57]\n"
"v_pk_mul_f32 v[2:3], v[2:3], s[56:57]\n"
"v_cvt_pk_bf16_f32 v0, v0, v1\n"
"v_cvt_pk_bf16_f32 v1, v2, v3\n"
"v_pk_mul_f32 v[4:5], v[4:5], s[56:57]\n"
"v_pk_mul_f32 v[6:7], v[6:7], s[56:57]\n"
"v_cvt_pk_bf16_f32 v2, v4, v5\n"
"v_cvt_pk_bf16_f32 v3, v6, v7\n"
"v_pk_mul_f32 v[8:9], v[8:9], s[56:57]\n"
"v_pk_mul_f32 v[10:11], v[10:11], s[56:57]\n"
"v_cvt_pk_bf16_f32 v4, v8, v9\n"
"v_cvt_pk_bf16_f32 v5, v10, v11\n"
"v_pk_mul_f32 v[12:13], v[12:13], s[56:57]\n"
"v_pk_mul_f32 v[14:15], v[14:15], s[56:57]\n"
"v_cvt_pk_bf16_f32 v6, v12, v13\n"
"v_cvt_pk_bf16_f32 v7, v14, v15\n"
"v_pk_mul_f32 v[16:17], v[16:17], s[56:57]\n"
"v_pk_mul_f32 v[18:19], v[18:19], s[56:57]\n"
"v_cvt_pk_bf16_f32 v8, v16, v17\n"
"v_cvt_pk_bf16_f32 v9, v18, v19\n"
"v_pk_mul_f32 v[20:21], v[20:21], s[56:57]\n"
"v_pk_mul_f32 v[22:23], v[22:23], s[56:57]\n"
"v_cvt_pk_bf16_f32 v10, v20, v21\n"
"v_cvt_pk_bf16_f32 v11, v22, v23\n"
"v_pk_mul_f32 v[24:25], v[24:25], s[56:57]\n"
"v_pk_mul_f32 v[26:27], v[26:27], s[56:57]\n"
"v_cvt_pk_bf16_f32 v12, v24, v25\n"
"v_cvt_pk_bf16_f32 v13, v26, v27\n"
"v_pk_mul_f32 v[28:29], v[28:29], s[56:57]\n"
"v_pk_mul_f32 v[30:31], v[30:31], s[56:57]\n"
"v_cvt_pk_bf16_f32 v14, v28, v29\n"
"v_cvt_pk_bf16_f32 v15, v30, v31\n"
"v_pk_mul_f32 v[32:33], v[32:33], s[56:57]\n"
"v_pk_mul_f32 v[34:35], v[34:35], s[56:57]\n"
"v_cvt_pk_bf16_f32 v16, v32, v33\n"
"v_cvt_pk_bf16_f32 v17, v34, v35\n"
"v_pk_mul_f32 v[36:37], v[36:37], s[56:57]\n"
"v_pk_mul_f32 v[38:39], v[38:39], s[56:57]\n"
"v_cvt_pk_bf16_f32 v18, v36, v37\n"
"v_cvt_pk_bf16_f32 v19, v38, v39\n"
"v_pk_mul_f32 v[40:41], v[40:41], s[56:57]\n"
"v_pk_mul_f32 v[42:43], v[42:43], s[56:57]\n"
"v_cvt_pk_bf16_f32 v20, v40, v41\n"
"v_cvt_pk_bf16_f32 v21, v42, v43\n"
"v_pk_mul_f32 v[44:45], v[44:45], s[56:57]\n"
"v_pk_mul_f32 v[46:47], v[46:47], s[56:57]\n"
"v_cvt_pk_bf16_f32 v22, v44, v45\n"
"v_cvt_pk_bf16_f32 v23, v46, v47\n"
"v_pk_mul_f32 v[48:49], v[48:49], s[56:57]\n"
"v_pk_mul_f32 v[50:51], v[50:51], s[56:57]\n"
"v_cvt_pk_bf16_f32 v24, v48, v49\n"
"v_cvt_pk_bf16_f32 v25, v50, v51\n"
"v_pk_mul_f32 v[52:53], v[52:53], s[56:57]\n"
"v_pk_mul_f32 v[54:55], v[54:55], s[56:57]\n"
"v_cvt_pk_bf16_f32 v26, v52, v53\n"
"v_cvt_pk_bf16_f32 v27, v54, v55\n"
"v_pk_mul_f32 v[56:57], v[56:57], s[56:57]\n"
"v_pk_mul_f32 v[58:59], v[58:59], s[56:57]\n"
"v_cvt_pk_bf16_f32 v28, v56, v57\n"
"v_cvt_pk_bf16_f32 v29, v58, v59\n"
"v_pk_mul_f32 v[60:61], v[60:61], s[56:57]\n"
"v_pk_mul_f32 v[62:63], v[62:63], s[56:57]\n"
"v_cvt_pk_bf16_f32 v30, v60, v61\n"
"v_cvt_pk_bf16_f32 v31, v62, v63\n"
"v_pk_mul_f32 v[64:65], v[64:65], s[56:57]\n"
"v_pk_mul_f32 v[66:67], v[66:67], s[56:57]\n"
"v_cvt_pk_bf16_f32 v32, v64, v65\n"
"v_cvt_pk_bf16_f32 v33, v66, v67\n"
"v_pk_mul_f32 v[68:69], v[68:69], s[56:57]\n"
"v_pk_mul_f32 v[70:71], v[70:71], s[56:57]\n"
"v_cvt_pk_bf16_f32 v34, v68, v69\n"
"v_cvt_pk_bf16_f32 v35, v70, v71\n"
"v_pk_mul_f32 v[72:73], v[72:73], s[56:57]\n"
"v_pk_mul_f32 v[74:75], v[74:75], s[56:57]\n"
"v_cvt_pk_bf16_f32 v36, v72, v73\n"
"v_cvt_pk_bf16_f32 v37, v74, v75\n"
"v_pk_mul_f32 v[76:77], v[76:77], s[56:57]\n"
"v_pk_mul_f32 v[78:79], v[78:79], s[56:57]\n"
"v_cvt_pk_bf16_f32 v38, v76, v77\n"
"v_cvt_pk_bf16_f32 v39, v78, v79\n"
"v_pk_mul_f32 v[80:81], v[80:81], s[56:57]\n"
"v_pk_mul_f32 v[82:83], v[82:83], s[56:57]\n"
"v_cvt_pk_bf16_f32 v40, v80, v81\n"
"v_cvt_pk_bf16_f32 v41, v82, v83\n"
"v_pk_mul_f32 v[84:85], v[84:85], s[56:57]\n"
"v_pk_mul_f32 v[86:87], v[86:87], s[56:57]\n"
"v_cvt_pk_bf16_f32 v42, v84, v85\n"
"v_cvt_pk_bf16_f32 v43, v86, v87\n"
"v_pk_mul_f32 v[88:89], v[88:89], s[56:57]\n"
"v_pk_mul_f32 v[90:91], v[90:91], s[56:57]\n"
"v_cvt_pk_bf16_f32 v44, v88, v89\n"
"v_cvt_pk_bf16_f32 v45, v90, v91\n"
"v_pk_mul_f32 v[92:93], v[92:93], s[56:57]\n"
"v_pk_mul_f32 v[94:95], v[94:95], s[56:57]\n"
"v_cvt_pk_bf16_f32 v46, v92, v93\n"
"v_cvt_pk_bf16_f32 v47, v94, v95\n"
"v_pk_mul_f32 v[96:97], v[96:97], s[56:57]\n"
"v_pk_mul_f32 v[98:99], v[98:99], s[56:57]\n"
"v_cvt_pk_bf16_f32 v48, v96, v97\n"
"v_cvt_pk_bf16_f32 v49, v98, v99\n"
"v_pk_mul_f32 v[100:101], v[100:101], s[56:57]\n"
"v_pk_mul_f32 v[102:103], v[102:103], s[56:57]\n"
"v_cvt_pk_bf16_f32 v50, v100, v101\n"
"v_cvt_pk_bf16_f32 v51, v102, v103\n"
"v_pk_mul_f32 v[104:105], v[104:105], s[56:57]\n"
"v_pk_mul_f32 v[106:107], v[106:107], s[56:57]\n"
"v_cvt_pk_bf16_f32 v52, v104, v105\n"
"v_cvt_pk_bf16_f32 v53, v106, v107\n"
"v_pk_mul_f32 v[108:109], v[108:109], s[56:57]\n"
"v_pk_mul_f32 v[110:111], v[110:111], s[56:57]\n"
"v_cvt_pk_bf16_f32 v54, v108, v109\n"
"v_cvt_pk_bf16_f32 v55, v110, v111\n"
"v_pk_mul_f32 v[112:113], v[112:113], s[56:57]\n"
"v_pk_mul_f32 v[114:115], v[114:115], s[56:57]\n"
"v_cvt_pk_bf16_f32 v56, v112, v113\n"
"v_cvt_pk_bf16_f32 v57, v114, v115\n"
"v_pk_mul_f32 v[116:117], v[116:117], s[56:57]\n"
"v_pk_mul_f32 v[118:119], v[118:119], s[56:57]\n"
"v_cvt_pk_bf16_f32 v58, v116, v117\n"
"v_cvt_pk_bf16_f32 v59, v118, v119\n"
"v_pk_mul_f32 v[120:121], v[120:121], s[56:57]\n"
"v_pk_mul_f32 v[122:123], v[122:123], s[56:57]\n"
"v_cvt_pk_bf16_f32 v60, v120, v121\n"
"v_cvt_pk_bf16_f32 v61, v122, v123\n"
"v_pk_mul_f32 v[124:125], v[124:125], s[56:57]\n"
"v_pk_mul_f32 v[126:127], v[126:127], s[56:57]\n"
"v_cvt_pk_bf16_f32 v62, v124, v125\n"
"v_cvt_pk_bf16_f32 v63, v126, v127\n"
"v_pk_mul_f32 v[128:129], v[128:129], s[56:57]\n"
"v_pk_mul_f32 v[130:131], v[130:131], s[56:57]\n"
"v_cvt_pk_bf16_f32 v64, v128, v129\n"
"v_cvt_pk_bf16_f32 v65, v130, v131\n"
"v_pk_mul_f32 v[132:133], v[132:133], s[56:57]\n"
"v_pk_mul_f32 v[134:135], v[134:135], s[56:57]\n"
"v_cvt_pk_bf16_f32 v66, v132, v133\n"
"v_cvt_pk_bf16_f32 v67, v134, v135\n"
"v_pk_mul_f32 v[136:137], v[136:137], s[56:57]\n"
"v_pk_mul_f32 v[138:139], v[138:139], s[56:57]\n"
"v_cvt_pk_bf16_f32 v68, v136, v137\n"
"v_cvt_pk_bf16_f32 v69, v138, v139\n"
"v_pk_mul_f32 v[140:141], v[140:141], s[56:57]\n"
"v_pk_mul_f32 v[142:143], v[142:143], s[56:57]\n"
"v_cvt_pk_bf16_f32 v70, v140, v141\n"
"v_cvt_pk_bf16_f32 v71, v142, v143\n"
"v_pk_mul_f32 v[144:145], v[144:145], s[56:57]\n"
"v_pk_mul_f32 v[146:147], v[146:147], s[56:57]\n"
"v_cvt_pk_bf16_f32 v72, v144, v145\n"
"v_cvt_pk_bf16_f32 v73, v146, v147\n"
"v_pk_mul_f32 v[148:149], v[148:149], s[56:57]\n"
"v_pk_mul_f32 v[150:151], v[150:151], s[56:57]\n"
"v_cvt_pk_bf16_f32 v74, v148, v149\n"
"v_cvt_pk_bf16_f32 v75, v150, v151\n"
"v_pk_mul_f32 v[152:153], v[152:153], s[56:57]\n"
"v_pk_mul_f32 v[154:155], v[154:155], s[56:57]\n"
"v_cvt_pk_bf16_f32 v76, v152, v153\n"
"v_cvt_pk_bf16_f32 v77, v154, v155\n"
"v_pk_mul_f32 v[156:157], v[156:157], s[56:57]\n"
"v_pk_mul_f32 v[158:159], v[158:159], s[56:57]\n"
"v_cvt_pk_bf16_f32 v78, v156, v157\n"
"v_cvt_pk_bf16_f32 v79, v158, v159\n"
"v_pk_mul_f32 v[160:161], v[160:161], s[56:57]\n"
"v_pk_mul_f32 v[162:163], v[162:163], s[56:57]\n"
"v_cvt_pk_bf16_f32 v80, v160, v161\n"
"v_cvt_pk_bf16_f32 v81, v162, v163\n"
"v_pk_mul_f32 v[164:165], v[164:165], s[56:57]\n"
"v_pk_mul_f32 v[166:167], v[166:167], s[56:57]\n"
"v_cvt_pk_bf16_f32 v82, v164, v165\n"
"v_cvt_pk_bf16_f32 v83, v166, v167\n"
"v_pk_mul_f32 v[168:169], v[168:169], s[56:57]\n"
"v_pk_mul_f32 v[170:171], v[170:171], s[56:57]\n"
"v_cvt_pk_bf16_f32 v84, v168, v169\n"
"v_cvt_pk_bf16_f32 v85, v170, v171\n"
"v_pk_mul_f32 v[172:173], v[172:173], s[56:57]\n"
"v_pk_mul_f32 v[174:175], v[174:175], s[56:57]\n"
"v_cvt_pk_bf16_f32 v86, v172, v173\n"
"v_cvt_pk_bf16_f32 v87, v174, v175\n"
"v_pk_mul_f32 v[176:177], v[176:177], s[56:57]\n"
"v_pk_mul_f32 v[178:179], v[178:179], s[56:57]\n"
"v_cvt_pk_bf16_f32 v88, v176, v177\n"
"v_cvt_pk_bf16_f32 v89, v178, v179\n"
"v_pk_mul_f32 v[180:181], v[180:181], s[56:57]\n"
"v_pk_mul_f32 v[182:183], v[182:183], s[56:57]\n"
"v_cvt_pk_bf16_f32 v90, v180, v181\n"
"v_cvt_pk_bf16_f32 v91, v182, v183\n"
"v_pk_mul_f32 v[184:185], v[184:185], s[56:57]\n"
"v_pk_mul_f32 v[186:187], v[186:187], s[56:57]\n"
"v_cvt_pk_bf16_f32 v92, v184, v185\n"
"v_cvt_pk_bf16_f32 v93, v186, v187\n"
"v_pk_mul_f32 v[188:189], v[188:189], s[56:57]\n"
"v_pk_mul_f32 v[190:191], v[190:191], s[56:57]\n"
"v_cvt_pk_bf16_f32 v94, v188, v189\n"
"v_cvt_pk_bf16_f32 v95, v190, v191\n"
"v_pk_mul_f32 v[192:193], v[192:193], s[56:57]\n"
"v_pk_mul_f32 v[194:195], v[194:195], s[56:57]\n"
"v_cvt_pk_bf16_f32 v192, v192, v193\n"
"v_cvt_pk_bf16_f32 v193, v194, v195\n"
"v_pk_mul_f32 v[196:197], v[196:197], s[56:57]\n"
"v_pk_mul_f32 v[198:199], v[198:199], s[56:57]\n"
"v_cvt_pk_bf16_f32 v194, v196, v197\n"
"v_cvt_pk_bf16_f32 v195, v198, v199\n"
"v_pk_mul_f32 v[200:201], v[200:201], s[56:57]\n"
"v_pk_mul_f32 v[202:203], v[202:203], s[56:57]\n"
"v_cvt_pk_bf16_f32 v196, v200, v201\n"
"v_cvt_pk_bf16_f32 v197, v202, v203\n"
"v_pk_mul_f32 v[204:205], v[204:205], s[56:57]\n"
"v_pk_mul_f32 v[206:207], v[206:207], s[56:57]\n"
"v_cvt_pk_bf16_f32 v198, v204, v205\n"
"v_cvt_pk_bf16_f32 v199, v206, v207\n"
"s_mov_b32 exec_lo, 0xffff0000\n"
"s_mov_b32 exec_hi, 0xffff0000\n"
"v_swap_b32 v192, v194\n"
"v_swap_b32 v193, v195\n"
"v_swap_b32 v196, v198\n"
"v_swap_b32 v197, v199\n"
"v_swap_b32 v0, v2\n"
"v_swap_b32 v1, v3\n"
"v_swap_b32 v4, v6\n"
"v_swap_b32 v5, v7\n"
"v_swap_b32 v8, v10\n"
"v_swap_b32 v9, v11\n"
"v_swap_b32 v12, v14\n"
"v_swap_b32 v13, v15\n"
"v_swap_b32 v16, v18\n"
"v_swap_b32 v17, v19\n"
"v_swap_b32 v20, v22\n"
"v_swap_b32 v21, v23\n"
"v_swap_b32 v24, v26\n"
"v_swap_b32 v25, v27\n"
"v_swap_b32 v28, v30\n"
"v_swap_b32 v29, v31\n"
"v_swap_b32 v32, v34\n"
"v_swap_b32 v33, v35\n"
"v_swap_b32 v36, v38\n"
"v_swap_b32 v37, v39\n"
"v_swap_b32 v40, v42\n"
"v_swap_b32 v41, v43\n"
"v_swap_b32 v44, v46\n"
"v_swap_b32 v45, v47\n"
"v_swap_b32 v48, v50\n"
"v_swap_b32 v49, v51\n"
"v_swap_b32 v52, v54\n"
"v_swap_b32 v53, v55\n"
"v_swap_b32 v56, v58\n"
"v_swap_b32 v57, v59\n"
"v_swap_b32 v60, v62\n"
"v_swap_b32 v61, v63\n"
"v_swap_b32 v64, v66\n"
"v_swap_b32 v65, v67\n"
"v_swap_b32 v68, v70\n"
"v_swap_b32 v69, v71\n"
"v_swap_b32 v72, v74\n"
"v_swap_b32 v73, v75\n"
"v_swap_b32 v76, v78\n"
"v_swap_b32 v77, v79\n"
"v_swap_b32 v80, v82\n"
"v_swap_b32 v81, v83\n"
"v_swap_b32 v84, v86\n"
"v_swap_b32 v85, v87\n"
"v_swap_b32 v88, v90\n"
"v_swap_b32 v89, v91\n"
"v_swap_b32 v92, v94\n"
"v_swap_b32 v93, v95\n"
"s_mov_b64 exec, -1\n"
"ds_write_b128 v252, v[192:195]\n"
"ds_write_b128 v252, v[196:199] offset:1024\n"
"v_accvgpr_write_b32 a0, v0\n"
"v_accvgpr_write_b32 a1, v1\n"
"v_accvgpr_write_b32 a2, v2\n"
"v_accvgpr_write_b32 a3, v3\n"
"v_accvgpr_write_b32 a4, v4\n"
"v_accvgpr_write_b32 a5, v5\n"
"v_accvgpr_write_b32 a6, v6\n"
"v_accvgpr_write_b32 a7, v7\n"
"v_accvgpr_write_b32 a8, v8\n"
"v_accvgpr_write_b32 a9, v9\n"
"v_accvgpr_write_b32 a10, v10\n"
"v_accvgpr_write_b32 a11, v11\n"
"v_accvgpr_write_b32 a12, v12\n"
"v_accvgpr_write_b32 a13, v13\n"
"v_accvgpr_write_b32 a14, v14\n"
"v_accvgpr_write_b32 a15, v15\n"
"v_accvgpr_write_b32 a16, v16\n"
"v_accvgpr_write_b32 a17, v17\n"
"v_accvgpr_write_b32 a18, v18\n"
"v_accvgpr_write_b32 a19, v19\n"
"v_accvgpr_write_b32 a20, v20\n"
"v_accvgpr_write_b32 a21, v21\n"
"v_accvgpr_write_b32 a22, v22\n"
"v_accvgpr_write_b32 a23, v23\n"
"v_accvgpr_write_b32 a24, v24\n"
"v_accvgpr_write_b32 a25, v25\n"
"v_accvgpr_write_b32 a26, v26\n"
"v_accvgpr_write_b32 a27, v27\n"
"v_accvgpr_write_b32 a28, v28\n"
"v_accvgpr_write_b32 a29, v29\n"
"v_accvgpr_write_b32 a30, v30\n"
"v_accvgpr_write_b32 a31, v31\n"
"v_accvgpr_write_b32 a32, v32\n"
"v_accvgpr_write_b32 a33, v33\n"
"v_accvgpr_write_b32 a34, v34\n"
"v_accvgpr_write_b32 a35, v35\n"
"v_accvgpr_write_b32 a36, v36\n"
"v_accvgpr_write_b32 a37, v37\n"
"v_accvgpr_write_b32 a38, v38\n"
"v_accvgpr_write_b32 a39, v39\n"
"v_accvgpr_write_b32 a40, v40\n"
"v_accvgpr_write_b32 a41, v41\n"
"v_accvgpr_write_b32 a42, v42\n"
"v_accvgpr_write_b32 a43, v43\n"
"v_accvgpr_write_b32 a44, v44\n"
"v_accvgpr_write_b32 a45, v45\n"
"v_accvgpr_write_b32 a46, v46\n"
"v_accvgpr_write_b32 a47, v47\n"
"v_accvgpr_write_b32 a48, v48\n"
"v_accvgpr_write_b32 a49, v49\n"
"v_accvgpr_write_b32 a50, v50\n"
"v_accvgpr_write_b32 a51, v51\n"
"v_accvgpr_write_b32 a52, v52\n"
"v_accvgpr_write_b32 a53, v53\n"
"v_accvgpr_write_b32 a54, v54\n"
"v_accvgpr_write_b32 a55, v55\n"
"v_accvgpr_write_b32 a56, v56\n"
"v_accvgpr_write_b32 a57, v57\n"
"v_accvgpr_write_b32 a58, v58\n"
"v_accvgpr_write_b32 a59, v59\n"
"v_accvgpr_write_b32 a60, v60\n"
"v_accvgpr_write_b32 a61, v61\n"
"v_accvgpr_write_b32 a62, v62\n"
"v_accvgpr_write_b32 a63, v63\n"
"v_accvgpr_write_b32 a64, v64\n"
"v_accvgpr_write_b32 a65, v65\n"
"v_accvgpr_write_b32 a66, v66\n"
"v_accvgpr_write_b32 a67, v67\n"
"v_accvgpr_write_b32 a68, v68\n"
"v_accvgpr_write_b32 a69, v69\n"
"v_accvgpr_write_b32 a70, v70\n"
"v_accvgpr_write_b32 a71, v71\n"
"v_accvgpr_write_b32 a72, v72\n"
"v_accvgpr_write_b32 a73, v73\n"
"v_accvgpr_write_b32 a74, v74\n"
"v_accvgpr_write_b32 a75, v75\n"
"v_accvgpr_write_b32 a76, v76\n"
"v_accvgpr_write_b32 a77, v77\n"
"v_accvgpr_write_b32 a78, v78\n"
"v_accvgpr_write_b32 a79, v79\n"
"v_accvgpr_write_b32 a80, v80\n"
"v_accvgpr_write_b32 a81, v81\n"
"v_accvgpr_write_b32 a82, v82\n"
"v_accvgpr_write_b32 a83, v83\n"
"v_accvgpr_write_b32 a84, v84\n"
"v_accvgpr_write_b32 a85, v85\n"
"v_accvgpr_write_b32 a86, v86\n"
"v_accvgpr_write_b32 a87, v87\n"
"v_accvgpr_write_b32 a88, v88\n"
"v_accvgpr_write_b32 a89, v89\n"
"v_accvgpr_write_b32 a90, v90\n"
"v_accvgpr_write_b32 a91, v91\n"
"v_accvgpr_write_b32 a92, v92\n"
"v_accvgpr_write_b32 a93, v93\n"
"v_accvgpr_write_b32 a94, v94\n"
"v_accvgpr_write_b32 a95, v95\n"
"s_lshl_b32 s25, s22, 12\n"
"s_add_u32 s25, s25, 154496\n"
"v_add_u32 v252, s25, v251\n"
"s_waitcnt lgkmcnt(0)\n"
"s_barrier\n"
"ds_read_b128 a[96:99], v252\n"
"ds_read_b128 a[100:103], v252 offset:1024\n"
"ds_read_b128 a[104:107], v252 offset:2048\n"
"ds_read_b128 a[108:111], v252 offset:3072\n"
"L1_attn_h1:\n"
"s_bfe_u32 s35, s20, 196611\n"
"s_mul_i32 s35, s35, 128\n"
"s_add_u32 s35, s35, 0\n"
"s_mov_b32 s66, 1160\n"
"ds_read_b128 a[112:115], v244\n"
"ds_read_b128 a[128:131], v244 offset:4352\n"
"ds_read_b128 a[116:119], v244 offset:64\n"
"ds_read_b128 a[132:135], v244 offset:4416\n"
"ds_read_b128 a[120:123], v244 offset:128\n"
"ds_read_b128 a[136:139], v244 offset:4480\n"
"ds_read_b128 a[124:127], v244 offset:192\n"
"ds_read_b128 a[140:143], v244 offset:4544\n"
"s_waitcnt lgkmcnt(7)\n"
"v_mfma_f32_16x16x32_bf16 v[192:195], a[112:115], a[0:3], v[216:219]\n"
"s_waitcnt lgkmcnt(5)\n"
"v_mfma_f32_16x16x32_bf16 v[192:195], a[116:119], a[4:7], v[192:195]\n"
"s_waitcnt lgkmcnt(3)\n"
"v_mfma_f32_16x16x32_bf16 v[192:195], a[120:123], a[8:11], v[192:195]\n"
"s_waitcnt lgkmcnt(1)\n"
"v_mfma_f32_16x16x32_bf16 v[192:195], a[124:127], a[12:15], v[192:195]\n"
"v_mfma_f32_16x16x32_bf16 v[196:199], a[128:131], a[0:3], v[216:219]\n"
"v_mfma_f32_16x16x32_bf16 v[196:199], a[132:135], a[4:7], v[196:199]\n"
"v_mfma_f32_16x16x32_bf16 v[196:199], a[136:139], a[8:11], v[196:199]\n"
"s_waitcnt lgkmcnt(0)\n"
"v_mfma_f32_16x16x32_bf16 v[196:199], a[140:143], a[12:15], v[196:199]\n"
"s_nop 7\n"
"v_mfma_f32_16x16x32_bf16 v[200:203], a[112:115], a[16:19], v[220:223]\n"
"v_exp_f32 v192, v192\n"
"v_mfma_f32_16x16x32_bf16 v[200:203], a[116:119], a[20:23], v[200:203]\n"
"v_exp_f32 v193, v193\n"
"v_mfma_f32_16x16x32_bf16 v[200:203], a[120:123], a[24:27], v[200:203]\n"
"v_exp_f32 v194, v194\n"
"v_mfma_f32_16x16x32_bf16 v[200:203], a[124:127], a[28:31], v[200:203]\n"
"v_exp_f32 v195, v195\n"
"v_mfma_f32_16x16x32_bf16 v[204:207], a[128:131], a[16:19], v[220:223]\n"
"v_exp_f32 v196, v196\n"
"v_mfma_f32_16x16x32_bf16 v[204:207], a[132:135], a[20:23], v[204:207]\n"
"v_exp_f32 v197, v197\n"
"v_mfma_f32_16x16x32_bf16 v[204:207], a[136:139], a[24:27], v[204:207]\n"
"v_exp_f32 v198, v198\n"
"v_mfma_f32_16x16x32_bf16 v[204:207], a[140:143], a[28:31], v[204:207]\n"
"v_exp_f32 v199, v199\n"
"ds_read_b64_tr_b16 a[144:145], v246\n"
"ds_read_b64_tr_b16 a[146:147], v246 offset:4224\n"
"ds_read_b64_tr_b16 a[148:149], v246 offset:32\n"
"v_cvt_pk_bf16_f32 v208, v192, v193\n"
"ds_read_b64_tr_b16 a[150:151], v246 offset:4256\n"
"ds_read_b64_tr_b16 a[152:153], v246 offset:64\n"
"ds_read_b64_tr_b16 a[154:155], v246 offset:4288\n"
"v_cvt_pk_bf16_f32 v209, v194, v195\n"
"ds_read_b64_tr_b16 a[156:157], v246 offset:96\n"
"ds_read_b64_tr_b16 a[158:159], v246 offset:4320\n"
"ds_read_b64_tr_b16 a[160:161], v248\n"
"v_cvt_pk_bf16_f32 v210, v196, v197\n"
"ds_read_b64_tr_b16 a[162:163], v248 offset:4224\n"
"ds_read_b64_tr_b16 a[164:165], v248 offset:32\n"
"ds_read_b64_tr_b16 a[166:167], v248 offset:4256\n"
"v_cvt_pk_bf16_f32 v211, v198, v199\n"
"ds_read_b64_tr_b16 a[168:169], v248 offset:64\n"
"ds_read_b64_tr_b16 a[170:171], v248 offset:4288\n"
"v_mfma_f32_16x16x32_bf16 v[192:195], a[112:115], a[32:35], v[224:227]\n"
"ds_read_b64_tr_b16 a[172:173], v248 offset:96\n"
"ds_read_b64_tr_b16 a[174:175], v248 offset:4320\n"
"v_mfma_f32_16x16x32_bf16 v[192:195], a[116:119], a[36:39], v[192:195]\n"
"v_exp_f32 v200, v200\n"
"v_mfma_f32_16x16x32_bf16 v[192:195], a[120:123], a[40:43], v[192:195]\n"
"s_memrealtime s[28:29]\n"
"v_exp_f32 v201, v201\n"
"v_mfma_f32_16x16x32_bf16 v[192:195], a[124:127], a[44:47], v[192:195]\n"
"v_exp_f32 v202, v202\n"
"v_mfma_f32_16x16x32_bf16 v[196:199], a[128:131], a[32:35], v[224:227]\n"
"v_exp_f32 v203, v203\n"
"v_mfma_f32_16x16x32_bf16 v[196:199], a[132:135], a[36:39], v[196:199]\n"
"v_exp_f32 v204, v204\n"
"v_mfma_f32_16x16x32_bf16 v[196:199], a[136:139], a[40:43], v[196:199]\n"
"v_exp_f32 v205, v205\n"
"v_mfma_f32_16x16x32_bf16 v[196:199], a[140:143], a[44:47], v[196:199]\n"
"s_waitcnt lgkmcnt(14)\n"
"v_exp_f32 v206, v206\n"
"v_mfma_f32_16x16x32_bf16 v[0:3], a[144:147], v[208:211], 0\n"
"s_waitcnt lgkmcnt(12)\n"
"v_exp_f32 v207, v207\n"
"v_mfma_f32_16x16x32_bf16 v[4:7], a[148:151], v[208:211], 0\n"
"s_waitcnt lgkmcnt(10)\n"
"v_cvt_pk_bf16_f32 v212, v200, v201\n"
"v_cvt_pk_bf16_f32 v213, v202, v203\n"
"v_mfma_f32_16x16x32_bf16 v[8:11], a[152:155], v[208:211], 0\n"
"s_waitcnt lgkmcnt(8)\n"
"v_cvt_pk_bf16_f32 v214, v204, v205\n"
"v_cvt_pk_bf16_f32 v215, v206, v207\n"
"v_mfma_f32_16x16x32_bf16 v[12:15], a[156:159], v[208:211], 0\n"
"s_waitcnt lgkmcnt(6)\n"
"v_mfma_f32_16x16x32_bf16 v[16:19], a[160:163], v[208:211], 0\n"
"s_waitcnt lgkmcnt(4)\n"
"v_mfma_f32_16x16x32_bf16 v[20:23], a[164:167], v[208:211], 0\n"
"s_waitcnt lgkmcnt(2)\n"
"v_mfma_f32_16x16x32_bf16 v[24:27], a[168:171], v[208:211], 0\n"
"s_waitcnt lgkmcnt(0)\n"
"v_mfma_f32_16x16x32_bf16 v[28:31], a[172:175], v[208:211], 0\n"
"v_mfma_f32_16x16x32_bf16 a[208:211], a[236:239], v[208:211], 0\n"
"v_mfma_f32_16x16x32_bf16 v[200:203], a[112:115], a[48:51], v[228:231]\n"
"v_exp_f32 v192, v192\n"
"v_mfma_f32_16x16x32_bf16 v[200:203], a[116:119], a[52:55], v[200:203]\n"
"v_exp_f32 v193, v193\n"
"v_mfma_f32_16x16x32_bf16 v[200:203], a[120:123], a[56:59], v[200:203]\n"
"v_exp_f32 v194, v194\n"
"v_mfma_f32_16x16x32_bf16 v[200:203], a[124:127], a[60:63], v[200:203]\n"
"v_exp_f32 v195, v195\n"
"v_mfma_f32_16x16x32_bf16 v[204:207], a[128:131], a[48:51], v[228:231]\n"
"v_exp_f32 v196, v196\n"
"v_mfma_f32_16x16x32_bf16 v[204:207], a[132:135], a[52:55], v[204:207]\n"
"v_exp_f32 v197, v197\n"
"v_mfma_f32_16x16x32_bf16 v[204:207], a[136:139], a[56:59], v[204:207]\n"
"v_exp_f32 v198, v198\n"
"v_mfma_f32_16x16x32_bf16 v[204:207], a[140:143], a[60:63], v[204:207]\n"
"v_exp_f32 v199, v199\n"
"v_mfma_f32_16x16x32_bf16 v[32:35], a[144:147], v[212:215], 0\n"
"v_mfma_f32_16x16x32_bf16 v[36:39], a[148:151], v[212:215], 0\n"
"v_cvt_pk_bf16_f32 v208, v192, v193\n"
"v_cvt_pk_bf16_f32 v209, v194, v195\n"
"v_mfma_f32_16x16x32_bf16 v[40:43], a[152:155], v[212:215], 0\n"
"v_cvt_pk_bf16_f32 v210, v196, v197\n"
"v_cvt_pk_bf16_f32 v211, v198, v199\n"
"v_mfma_f32_16x16x32_bf16 v[44:47], a[156:159], v[212:215], 0\n"
"s_lshl_b32 s33, s28, 3\n"
"v_mfma_f32_16x16x32_bf16 v[48:51], a[160:163], v[212:215], 0\n"
"s_mov_b32 s32, s35\n"
"v_mfma_f32_16x16x32_bf16 v[52:55], a[164:167], v[212:215], 0\n"
"s_max_i32 s30, s32, -400\n"
"v_mfma_f32_16x16x32_bf16 v[56:59], a[168:171], v[212:215], 0\n"
"s_add_i32 s30, s30, s33\n"
"v_mfma_f32_16x16x32_bf16 v[60:63], a[172:175], v[212:215], 0\n"
"s_add_i32 s30, s30, s66\n"
"v_mfma_f32_16x16x32_bf16 a[212:215], a[236:239], v[212:215], 0\n"
"v_mfma_f32_16x16x32_bf16 v[192:195], a[112:115], a[64:67], v[232:235]\n"
"v_exp_f32 v200, v200\n"
"v_mfma_f32_16x16x32_bf16 v[192:195], a[116:119], a[68:71], v[192:195]\n"
"v_exp_f32 v201, v201\n"
"v_mfma_f32_16x16x32_bf16 v[192:195], a[120:123], a[72:75], v[192:195]\n"
"v_exp_f32 v202, v202\n"
"v_mfma_f32_16x16x32_bf16 v[192:195], a[124:127], a[76:79], v[192:195]\n"
"v_exp_f32 v203, v203\n"
"v_mfma_f32_16x16x32_bf16 v[196:199], a[128:131], a[64:67], v[232:235]\n"
"v_exp_f32 v204, v204\n"
"v_mfma_f32_16x16x32_bf16 v[196:199], a[132:135], a[68:71], v[196:199]\n"
"v_exp_f32 v205, v205\n"
"v_mfma_f32_16x16x32_bf16 v[196:199], a[136:139], a[72:75], v[196:199]\n"
"v_exp_f32 v206, v206\n"
"v_mfma_f32_16x16x32_bf16 v[196:199], a[140:143], a[76:79], v[196:199]\n"
"v_exp_f32 v207, v207\n"
"v_mfma_f32_16x16x32_bf16 v[64:67], a[144:147], v[208:211], 0\n"
"v_mfma_f32_16x16x32_bf16 v[68:71], a[148:151], v[208:211], 0\n"
"v_cvt_pk_bf16_f32 v212, v200, v201\n"
"v_cvt_pk_bf16_f32 v213, v202, v203\n"
"v_mfma_f32_16x16x32_bf16 v[72:75], a[152:155], v[208:211], 0\n"
"v_cvt_pk_bf16_f32 v214, v204, v205\n"
"v_cvt_pk_bf16_f32 v215, v206, v207\n"
"v_mfma_f32_16x16x32_bf16 v[76:79], a[156:159], v[208:211], 0\n"
"s_sub_i32 s66, s66, 15\n"
"v_mfma_f32_16x16x32_bf16 v[80:83], a[160:163], v[208:211], 0\n"
"s_max_i32 s32, s32, 0\n"
"v_mfma_f32_16x16x32_bf16 v[84:87], a[164:167], v[208:211], 0\n"
"s_mul_i32 s32, s32, 13\n"
"v_mfma_f32_16x16x32_bf16 v[88:91], a[168:171], v[208:211], 0\n"
"s_lshr_b32 s32, s32, 8\n"
"v_mfma_f32_16x16x32_bf16 v[92:95], a[172:175], v[208:211], 0\n"
"s_min_u32 s31, s32, 24\n"
"v_mfma_f32_16x16x32_bf16 a[216:219], a[236:239], v[208:211], 0\n"
"1:\n"
"s_sub_u32 s31, s31, 1\n"
"s_cbranch_scc1 2f\n"
"s_nop 7\n"
"s_branch 1b\n"
"2:\n"
"s_barrier\n"
"v_mfma_f32_16x16x32_bf16 v[200:203], a[112:115], a[80:83], v[236:239]\n"
"v_exp_f32 v192, v192\n"
"v_mfma_f32_16x16x32_bf16 v[200:203], a[116:119], a[84:87], v[200:203]\n"
"ds_read_b128 a[112:115], v244 offset:34304\n"
"ds_read_b128 a[116:119], v244 offset:34368\n"
"v_mfma_f32_16x16x32_bf16 v[200:203], a[120:123], a[88:91], v[200:203]\n"
"v_exp_f32 v193, v193\n"
"v_mfma_f32_16x16x32_bf16 v[200:203], a[124:127], a[92:95], v[200:203]\n"
"ds_read_b128 a[120:123], v244 offset:34432\n"
"ds_read_b128 a[124:127], v244 offset:34496\n"
"v_mfma_f32_16x16x32_bf16 v[204:207], a[128:131], a[80:83], v[236:239]\n"
"v_exp_f32 v194, v194\n"
"v_mfma_f32_16x16x32_bf16 v[204:207], a[132:135], a[84:87], v[204:207]\n"
"ds_read_b128 a[128:131], v244 offset:38656\n"
"ds_read_b128 a[132:135], v244 offset:38720\n"
"v_mfma_f32_16x16x32_bf16 v[204:207], a[136:139], a[88:91], v[204:207]\n"
"v_exp_f32 v195, v195\n"
"v_mfma_f32_16x16x32_bf16 v[204:207], a[140:143], a[92:95], v[204:207]\n"
"ds_read_b128 a[136:139], v244 offset:38784\n"
"ds_read_b128 a[140:143], v244 offset:38848\n"
"v_mfma_f32_16x16x32_bf16 v[96:99], a[144:147], v[212:215], 0\n"
"v_exp_f32 v196, v196\n"
"v_mfma_f32_16x16x32_bf16 v[100:103], a[148:151], v[212:215], 0\n"
"v_exp_f32 v197, v197\n"
"v_mfma_f32_16x16x32_bf16 v[104:107], a[152:155], v[212:215], 0\n"
"v_exp_f32 v198, v198\n"
"v_mfma_f32_16x16x32_bf16 v[108:111], a[156:159], v[212:215], 0\n"
"v_exp_f32 v199, v199\n"
"v_mfma_f32_16x16x32_bf16 v[112:115], a[160:163], v[212:215], 0\n"
"v_cvt_pk_bf16_f32 v208, v192, v193\n"
"v_cvt_pk_bf16_f32 v209, v194, v195\n"
"v_mfma_f32_16x16x32_bf16 v[116:119], a[164:167], v[212:215], 0\n"
"v_cvt_pk_bf16_f32 v210, v196, v197\n"
"v_cvt_pk_bf16_f32 v211, v198, v199\n"
"v_mfma_f32_16x16x32_bf16 v[120:123], a[168:171], v[212:215], 0\n"
"v_mfma_f32_16x16x32_bf16 v[124:127], a[172:175], v[212:215], 0\n"
"v_mfma_f32_16x16x32_bf16 a[220:223], a[236:239], v[212:215], 0\n"
"s_waitcnt lgkmcnt(7)\n"
"v_mfma_f32_16x16x32_bf16 v[192:195], a[112:115], a[0:3], v[216:219]\n"
"s_waitcnt lgkmcnt(6)\n"
"v_exp_f32 v200, v200\n"
"v_mfma_f32_16x16x32_bf16 v[192:195], a[116:119], a[4:7], v[192:195]\n"
"s_waitcnt lgkmcnt(5)\n"
"v_exp_f32 v201, v201\n"
"v_mfma_f32_16x16x32_bf16 v[192:195], a[120:123], a[8:11], v[192:195]\n"
"s_waitcnt lgkmcnt(4)\n"
"v_exp_f32 v202, v202\n"
"v_mfma_f32_16x16x32_bf16 v[192:195], a[124:127], a[12:15], v[192:195]\n"
"s_waitcnt lgkmcnt(3)\n"
"v_exp_f32 v203, v203\n"
"v_mfma_f32_16x16x32_bf16 v[196:199], a[128:131], a[0:3], v[216:219]\n"
"s_waitcnt lgkmcnt(2)\n"
"v_exp_f32 v204, v204\n"
"v_mfma_f32_16x16x32_bf16 v[196:199], a[132:135], a[4:7], v[196:199]\n"
"s_waitcnt lgkmcnt(1)\n"
"v_exp_f32 v205, v205\n"
"v_mfma_f32_16x16x32_bf16 v[196:199], a[136:139], a[8:11], v[196:199]\n"
"s_waitcnt lgkmcnt(0)\n"
"v_exp_f32 v206, v206\n"
"v_mfma_f32_16x16x32_bf16 v[196:199], a[140:143], a[12:15], v[196:199]\n"
"v_exp_f32 v207, v207\n"
"v_mfma_f32_16x16x32_bf16 v[128:131], a[144:147], v[208:211], 0\n"
"v_mfma_f32_16x16x32_bf16 v[132:135], a[148:151], v[208:211], 0\n"
"s_add_u32 m0, s50, 8704\n"
"v_cvt_pk_bf16_f32 v212, v200, v201\n"
"v_mfma_f32_16x16x32_bf16 v[136:139], a[152:155], v[208:211], 0\n"
"buffer_load_dwordx4 v250, s[36:39], s53 offen nt lds\n"
"v_mfma_f32_16x16x32_bf16 v[140:143], a[156:159], v[208:211], 0\n"
"s_add_u32 m0, s50, 9504\n"
"v_cvt_pk_bf16_f32 v213, v202, v203\n"
"v_mfma_f32_16x16x32_bf16 v[144:147], a[160:163], v[208:211], 0\n"
"buffer_load_dwordx4 v250, s[36:39], s53 offen offset:256 nt lds\n"
"v_mfma_f32_16x16x32_bf16 v[148:151], a[164:167], v[208:211], 0\n"
"s_add_u32 m0, s50, 10304\n"
"v_cvt_pk_bf16_f32 v214, v204, v205\n"
"v_mfma_f32_16x16x32_bf16 v[152:155], a[168:171], v[208:211], 0\n"
"buffer_load_dwordx4 v250, s[36:39], s53 offen offset:512 nt lds\n"
"v_mfma_f32_16x16x32_bf16 v[156:159], a[172:175], v[208:211], 0\n"
"s_add_u32 m0, s50, 11104\n"
"v_cvt_pk_bf16_f32 v215, v206, v207\n"
"v_mfma_f32_16x16x32_bf16 a[224:227], a[236:239], v[208:211], 0\n"
"buffer_load_dwordx4 v250, s[36:39], s53 offen offset:768 nt lds\n"
"v_mfma_f32_16x16x32_bf16 v[200:203], a[112:115], a[16:19], v[220:223]\n"
"v_exp_f32 v192, v192\n"
"v_mfma_f32_16x16x32_bf16 v[200:203], a[116:119], a[20:23], v[200:203]\n"
"v_exp_f32 v193, v193\n"
"v_mfma_f32_16x16x32_bf16 v[200:203], a[120:123], a[24:27], v[200:203]\n"
"v_exp_f32 v194, v194\n"
"v_mfma_f32_16x16x32_bf16 v[200:203], a[124:127], a[28:31], v[200:203]\n"
"v_exp_f32 v195, v195\n"
"v_mfma_f32_16x16x32_bf16 v[204:207], a[128:131], a[16:19], v[220:223]\n"
"v_exp_f32 v196, v196\n"
"v_mfma_f32_16x16x32_bf16 v[204:207], a[132:135], a[20:23], v[204:207]\n"
"v_exp_f32 v197, v197\n"
"v_mfma_f32_16x16x32_bf16 v[204:207], a[136:139], a[24:27], v[204:207]\n"
"v_exp_f32 v198, v198\n"
"v_mfma_f32_16x16x32_bf16 v[204:207], a[140:143], a[28:31], v[204:207]\n"
"v_exp_f32 v199, v199\n"
"v_mfma_f32_16x16x32_bf16 v[160:163], a[144:147], v[212:215], 0\n"
"ds_read_b64_tr_b16 a[144:145], v246 offset:34304\n"
"ds_read_b64_tr_b16 a[146:147], v246 offset:38528\n"
"v_mfma_f32_16x16x32_bf16 v[164:167], a[148:151], v[212:215], 0\n"
"ds_read_b64_tr_b16 a[148:149], v246 offset:34336\n"
"v_cvt_pk_bf16_f32 v208, v192, v193\n"
"v_mfma_f32_16x16x32_bf16 v[168:171], a[152:155], v[212:215], 0\n"
"ds_read_b64_tr_b16 a[150:151], v246 offset:38560\n"
"ds_read_b64_tr_b16 a[152:153], v246 offset:34368\n"
"v_mfma_f32_16x16x32_bf16 v[172:175], a[156:159], v[212:215], 0\n"
"ds_read_b64_tr_b16 a[154:155], v246 offset:38592\n"
"v_cvt_pk_bf16_f32 v209, v194, v195\n"
"v_mfma_f32_16x16x32_bf16 v[176:179], a[160:163], v[212:215], 0\n"
"ds_read_b64_tr_b16 a[156:157], v246 offset:34400\n"
"ds_read_b64_tr_b16 a[158:159], v246 offset:38624\n"
"v_mfma_f32_16x16x32_bf16 v[180:183], a[164:167], v[212:215], 0\n"
"ds_read_b64_tr_b16 a[160:161], v248 offset:34304\n"
"v_cvt_pk_bf16_f32 v210, v196, v197\n"
"v_mfma_f32_16x16x32_bf16 v[184:187], a[168:171], v[212:215], 0\n"
"ds_read_b64_tr_b16 a[162:163], v248 offset:38528\n"
"ds_read_b64_tr_b16 a[164:165], v248 offset:34336\n"
"v_mfma_f32_16x16x32_bf16 v[188:191], a[172:175], v[212:215], 0\n"
"ds_read_b64_tr_b16 a[166:167], v248 offset:38560\n"
"v_cvt_pk_bf16_f32 v211, v198, v199\n"
"v_mfma_f32_16x16x32_bf16 a[228:231], a[236:239], v[212:215], 0\n"
"ds_read_b64_tr_b16 a[168:169], v248 offset:34368\n"
"ds_read_b64_tr_b16 a[170:171], v248 offset:38592\n"
"v_mfma_f32_16x16x32_bf16 v[192:195], a[112:115], a[32:35], v[224:227]\n"
"ds_read_b64_tr_b16 a[172:173], v248 offset:34400\n"
"ds_read_b64_tr_b16 a[174:175], v248 offset:38624\n"
"v_mfma_f32_16x16x32_bf16 v[192:195], a[116:119], a[36:39], v[192:195]\n"
"v_exp_f32 v200, v200\n"
"v_mfma_f32_16x16x32_bf16 v[192:195], a[120:123], a[40:43], v[192:195]\n"
"v_exp_f32 v201, v201\n"
"v_mfma_f32_16x16x32_bf16 v[192:195], a[124:127], a[44:47], v[192:195]\n"
"v_exp_f32 v202, v202\n"
"v_mfma_f32_16x16x32_bf16 v[196:199], a[128:131], a[32:35], v[224:227]\n"
"v_exp_f32 v203, v203\n"
"v_mfma_f32_16x16x32_bf16 v[196:199], a[132:135], a[36:39], v[196:199]\n"
"v_exp_f32 v204, v204\n"
"v_mfma_f32_16x16x32_bf16 v[196:199], a[136:139], a[40:43], v[196:199]\n"
"v_exp_f32 v205, v205\n"
"v_mfma_f32_16x16x32_bf16 v[196:199], a[140:143], a[44:47], v[196:199]\n"
"s_waitcnt lgkmcnt(14)\n"
"v_exp_f32 v206, v206\n"
"v_mfma_f32_16x16x32_bf16 v[0:3], a[144:147], v[208:211], v[0:3]\n"
"s_waitcnt lgkmcnt(12)\n"
"v_exp_f32 v207, v207\n"
"v_mfma_f32_16x16x32_bf16 v[4:7], a[148:151], v[208:211], v[4:7]\n"
"s_waitcnt lgkmcnt(10)\n"
"s_add_u32 m0, s50, 12928\n"
"v_cvt_pk_bf16_f32 v212, v200, v201\n"
"v_mfma_f32_16x16x32_bf16 v[8:11], a[152:155], v[208:211], v[8:11]\n"
"s_waitcnt lgkmcnt(8)\n"
"buffer_load_dwordx4 v250, s[36:39], s54 offen nt lds\n"
"s_add_u32 s53, s53, 8192\n"
"v_mfma_f32_16x16x32_bf16 v[12:15], a[156:159], v[208:211], v[12:15]\n"
"s_waitcnt lgkmcnt(6)\n"
"s_add_u32 m0, s50, 13728\n"
"v_cvt_pk_bf16_f32 v213, v202, v203\n"
"v_mfma_f32_16x16x32_bf16 v[16:19], a[160:163], v[208:211], v[16:19]\n"
"s_waitcnt lgkmcnt(4)\n"
"buffer_load_dwordx4 v250, s[36:39], s54 offen offset:256 nt lds\n"
"s_and_b32 s53, s53, 258047\n"
"v_mfma_f32_16x16x32_bf16 v[20:23], a[164:167], v[208:211], v[20:23]\n"
"s_waitcnt lgkmcnt(2)\n"
"s_add_u32 m0, s50, 14528\n"
"v_cvt_pk_bf16_f32 v214, v204, v205\n"
"v_mfma_f32_16x16x32_bf16 v[24:27], a[168:171], v[208:211], v[24:27]\n"
"s_waitcnt lgkmcnt(0)\n"
"buffer_load_dwordx4 v250, s[36:39], s54 offen offset:512 nt lds\n"
"v_mfma_f32_16x16x32_bf16 v[28:31], a[172:175], v[208:211], v[28:31]\n"
"s_add_u32 m0, s50, 15328\n"
"v_cvt_pk_bf16_f32 v215, v206, v207\n"
"v_mfma_f32_16x16x32_bf16 a[208:211], a[236:239], v[208:211], a[208:211]\n"
"buffer_load_dwordx4 v250, s[36:39], s54 offen offset:768 nt lds\n"
"s_lshl_b32 s38, s38, 1\n"
"s_add_u32 s54, s53, 4096\n"
"v_mfma_f32_16x16x32_bf16 v[200:203], a[112:115], a[48:51], v[228:231]\n"
"v_exp_f32 v192, v192\n"
"v_mfma_f32_16x16x32_bf16 v[200:203], a[116:119], a[52:55], v[200:203]\n"
"v_exp_f32 v193, v193\n"
"v_mfma_f32_16x16x32_bf16 v[200:203], a[120:123], a[56:59], v[200:203]\n"
"s_memrealtime s[28:29]\n"
"v_exp_f32 v194, v194\n"
"v_mfma_f32_16x16x32_bf16 v[200:203], a[124:127], a[60:63], v[200:203]\n"
"v_exp_f32 v195, v195\n"
"v_mfma_f32_16x16x32_bf16 v[204:207], a[128:131], a[48:51], v[228:231]\n"
"v_exp_f32 v196, v196\n"
"v_mfma_f32_16x16x32_bf16 v[204:207], a[132:135], a[52:55], v[204:207]\n"
"v_exp_f32 v197, v197\n"
"v_mfma_f32_16x16x32_bf16 v[204:207], a[136:139], a[56:59], v[204:207]\n"
"v_exp_f32 v198, v198\n"
"v_mfma_f32_16x16x32_bf16 v[204:207], a[140:143], a[60:63], v[204:207]\n"
"v_exp_f32 v199, v199\n"
"v_mfma_f32_16x16x32_bf16 v[32:35], a[144:147], v[212:215], v[32:35]\n"
"v_mfma_f32_16x16x32_bf16 v[36:39], a[148:151], v[212:215], v[36:39]\n"
"v_cvt_pk_bf16_f32 v208, v192, v193\n"
"v_cvt_pk_bf16_f32 v209, v194, v195\n"
"v_mfma_f32_16x16x32_bf16 v[40:43], a[152:155], v[212:215], v[40:43]\n"
"v_cvt_pk_bf16_f32 v210, v196, v197\n"
"v_cvt_pk_bf16_f32 v211, v198, v199\n"
"v_mfma_f32_16x16x32_bf16 v[44:47], a[156:159], v[212:215], v[44:47]\n"
"v_mfma_f32_16x16x32_bf16 v[48:51], a[160:163], v[212:215], v[48:51]\n"
"v_mfma_f32_16x16x32_bf16 v[52:55], a[164:167], v[212:215], v[52:55]\n"
"v_mfma_f32_16x16x32_bf16 v[56:59], a[168:171], v[212:215], v[56:59]\n"
"v_mfma_f32_16x16x32_bf16 v[60:63], a[172:175], v[212:215], v[60:63]\n"
"v_mfma_f32_16x16x32_bf16 a[212:215], a[236:239], v[212:215], a[212:215]\n"
"v_mfma_f32_16x16x32_bf16 v[192:195], a[112:115], a[64:67], v[232:235]\n"
"v_exp_f32 v200, v200\n"
"v_mfma_f32_16x16x32_bf16 v[192:195], a[116:119], a[68:71], v[192:195]\n"
"v_exp_f32 v201, v201\n"
"v_mfma_f32_16x16x32_bf16 v[192:195], a[120:123], a[72:75], v[192:195]\n"
"v_exp_f32 v202, v202\n"
"v_mfma_f32_16x16x32_bf16 v[192:195], a[124:127], a[76:79], v[192:195]\n"
"v_exp_f32 v203, v203\n"
"v_mfma_f32_16x16x32_bf16 v[196:199], a[128:131], a[64:67], v[232:235]\n"
"v_exp_f32 v204, v204\n"
"v_mfma_f32_16x16x32_bf16 v[196:199], a[132:135], a[68:71], v[196:199]\n"
"v_exp_f32 v205, v205\n"
"v_mfma_f32_16x16x32_bf16 v[196:199], a[136:139], a[72:75], v[196:199]\n"
"v_exp_f32 v206, v206\n"
"v_mfma_f32_16x16x32_bf16 v[196:199], a[140:143], a[76:79], v[196:199]\n"
"v_exp_f32 v207, v207\n"
"v_mfma_f32_16x16x32_bf16 v[64:67], a[144:147], v[208:211], v[64:67]\n"
"v_mfma_f32_16x16x32_bf16 v[68:71], a[148:151], v[208:211], v[68:71]\n"
"v_cvt_pk_bf16_f32 v212, v200, v201\n"
"v_cvt_pk_bf16_f32 v213, v202, v203\n"
"v_mfma_f32_16x16x32_bf16 v[72:75], a[152:155], v[208:211], v[72:75]\n"
"v_cvt_pk_bf16_f32 v214, v204, v205\n"
"v_cvt_pk_bf16_f32 v215, v206, v207\n"
"v_mfma_f32_16x16x32_bf16 v[76:79], a[156:159], v[208:211], v[76:79]\n"
"s_lshl_b32 s33, s28, 3\n"
"v_mfma_f32_16x16x32_bf16 v[80:83], a[160:163], v[208:211], v[80:83]\n"
"s_sub_i32 s32, s30, s33\n"
"v_mfma_f32_16x16x32_bf16 v[84:87], a[164:167], v[208:211], v[84:87]\n"
"s_max_i32 s30, s32, -400\n"
"v_mfma_f32_16x16x32_bf16 v[88:91], a[168:171], v[208:211], v[88:91]\n"
"s_add_i32 s30, s30, s33\n"
"v_mfma_f32_16x16x32_bf16 v[92:95], a[172:175], v[208:211], v[92:95]\n"
"s_add_i32 s30, s30, s66\n"
"v_mfma_f32_16x16x32_bf16 a[216:219], a[236:239], v[208:211], a[216:219]\n"
"v_mfma_f32_16x16x32_bf16 v[200:203], a[112:115], a[80:83], v[236:239]\n"
"v_exp_f32 v192, v192\n"
"v_mfma_f32_16x16x32_bf16 v[200:203], a[116:119], a[84:87], v[200:203]\n"
"v_exp_f32 v193, v193\n"
"v_mfma_f32_16x16x32_bf16 v[200:203], a[120:123], a[88:91], v[200:203]\n"
"v_exp_f32 v194, v194\n"
"v_mfma_f32_16x16x32_bf16 v[200:203], a[124:127], a[92:95], v[200:203]\n"
"v_exp_f32 v195, v195\n"
"v_mfma_f32_16x16x32_bf16 v[204:207], a[128:131], a[80:83], v[236:239]\n"
"v_exp_f32 v196, v196\n"
"v_mfma_f32_16x16x32_bf16 v[204:207], a[132:135], a[84:87], v[204:207]\n"
"v_exp_f32 v197, v197\n"
"v_mfma_f32_16x16x32_bf16 v[204:207], a[136:139], a[88:91], v[204:207]\n"
"v_exp_f32 v198, v198\n"
"v_mfma_f32_16x16x32_bf16 v[204:207], a[140:143], a[92:95], v[204:207]\n"
"v_exp_f32 v199, v199\n"
"v_mfma_f32_16x16x32_bf16 v[96:99], a[144:147], v[212:215], v[96:99]\n"
"v_mfma_f32_16x16x32_bf16 v[100:103], a[148:151], v[212:215], v[100:103]\n"
"v_cvt_pk_bf16_f32 v208, v192, v193\n"
"v_cvt_pk_bf16_f32 v209, v194, v195\n"
"v_mfma_f32_16x16x32_bf16 v[104:107], a[152:155], v[212:215], v[104:107]\n"
"v_cvt_pk_bf16_f32 v210, v196, v197\n"
"v_cvt_pk_bf16_f32 v211, v198, v199\n"
"v_mfma_f32_16x16x32_bf16 v[108:111], a[156:159], v[212:215], v[108:111]\n"
"s_sub_i32 s66, s66, 15\n"
"v_mfma_f32_16x16x32_bf16 v[112:115], a[160:163], v[212:215], v[112:115]\n"
"s_max_i32 s32, s32, 0\n"
"v_mfma_f32_16x16x32_bf16 v[116:119], a[164:167], v[212:215], v[116:119]\n"
"s_mul_i32 s32, s32, 13\n"
"v_mfma_f32_16x16x32_bf16 v[120:123], a[168:171], v[212:215], v[120:123]\n"
"s_lshr_b32 s32, s32, 8\n"
"v_mfma_f32_16x16x32_bf16 v[124:127], a[172:175], v[212:215], v[124:127]\n"
"s_min_u32 s31, s32, 24\n"
"v_mfma_f32_16x16x32_bf16 a[220:223], a[236:239], v[212:215], a[220:223]\n"
"1:\n"
"s_sub_u32 s31, s31, 1\n"
"s_cbranch_scc1 2f\n"
"s_nop 7\n"
"s_branch 1b\n"
"2:\n"
"s_barrier\n"
"v_mfma_f32_16x16x32_bf16 v[192:195], a[112:115], a[96:99], v[240:243]\n"
"v_exp_f32 v200, v200\n"
"v_mfma_f32_16x16x32_bf16 v[192:195], a[116:119], a[100:103], v[192:195]\n"
"ds_read_b128 a[112:115], v245\n"
"ds_read_b128 a[116:119], v245 offset:64\n"
"v_mfma_f32_16x16x32_bf16 v[192:195], a[120:123], a[104:107], v[192:195]\n"
"v_exp_f32 v201, v201\n"
"v_mfma_f32_16x16x32_bf16 v[192:195], a[124:127], a[108:111], v[192:195]\n"
"ds_read_b128 a[120:123], v245 offset:128\n"
"ds_read_b128 a[124:127], v245 offset:192\n"
"v_mfma_f32_16x16x32_bf16 v[196:199], a[128:131], a[96:99], v[240:243]\n"
"v_exp_f32 v202, v202\n"
"v_mfma_f32_16x16x32_bf16 v[196:199], a[132:135], a[100:103], v[196:199]\n"
"ds_read_b128 a[128:131], v245 offset:4352\n"
"ds_read_b128 a[132:135], v245 offset:4416\n"
"v_mfma_f32_16x16x32_bf16 v[196:199], a[136:139], a[104:107], v[196:199]\n"
"v_exp_f32 v203, v203\n"
"v_mfma_f32_16x16x32_bf16 v[196:199], a[140:143], a[108:111], v[196:199]\n"
"ds_read_b128 a[136:139], v245 offset:4480\n"
"ds_read_b128 a[140:143], v245 offset:4544\n"
"v_mfma_f32_16x16x32_bf16 v[128:131], a[144:147], v[208:211], v[128:131]\n"
"v_exp_f32 v204, v204\n"
"v_mfma_f32_16x16x32_bf16 v[132:135], a[148:151], v[208:211], v[132:135]\n"
"v_exp_f32 v205, v205\n"
"v_mfma_f32_16x16x32_bf16 v[136:139], a[152:155], v[208:211], v[136:139]\n"
"v_exp_f32 v206, v206\n"
"v_mfma_f32_16x16x32_bf16 v[140:143], a[156:159], v[208:211], v[140:143]\n"
"v_exp_f32 v207, v207\n"
"v_mfma_f32_16x16x32_bf16 v[144:147], a[160:163], v[208:211], v[144:147]\n"
"v_cvt_pk_bf16_f32 v212, v200, v201\n"
"v_cvt_pk_bf16_f32 v213, v202, v203\n"
"v_mfma_f32_16x16x32_bf16 v[148:151], a[164:167], v[208:211], v[148:151]\n"
"v_cvt_pk_bf16_f32 v214, v204, v205\n"
"v_cvt_pk_bf16_f32 v215, v206, v207\n"
"v_mfma_f32_16x16x32_bf16 v[152:155], a[168:171], v[208:211], v[152:155]\n"
"v_mfma_f32_16x16x32_bf16 v[156:159], a[172:175], v[208:211], v[156:159]\n"
"v_mfma_f32_16x16x32_bf16 a[224:227], a[236:239], v[208:211], a[224:227]\n"
"s_waitcnt lgkmcnt(7)\n"
"v_mfma_f32_16x16x32_bf16 v[200:203], a[112:115], a[0:3], v[216:219]\n"
"s_waitcnt lgkmcnt(6)\n"
"v_exp_f32 v192, v192\n"
"v_mfma_f32_16x16x32_bf16 v[200:203], a[116:119], a[4:7], v[200:203]\n"
"s_waitcnt lgkmcnt(5)\n"
"v_exp_f32 v193, v193\n"
"v_mfma_f32_16x16x32_bf16 v[200:203], a[120:123], a[8:11], v[200:203]\n"
"s_waitcnt lgkmcnt(4)\n"
"v_exp_f32 v194, v194\n"
"v_mfma_f32_16x16x32_bf16 v[200:203], a[124:127], a[12:15], v[200:203]\n"
"s_waitcnt lgkmcnt(3)\n"
"v_exp_f32 v195, v195\n"
"v_mfma_f32_16x16x32_bf16 v[204:207], a[128:131], a[0:3], v[216:219]\n"
"s_waitcnt lgkmcnt(2)\n"
"v_exp_f32 v196, v196\n"
"v_mfma_f32_16x16x32_bf16 v[204:207], a[132:135], a[4:7], v[204:207]\n"
"s_waitcnt lgkmcnt(1)\n"
"v_exp_f32 v197, v197\n"
"v_mfma_f32_16x16x32_bf16 v[204:207], a[136:139], a[8:11], v[204:207]\n"
"s_waitcnt lgkmcnt(0)\n"
"v_exp_f32 v198, v198\n"
"v_mfma_f32_16x16x32_bf16 v[204:207], a[140:143], a[12:15], v[204:207]\n"
"v_exp_f32 v199, v199\n"
"v_mfma_f32_16x16x32_bf16 v[160:163], a[144:147], v[212:215], v[160:163]\n"
"v_mfma_f32_16x16x32_bf16 v[164:167], a[148:151], v[212:215], v[164:167]\n"
"s_add_u32 m0, s50, 43008\n"
"v_cvt_pk_bf16_f32 v208, v192, v193\n"
"v_mfma_f32_16x16x32_bf16 v[168:171], a[152:155], v[212:215], v[168:171]\n"
"buffer_load_dwordx4 v250, s[36:39], s53 offen nt lds\n"
"v_mfma_f32_16x16x32_bf16 v[172:175], a[156:159], v[212:215], v[172:175]\n"
"s_add_u32 m0, s50, 43808\n"
"v_cvt_pk_bf16_f32 v209, v194, v195\n"
"v_mfma_f32_16x16x32_bf16 v[176:179], a[160:163], v[212:215], v[176:179]\n"
"buffer_load_dwordx4 v250, s[36:39], s53 offen offset:256 nt lds\n"
"v_mfma_f32_16x16x32_bf16 v[180:183], a[164:167], v[212:215], v[180:183]\n"
"s_add_u32 m0, s50, 44608\n"
"v_cvt_pk_bf16_f32 v210, v196, v197\n"
"v_mfma_f32_16x16x32_bf16 v[184:187], a[168:171], v[212:215], v[184:187]\n"
"buffer_load_dwordx4 v250, s[36:39], s53 offen offset:512 nt lds\n"
"v_mfma_f32_16x16x32_bf16 v[188:191], a[172:175], v[212:215], v[188:191]\n"
"s_add_u32 m0, s50, 45408\n"
"v_cvt_pk_bf16_f32 v211, v198, v199\n"
"v_mfma_f32_16x16x32_bf16 a[228:231], a[236:239], v[212:215], a[228:231]\n"
"buffer_load_dwordx4 v250, s[36:39], s53 offen offset:768 nt lds\n"
"v_mfma_f32_16x16x32_bf16 v[192:195], a[112:115], a[16:19], v[220:223]\n"
"v_exp_f32 v200, v200\n"
"v_mfma_f32_16x16x32_bf16 v[192:195], a[116:119], a[20:23], v[192:195]\n"
"v_exp_f32 v201, v201\n"
"v_mfma_f32_16x16x32_bf16 v[192:195], a[120:123], a[24:27], v[192:195]\n"
"v_exp_f32 v202, v202\n"
"v_mfma_f32_16x16x32_bf16 v[192:195], a[124:127], a[28:31], v[192:195]\n"
"v_exp_f32 v203, v203\n"
"v_mfma_f32_16x16x32_bf16 v[196:199], a[128:131], a[16:19], v[220:223]\n"
"v_exp_f32 v204, v204\n"
"v_mfma_f32_16x16x32_bf16 v[196:199], a[132:135], a[20:23], v[196:199]\n"
"v_exp_f32 v205, v205\n"
"v_mfma_f32_16x16x32_bf16 v[196:199], a[136:139], a[24:27], v[196:199]\n"
"v_exp_f32 v206, v206\n"
"v_mfma_f32_16x16x32_bf16 v[196:199], a[140:143], a[28:31], v[196:199]\n"
"v_exp_f32 v207, v207\n"
"v_mfma_f32_16x16x32_bf16 a[176:179], a[144:147], v[208:211], 0\n"
"ds_read_b64_tr_b16 a[144:145], v247\n"
"ds_read_b64_tr_b16 a[146:147], v247 offset:4224\n"
"v_mfma_f32_16x16x32_bf16 a[180:183], a[148:151], v[208:211], 0\n"
"ds_read_b64_tr_b16 a[148:149], v247 offset:32\n"
"v_cvt_pk_bf16_f32 v212, v200, v201\n"
"v_mfma_f32_16x16x32_bf16 a[184:187], a[152:155], v[208:211], 0\n"
"ds_read_b64_tr_b16 a[150:151], v247 offset:4256\n"
"ds_read_b64_tr_b16 a[152:153], v247 offset:64\n"
"v_mfma_f32_16x16x32_bf16 a[188:191], a[156:159], v[208:211], 0\n"
"ds_read_b64_tr_b16 a[154:155], v247 offset:4288\n"
"v_cvt_pk_bf16_f32 v213, v202, v203\n"
"v_mfma_f32_16x16x32_bf16 a[192:195], a[160:163], v[208:211], 0\n"
"ds_read_b64_tr_b16 a[156:157], v247 offset:96\n"
"ds_read_b64_tr_b16 a[158:159], v247 offset:4320\n"
"v_mfma_f32_16x16x32_bf16 a[196:199], a[164:167], v[208:211], 0\n"
"ds_read_b64_tr_b16 a[160:161], v249\n"
"v_cvt_pk_bf16_f32 v214, v204, v205\n"
"v_mfma_f32_16x16x32_bf16 a[200:203], a[168:171], v[208:211], 0\n"
"ds_read_b64_tr_b16 a[162:163], v249 offset:4224\n"
"ds_read_b64_tr_b16 a[164:165], v249 offset:32\n"
"v_mfma_f32_16x16x32_bf16 a[204:207], a[172:175], v[208:211], 0\n"
"ds_read_b64_tr_b16 a[166:167], v249 offset:4256\n"
"v_cvt_pk_bf16_f32 v215, v206, v207\n"
"v_mfma_f32_16x16x32_bf16 a[232:235], a[236:239], v[208:211], 0\n"
"ds_read_b64_tr_b16 a[168:169], v249 offset:64\n"
"ds_read_b64_tr_b16 a[170:171], v249 offset:4288\n"
"v_mfma_f32_16x16x32_bf16 v[200:203], a[112:115], a[32:35], v[224:227]\n"
"ds_read_b64_tr_b16 a[172:173], v249 offset:96\n"
"ds_read_b64_tr_b16 a[174:175], v249 offset:4320\n"
"v_mfma_f32_16x16x32_bf16 v[200:203], a[116:119], a[36:39], v[200:203]\n"
"v_exp_f32 v192, v192\n"
"v_mfma_f32_16x16x32_bf16 v[200:203], a[120:123], a[40:43], v[200:203]\n"
"s_memrealtime s[28:29]\n"
"v_exp_f32 v193, v193\n"
"v_mfma_f32_16x16x32_bf16 v[200:203], a[124:127], a[44:47], v[200:203]\n"
"v_exp_f32 v194, v194\n"
"v_mfma_f32_16x16x32_bf16 v[204:207], a[128:131], a[32:35], v[224:227]\n"
"v_exp_f32 v195, v195\n"
"v_mfma_f32_16x16x32_bf16 v[204:207], a[132:135], a[36:39], v[204:207]\n"
"v_exp_f32 v196, v196\n"
"v_mfma_f32_16x16x32_bf16 v[204:207], a[136:139], a[40:43], v[204:207]\n"
"v_exp_f32 v197, v197\n"
"v_mfma_f32_16x16x32_bf16 v[204:207], a[140:143], a[44:47], v[204:207]\n"
"s_waitcnt lgkmcnt(14)\n"
"v_exp_f32 v198, v198\n"
"v_mfma_f32_16x16x32_bf16 v[0:3], a[144:147], v[212:215], v[0:3]\n"
"s_waitcnt lgkmcnt(12)\n"
"v_exp_f32 v199, v199\n"
"v_mfma_f32_16x16x32_bf16 v[4:7], a[148:151], v[212:215], v[4:7]\n"
"s_waitcnt lgkmcnt(10)\n"
"s_add_u32 m0, s50, 47232\n"
"v_cvt_pk_bf16_f32 v208, v192, v193\n"
"v_mfma_f32_16x16x32_bf16 v[8:11], a[152:155], v[212:215], v[8:11]\n"
"s_waitcnt lgkmcnt(8)\n"
"buffer_load_dwordx4 v250, s[36:39], s54 offen nt lds\n"
"s_add_u32 s53, s53, 8192\n"
"v_mfma_f32_16x16x32_bf16 v[12:15], a[156:159], v[212:215], v[12:15]\n"
"s_waitcnt lgkmcnt(6)\n"
"s_add_u32 m0, s50, 48032\n"
"v_cvt_pk_bf16_f32 v209, v194, v195\n"
"v_mfma_f32_16x16x32_bf16 v[16:19], a[160:163], v[212:215], v[16:19]\n"
"s_waitcnt lgkmcnt(4)\n"
"buffer_load_dwordx4 v250, s[36:39], s54 offen offset:256 nt lds\n"
"s_and_b32 s53, s53, 258047\n"
"v_mfma_f32_16x16x32_bf16 v[20:23], a[164:167], v[212:215], v[20:23]\n"
"s_waitcnt lgkmcnt(2)\n"
"s_add_u32 m0, s50, 48832\n"
"v_cvt_pk_bf16_f32 v210, v196, v197\n"
"v_mfma_f32_16x16x32_bf16 v[24:27], a[168:171], v[212:215], v[24:27]\n"
"s_waitcnt lgkmcnt(0)\n"
"buffer_load_dwordx4 v250, s[36:39], s54 offen offset:512 nt lds\n"
"v_mfma_f32_16x16x32_bf16 v[28:31], a[172:175], v[212:215], v[28:31]\n"
"s_add_u32 m0, s50, 49632\n"
"v_cvt_pk_bf16_f32 v211, v198, v199\n"
"v_mfma_f32_16x16x32_bf16 a[208:211], a[236:239], v[212:215], a[208:211]\n"
"buffer_load_dwordx4 v250, s[36:39], s54 offen offset:768 nt lds\n"
"s_lshl_b32 s38, s38, 1\n"
"s_add_u32 s54, s53, 4096\n"
"v_mfma_f32_16x16x32_bf16 v[192:195], a[112:115], a[48:51], v[228:231]\n"
"v_exp_f32 v200, v200\n"
"v_mfma_f32_16x16x32_bf16 v[192:195], a[116:119], a[52:55], v[192:195]\n"
"v_exp_f32 v201, v201\n"
"v_mfma_f32_16x16x32_bf16 v[192:195], a[120:123], a[56:59], v[192:195]\n"
"v_exp_f32 v202, v202\n"
"v_mfma_f32_16x16x32_bf16 v[192:195], a[124:127], a[60:63], v[192:195]\n"
"v_exp_f32 v203, v203\n"
"v_mfma_f32_16x16x32_bf16 v[196:199], a[128:131], a[48:51], v[228:231]\n"
"v_exp_f32 v204, v204\n"
"v_mfma_f32_16x16x32_bf16 v[196:199], a[132:135], a[52:55], v[196:199]\n"
"v_exp_f32 v205, v205\n"
"v_mfma_f32_16x16x32_bf16 v[196:199], a[136:139], a[56:59], v[196:199]\n"
"v_exp_f32 v206, v206\n"
"v_mfma_f32_16x16x32_bf16 v[196:199], a[140:143], a[60:63], v[196:199]\n"
"v_exp_f32 v207, v207\n"
"v_mfma_f32_16x16x32_bf16 v[32:35], a[144:147], v[208:211], v[32:35]\n"
"v_mfma_f32_16x16x32_bf16 v[36:39], a[148:151], v[208:211], v[36:39]\n"
"v_cvt_pk_bf16_f32 v212, v200, v201\n"
"v_cvt_pk_bf16_f32 v213, v202, v203\n"
"v_mfma_f32_16x16x32_bf16 v[40:43], a[152:155], v[208:211], v[40:43]\n"
"v_cvt_pk_bf16_f32 v214, v204, v205\n"
"v_cvt_pk_bf16_f32 v215, v206, v207\n"
"v_mfma_f32_16x16x32_bf16 v[44:47], a[156:159], v[208:211], v[44:47]\n"
"s_lshl_b32 s33, s28, 3\n"
"v_mfma_f32_16x16x32_bf16 v[48:51], a[160:163], v[208:211], v[48:51]\n"
"s_sub_i32 s32, s30, s33\n"
"v_mfma_f32_16x16x32_bf16 v[52:55], a[164:167], v[208:211], v[52:55]\n"
"s_max_i32 s30, s32, -400\n"
"v_mfma_f32_16x16x32_bf16 v[56:59], a[168:171], v[208:211], v[56:59]\n"
"s_add_i32 s30, s30, s33\n"
"v_mfma_f32_16x16x32_bf16 v[60:63], a[172:175], v[208:211], v[60:63]\n"
"s_add_i32 s30, s30, s66\n"
"v_mfma_f32_16x16x32_bf16 a[212:215], a[236:239], v[208:211], a[212:215]\n"
"v_mfma_f32_16x16x32_bf16 v[200:203], a[112:115], a[64:67], v[232:235]\n"
"v_exp_f32 v192, v192\n"
"v_mfma_f32_16x16x32_bf16 v[200:203], a[116:119], a[68:71], v[200:203]\n"
"v_exp_f32 v193, v193\n"
"v_mfma_f32_16x16x32_bf16 v[200:203], a[120:123], a[72:75], v[200:203]\n"
"v_exp_f32 v194, v194\n"
"v_mfma_f32_16x16x32_bf16 v[200:203], a[124:127], a[76:79], v[200:203]\n"
"v_exp_f32 v195, v195\n"
"v_mfma_f32_16x16x32_bf16 v[204:207], a[128:131], a[64:67], v[232:235]\n"
"v_exp_f32 v196, v196\n"
"v_mfma_f32_16x16x32_bf16 v[204:207], a[132:135], a[68:71], v[204:207]\n"
"v_exp_f32 v197, v197\n"
"v_mfma_f32_16x16x32_bf16 v[204:207], a[136:139], a[72:75], v[204:207]\n"
"v_exp_f32 v198, v198\n"
"v_mfma_f32_16x16x32_bf16 v[204:207], a[140:143], a[76:79], v[204:207]\n"
"v_exp_f32 v199, v199\n"
"v_mfma_f32_16x16x32_bf16 v[64:67], a[144:147], v[212:215], v[64:67]\n"
"v_mfma_f32_16x16x32_bf16 v[68:71], a[148:151], v[212:215], v[68:71]\n"
"v_cvt_pk_bf16_f32 v208, v192, v193\n"
"v_cvt_pk_bf16_f32 v209, v194, v195\n"
"v_mfma_f32_16x16x32_bf16 v[72:75], a[152:155], v[212:215], v[72:75]\n"
"v_cvt_pk_bf16_f32 v210, v196, v197\n"
"v_cvt_pk_bf16_f32 v211, v198, v199\n"
"v_mfma_f32_16x16x32_bf16 v[76:79], a[156:159], v[212:215], v[76:79]\n"
"s_sub_i32 s66, s66, 15\n"
"v_mfma_f32_16x16x32_bf16 v[80:83], a[160:163], v[212:215], v[80:83]\n"
"s_max_i32 s32, s32, 0\n"
"v_mfma_f32_16x16x32_bf16 v[84:87], a[164:167], v[212:215], v[84:87]\n"
"s_mul_i32 s32, s32, 13\n"
"v_mfma_f32_16x16x32_bf16 v[88:91], a[168:171], v[212:215], v[88:91]\n"
"s_lshr_b32 s32, s32, 8\n"
"v_mfma_f32_16x16x32_bf16 v[92:95], a[172:175], v[212:215], v[92:95]\n"
"s_min_u32 s31, s32, 24\n"
"v_mfma_f32_16x16x32_bf16 a[216:219], a[236:239], v[212:215], a[216:219]\n"
"1:\n"
"s_sub_u32 s31, s31, 1\n"
"s_cbranch_scc1 2f\n"
"s_nop 7\n"
"s_branch 1b\n"
"2:\n"
"s_barrier\n"
"v_mfma_f32_16x16x32_bf16 v[192:195], a[112:115], a[80:83], v[236:239]\n"
"v_exp_f32 v200, v200\n"
"v_mfma_f32_16x16x32_bf16 v[192:195], a[116:119], a[84:87], v[192:195]\n"
"ds_read_b128 a[112:115], v245 offset:34304\n"
"ds_read_b128 a[116:119], v245 offset:34368\n"
"v_mfma_f32_16x16x32_bf16 v[192:195], a[120:123], a[88:91], v[192:195]\n"
"v_exp_f32 v201, v201\n"
"v_mfma_f32_16x16x32_bf16 v[192:195], a[124:127], a[92:95], v[192:195]\n"
"ds_read_b128 a[120:123], v245 offset:34432\n"
"ds_read_b128 a[124:127], v245 offset:34496\n"
"v_mfma_f32_16x16x32_bf16 v[196:199], a[128:131], a[80:83], v[236:239]\n"
"v_exp_f32 v202, v202\n"
"v_mfma_f32_16x16x32_bf16 v[196:199], a[132:135], a[84:87], v[196:199]\n"
"ds_read_b128 a[128:131], v245 offset:38656\n"
"ds_read_b128 a[132:135], v245 offset:38720\n"
"v_mfma_f32_16x16x32_bf16 v[196:199], a[136:139], a[88:91], v[196:199]\n"
"v_exp_f32 v203, v203\n"
"v_mfma_f32_16x16x32_bf16 v[196:199], a[140:143], a[92:95], v[196:199]\n"
"ds_read_b128 a[136:139], v245 offset:38784\n"
"ds_read_b128 a[140:143], v245 offset:38848\n"
"v_mfma_f32_16x16x32_bf16 v[96:99], a[144:147], v[208:211], v[96:99]\n"
"v_exp_f32 v204, v204\n"
"v_mfma_f32_16x16x32_bf16 v[100:103], a[148:151], v[208:211], v[100:103]\n"
"v_exp_f32 v205, v205\n"
"v_mfma_f32_16x16x32_bf16 v[104:107], a[152:155], v[208:211], v[104:107]\n"
"v_exp_f32 v206, v206\n"
"v_mfma_f32_16x16x32_bf16 v[108:111], a[156:159], v[208:211], v[108:111]\n"
"v_exp_f32 v207, v207\n"
"v_mfma_f32_16x16x32_bf16 v[112:115], a[160:163], v[208:211], v[112:115]\n"
"v_cvt_pk_bf16_f32 v212, v200, v201\n"
"v_cvt_pk_bf16_f32 v213, v202, v203\n"
"v_mfma_f32_16x16x32_bf16 v[116:119], a[164:167], v[208:211], v[116:119]\n"
"v_cvt_pk_bf16_f32 v214, v204, v205\n"
"v_cvt_pk_bf16_f32 v215, v206, v207\n"
"v_mfma_f32_16x16x32_bf16 v[120:123], a[168:171], v[208:211], v[120:123]\n"
"v_mfma_f32_16x16x32_bf16 v[124:127], a[172:175], v[208:211], v[124:127]\n"
"v_mfma_f32_16x16x32_bf16 a[220:223], a[236:239], v[208:211], a[220:223]\n"
"s_waitcnt lgkmcnt(7)\n"
"v_mfma_f32_16x16x32_bf16 v[200:203], a[112:115], a[0:3], v[216:219]\n"
"s_waitcnt lgkmcnt(6)\n"
"v_exp_f32 v192, v192\n"
"v_mfma_f32_16x16x32_bf16 v[200:203], a[116:119], a[4:7], v[200:203]\n"
"s_waitcnt lgkmcnt(5)\n"
"v_exp_f32 v193, v193\n"
"v_mfma_f32_16x16x32_bf16 v[200:203], a[120:123], a[8:11], v[200:203]\n"
"s_waitcnt lgkmcnt(4)\n"
"v_exp_f32 v194, v194\n"
"v_mfma_f32_16x16x32_bf16 v[200:203], a[124:127], a[12:15], v[200:203]\n"
"s_waitcnt lgkmcnt(3)\n"
"v_exp_f32 v195, v195\n"
"v_mfma_f32_16x16x32_bf16 v[204:207], a[128:131], a[0:3], v[216:219]\n"
"s_waitcnt lgkmcnt(2)\n"
"v_exp_f32 v196, v196\n"
"v_mfma_f32_16x16x32_bf16 v[204:207], a[132:135], a[4:7], v[204:207]\n"
"s_waitcnt lgkmcnt(1)\n"
"v_exp_f32 v197, v197\n"
"v_mfma_f32_16x16x32_bf16 v[204:207], a[136:139], a[8:11], v[204:207]\n"
"s_waitcnt lgkmcnt(0)\n"
"v_exp_f32 v198, v198\n"
"v_mfma_f32_16x16x32_bf16 v[204:207], a[140:143], a[12:15], v[204:207]\n"
"v_exp_f32 v199, v199\n"
"v_mfma_f32_16x16x32_bf16 v[128:131], a[144:147], v[212:215], v[128:131]\n"
"v_mfma_f32_16x16x32_bf16 v[132:135], a[148:151], v[212:215], v[132:135]\n"
"s_add_u32 m0, s50, 77312\n"
"v_cvt_pk_bf16_f32 v208, v192, v193\n"
"v_mfma_f32_16x16x32_bf16 v[136:139], a[152:155], v[212:215], v[136:139]\n"
"buffer_load_dwordx4 v250, s[36:39], s53 offen nt lds\n"
"v_mfma_f32_16x16x32_bf16 v[140:143], a[156:159], v[212:215], v[140:143]\n"
"s_add_u32 m0, s50, 78112\n"
"v_cvt_pk_bf16_f32 v209, v194, v195\n"
"v_mfma_f32_16x16x32_bf16 v[144:147], a[160:163], v[212:215], v[144:147]\n"
"buffer_load_dwordx4 v250, s[36:39], s53 offen offset:256 nt lds\n"
"v_mfma_f32_16x16x32_bf16 v[148:151], a[164:167], v[212:215], v[148:151]\n"
"s_add_u32 m0, s50, 78912\n"
"v_cvt_pk_bf16_f32 v210, v196, v197\n"
"v_mfma_f32_16x16x32_bf16 v[152:155], a[168:171], v[212:215], v[152:155]\n"
"buffer_load_dwordx4 v250, s[36:39], s53 offen offset:512 nt lds\n"
"v_mfma_f32_16x16x32_bf16 v[156:159], a[172:175], v[212:215], v[156:159]\n"
"s_add_u32 m0, s50, 79712\n"
"v_cvt_pk_bf16_f32 v211, v198, v199\n"
"v_mfma_f32_16x16x32_bf16 a[224:227], a[236:239], v[212:215], a[224:227]\n"
"buffer_load_dwordx4 v250, s[36:39], s53 offen offset:768 nt lds\n"
"v_mfma_f32_16x16x32_bf16 v[192:195], a[112:115], a[16:19], v[220:223]\n"
"v_exp_f32 v200, v200\n"
"v_mfma_f32_16x16x32_bf16 v[192:195], a[116:119], a[20:23], v[192:195]\n"
"v_exp_f32 v201, v201\n"
"v_mfma_f32_16x16x32_bf16 v[192:195], a[120:123], a[24:27], v[192:195]\n"
"v_exp_f32 v202, v202\n"
"v_mfma_f32_16x16x32_bf16 v[192:195], a[124:127], a[28:31], v[192:195]\n"
"v_exp_f32 v203, v203\n"
"v_mfma_f32_16x16x32_bf16 v[196:199], a[128:131], a[16:19], v[220:223]\n"
"v_exp_f32 v204, v204\n"
"v_mfma_f32_16x16x32_bf16 v[196:199], a[132:135], a[20:23], v[196:199]\n"
"v_exp_f32 v205, v205\n"
"v_mfma_f32_16x16x32_bf16 v[196:199], a[136:139], a[24:27], v[196:199]\n"
"v_exp_f32 v206, v206\n"
"v_mfma_f32_16x16x32_bf16 v[196:199], a[140:143], a[28:31], v[196:199]\n"
"v_exp_f32 v207, v207\n"
"v_mfma_f32_16x16x32_bf16 v[160:163], a[144:147], v[208:211], v[160:163]\n"
"ds_read_b64_tr_b16 a[144:145], v247 offset:34304\n"
"ds_read_b64_tr_b16 a[146:147], v247 offset:38528\n"
"v_mfma_f32_16x16x32_bf16 v[164:167], a[148:151], v[208:211], v[164:167]\n"
"ds_read_b64_tr_b16 a[148:149], v247 offset:34336\n"
"v_cvt_pk_bf16_f32 v212, v200, v201\n"
"v_mfma_f32_16x16x32_bf16 v[168:171], a[152:155], v[208:211], v[168:171]\n"
"ds_read_b64_tr_b16 a[150:151], v247 offset:38560\n"
"ds_read_b64_tr_b16 a[152:153], v247 offset:34368\n"
"v_mfma_f32_16x16x32_bf16 v[172:175], a[156:159], v[208:211], v[172:175]\n"
"ds_read_b64_tr_b16 a[154:155], v247 offset:38592\n"
"v_cvt_pk_bf16_f32 v213, v202, v203\n"
"v_mfma_f32_16x16x32_bf16 v[176:179], a[160:163], v[208:211], v[176:179]\n"
"ds_read_b64_tr_b16 a[156:157], v247 offset:34400\n"
"ds_read_b64_tr_b16 a[158:159], v247 offset:38624\n"
"v_mfma_f32_16x16x32_bf16 v[180:183], a[164:167], v[208:211], v[180:183]\n"
"ds_read_b64_tr_b16 a[160:161], v249 offset:34304\n"
"v_cvt_pk_bf16_f32 v214, v204, v205\n"
"v_mfma_f32_16x16x32_bf16 v[184:187], a[168:171], v[208:211], v[184:187]\n"
"ds_read_b64_tr_b16 a[162:163], v249 offset:38528\n"
"ds_read_b64_tr_b16 a[164:165], v249 offset:34336\n"
"v_mfma_f32_16x16x32_bf16 v[188:191], a[172:175], v[208:211], v[188:191]\n"
"ds_read_b64_tr_b16 a[166:167], v249 offset:38560\n"
"v_cvt_pk_bf16_f32 v215, v206, v207\n"
"v_mfma_f32_16x16x32_bf16 a[228:231], a[236:239], v[208:211], a[228:231]\n"
"ds_read_b64_tr_b16 a[168:169], v249 offset:34368\n"
"ds_read_b64_tr_b16 a[170:171], v249 offset:38592\n"
"v_mfma_f32_16x16x32_bf16 v[200:203], a[112:115], a[32:35], v[224:227]\n"
"ds_read_b64_tr_b16 a[172:173], v249 offset:34400\n"
"ds_read_b64_tr_b16 a[174:175], v249 offset:38624\n"
"v_mfma_f32_16x16x32_bf16 v[200:203], a[116:119], a[36:39], v[200:203]\n"
"v_exp_f32 v192, v192\n"
"v_mfma_f32_16x16x32_bf16 v[200:203], a[120:123], a[40:43], v[200:203]\n"
"v_exp_f32 v193, v193\n"
"v_mfma_f32_16x16x32_bf16 v[200:203], a[124:127], a[44:47], v[200:203]\n"
"v_exp_f32 v194, v194\n"
"v_mfma_f32_16x16x32_bf16 v[204:207], a[128:131], a[32:35], v[224:227]\n"
"v_exp_f32 v195, v195\n"
"v_mfma_f32_16x16x32_bf16 v[204:207], a[132:135], a[36:39], v[204:207]\n"
"v_exp_f32 v196, v196\n"
"v_mfma_f32_16x16x32_bf16 v[204:207], a[136:139], a[40:43], v[204:207]\n"
"v_exp_f32 v197, v197\n"
"v_mfma_f32_16x16x32_bf16 v[204:207], a[140:143], a[44:47], v[204:207]\n"
"s_waitcnt lgkmcnt(14)\n"
"v_exp_f32 v198, v198\n"
"v_mfma_f32_16x16x32_bf16 v[0:3], a[144:147], v[212:215], v[0:3]\n"
"s_waitcnt lgkmcnt(12)\n"
"v_exp_f32 v199, v199\n"
"v_mfma_f32_16x16x32_bf16 v[4:7], a[148:151], v[212:215], v[4:7]\n"
"s_waitcnt lgkmcnt(10)\n"
"s_add_u32 m0, s50, 81536\n"
"v_cvt_pk_bf16_f32 v208, v192, v193\n"
"v_mfma_f32_16x16x32_bf16 v[8:11], a[152:155], v[212:215], v[8:11]\n"
"s_waitcnt lgkmcnt(8)\n"
"buffer_load_dwordx4 v250, s[36:39], s54 offen nt lds\n"
"s_add_u32 s53, s53, 8192\n"
"v_mfma_f32_16x16x32_bf16 v[12:15], a[156:159], v[212:215], v[12:15]\n"
"s_waitcnt lgkmcnt(6)\n"
"s_add_u32 m0, s50, 82336\n"
"v_cvt_pk_bf16_f32 v209, v194, v195\n"
"v_mfma_f32_16x16x32_bf16 v[16:19], a[160:163], v[212:215], v[16:19]\n"
"s_waitcnt lgkmcnt(4)\n"
"buffer_load_dwordx4 v250, s[36:39], s54 offen offset:256 nt lds\n"
"s_and_b32 s53, s53, 258047\n"
"v_mfma_f32_16x16x32_bf16 v[20:23], a[164:167], v[212:215], v[20:23]\n"
"s_waitcnt lgkmcnt(2)\n"
"s_add_u32 m0, s50, 83136\n"
"v_cvt_pk_bf16_f32 v210, v196, v197\n"
"v_mfma_f32_16x16x32_bf16 v[24:27], a[168:171], v[212:215], v[24:27]\n"
"s_waitcnt lgkmcnt(0)\n"
"buffer_load_dwordx4 v250, s[36:39], s54 offen offset:512 nt lds\n"
"v_mfma_f32_16x16x32_bf16 v[28:31], a[172:175], v[212:215], v[28:31]\n"
"s_add_u32 m0, s50, 83936\n"
"v_cvt_pk_bf16_f32 v211, v198, v199\n"
"v_mfma_f32_16x16x32_bf16 a[208:211], a[236:239], v[212:215], a[208:211]\n"
"buffer_load_dwordx4 v250, s[36:39], s54 offen offset:768 nt lds\n"
"s_lshl_b32 s38, s38, 1\n"
"s_add_u32 s54, s53, 4096\n"
"v_mfma_f32_16x16x32_bf16 v[192:195], a[112:115], a[48:51], v[228:231]\n"
"v_exp_f32 v200, v200\n"
"v_mfma_f32_16x16x32_bf16 v[192:195], a[116:119], a[52:55], v[192:195]\n"
"v_exp_f32 v201, v201\n"
"v_mfma_f32_16x16x32_bf16 v[192:195], a[120:123], a[56:59], v[192:195]\n"
"s_memrealtime s[28:29]\n"
"v_exp_f32 v202, v202\n"
"v_mfma_f32_16x16x32_bf16 v[192:195], a[124:127], a[60:63], v[192:195]\n"
"v_exp_f32 v203, v203\n"
"v_mfma_f32_16x16x32_bf16 v[196:199], a[128:131], a[48:51], v[228:231]\n"
"v_exp_f32 v204, v204\n"
"v_mfma_f32_16x16x32_bf16 v[196:199], a[132:135], a[52:55], v[196:199]\n"
"v_exp_f32 v205, v205\n"
"v_mfma_f32_16x16x32_bf16 v[196:199], a[136:139], a[56:59], v[196:199]\n"
"v_exp_f32 v206, v206\n"
"v_mfma_f32_16x16x32_bf16 v[196:199], a[140:143], a[60:63], v[196:199]\n"
"v_exp_f32 v207, v207\n"
"v_mfma_f32_16x16x32_bf16 v[32:35], a[144:147], v[208:211], v[32:35]\n"
"v_mfma_f32_16x16x32_bf16 v[36:39], a[148:151], v[208:211], v[36:39]\n"
"v_cvt_pk_bf16_f32 v212, v200, v201\n"
"v_cvt_pk_bf16_f32 v213, v202, v203\n"
"v_mfma_f32_16x16x32_bf16 v[40:43], a[152:155], v[208:211], v[40:43]\n"
"v_cvt_pk_bf16_f32 v214, v204, v205\n"
"v_cvt_pk_bf16_f32 v215, v206, v207\n"
"v_mfma_f32_16x16x32_bf16 v[44:47], a[156:159], v[208:211], v[44:47]\n"
"v_mfma_f32_16x16x32_bf16 v[48:51], a[160:163], v[208:211], v[48:51]\n"
"v_mfma_f32_16x16x32_bf16 v[52:55], a[164:167], v[208:211], v[52:55]\n"
"v_mfma_f32_16x16x32_bf16 v[56:59], a[168:171], v[208:211], v[56:59]\n"
"v_mfma_f32_16x16x32_bf16 v[60:63], a[172:175], v[208:211], v[60:63]\n"
"v_mfma_f32_16x16x32_bf16 a[212:215], a[236:239], v[208:211], a[212:215]\n"
"v_mfma_f32_16x16x32_bf16 v[200:203], a[112:115], a[64:67], v[232:235]\n"
"v_exp_f32 v192, v192\n"
"v_mfma_f32_16x16x32_bf16 v[200:203], a[116:119], a[68:71], v[200:203]\n"
"v_exp_f32 v193, v193\n"
"v_mfma_f32_16x16x32_bf16 v[200:203], a[120:123], a[72:75], v[200:203]\n"
"v_exp_f32 v194, v194\n"
"v_mfma_f32_16x16x32_bf16 v[200:203], a[124:127], a[76:79], v[200:203]\n"
"v_exp_f32 v195, v195\n"
"v_mfma_f32_16x16x32_bf16 v[204:207], a[128:131], a[64:67], v[232:235]\n"
"v_exp_f32 v196, v196\n"
"v_mfma_f32_16x16x32_bf16 v[204:207], a[132:135], a[68:71], v[204:207]\n"
"v_exp_f32 v197, v197\n"
"v_mfma_f32_16x16x32_bf16 v[204:207], a[136:139], a[72:75], v[204:207]\n"
"v_exp_f32 v198, v198\n"
"v_mfma_f32_16x16x32_bf16 v[204:207], a[140:143], a[76:79], v[204:207]\n"
"v_exp_f32 v199, v199\n"
"v_mfma_f32_16x16x32_bf16 v[64:67], a[144:147], v[212:215], v[64:67]\n"
"v_mfma_f32_16x16x32_bf16 v[68:71], a[148:151], v[212:215], v[68:71]\n"
"v_cvt_pk_bf16_f32 v208, v192, v193\n"
"v_cvt_pk_bf16_f32 v209, v194, v195\n"
"v_mfma_f32_16x16x32_bf16 v[72:75], a[152:155], v[212:215], v[72:75]\n"
"v_cvt_pk_bf16_f32 v210, v196, v197\n"
"v_cvt_pk_bf16_f32 v211, v198, v199\n"
"v_mfma_f32_16x16x32_bf16 v[76:79], a[156:159], v[212:215], v[76:79]\n"
"s_lshl_b32 s33, s28, 3\n"
"v_mfma_f32_16x16x32_bf16 v[80:83], a[160:163], v[212:215], v[80:83]\n"
"s_sub_i32 s32, s30, s33\n"
"v_mfma_f32_16x16x32_bf16 v[84:87], a[164:167], v[212:215], v[84:87]\n"
"s_max_i32 s30, s32, -400\n"
"v_mfma_f32_16x16x32_bf16 v[88:91], a[168:171], v[212:215], v[88:91]\n"
"s_add_i32 s30, s30, s33\n"
"v_mfma_f32_16x16x32_bf16 v[92:95], a[172:175], v[212:215], v[92:95]\n"
"s_add_i32 s30, s30, s66\n"
"v_mfma_f32_16x16x32_bf16 a[216:219], a[236:239], v[212:215], a[216:219]\n"
"v_mfma_f32_16x16x32_bf16 v[192:195], a[112:115], a[80:83], v[236:239]\n"
"v_exp_f32 v200, v200\n"
"v_mfma_f32_16x16x32_bf16 v[192:195], a[116:119], a[84:87], v[192:195]\n"
"v_exp_f32 v201, v201\n"
"v_mfma_f32_16x16x32_bf16 v[192:195], a[120:123], a[88:91], v[192:195]\n"
"v_exp_f32 v202, v202\n"
"v_mfma_f32_16x16x32_bf16 v[192:195], a[124:127], a[92:95], v[192:195]\n"
"v_exp_f32 v203, v203\n"
"v_mfma_f32_16x16x32_bf16 v[196:199], a[128:131], a[80:83], v[236:239]\n"
"v_exp_f32 v204, v204\n"
"v_mfma_f32_16x16x32_bf16 v[196:199], a[132:135], a[84:87], v[196:199]\n"
"v_exp_f32 v205, v205\n"
"v_mfma_f32_16x16x32_bf16 v[196:199], a[136:139], a[88:91], v[196:199]\n"
"v_exp_f32 v206, v206\n"
"v_mfma_f32_16x16x32_bf16 v[196:199], a[140:143], a[92:95], v[196:199]\n"
"v_exp_f32 v207, v207\n"
"v_mfma_f32_16x16x32_bf16 v[96:99], a[144:147], v[208:211], v[96:99]\n"
"v_mfma_f32_16x16x32_bf16 v[100:103], a[148:151], v[208:211], v[100:103]\n"
"v_cvt_pk_bf16_f32 v212, v200, v201\n"
"v_cvt_pk_bf16_f32 v213, v202, v203\n"
"v_mfma_f32_16x16x32_bf16 v[104:107], a[152:155], v[208:211], v[104:107]\n"
"v_cvt_pk_bf16_f32 v214, v204, v205\n"
"v_cvt_pk_bf16_f32 v215, v206, v207\n"
"v_mfma_f32_16x16x32_bf16 v[108:111], a[156:159], v[208:211], v[108:111]\n"
"s_sub_i32 s66, s66, 15\n"
"v_mfma_f32_16x16x32_bf16 v[112:115], a[160:163], v[208:211], v[112:115]\n"
"s_max_i32 s32, s32, 0\n"
"v_mfma_f32_16x16x32_bf16 v[116:119], a[164:167], v[208:211], v[116:119]\n"
"s_mul_i32 s32, s32, 13\n"
"v_mfma_f32_16x16x32_bf16 v[120:123], a[168:171], v[208:211], v[120:123]\n"
"s_lshr_b32 s32, s32, 8\n"
"v_mfma_f32_16x16x32_bf16 v[124:127], a[172:175], v[208:211], v[124:127]\n"
"s_min_u32 s31, s32, 24\n"
"v_mfma_f32_16x16x32_bf16 a[220:223], a[236:239], v[208:211], a[220:223]\n"
"1:\n"
"s_sub_u32 s31, s31, 1\n"
"s_cbranch_scc1 2f\n"
"s_nop 7\n"
"s_branch 1b\n"
"2:\n"
"s_waitcnt vmcnt(16)\n"
"s_barrier\n"
"v_mfma_f32_16x16x32_bf16 v[200:203], a[112:115], a[96:99], v[240:243]\n"
"v_exp_f32 v192, v192\n"
"v_mfma_f32_16x16x32_bf16 v[200:203], a[116:119], a[100:103], v[200:203]\n"
"ds_read_b128 a[112:115], v244\n"
"ds_read_b128 a[116:119], v244 offset:64\n"
"v_mfma_f32_16x16x32_bf16 v[200:203], a[120:123], a[104:107], v[200:203]\n"
"v_exp_f32 v193, v193\n"
"v_mfma_f32_16x16x32_bf16 v[200:203], a[124:127], a[108:111], v[200:203]\n"
"ds_read_b128 a[120:123], v244 offset:128\n"
"ds_read_b128 a[124:127], v244 offset:192\n"
"v_mfma_f32_16x16x32_bf16 v[204:207], a[128:131], a[96:99], v[240:243]\n"
"v_exp_f32 v194, v194\n"
"v_mfma_f32_16x16x32_bf16 v[204:207], a[132:135], a[100:103], v[204:207]\n"
"ds_read_b128 a[128:131], v244 offset:4352\n"
"ds_read_b128 a[132:135], v244 offset:4416\n"
"v_mfma_f32_16x16x32_bf16 v[204:207], a[136:139], a[104:107], v[204:207]\n"
"v_exp_f32 v195, v195\n"
"v_mfma_f32_16x16x32_bf16 v[204:207], a[140:143], a[108:111], v[204:207]\n"
"ds_read_b128 a[136:139], v244 offset:4480\n"
"ds_read_b128 a[140:143], v244 offset:4544\n"
"v_mfma_f32_16x16x32_bf16 v[128:131], a[144:147], v[212:215], v[128:131]\n"
"v_exp_f32 v196, v196\n"
"v_mfma_f32_16x16x32_bf16 v[132:135], a[148:151], v[212:215], v[132:135]\n"
"v_exp_f32 v197, v197\n"
"v_mfma_f32_16x16x32_bf16 v[136:139], a[152:155], v[212:215], v[136:139]\n"
"v_exp_f32 v198, v198\n"
"v_mfma_f32_16x16x32_bf16 v[140:143], a[156:159], v[212:215], v[140:143]\n"
"v_exp_f32 v199, v199\n"
"v_mfma_f32_16x16x32_bf16 v[144:147], a[160:163], v[212:215], v[144:147]\n"
"v_cvt_pk_bf16_f32 v208, v192, v193\n"
"v_cvt_pk_bf16_f32 v209, v194, v195\n"
"v_mfma_f32_16x16x32_bf16 v[148:151], a[164:167], v[212:215], v[148:151]\n"
"v_cvt_pk_bf16_f32 v210, v196, v197\n"
"v_cvt_pk_bf16_f32 v211, v198, v199\n"
"v_mfma_f32_16x16x32_bf16 v[152:155], a[168:171], v[212:215], v[152:155]\n"
"v_mfma_f32_16x16x32_bf16 v[156:159], a[172:175], v[212:215], v[156:159]\n"
"v_mfma_f32_16x16x32_bf16 a[224:227], a[236:239], v[212:215], a[224:227]\n"
"s_waitcnt lgkmcnt(7)\n"
"v_mfma_f32_16x16x32_bf16 v[192:195], a[112:115], a[0:3], v[216:219]\n"
"s_waitcnt lgkmcnt(6)\n"
"v_exp_f32 v200, v200\n"
"v_mfma_f32_16x16x32_bf16 v[192:195], a[116:119], a[4:7], v[192:195]\n"
"s_waitcnt lgkmcnt(5)\n"
"v_exp_f32 v201, v201\n"
"v_mfma_f32_16x16x32_bf16 v[192:195], a[120:123], a[8:11], v[192:195]\n"
"s_waitcnt lgkmcnt(4)\n"
"v_exp_f32 v202, v202\n"
"v_mfma_f32_16x16x32_bf16 v[192:195], a[124:127], a[12:15], v[192:195]\n"
"s_waitcnt lgkmcnt(3)\n"
"v_exp_f32 v203, v203\n"
"v_mfma_f32_16x16x32_bf16 v[196:199], a[128:131], a[0:3], v[216:219]\n"
"s_waitcnt lgkmcnt(2)\n"
"v_exp_f32 v204, v204\n"
"v_mfma_f32_16x16x32_bf16 v[196:199], a[132:135], a[4:7], v[196:199]\n"
"s_waitcnt lgkmcnt(1)\n"
"v_exp_f32 v205, v205\n"
"v_mfma_f32_16x16x32_bf16 v[196:199], a[136:139], a[8:11], v[196:199]\n"
"s_waitcnt lgkmcnt(0)\n"
"v_exp_f32 v206, v206\n"
"v_mfma_f32_16x16x32_bf16 v[196:199], a[140:143], a[12:15], v[196:199]\n"
"v_exp_f32 v207, v207\n"
"v_mfma_f32_16x16x32_bf16 v[160:163], a[144:147], v[208:211], v[160:163]\n"
"v_mfma_f32_16x16x32_bf16 v[164:167], a[148:151], v[208:211], v[164:167]\n"
"s_add_u32 m0, s50, 111616\n"
"v_cvt_pk_bf16_f32 v212, v200, v201\n"
"v_mfma_f32_16x16x32_bf16 v[168:171], a[152:155], v[208:211], v[168:171]\n"
"buffer_load_dwordx4 v250, s[36:39], s53 offen nt lds\n"
"v_mfma_f32_16x16x32_bf16 v[172:175], a[156:159], v[208:211], v[172:175]\n"
"s_add_u32 m0, s50, 112416\n"
"v_cvt_pk_bf16_f32 v213, v202, v203\n"
"v_mfma_f32_16x16x32_bf16 v[176:179], a[160:163], v[208:211], v[176:179]\n"
"buffer_load_dwordx4 v250, s[36:39], s53 offen offset:256 nt lds\n"
"v_mfma_f32_16x16x32_bf16 v[180:183], a[164:167], v[208:211], v[180:183]\n"
"s_add_u32 m0, s50, 113216\n"
"v_cvt_pk_bf16_f32 v214, v204, v205\n"
"v_mfma_f32_16x16x32_bf16 v[184:187], a[168:171], v[208:211], v[184:187]\n"
"buffer_load_dwordx4 v250, s[36:39], s53 offen offset:512 nt lds\n"
"v_mfma_f32_16x16x32_bf16 v[188:191], a[172:175], v[208:211], v[188:191]\n"
"s_add_u32 m0, s50, 114016\n"
"v_cvt_pk_bf16_f32 v215, v206, v207\n"
"v_mfma_f32_16x16x32_bf16 a[228:231], a[236:239], v[208:211], a[228:231]\n"
"buffer_load_dwordx4 v250, s[36:39], s53 offen offset:768 nt lds\n"
"s_mov_b32 s55, 6\n"
".p2align 6\n"
"L1_loop_h1:\n"
"v_mfma_f32_16x16x32_bf16 v[200:203], a[112:115], a[16:19], v[220:223]\n"
"v_exp_f32 v192, v192\n"
"v_mfma_f32_16x16x32_bf16 v[200:203], a[116:119], a[20:23], v[200:203]\n"
"v_exp_f32 v193, v193\n"
"v_mfma_f32_16x16x32_bf16 v[200:203], a[120:123], a[24:27], v[200:203]\n"
"v_exp_f32 v194, v194\n"
"v_mfma_f32_16x16x32_bf16 v[200:203], a[124:127], a[28:31], v[200:203]\n"
"v_exp_f32 v195, v195\n"
"v_mfma_f32_16x16x32_bf16 v[204:207], a[128:131], a[16:19], v[220:223]\n"
"v_exp_f32 v196, v196\n"
"v_mfma_f32_16x16x32_bf16 v[204:207], a[132:135], a[20:23], v[204:207]\n"
"v_exp_f32 v197, v197\n"
"v_mfma_f32_16x16x32_bf16 v[204:207], a[136:139], a[24:27], v[204:207]\n"
"v_exp_f32 v198, v198\n"
"v_mfma_f32_16x16x32_bf16 v[204:207], a[140:143], a[28:31], v[204:207]\n"
"v_exp_f32 v199, v199\n"
"v_mfma_f32_16x16x32_bf16 a[176:179], a[144:147], v[212:215], a[176:179]\n"
"ds_read_b64_tr_b16 a[144:145], v246\n"
"ds_read_b64_tr_b16 a[146:147], v246 offset:4224\n"
"v_mfma_f32_16x16x32_bf16 a[180:183], a[148:151], v[212:215], a[180:183]\n"
"ds_read_b64_tr_b16 a[148:149], v246 offset:32\n"
"v_cvt_pk_bf16_f32 v208, v192, v193\n"
"v_mfma_f32_16x16x32_bf16 a[184:187], a[152:155], v[212:215], a[184:187]\n"
"ds_read_b64_tr_b16 a[150:151], v246 offset:4256\n"
"ds_read_b64_tr_b16 a[152:153], v246 offset:64\n"
"v_mfma_f32_16x16x32_bf16 a[188:191], a[156:159], v[212:215], a[188:191]\n"
"ds_read_b64_tr_b16 a[154:155], v246 offset:4288\n"
"v_cvt_pk_bf16_f32 v209, v194, v195\n"
"v_mfma_f32_16x16x32_bf16 a[192:195], a[160:163], v[212:215], a[192:195]\n"
"ds_read_b64_tr_b16 a[156:157], v246 offset:96\n"
"ds_read_b64_tr_b16 a[158:159], v246 offset:4320\n"
"v_mfma_f32_16x16x32_bf16 a[196:199], a[164:167], v[212:215], a[196:199]\n"
"ds_read_b64_tr_b16 a[160:161], v248\n"
"v_cvt_pk_bf16_f32 v210, v196, v197\n"
"v_mfma_f32_16x16x32_bf16 a[200:203], a[168:171], v[212:215], a[200:203]\n"
"ds_read_b64_tr_b16 a[162:163], v248 offset:4224\n"
"ds_read_b64_tr_b16 a[164:165], v248 offset:32\n"
"v_mfma_f32_16x16x32_bf16 a[204:207], a[172:175], v[212:215], a[204:207]\n"
"ds_read_b64_tr_b16 a[166:167], v248 offset:4256\n"
"v_cvt_pk_bf16_f32 v211, v198, v199\n"
"v_mfma_f32_16x16x32_bf16 a[232:235], a[236:239], v[212:215], a[232:235]\n"
"ds_read_b64_tr_b16 a[168:169], v248 offset:64\n"
"ds_read_b64_tr_b16 a[170:171], v248 offset:4288\n"
"v_mfma_f32_16x16x32_bf16 v[192:195], a[112:115], a[32:35], v[224:227]\n"
"ds_read_b64_tr_b16 a[172:173], v248 offset:96\n"
"ds_read_b64_tr_b16 a[174:175], v248 offset:4320\n"
"v_mfma_f32_16x16x32_bf16 v[192:195], a[116:119], a[36:39], v[192:195]\n"
"v_exp_f32 v200, v200\n"
"v_mfma_f32_16x16x32_bf16 v[192:195], a[120:123], a[40:43], v[192:195]\n"
"s_memrealtime s[28:29]\n"
"v_exp_f32 v201, v201\n"
"v_mfma_f32_16x16x32_bf16 v[192:195], a[124:127], a[44:47], v[192:195]\n"
"v_exp_f32 v202, v202\n"
"v_mfma_f32_16x16x32_bf16 v[196:199], a[128:131], a[32:35], v[224:227]\n"
"v_exp_f32 v203, v203\n"
"v_mfma_f32_16x16x32_bf16 v[196:199], a[132:135], a[36:39], v[196:199]\n"
"v_exp_f32 v204, v204\n"
"v_mfma_f32_16x16x32_bf16 v[196:199], a[136:139], a[40:43], v[196:199]\n"
"v_exp_f32 v205, v205\n"
"v_mfma_f32_16x16x32_bf16 v[196:199], a[140:143], a[44:47], v[196:199]\n"
"s_waitcnt lgkmcnt(14)\n"
"v_exp_f32 v206, v206\n"
"v_mfma_f32_16x16x32_bf16 v[0:3], a[144:147], v[208:211], v[0:3]\n"
"s_waitcnt lgkmcnt(12)\n"
"v_exp_f32 v207, v207\n"
"v_mfma_f32_16x16x32_bf16 v[4:7], a[148:151], v[208:211], v[4:7]\n"
"s_waitcnt lgkmcnt(10)\n"
"s_add_u32 m0, s50, 115840\n"
"v_cvt_pk_bf16_f32 v212, v200, v201\n"
"v_mfma_f32_16x16x32_bf16 v[8:11], a[152:155], v[208:211], v[8:11]\n"
"s_waitcnt lgkmcnt(8)\n"
"buffer_load_dwordx4 v250, s[36:39], s54 offen nt lds\n"
"s_add_u32 s53, s53, 8192\n"
"v_mfma_f32_16x16x32_bf16 v[12:15], a[156:159], v[208:211], v[12:15]\n"
"s_waitcnt lgkmcnt(6)\n"
"s_add_u32 m0, s50, 116640\n"
"v_cvt_pk_bf16_f32 v213, v202, v203\n"
"v_mfma_f32_16x16x32_bf16 v[16:19], a[160:163], v[208:211], v[16:19]\n"
"s_waitcnt lgkmcnt(4)\n"
"buffer_load_dwordx4 v250, s[36:39], s54 offen offset:256 nt lds\n"
"s_and_b32 s53, s53, 258047\n"
"v_mfma_f32_16x16x32_bf16 v[20:23], a[164:167], v[208:211], v[20:23]\n"
"s_waitcnt lgkmcnt(2)\n"
"s_add_u32 m0, s50, 117440\n"
"v_cvt_pk_bf16_f32 v214, v204, v205\n"
"v_mfma_f32_16x16x32_bf16 v[24:27], a[168:171], v[208:211], v[24:27]\n"
"s_waitcnt lgkmcnt(0)\n"
"buffer_load_dwordx4 v250, s[36:39], s54 offen offset:512 nt lds\n"
"v_mfma_f32_16x16x32_bf16 v[28:31], a[172:175], v[208:211], v[28:31]\n"
"s_add_u32 m0, s50, 118240\n"
"v_cvt_pk_bf16_f32 v215, v206, v207\n"
"v_mfma_f32_16x16x32_bf16 a[208:211], a[236:239], v[208:211], a[208:211]\n"
"buffer_load_dwordx4 v250, s[36:39], s54 offen offset:768 nt lds\n"
"s_lshl_b32 s38, s38, 1\n"
"s_add_u32 s54, s53, 4096\n"
"v_mfma_f32_16x16x32_bf16 v[200:203], a[112:115], a[48:51], v[228:231]\n"
"v_exp_f32 v192, v192\n"
"v_mfma_f32_16x16x32_bf16 v[200:203], a[116:119], a[52:55], v[200:203]\n"
"v_exp_f32 v193, v193\n"
"v_mfma_f32_16x16x32_bf16 v[200:203], a[120:123], a[56:59], v[200:203]\n"
"v_exp_f32 v194, v194\n"
"v_mfma_f32_16x16x32_bf16 v[200:203], a[124:127], a[60:63], v[200:203]\n"
"v_exp_f32 v195, v195\n"
"v_mfma_f32_16x16x32_bf16 v[204:207], a[128:131], a[48:51], v[228:231]\n"
"v_exp_f32 v196, v196\n"
"v_mfma_f32_16x16x32_bf16 v[204:207], a[132:135], a[52:55], v[204:207]\n"
"v_exp_f32 v197, v197\n"
"v_mfma_f32_16x16x32_bf16 v[204:207], a[136:139], a[56:59], v[204:207]\n"
"v_exp_f32 v198, v198\n"
"v_mfma_f32_16x16x32_bf16 v[204:207], a[140:143], a[60:63], v[204:207]\n"
"v_exp_f32 v199, v199\n"
"v_mfma_f32_16x16x32_bf16 v[32:35], a[144:147], v[212:215], v[32:35]\n"
"v_mfma_f32_16x16x32_bf16 v[36:39], a[148:151], v[212:215], v[36:39]\n"
"v_cvt_pk_bf16_f32 v208, v192, v193\n"
"v_cvt_pk_bf16_f32 v209, v194, v195\n"
"v_mfma_f32_16x16x32_bf16 v[40:43], a[152:155], v[212:215], v[40:43]\n"
"v_cvt_pk_bf16_f32 v210, v196, v197\n"
"v_cvt_pk_bf16_f32 v211, v198, v199\n"
"v_mfma_f32_16x16x32_bf16 v[44:47], a[156:159], v[212:215], v[44:47]\n"
"s_lshl_b32 s33, s28, 3\n"
"v_mfma_f32_16x16x32_bf16 v[48:51], a[160:163], v[212:215], v[48:51]\n"
"s_sub_i32 s32, s30, s33\n"
"v_mfma_f32_16x16x32_bf16 v[52:55], a[164:167], v[212:215], v[52:55]\n"
"s_max_i32 s30, s32, -400\n"
"v_mfma_f32_16x16x32_bf16 v[56:59], a[168:171], v[212:215], v[56:59]\n"
"s_add_i32 s30, s30, s33\n"
"v_mfma_f32_16x16x32_bf16 v[60:63], a[172:175], v[212:215], v[60:63]\n"
"s_add_i32 s30, s30, s66\n"
"v_mfma_f32_16x16x32_bf16 a[212:215], a[236:239], v[212:215], a[212:215]\n"
"v_mfma_f32_16x16x32_bf16 v[192:195], a[112:115], a[64:67], v[232:235]\n"
"v_exp_f32 v200, v200\n"
"v_mfma_f32_16x16x32_bf16 v[192:195], a[116:119], a[68:71], v[192:195]\n"
"v_exp_f32 v201, v201\n"
"v_mfma_f32_16x16x32_bf16 v[192:195], a[120:123], a[72:75], v[192:195]\n"
"v_exp_f32 v202, v202\n"
"v_mfma_f32_16x16x32_bf16 v[192:195], a[124:127], a[76:79], v[192:195]\n"
"v_exp_f32 v203, v203\n"
"v_mfma_f32_16x16x32_bf16 v[196:199], a[128:131], a[64:67], v[232:235]\n"
"v_exp_f32 v204, v204\n"
"v_mfma_f32_16x16x32_bf16 v[196:199], a[132:135], a[68:71], v[196:199]\n"
"v_exp_f32 v205, v205\n"
"v_mfma_f32_16x16x32_bf16 v[196:199], a[136:139], a[72:75], v[196:199]\n"
"v_exp_f32 v206, v206\n"
"v_mfma_f32_16x16x32_bf16 v[196:199], a[140:143], a[76:79], v[196:199]\n"
"v_exp_f32 v207, v207\n"
"v_mfma_f32_16x16x32_bf16 v[64:67], a[144:147], v[208:211], v[64:67]\n"
"v_mfma_f32_16x16x32_bf16 v[68:71], a[148:151], v[208:211], v[68:71]\n"
"v_cvt_pk_bf16_f32 v212, v200, v201\n"
"v_cvt_pk_bf16_f32 v213, v202, v203\n"
"v_mfma_f32_16x16x32_bf16 v[72:75], a[152:155], v[208:211], v[72:75]\n"
"v_cvt_pk_bf16_f32 v214, v204, v205\n"
"v_cvt_pk_bf16_f32 v215, v206, v207\n"
"v_mfma_f32_16x16x32_bf16 v[76:79], a[156:159], v[208:211], v[76:79]\n"
"s_sub_i32 s66, s66, 15\n"
"v_mfma_f32_16x16x32_bf16 v[80:83], a[160:163], v[208:211], v[80:83]\n"
"s_max_i32 s32, s32, 0\n"
"v_mfma_f32_16x16x32_bf16 v[84:87], a[164:167], v[208:211], v[84:87]\n"
"s_mul_i32 s32, s32, 13\n"
"v_mfma_f32_16x16x32_bf16 v[88:91], a[168:171], v[208:211], v[88:91]\n"
"s_lshr_b32 s32, s32, 8\n"
"v_mfma_f32_16x16x32_bf16 v[92:95], a[172:175], v[208:211], v[92:95]\n"
"s_min_u32 s31, s32, 24\n"
"v_mfma_f32_16x16x32_bf16 a[216:219], a[236:239], v[208:211], a[216:219]\n"
"1:\n"
"s_sub_u32 s31, s31, 1\n"
"s_cbranch_scc1 2f\n"
"s_nop 7\n"
"s_branch 1b\n"
"2:\n"
"s_waitcnt vmcnt(16)\n"
"s_barrier\n"
"v_mfma_f32_16x16x32_bf16 v[200:203], a[112:115], a[80:83], v[236:239]\n"
"v_exp_f32 v192, v192\n"
"v_mfma_f32_16x16x32_bf16 v[200:203], a[116:119], a[84:87], v[200:203]\n"
"ds_read_b128 a[112:115], v244 offset:34304\n"
"ds_read_b128 a[116:119], v244 offset:34368\n"
"v_mfma_f32_16x16x32_bf16 v[200:203], a[120:123], a[88:91], v[200:203]\n"
"v_exp_f32 v193, v193\n"
"v_mfma_f32_16x16x32_bf16 v[200:203], a[124:127], a[92:95], v[200:203]\n"
"ds_read_b128 a[120:123], v244 offset:34432\n"
"ds_read_b128 a[124:127], v244 offset:34496\n"
"v_mfma_f32_16x16x32_bf16 v[204:207], a[128:131], a[80:83], v[236:239]\n"
"v_exp_f32 v194, v194\n"
"v_mfma_f32_16x16x32_bf16 v[204:207], a[132:135], a[84:87], v[204:207]\n"
"ds_read_b128 a[128:131], v244 offset:38656\n"
"ds_read_b128 a[132:135], v244 offset:38720\n"
"v_mfma_f32_16x16x32_bf16 v[204:207], a[136:139], a[88:91], v[204:207]\n"
"v_exp_f32 v195, v195\n"
"v_mfma_f32_16x16x32_bf16 v[204:207], a[140:143], a[92:95], v[204:207]\n"
"ds_read_b128 a[136:139], v244 offset:38784\n"
"ds_read_b128 a[140:143], v244 offset:38848\n"
"v_mfma_f32_16x16x32_bf16 v[96:99], a[144:147], v[212:215], v[96:99]\n"
"v_exp_f32 v196, v196\n"
"v_mfma_f32_16x16x32_bf16 v[100:103], a[148:151], v[212:215], v[100:103]\n"
"v_exp_f32 v197, v197\n"
"v_mfma_f32_16x16x32_bf16 v[104:107], a[152:155], v[212:215], v[104:107]\n"
"v_exp_f32 v198, v198\n"
"v_mfma_f32_16x16x32_bf16 v[108:111], a[156:159], v[212:215], v[108:111]\n"
"v_exp_f32 v199, v199\n"
"v_mfma_f32_16x16x32_bf16 v[112:115], a[160:163], v[212:215], v[112:115]\n"
"v_cvt_pk_bf16_f32 v208, v192, v193\n"
"v_cvt_pk_bf16_f32 v209, v194, v195\n"
"v_mfma_f32_16x16x32_bf16 v[116:119], a[164:167], v[212:215], v[116:119]\n"
"v_cvt_pk_bf16_f32 v210, v196, v197\n"
"v_cvt_pk_bf16_f32 v211, v198, v199\n"
"v_mfma_f32_16x16x32_bf16 v[120:123], a[168:171], v[212:215], v[120:123]\n"
"v_mfma_f32_16x16x32_bf16 v[124:127], a[172:175], v[212:215], v[124:127]\n"
"v_mfma_f32_16x16x32_bf16 a[220:223], a[236:239], v[212:215], a[220:223]\n"
"s_waitcnt lgkmcnt(7)\n"
"v_mfma_f32_16x16x32_bf16 v[192:195], a[112:115], a[0:3], v[216:219]\n"
"s_waitcnt lgkmcnt(6)\n"
"v_exp_f32 v200, v200\n"
"v_mfma_f32_16x16x32_bf16 v[192:195], a[116:119], a[4:7], v[192:195]\n"
"s_waitcnt lgkmcnt(5)\n"
"v_exp_f32 v201, v201\n"
"v_mfma_f32_16x16x32_bf16 v[192:195], a[120:123], a[8:11], v[192:195]\n"
"s_waitcnt lgkmcnt(4)\n"
"v_exp_f32 v202, v202\n"
"v_mfma_f32_16x16x32_bf16 v[192:195], a[124:127], a[12:15], v[192:195]\n"
"s_waitcnt lgkmcnt(3)\n"
"v_exp_f32 v203, v203\n"
"v_mfma_f32_16x16x32_bf16 v[196:199], a[128:131], a[0:3], v[216:219]\n"
"s_waitcnt lgkmcnt(2)\n"
"v_exp_f32 v204, v204\n"
"v_mfma_f32_16x16x32_bf16 v[196:199], a[132:135], a[4:7], v[196:199]\n"
"s_waitcnt lgkmcnt(1)\n"
"v_exp_f32 v205, v205\n"
"v_mfma_f32_16x16x32_bf16 v[196:199], a[136:139], a[8:11], v[196:199]\n"
"s_waitcnt lgkmcnt(0)\n"
"v_exp_f32 v206, v206\n"
"v_mfma_f32_16x16x32_bf16 v[196:199], a[140:143], a[12:15], v[196:199]\n"
"v_exp_f32 v207, v207\n"
"v_mfma_f32_16x16x32_bf16 v[128:131], a[144:147], v[208:211], v[128:131]\n"
"v_mfma_f32_16x16x32_bf16 v[132:135], a[148:151], v[208:211], v[132:135]\n"
"s_add_u32 m0, s50, 8704\n"
"v_cvt_pk_bf16_f32 v212, v200, v201\n"
"v_mfma_f32_16x16x32_bf16 v[136:139], a[152:155], v[208:211], v[136:139]\n"
"buffer_load_dwordx4 v250, s[36:39], s53 offen nt lds\n"
"v_mfma_f32_16x16x32_bf16 v[140:143], a[156:159], v[208:211], v[140:143]\n"
"s_add_u32 m0, s50, 9504\n"
"v_cvt_pk_bf16_f32 v213, v202, v203\n"
"v_mfma_f32_16x16x32_bf16 v[144:147], a[160:163], v[208:211], v[144:147]\n"
"buffer_load_dwordx4 v250, s[36:39], s53 offen offset:256 nt lds\n"
"v_mfma_f32_16x16x32_bf16 v[148:151], a[164:167], v[208:211], v[148:151]\n"
"s_add_u32 m0, s50, 10304\n"
"v_cvt_pk_bf16_f32 v214, v204, v205\n"
"v_mfma_f32_16x16x32_bf16 v[152:155], a[168:171], v[208:211], v[152:155]\n"
"buffer_load_dwordx4 v250, s[36:39], s53 offen offset:512 nt lds\n"
"v_mfma_f32_16x16x32_bf16 v[156:159], a[172:175], v[208:211], v[156:159]\n"
"s_add_u32 m0, s50, 11104\n"
"v_cvt_pk_bf16_f32 v215, v206, v207\n"
"v_mfma_f32_16x16x32_bf16 a[224:227], a[236:239], v[208:211], a[224:227]\n"
"buffer_load_dwordx4 v250, s[36:39], s53 offen offset:768 nt lds\n"
"v_mfma_f32_16x16x32_bf16 v[200:203], a[112:115], a[16:19], v[220:223]\n"
"v_exp_f32 v192, v192\n"
"v_mfma_f32_16x16x32_bf16 v[200:203], a[116:119], a[20:23], v[200:203]\n"
"v_exp_f32 v193, v193\n"
"v_mfma_f32_16x16x32_bf16 v[200:203], a[120:123], a[24:27], v[200:203]\n"
"v_exp_f32 v194, v194\n"
"v_mfma_f32_16x16x32_bf16 v[200:203], a[124:127], a[28:31], v[200:203]\n"
"v_exp_f32 v195, v195\n"
"v_mfma_f32_16x16x32_bf16 v[204:207], a[128:131], a[16:19], v[220:223]\n"
"v_exp_f32 v196, v196\n"
"v_mfma_f32_16x16x32_bf16 v[204:207], a[132:135], a[20:23], v[204:207]\n"
"v_exp_f32 v197, v197\n"
"v_mfma_f32_16x16x32_bf16 v[204:207], a[136:139], a[24:27], v[204:207]\n"
"v_exp_f32 v198, v198\n"
"v_mfma_f32_16x16x32_bf16 v[204:207], a[140:143], a[28:31], v[204:207]\n"
"v_exp_f32 v199, v199\n"
"v_mfma_f32_16x16x32_bf16 v[160:163], a[144:147], v[212:215], v[160:163]\n"
"ds_read_b64_tr_b16 a[144:145], v246 offset:34304\n"
"ds_read_b64_tr_b16 a[146:147], v246 offset:38528\n"
"v_mfma_f32_16x16x32_bf16 v[164:167], a[148:151], v[212:215], v[164:167]\n"
"ds_read_b64_tr_b16 a[148:149], v246 offset:34336\n"
"v_cvt_pk_bf16_f32 v208, v192, v193\n"
"v_mfma_f32_16x16x32_bf16 v[168:171], a[152:155], v[212:215], v[168:171]\n"
"ds_read_b64_tr_b16 a[150:151], v246 offset:38560\n"
"ds_read_b64_tr_b16 a[152:153], v246 offset:34368\n"
"v_mfma_f32_16x16x32_bf16 v[172:175], a[156:159], v[212:215], v[172:175]\n"
"ds_read_b64_tr_b16 a[154:155], v246 offset:38592\n"
"v_cvt_pk_bf16_f32 v209, v194, v195\n"
"v_mfma_f32_16x16x32_bf16 v[176:179], a[160:163], v[212:215], v[176:179]\n"
"ds_read_b64_tr_b16 a[156:157], v246 offset:34400\n"
"ds_read_b64_tr_b16 a[158:159], v246 offset:38624\n"
"v_mfma_f32_16x16x32_bf16 v[180:183], a[164:167], v[212:215], v[180:183]\n"
"ds_read_b64_tr_b16 a[160:161], v248 offset:34304\n"
"v_cvt_pk_bf16_f32 v210, v196, v197\n"
"v_mfma_f32_16x16x32_bf16 v[184:187], a[168:171], v[212:215], v[184:187]\n"
"ds_read_b64_tr_b16 a[162:163], v248 offset:38528\n"
"ds_read_b64_tr_b16 a[164:165], v248 offset:34336\n"
"v_mfma_f32_16x16x32_bf16 v[188:191], a[172:175], v[212:215], v[188:191]\n"
"ds_read_b64_tr_b16 a[166:167], v248 offset:38560\n"
"v_cvt_pk_bf16_f32 v211, v198, v199\n"
"v_mfma_f32_16x16x32_bf16 a[228:231], a[236:239], v[212:215], a[228:231]\n"
"ds_read_b64_tr_b16 a[168:169], v248 offset:34368\n"
"ds_read_b64_tr_b16 a[170:171], v248 offset:38592\n"
"v_mfma_f32_16x16x32_bf16 v[192:195], a[112:115], a[32:35], v[224:227]\n"
"ds_read_b64_tr_b16 a[172:173], v248 offset:34400\n"
"ds_read_b64_tr_b16 a[174:175], v248 offset:38624\n"
"v_mfma_f32_16x16x32_bf16 v[192:195], a[116:119], a[36:39], v[192:195]\n"
"v_exp_f32 v200, v200\n"
"v_mfma_f32_16x16x32_bf16 v[192:195], a[120:123], a[40:43], v[192:195]\n"
"v_exp_f32 v201, v201\n"
"v_mfma_f32_16x16x32_bf16 v[192:195], a[124:127], a[44:47], v[192:195]\n"
"v_exp_f32 v202, v202\n"
"v_mfma_f32_16x16x32_bf16 v[196:199], a[128:131], a[32:35], v[224:227]\n"
"v_exp_f32 v203, v203\n"
"v_mfma_f32_16x16x32_bf16 v[196:199], a[132:135], a[36:39], v[196:199]\n"
"v_exp_f32 v204, v204\n"
"v_mfma_f32_16x16x32_bf16 v[196:199], a[136:139], a[40:43], v[196:199]\n"
"v_exp_f32 v205, v205\n"
"v_mfma_f32_16x16x32_bf16 v[196:199], a[140:143], a[44:47], v[196:199]\n"
"s_waitcnt lgkmcnt(14)\n"
"v_exp_f32 v206, v206\n"
"v_mfma_f32_16x16x32_bf16 v[0:3], a[144:147], v[208:211], v[0:3]\n"
"s_waitcnt lgkmcnt(12)\n"
"v_exp_f32 v207, v207\n"
"v_mfma_f32_16x16x32_bf16 v[4:7], a[148:151], v[208:211], v[4:7]\n"
"s_waitcnt lgkmcnt(10)\n"
"s_add_u32 m0, s50, 12928\n"
"v_cvt_pk_bf16_f32 v212, v200, v201\n"
"v_mfma_f32_16x16x32_bf16 v[8:11], a[152:155], v[208:211], v[8:11]\n"
"s_waitcnt lgkmcnt(8)\n"
"buffer_load_dwordx4 v250, s[36:39], s54 offen nt lds\n"
"s_add_u32 s53, s53, 8192\n"
"v_mfma_f32_16x16x32_bf16 v[12:15], a[156:159], v[208:211], v[12:15]\n"
"s_waitcnt lgkmcnt(6)\n"
"s_add_u32 m0, s50, 13728\n"
"v_cvt_pk_bf16_f32 v213, v202, v203\n"
"v_mfma_f32_16x16x32_bf16 v[16:19], a[160:163], v[208:211], v[16:19]\n"
"s_waitcnt lgkmcnt(4)\n"
"buffer_load_dwordx4 v250, s[36:39], s54 offen offset:256 nt lds\n"
"s_and_b32 s53, s53, 258047\n"
"v_mfma_f32_16x16x32_bf16 v[20:23], a[164:167], v[208:211], v[20:23]\n"
"s_waitcnt lgkmcnt(2)\n"
"s_add_u32 m0, s50, 14528\n"
"v_cvt_pk_bf16_f32 v214, v204, v205\n"
"v_mfma_f32_16x16x32_bf16 v[24:27], a[168:171], v[208:211], v[24:27]\n"
"s_waitcnt lgkmcnt(0)\n"
"buffer_load_dwordx4 v250, s[36:39], s54 offen offset:512 nt lds\n"
"v_mfma_f32_16x16x32_bf16 v[28:31], a[172:175], v[208:211], v[28:31]\n"
"s_add_u32 m0, s50, 15328\n"
"v_cvt_pk_bf16_f32 v215, v206, v207\n"
"v_mfma_f32_16x16x32_bf16 a[208:211], a[236:239], v[208:211], a[208:211]\n"
"buffer_load_dwordx4 v250, s[36:39], s54 offen offset:768 nt lds\n"
"s_lshl_b32 s38, s38, 1\n"
"s_add_u32 s54, s53, 4096\n"
"v_mfma_f32_16x16x32_bf16 v[200:203], a[112:115], a[48:51], v[228:231]\n"
"v_exp_f32 v192, v192\n"
"v_mfma_f32_16x16x32_bf16 v[200:203], a[116:119], a[52:55], v[200:203]\n"
"v_exp_f32 v193, v193\n"
"v_mfma_f32_16x16x32_bf16 v[200:203], a[120:123], a[56:59], v[200:203]\n"
"s_memrealtime s[28:29]\n"
"v_exp_f32 v194, v194\n"
"v_mfma_f32_16x16x32_bf16 v[200:203], a[124:127], a[60:63], v[200:203]\n"
"v_exp_f32 v195, v195\n"
"v_mfma_f32_16x16x32_bf16 v[204:207], a[128:131], a[48:51], v[228:231]\n"
"v_exp_f32 v196, v196\n"
"v_mfma_f32_16x16x32_bf16 v[204:207], a[132:135], a[52:55], v[204:207]\n"
"v_exp_f32 v197, v197\n"
"v_mfma_f32_16x16x32_bf16 v[204:207], a[136:139], a[56:59], v[204:207]\n"
"v_exp_f32 v198, v198\n"
"v_mfma_f32_16x16x32_bf16 v[204:207], a[140:143], a[60:63], v[204:207]\n"
"v_exp_f32 v199, v199\n"
"v_mfma_f32_16x16x32_bf16 v[32:35], a[144:147], v[212:215], v[32:35]\n"
"v_mfma_f32_16x16x32_bf16 v[36:39], a[148:151], v[212:215], v[36:39]\n"
"v_cvt_pk_bf16_f32 v208, v192, v193\n"
"v_cvt_pk_bf16_f32 v209, v194, v195\n"
"v_mfma_f32_16x16x32_bf16 v[40:43], a[152:155], v[212:215], v[40:43]\n"
"v_cvt_pk_bf16_f32 v210, v196, v197\n"
"v_cvt_pk_bf16_f32 v211, v198, v199\n"
"v_mfma_f32_16x16x32_bf16 v[44:47], a[156:159], v[212:215], v[44:47]\n"
"v_mfma_f32_16x16x32_bf16 v[48:51], a[160:163], v[212:215], v[48:51]\n"
"v_mfma_f32_16x16x32_bf16 v[52:55], a[164:167], v[212:215], v[52:55]\n"
"v_mfma_f32_16x16x32_bf16 v[56:59], a[168:171], v[212:215], v[56:59]\n"
"v_mfma_f32_16x16x32_bf16 v[60:63], a[172:175], v[212:215], v[60:63]\n"
"v_mfma_f32_16x16x32_bf16 a[212:215], a[236:239], v[212:215], a[212:215]\n"
"v_mfma_f32_16x16x32_bf16 v[192:195], a[112:115], a[64:67], v[232:235]\n"
"v_exp_f32 v200, v200\n"
"v_mfma_f32_16x16x32_bf16 v[192:195], a[116:119], a[68:71], v[192:195]\n"
"v_exp_f32 v201, v201\n"
"v_mfma_f32_16x16x32_bf16 v[192:195], a[120:123], a[72:75], v[192:195]\n"
"v_exp_f32 v202, v202\n"
"v_mfma_f32_16x16x32_bf16 v[192:195], a[124:127], a[76:79], v[192:195]\n"
"v_exp_f32 v203, v203\n"
"v_mfma_f32_16x16x32_bf16 v[196:199], a[128:131], a[64:67], v[232:235]\n"
"v_exp_f32 v204, v204\n"
"v_mfma_f32_16x16x32_bf16 v[196:199], a[132:135], a[68:71], v[196:199]\n"
"v_exp_f32 v205, v205\n"
"v_mfma_f32_16x16x32_bf16 v[196:199], a[136:139], a[72:75], v[196:199]\n"
"v_exp_f32 v206, v206\n"
"v_mfma_f32_16x16x32_bf16 v[196:199], a[140:143], a[76:79], v[196:199]\n"
"v_exp_f32 v207, v207\n"
"v_mfma_f32_16x16x32_bf16 v[64:67], a[144:147], v[208:211], v[64:67]\n"
"v_mfma_f32_16x16x32_bf16 v[68:71], a[148:151], v[208:211], v[68:71]\n"
"v_cvt_pk_bf16_f32 v212, v200, v201\n"
"v_cvt_pk_bf16_f32 v213, v202, v203\n"
"v_mfma_f32_16x16x32_bf16 v[72:75], a[152:155], v[208:211], v[72:75]\n"
"v_cvt_pk_bf16_f32 v214, v204, v205\n"
"v_cvt_pk_bf16_f32 v215, v206, v207\n"
"v_mfma_f32_16x16x32_bf16 v[76:79], a[156:159], v[208:211], v[76:79]\n"
"s_lshl_b32 s33, s28, 3\n"
"v_mfma_f32_16x16x32_bf16 v[80:83], a[160:163], v[208:211], v[80:83]\n"
"s_sub_i32 s32, s30, s33\n"
"v_mfma_f32_16x16x32_bf16 v[84:87], a[164:167], v[208:211], v[84:87]\n"
"s_max_i32 s30, s32, -400\n"
"v_mfma_f32_16x16x32_bf16 v[88:91], a[168:171], v[208:211], v[88:91]\n"
"s_add_i32 s30, s30, s33\n"
"v_mfma_f32_16x16x32_bf16 v[92:95], a[172:175], v[208:211], v[92:95]\n"
"s_add_i32 s30, s30, s66\n"
"v_mfma_f32_16x16x32_bf16 a[216:219], a[236:239], v[208:211], a[216:219]\n"
"v_mfma_f32_16x16x32_bf16 v[200:203], a[112:115], a[80:83], v[236:239]\n"
"v_exp_f32 v192, v192\n"
"v_mfma_f32_16x16x32_bf16 v[200:203], a[116:119], a[84:87], v[200:203]\n"
"v_exp_f32 v193, v193\n"
"v_mfma_f32_16x16x32_bf16 v[200:203], a[120:123], a[88:91], v[200:203]\n"
"v_exp_f32 v194, v194\n"
"v_mfma_f32_16x16x32_bf16 v[200:203], a[124:127], a[92:95], v[200:203]\n"
"v_exp_f32 v195, v195\n"
"v_mfma_f32_16x16x32_bf16 v[204:207], a[128:131], a[80:83], v[236:239]\n"
"v_exp_f32 v196, v196\n"
"v_mfma_f32_16x16x32_bf16 v[204:207], a[132:135], a[84:87], v[204:207]\n"
"v_exp_f32 v197, v197\n"
"v_mfma_f32_16x16x32_bf16 v[204:207], a[136:139], a[88:91], v[204:207]\n"
"v_exp_f32 v198, v198\n"
"v_mfma_f32_16x16x32_bf16 v[204:207], a[140:143], a[92:95], v[204:207]\n"
"v_exp_f32 v199, v199\n"
"v_mfma_f32_16x16x32_bf16 v[96:99], a[144:147], v[212:215], v[96:99]\n"
"v_mfma_f32_16x16x32_bf16 v[100:103], a[148:151], v[212:215], v[100:103]\n"
"v_cvt_pk_bf16_f32 v208, v192, v193\n"
"v_cvt_pk_bf16_f32 v209, v194, v195\n"
"v_mfma_f32_16x16x32_bf16 v[104:107], a[152:155], v[212:215], v[104:107]\n"
"v_cvt_pk_bf16_f32 v210, v196, v197\n"
"v_cvt_pk_bf16_f32 v211, v198, v199\n"
"v_mfma_f32_16x16x32_bf16 v[108:111], a[156:159], v[212:215], v[108:111]\n"
"s_sub_i32 s66, s66, 15\n"
"v_mfma_f32_16x16x32_bf16 v[112:115], a[160:163], v[212:215], v[112:115]\n"
"s_max_i32 s32, s32, 0\n"
"v_mfma_f32_16x16x32_bf16 v[116:119], a[164:167], v[212:215], v[116:119]\n"
"s_mul_i32 s32, s32, 13\n"
"v_mfma_f32_16x16x32_bf16 v[120:123], a[168:171], v[212:215], v[120:123]\n"
"s_lshr_b32 s32, s32, 8\n"
"v_mfma_f32_16x16x32_bf16 v[124:127], a[172:175], v[212:215], v[124:127]\n"
"s_min_u32 s31, s32, 24\n"
"v_mfma_f32_16x16x32_bf16 a[220:223], a[236:239], v[212:215], a[220:223]\n"
"1:\n"
"s_sub_u32 s31, s31, 1\n"
"s_cbranch_scc1 2f\n"
"s_nop 7\n"
"s_branch 1b\n"
"2:\n"
"s_waitcnt vmcnt(16)\n"
"s_barrier\n"
"s_cmp_lg_u32 s55, 0\n"
"s_cbranch_scc1 L1_npf_h1\n"
"buffer_load_dwordx4 a[240:243], v251, s[60:63], 0 offen\n"
"buffer_load_dwordx4 a[240:243], v251, s[60:63], 0 offen offset:2048\n"
"buffer_load_dwordx4 a[240:243], v254, s[60:63], 0 offen\n"
"buffer_load_dwordx4 a[240:243], v254, s[60:63], 0 offen offset:2048\n"
"global_load_dword a240, v255, s[100:101]\n"
"L1_npf_h1:\n"
"v_mfma_f32_16x16x32_bf16 v[192:195], a[112:115], a[96:99], v[240:243]\n"
"v_exp_f32 v200, v200\n"
"v_mfma_f32_16x16x32_bf16 v[192:195], a[116:119], a[100:103], v[192:195]\n"
"ds_read_b128 a[112:115], v245\n"
"ds_read_b128 a[116:119], v245 offset:64\n"
"v_mfma_f32_16x16x32_bf16 v[192:195], a[120:123], a[104:107], v[192:195]\n"
"v_exp_f32 v201, v201\n"
"v_mfma_f32_16x16x32_bf16 v[192:195], a[124:127], a[108:111], v[192:195]\n"
"ds_read_b128 a[120:123], v245 offset:128\n"
"ds_read_b128 a[124:127], v245 offset:192\n"
"v_mfma_f32_16x16x32_bf16 v[196:199], a[128:131], a[96:99], v[240:243]\n"
"v_exp_f32 v202, v202\n"
"v_mfma_f32_16x16x32_bf16 v[196:199], a[132:135], a[100:103], v[196:199]\n"
"ds_read_b128 a[128:131], v245 offset:4352\n"
"ds_read_b128 a[132:135], v245 offset:4416\n"
"v_mfma_f32_16x16x32_bf16 v[196:199], a[136:139], a[104:107], v[196:199]\n"
"v_exp_f32 v203, v203\n"
"v_mfma_f32_16x16x32_bf16 v[196:199], a[140:143], a[108:111], v[196:199]\n"
"ds_read_b128 a[136:139], v245 offset:4480\n"
"ds_read_b128 a[140:143], v245 offset:4544\n"
"v_mfma_f32_16x16x32_bf16 v[128:131], a[144:147], v[208:211], v[128:131]\n"
"v_exp_f32 v204, v204\n"
"v_mfma_f32_16x16x32_bf16 v[132:135], a[148:151], v[208:211], v[132:135]\n"
"v_exp_f32 v205, v205\n"
"v_mfma_f32_16x16x32_bf16 v[136:139], a[152:155], v[208:211], v[136:139]\n"
"v_exp_f32 v206, v206\n"
"v_mfma_f32_16x16x32_bf16 v[140:143], a[156:159], v[208:211], v[140:143]\n"
"v_exp_f32 v207, v207\n"
"v_mfma_f32_16x16x32_bf16 v[144:147], a[160:163], v[208:211], v[144:147]\n"
"v_cvt_pk_bf16_f32 v212, v200, v201\n"
"v_cvt_pk_bf16_f32 v213, v202, v203\n"
"v_mfma_f32_16x16x32_bf16 v[148:151], a[164:167], v[208:211], v[148:151]\n"
"v_cvt_pk_bf16_f32 v214, v204, v205\n"
"v_cvt_pk_bf16_f32 v215, v206, v207\n"
"v_mfma_f32_16x16x32_bf16 v[152:155], a[168:171], v[208:211], v[152:155]\n"
"v_mfma_f32_16x16x32_bf16 v[156:159], a[172:175], v[208:211], v[156:159]\n"
"v_mfma_f32_16x16x32_bf16 a[224:227], a[236:239], v[208:211], a[224:227]\n"
"s_waitcnt lgkmcnt(7)\n"
"v_mfma_f32_16x16x32_bf16 v[200:203], a[112:115], a[0:3], v[216:219]\n"
"s_waitcnt lgkmcnt(6)\n"
"v_exp_f32 v192, v192\n"
"v_mfma_f32_16x16x32_bf16 v[200:203], a[116:119], a[4:7], v[200:203]\n"
"s_waitcnt lgkmcnt(5)\n"
"v_exp_f32 v193, v193\n"
"v_mfma_f32_16x16x32_bf16 v[200:203], a[120:123], a[8:11], v[200:203]\n"
"s_waitcnt lgkmcnt(4)\n"
"v_exp_f32 v194, v194\n"
"v_mfma_f32_16x16x32_bf16 v[200:203], a[124:127], a[12:15], v[200:203]\n"
"s_waitcnt lgkmcnt(3)\n"
"v_exp_f32 v195, v195\n"
"v_mfma_f32_16x16x32_bf16 v[204:207], a[128:131], a[0:3], v[216:219]\n"
"s_waitcnt lgkmcnt(2)\n"
"v_exp_f32 v196, v196\n"
"v_mfma_f32_16x16x32_bf16 v[204:207], a[132:135], a[4:7], v[204:207]\n"
"s_waitcnt lgkmcnt(1)\n"
"v_exp_f32 v197, v197\n"
"v_mfma_f32_16x16x32_bf16 v[204:207], a[136:139], a[8:11], v[204:207]\n"
"s_waitcnt lgkmcnt(0)\n"
"v_exp_f32 v198, v198\n"
"v_mfma_f32_16x16x32_bf16 v[204:207], a[140:143], a[12:15], v[204:207]\n"
"v_exp_f32 v199, v199\n"
"v_mfma_f32_16x16x32_bf16 v[160:163], a[144:147], v[212:215], v[160:163]\n"
"v_mfma_f32_16x16x32_bf16 v[164:167], a[148:151], v[212:215], v[164:167]\n"
"s_add_u32 m0, s50, 43008\n"
"v_cvt_pk_bf16_f32 v208, v192, v193\n"
"v_mfma_f32_16x16x32_bf16 v[168:171], a[152:155], v[212:215], v[168:171]\n"
"buffer_load_dwordx4 v250, s[36:39], s53 offen nt lds\n"
"v_mfma_f32_16x16x32_bf16 v[172:175], a[156:159], v[212:215], v[172:175]\n"
"s_add_u32 m0, s50, 43808\n"
"v_cvt_pk_bf16_f32 v209, v194, v195\n"
"v_mfma_f32_16x16x32_bf16 v[176:179], a[160:163], v[212:215], v[176:179]\n"
"buffer_load_dwordx4 v250, s[36:39], s53 offen offset:256 nt lds\n"
"v_mfma_f32_16x16x32_bf16 v[180:183], a[164:167], v[212:215], v[180:183]\n"
"s_add_u32 m0, s50, 44608\n"
"v_cvt_pk_bf16_f32 v210, v196, v197\n"
"v_mfma_f32_16x16x32_bf16 v[184:187], a[168:171], v[212:215], v[184:187]\n"
"buffer_load_dwordx4 v250, s[36:39], s53 offen offset:512 nt lds\n"
"v_mfma_f32_16x16x32_bf16 v[188:191], a[172:175], v[212:215], v[188:191]\n"
"s_add_u32 m0, s50, 45408\n"
"v_cvt_pk_bf16_f32 v211, v198, v199\n"
"v_mfma_f32_16x16x32_bf16 a[228:231], a[236:239], v[212:215], a[228:231]\n"
"buffer_load_dwordx4 v250, s[36:39], s53 offen offset:768 nt lds\n"
"v_mfma_f32_16x16x32_bf16 v[192:195], a[112:115], a[16:19], v[220:223]\n"
"v_exp_f32 v200, v200\n"
"v_mfma_f32_16x16x32_bf16 v[192:195], a[116:119], a[20:23], v[192:195]\n"
"v_exp_f32 v201, v201\n"
"v_mfma_f32_16x16x32_bf16 v[192:195], a[120:123], a[24:27], v[192:195]\n"
"v_exp_f32 v202, v202\n"
"v_mfma_f32_16x16x32_bf16 v[192:195], a[124:127], a[28:31], v[192:195]\n"
"v_exp_f32 v203, v203\n"
"v_mfma_f32_16x16x32_bf16 v[196:199], a[128:131], a[16:19], v[220:223]\n"
"v_exp_f32 v204, v204\n"
"v_mfma_f32_16x16x32_bf16 v[196:199], a[132:135], a[20:23], v[196:199]\n"
"v_exp_f32 v205, v205\n"
"v_mfma_f32_16x16x32_bf16 v[196:199], a[136:139], a[24:27], v[196:199]\n"
"v_exp_f32 v206, v206\n"
"v_mfma_f32_16x16x32_bf16 v[196:199], a[140:143], a[28:31], v[196:199]\n"
"v_exp_f32 v207, v207\n"
"v_mfma_f32_16x16x32_bf16 a[176:179], a[144:147], v[208:211], a[176:179]\n"
"ds_read_b64_tr_b16 a[144:145], v247\n"
"ds_read_b64_tr_b16 a[146:147], v247 offset:4224\n"
"v_mfma_f32_16x16x32_bf16 a[180:183], a[148:151], v[208:211], a[180:183]\n"
"ds_read_b64_tr_b16 a[148:149], v247 offset:32\n"
"v_cvt_pk_bf16_f32 v212, v200, v201\n"
"v_mfma_f32_16x16x32_bf16 a[184:187], a[152:155], v[208:211], a[184:187]\n"
"ds_read_b64_tr_b16 a[150:151], v247 offset:4256\n"
"ds_read_b64_tr_b16 a[152:153], v247 offset:64\n"
"v_mfma_f32_16x16x32_bf16 a[188:191], a[156:159], v[208:211], a[188:191]\n"
"ds_read_b64_tr_b16 a[154:155], v247 offset:4288\n"
"v_cvt_pk_bf16_f32 v213, v202, v203\n"
"v_mfma_f32_16x16x32_bf16 a[192:195], a[160:163], v[208:211], a[192:195]\n"
"ds_read_b64_tr_b16 a[156:157], v247 offset:96\n"
"ds_read_b64_tr_b16 a[158:159], v247 offset:4320\n"
"v_mfma_f32_16x16x32_bf16 a[196:199], a[164:167], v[208:211], a[196:199]\n"
"ds_read_b64_tr_b16 a[160:161], v249\n"
"v_cvt_pk_bf16_f32 v214, v204, v205\n"
"v_mfma_f32_16x16x32_bf16 a[200:203], a[168:171], v[208:211], a[200:203]\n"
"ds_read_b64_tr_b16 a[162:163], v249 offset:4224\n"
"ds_read_b64_tr_b16 a[164:165], v249 offset:32\n"
"v_mfma_f32_16x16x32_bf16 a[204:207], a[172:175], v[208:211], a[204:207]\n"
"ds_read_b64_tr_b16 a[166:167], v249 offset:4256\n"
"v_cvt_pk_bf16_f32 v215, v206, v207\n"
"v_mfma_f32_16x16x32_bf16 a[232:235], a[236:239], v[208:211], a[232:235]\n"
"ds_read_b64_tr_b16 a[168:169], v249 offset:64\n"
"ds_read_b64_tr_b16 a[170:171], v249 offset:4288\n"
"v_mfma_f32_16x16x32_bf16 v[200:203], a[112:115], a[32:35], v[224:227]\n"
"ds_read_b64_tr_b16 a[172:173], v249 offset:96\n"
"ds_read_b64_tr_b16 a[174:175], v249 offset:4320\n"
"v_mfma_f32_16x16x32_bf16 v[200:203], a[116:119], a[36:39], v[200:203]\n"
"v_exp_f32 v192, v192\n"
"v_mfma_f32_16x16x32_bf16 v[200:203], a[120:123], a[40:43], v[200:203]\n"
"s_memrealtime s[28:29]\n"
"v_exp_f32 v193, v193\n"
"v_mfma_f32_16x16x32_bf16 v[200:203], a[124:127], a[44:47], v[200:203]\n"
"v_exp_f32 v194, v194\n"
"v_mfma_f32_16x16x32_bf16 v[204:207], a[128:131], a[32:35], v[224:227]\n"
"v_exp_f32 v195, v195\n"
"v_mfma_f32_16x16x32_bf16 v[204:207], a[132:135], a[36:39], v[204:207]\n"
"v_exp_f32 v196, v196\n"
"v_mfma_f32_16x16x32_bf16 v[204:207], a[136:139], a[40:43], v[204:207]\n"
"v_exp_f32 v197, v197\n"
"v_mfma_f32_16x16x32_bf16 v[204:207], a[140:143], a[44:47], v[204:207]\n"
"s_waitcnt lgkmcnt(14)\n"
"v_exp_f32 v198, v198\n"
"v_mfma_f32_16x16x32_bf16 v[0:3], a[144:147], v[212:215], v[0:3]\n"
"s_waitcnt lgkmcnt(12)\n"
"v_exp_f32 v199, v199\n"
"v_mfma_f32_16x16x32_bf16 v[4:7], a[148:151], v[212:215], v[4:7]\n"
"s_waitcnt lgkmcnt(10)\n"
"s_add_u32 m0, s50, 47232\n"
"v_cvt_pk_bf16_f32 v208, v192, v193\n"
"v_mfma_f32_16x16x32_bf16 v[8:11], a[152:155], v[212:215], v[8:11]\n"
"s_waitcnt lgkmcnt(8)\n"
"buffer_load_dwordx4 v250, s[36:39], s54 offen nt lds\n"
"s_add_u32 s53, s53, 8192\n"
"v_mfma_f32_16x16x32_bf16 v[12:15], a[156:159], v[212:215], v[12:15]\n"
"s_waitcnt lgkmcnt(6)\n"
"s_add_u32 m0, s50, 48032\n"
"v_cvt_pk_bf16_f32 v209, v194, v195\n"
"v_mfma_f32_16x16x32_bf16 v[16:19], a[160:163], v[212:215], v[16:19]\n"
"s_waitcnt lgkmcnt(4)\n"
"buffer_load_dwordx4 v250, s[36:39], s54 offen offset:256 nt lds\n"
"s_and_b32 s53, s53, 258047\n"
"v_mfma_f32_16x16x32_bf16 v[20:23], a[164:167], v[212:215], v[20:23]\n"
"s_waitcnt lgkmcnt(2)\n"
"s_add_u32 m0, s50, 48832\n"
"v_cvt_pk_bf16_f32 v210, v196, v197\n"
"v_mfma_f32_16x16x32_bf16 v[24:27], a[168:171], v[212:215], v[24:27]\n"
"s_waitcnt lgkmcnt(0)\n"
"buffer_load_dwordx4 v250, s[36:39], s54 offen offset:512 nt lds\n"
"v_mfma_f32_16x16x32_bf16 v[28:31], a[172:175], v[212:215], v[28:31]\n"
"s_add_u32 m0, s50, 49632\n"
"v_cvt_pk_bf16_f32 v211, v198, v199\n"
"v_mfma_f32_16x16x32_bf16 a[208:211], a[236:239], v[212:215], a[208:211]\n"
"buffer_load_dwordx4 v250, s[36:39], s54 offen offset:768 nt lds\n"
"s_lshl_b32 s38, s38, 1\n"
"s_add_u32 s54, s53, 4096\n"
"v_mfma_f32_16x16x32_bf16 v[192:195], a[112:115], a[48:51], v[228:231]\n"
"v_exp_f32 v200, v200\n"
"v_mfma_f32_16x16x32_bf16 v[192:195], a[116:119], a[52:55], v[192:195]\n"
"v_exp_f32 v201, v201\n"
"v_mfma_f32_16x16x32_bf16 v[192:195], a[120:123], a[56:59], v[192:195]\n"
"v_exp_f32 v202, v202\n"
"v_mfma_f32_16x16x32_bf16 v[192:195], a[124:127], a[60:63], v[192:195]\n"
"v_exp_f32 v203, v203\n"
"v_mfma_f32_16x16x32_bf16 v[196:199], a[128:131], a[48:51], v[228:231]\n"
"v_exp_f32 v204, v204\n"
"v_mfma_f32_16x16x32_bf16 v[196:199], a[132:135], a[52:55], v[196:199]\n"
"v_exp_f32 v205, v205\n"
"v_mfma_f32_16x16x32_bf16 v[196:199], a[136:139], a[56:59], v[196:199]\n"
"v_exp_f32 v206, v206\n"
"v_mfma_f32_16x16x32_bf16 v[196:199], a[140:143], a[60:63], v[196:199]\n"
"v_exp_f32 v207, v207\n"
"v_mfma_f32_16x16x32_bf16 v[32:35], a[144:147], v[208:211], v[32:35]\n"
"v_mfma_f32_16x16x32_bf16 v[36:39], a[148:151], v[208:211], v[36:39]\n"
"v_cvt_pk_bf16_f32 v212, v200, v201\n"
"v_cvt_pk_bf16_f32 v213, v202, v203\n"
"v_mfma_f32_16x16x32_bf16 v[40:43], a[152:155], v[208:211], v[40:43]\n"
"v_cvt_pk_bf16_f32 v214, v204, v205\n"
"v_cvt_pk_bf16_f32 v215, v206, v207\n"
"v_mfma_f32_16x16x32_bf16 v[44:47], a[156:159], v[208:211], v[44:47]\n"
"s_lshl_b32 s33, s28, 3\n"
"v_mfma_f32_16x16x32_bf16 v[48:51], a[160:163], v[208:211], v[48:51]\n"
"s_sub_i32 s32, s30, s33\n"
"v_mfma_f32_16x16x32_bf16 v[52:55], a[164:167], v[208:211], v[52:55]\n"
"s_max_i32 s30, s32, -400\n"
"v_mfma_f32_16x16x32_bf16 v[56:59], a[168:171], v[208:211], v[56:59]\n"
"s_add_i32 s30, s30, s33\n"
"v_mfma_f32_16x16x32_bf16 v[60:63], a[172:175], v[208:211], v[60:63]\n"
"s_add_i32 s30, s30, s66\n"
"v_mfma_f32_16x16x32_bf16 a[212:215], a[236:239], v[208:211], a[212:215]\n"
"s_cmp_lg_u32 s55, 0\n"
"s_cbranch_scc1 L1_npfb_h1\n"
"buffer_load_dwordx4 a[240:243], v251, s[60:63], 0 offen offset:1024\n"
"buffer_load_dwordx4 a[240:243], v251, s[60:63], 0 offen offset:3072\n"
"buffer_load_dwordx4 a[240:243], v254, s[60:63], 0 offen offset:1024\n"
"buffer_load_dwordx4 a[240:243], v254, s[60:63], 0 offen offset:3072\n"
"L1_npfb_h1:\n"
"v_mfma_f32_16x16x32_bf16 v[200:203], a[112:115], a[64:67], v[232:235]\n"
"v_exp_f32 v192, v192\n"
"v_mfma_f32_16x16x32_bf16 v[200:203], a[116:119], a[68:71], v[200:203]\n"
"v_exp_f32 v193, v193\n"
"v_mfma_f32_16x16x32_bf16 v[200:203], a[120:123], a[72:75], v[200:203]\n"
"v_exp_f32 v194, v194\n"
"v_mfma_f32_16x16x32_bf16 v[200:203], a[124:127], a[76:79], v[200:203]\n"
"v_exp_f32 v195, v195\n"
"v_mfma_f32_16x16x32_bf16 v[204:207], a[128:131], a[64:67], v[232:235]\n"
"v_exp_f32 v196, v196\n"
"v_mfma_f32_16x16x32_bf16 v[204:207], a[132:135], a[68:71], v[204:207]\n"
"v_exp_f32 v197, v197\n"
"v_mfma_f32_16x16x32_bf16 v[204:207], a[136:139], a[72:75], v[204:207]\n"
"v_exp_f32 v198, v198\n"
"v_mfma_f32_16x16x32_bf16 v[204:207], a[140:143], a[76:79], v[204:207]\n"
"v_exp_f32 v199, v199\n"
"v_mfma_f32_16x16x32_bf16 v[64:67], a[144:147], v[212:215], v[64:67]\n"
"v_mfma_f32_16x16x32_bf16 v[68:71], a[148:151], v[212:215], v[68:71]\n"
"v_cvt_pk_bf16_f32 v208, v192, v193\n"
"v_cvt_pk_bf16_f32 v209, v194, v195\n"
"v_mfma_f32_16x16x32_bf16 v[72:75], a[152:155], v[212:215], v[72:75]\n"
"v_cvt_pk_bf16_f32 v210, v196, v197\n"
"v_cvt_pk_bf16_f32 v211, v198, v199\n"
"v_mfma_f32_16x16x32_bf16 v[76:79], a[156:159], v[212:215], v[76:79]\n"
"s_sub_i32 s66, s66, 15\n"
"v_mfma_f32_16x16x32_bf16 v[80:83], a[160:163], v[212:215], v[80:83]\n"
"s_max_i32 s32, s32, 0\n"
"v_mfma_f32_16x16x32_bf16 v[84:87], a[164:167], v[212:215], v[84:87]\n"
"s_mul_i32 s32, s32, 13\n"
"v_mfma_f32_16x16x32_bf16 v[88:91], a[168:171], v[212:215], v[88:91]\n"
"s_lshr_b32 s32, s32, 8\n"
"v_mfma_f32_16x16x32_bf16 v[92:95], a[172:175], v[212:215], v[92:95]\n"
"s_min_u32 s31, s32, 24\n"
"v_mfma_f32_16x16x32_bf16 a[216:219], a[236:239], v[212:215], a[216:219]\n"
"1:\n"
"s_sub_u32 s31, s31, 1\n"
"s_cbranch_scc1 2f\n"
"s_nop 7\n"
"s_branch 1b\n"
"2:\n"
"s_waitcnt vmcnt(16)\n"
"s_barrier\n"
"v_mfma_f32_16x16x32_bf16 v[192:195], a[112:115], a[80:83], v[236:239]\n"
"v_exp_f32 v200, v200\n"
"v_mfma_f32_16x16x32_bf16 v[192:195], a[116:119], a[84:87], v[192:195]\n"
"ds_read_b128 a[112:115], v245 offset:34304\n"
"ds_read_b128 a[116:119], v245 offset:34368\n"
"v_mfma_f32_16x16x32_bf16 v[192:195], a[120:123], a[88:91], v[192:195]\n"
"v_exp_f32 v201, v201\n"
"v_mfma_f32_16x16x32_bf16 v[192:195], a[124:127], a[92:95], v[192:195]\n"
"ds_read_b128 a[120:123], v245 offset:34432\n"
"ds_read_b128 a[124:127], v245 offset:34496\n"
"v_mfma_f32_16x16x32_bf16 v[196:199], a[128:131], a[80:83], v[236:239]\n"
"v_exp_f32 v202, v202\n"
"v_mfma_f32_16x16x32_bf16 v[196:199], a[132:135], a[84:87], v[196:199]\n"
"ds_read_b128 a[128:131], v245 offset:38656\n"
"ds_read_b128 a[132:135], v245 offset:38720\n"
"v_mfma_f32_16x16x32_bf16 v[196:199], a[136:139], a[88:91], v[196:199]\n"
"v_exp_f32 v203, v203\n"
"v_mfma_f32_16x16x32_bf16 v[196:199], a[140:143], a[92:95], v[196:199]\n"
"ds_read_b128 a[136:139], v245 offset:38784\n"
"ds_read_b128 a[140:143], v245 offset:38848\n"
"v_mfma_f32_16x16x32_bf16 v[96:99], a[144:147], v[208:211], v[96:99]\n"
"v_exp_f32 v204, v204\n"
"v_mfma_f32_16x16x32_bf16 v[100:103], a[148:151], v[208:211], v[100:103]\n"
"v_exp_f32 v205, v205\n"
"v_mfma_f32_16x16x32_bf16 v[104:107], a[152:155], v[208:211], v[104:107]\n"
"v_exp_f32 v206, v206\n"
"v_mfma_f32_16x16x32_bf16 v[108:111], a[156:159], v[208:211], v[108:111]\n"
"v_exp_f32 v207, v207\n"
"v_mfma_f32_16x16x32_bf16 v[112:115], a[160:163], v[208:211], v[112:115]\n"
"v_cvt_pk_bf16_f32 v212, v200, v201\n"
"v_cvt_pk_bf16_f32 v213, v202, v203\n"
"v_mfma_f32_16x16x32_bf16 v[116:119], a[164:167], v[208:211], v[116:119]\n"
"v_cvt_pk_bf16_f32 v214, v204, v205\n"
"v_cvt_pk_bf16_f32 v215, v206, v207\n"
"v_mfma_f32_16x16x32_bf16 v[120:123], a[168:171], v[208:211], v[120:123]\n"
"v_mfma_f32_16x16x32_bf16 v[124:127], a[172:175], v[208:211], v[124:127]\n"
"v_mfma_f32_16x16x32_bf16 a[220:223], a[236:239], v[208:211], a[220:223]\n"
"s_waitcnt lgkmcnt(7)\n"
"v_mfma_f32_16x16x32_bf16 v[200:203], a[112:115], a[0:3], v[216:219]\n"
"s_waitcnt lgkmcnt(6)\n"
"v_exp_f32 v192, v192\n"
"v_mfma_f32_16x16x32_bf16 v[200:203], a[116:119], a[4:7], v[200:203]\n"
"s_waitcnt lgkmcnt(5)\n"
"v_exp_f32 v193, v193\n"
"v_mfma_f32_16x16x32_bf16 v[200:203], a[120:123], a[8:11], v[200:203]\n"
"s_waitcnt lgkmcnt(4)\n"
"v_exp_f32 v194, v194\n"
"v_mfma_f32_16x16x32_bf16 v[200:203], a[124:127], a[12:15], v[200:203]\n"
"s_waitcnt lgkmcnt(3)\n"
"v_exp_f32 v195, v195\n"
"v_mfma_f32_16x16x32_bf16 v[204:207], a[128:131], a[0:3], v[216:219]\n"
"s_waitcnt lgkmcnt(2)\n"
"v_exp_f32 v196, v196\n"
"v_mfma_f32_16x16x32_bf16 v[204:207], a[132:135], a[4:7], v[204:207]\n"
"s_waitcnt lgkmcnt(1)\n"
"v_exp_f32 v197, v197\n"
"v_mfma_f32_16x16x32_bf16 v[204:207], a[136:139], a[8:11], v[204:207]\n"
"s_waitcnt lgkmcnt(0)\n"
"v_exp_f32 v198, v198\n"
"v_mfma_f32_16x16x32_bf16 v[204:207], a[140:143], a[12:15], v[204:207]\n"
"v_exp_f32 v199, v199\n"
"v_mfma_f32_16x16x32_bf16 v[128:131], a[144:147], v[212:215], v[128:131]\n"
"v_mfma_f32_16x16x32_bf16 v[132:135], a[148:151], v[212:215], v[132:135]\n"
"s_add_u32 m0, s50, 77312\n"
"v_cvt_pk_bf16_f32 v208, v192, v193\n"
"v_mfma_f32_16x16x32_bf16 v[136:139], a[152:155], v[212:215], v[136:139]\n"
"buffer_load_dwordx4 v250, s[36:39], s53 offen nt lds\n"
"v_mfma_f32_16x16x32_bf16 v[140:143], a[156:159], v[212:215], v[140:143]\n"
"s_add_u32 m0, s50, 78112\n"
"v_cvt_pk_bf16_f32 v209, v194, v195\n"
"v_mfma_f32_16x16x32_bf16 v[144:147], a[160:163], v[212:215], v[144:147]\n"
"buffer_load_dwordx4 v250, s[36:39], s53 offen offset:256 nt lds\n"
"v_mfma_f32_16x16x32_bf16 v[148:151], a[164:167], v[212:215], v[148:151]\n"
"s_add_u32 m0, s50, 78912\n"
"v_cvt_pk_bf16_f32 v210, v196, v197\n"
"v_mfma_f32_16x16x32_bf16 v[152:155], a[168:171], v[212:215], v[152:155]\n"
"buffer_load_dwordx4 v250, s[36:39], s53 offen offset:512 nt lds\n"
"v_mfma_f32_16x16x32_bf16 v[156:159], a[172:175], v[212:215], v[156:159]\n"
"s_add_u32 m0, s50, 79712\n"
"v_cvt_pk_bf16_f32 v211, v198, v199\n"
"v_mfma_f32_16x16x32_bf16 a[224:227], a[236:239], v[212:215], a[224:227]\n"
"buffer_load_dwordx4 v250, s[36:39], s53 offen offset:768 nt lds\n"
"v_mfma_f32_16x16x32_bf16 v[192:195], a[112:115], a[16:19], v[220:223]\n"
"v_exp_f32 v200, v200\n"
"v_mfma_f32_16x16x32_bf16 v[192:195], a[116:119], a[20:23], v[192:195]\n"
"v_exp_f32 v201, v201\n"
"v_mfma_f32_16x16x32_bf16 v[192:195], a[120:123], a[24:27], v[192:195]\n"
"v_exp_f32 v202, v202\n"
"v_mfma_f32_16x16x32_bf16 v[192:195], a[124:127], a[28:31], v[192:195]\n"
"v_exp_f32 v203, v203\n"
"v_mfma_f32_16x16x32_bf16 v[196:199], a[128:131], a[16:19], v[220:223]\n"
"v_exp_f32 v204, v204\n"
"v_mfma_f32_16x16x32_bf16 v[196:199], a[132:135], a[20:23], v[196:199]\n"
"v_exp_f32 v205, v205\n"
"v_mfma_f32_16x16x32_bf16 v[196:199], a[136:139], a[24:27], v[196:199]\n"
"v_exp_f32 v206, v206\n"
"v_mfma_f32_16x16x32_bf16 v[196:199], a[140:143], a[28:31], v[196:199]\n"
"v_exp_f32 v207, v207\n"
"v_mfma_f32_16x16x32_bf16 v[160:163], a[144:147], v[208:211], v[160:163]\n"
"ds_read_b64_tr_b16 a[144:145], v247 offset:34304\n"
"ds_read_b64_tr_b16 a[146:147], v247 offset:38528\n"
"v_mfma_f32_16x16x32_bf16 v[164:167], a[148:151], v[208:211], v[164:167]\n"
"ds_read_b64_tr_b16 a[148:149], v247 offset:34336\n"
"v_cvt_pk_bf16_f32 v212, v200, v201\n"
"v_mfma_f32_16x16x32_bf16 v[168:171], a[152:155], v[208:211], v[168:171]\n"
"ds_read_b64_tr_b16 a[150:151], v247 offset:38560\n"
"ds_read_b64_tr_b16 a[152:153], v247 offset:34368\n"
"v_mfma_f32_16x16x32_bf16 v[172:175], a[156:159], v[208:211], v[172:175]\n"
"ds_read_b64_tr_b16 a[154:155], v247 offset:38592\n"
"v_cvt_pk_bf16_f32 v213, v202, v203\n"
"v_mfma_f32_16x16x32_bf16 v[176:179], a[160:163], v[208:211], v[176:179]\n"
"ds_read_b64_tr_b16 a[156:157], v247 offset:34400\n"
"ds_read_b64_tr_b16 a[158:159], v247 offset:38624\n"
"v_mfma_f32_16x16x32_bf16 v[180:183], a[164:167], v[208:211], v[180:183]\n"
"ds_read_b64_tr_b16 a[160:161], v249 offset:34304\n"
"v_cvt_pk_bf16_f32 v214, v204, v205\n"
"v_mfma_f32_16x16x32_bf16 v[184:187], a[168:171], v[208:211], v[184:187]\n"
"ds_read_b64_tr_b16 a[162:163], v249 offset:38528\n"
"ds_read_b64_tr_b16 a[164:165], v249 offset:34336\n"
"v_mfma_f32_16x16x32_bf16 v[188:191], a[172:175], v[208:211], v[188:191]\n"
"ds_read_b64_tr_b16 a[166:167], v249 offset:38560\n"
"v_cvt_pk_bf16_f32 v215, v206, v207\n"
"v_mfma_f32_16x16x32_bf16 a[228:231], a[236:239], v[208:211], a[228:231]\n"
"ds_read_b64_tr_b16 a[168:169], v249 offset:34368\n"
"ds_read_b64_tr_b16 a[170:171], v249 offset:38592\n"
"v_mfma_f32_16x16x32_bf16 v[200:203], a[112:115], a[32:35], v[224:227]\n"
"ds_read_b64_tr_b16 a[172:173], v249 offset:34400\n"
"ds_read_b64_tr_b16 a[174:175], v249 offset:38624\n"
"v_mfma_f32_16x16x32_bf16 v[200:203], a[116:119], a[36:39], v[200:203]\n"
"v_exp_f32 v192, v192\n"
"v_mfma_f32_16x16x32_bf16 v[200:203], a[120:123], a[40:43], v[200:203]\n"
"v_exp_f32 v193, v193\n"
"v_mfma_f32_16x16x32_bf16 v[200:203], a[124:127], a[44:47], v[200:203]\n"
"v_exp_f32 v194, v194\n"
"v_mfma_f32_16x16x32_bf16 v[204:207], a[128:131], a[32:35], v[224:227]\n"
"v_exp_f32 v195, v195\n"
"v_mfma_f32_16x16x32_bf16 v[204:207], a[132:135], a[36:39], v[204:207]\n"
"v_exp_f32 v196, v196\n"
"v_mfma_f32_16x16x32_bf16 v[204:207], a[136:139], a[40:43], v[204:207]\n"
"v_exp_f32 v197, v197\n"
"v_mfma_f32_16x16x32_bf16 v[204:207], a[140:143], a[44:47], v[204:207]\n"
"s_waitcnt lgkmcnt(14)\n"
"v_exp_f32 v198, v198\n"
"v_mfma_f32_16x16x32_bf16 v[0:3], a[144:147], v[212:215], v[0:3]\n"
"s_waitcnt lgkmcnt(12)\n"
"v_exp_f32 v199, v199\n"
"v_mfma_f32_16x16x32_bf16 v[4:7], a[148:151], v[212:215], v[4:7]\n"
"s_waitcnt lgkmcnt(10)\n"
"s_add_u32 m0, s50, 81536\n"
"v_cvt_pk_bf16_f32 v208, v192, v193\n"
"v_mfma_f32_16x16x32_bf16 v[8:11], a[152:155], v[212:215], v[8:11]\n"
"s_waitcnt lgkmcnt(8)\n"
"buffer_load_dwordx4 v250, s[36:39], s54 offen nt lds\n"
"s_add_u32 s53, s53, 8192\n"
"v_mfma_f32_16x16x32_bf16 v[12:15], a[156:159], v[212:215], v[12:15]\n"
"s_waitcnt lgkmcnt(6)\n"
"s_add_u32 m0, s50, 82336\n"
"v_cvt_pk_bf16_f32 v209, v194, v195\n"
"v_mfma_f32_16x16x32_bf16 v[16:19], a[160:163], v[212:215], v[16:19]\n"
"s_waitcnt lgkmcnt(4)\n"
"buffer_load_dwordx4 v250, s[36:39], s54 offen offset:256 nt lds\n"
"s_and_b32 s53, s53, 258047\n"
"v_mfma_f32_16x16x32_bf16 v[20:23], a[164:167], v[212:215], v[20:23]\n"
"s_waitcnt lgkmcnt(2)\n"
"s_add_u32 m0, s50, 83136\n"
"v_cvt_pk_bf16_f32 v210, v196, v197\n"
"v_mfma_f32_16x16x32_bf16 v[24:27], a[168:171], v[212:215], v[24:27]\n"
"s_waitcnt lgkmcnt(0)\n"
"buffer_load_dwordx4 v250, s[36:39], s54 offen offset:512 nt lds\n"
"v_mfma_f32_16x16x32_bf16 v[28:31], a[172:175], v[212:215], v[28:31]\n"
"s_add_u32 m0, s50, 83936\n"
"v_cvt_pk_bf16_f32 v211, v198, v199\n"
"v_mfma_f32_16x16x32_bf16 a[208:211], a[236:239], v[212:215], a[208:211]\n"
"buffer_load_dwordx4 v250, s[36:39], s54 offen offset:768 nt lds\n"
"s_lshl_b32 s38, s38, 1\n"
"s_add_u32 s54, s53, 4096\n"
"v_mfma_f32_16x16x32_bf16 v[192:195], a[112:115], a[48:51], v[228:231]\n"
"v_exp_f32 v200, v200\n"
"v_mfma_f32_16x16x32_bf16 v[192:195], a[116:119], a[52:55], v[192:195]\n"
"v_exp_f32 v201, v201\n"
"v_mfma_f32_16x16x32_bf16 v[192:195], a[120:123], a[56:59], v[192:195]\n"
"s_memrealtime s[28:29]\n"
"v_exp_f32 v202, v202\n"
"v_mfma_f32_16x16x32_bf16 v[192:195], a[124:127], a[60:63], v[192:195]\n"
"v_exp_f32 v203, v203\n"
"v_mfma_f32_16x16x32_bf16 v[196:199], a[128:131], a[48:51], v[228:231]\n"
"v_exp_f32 v204, v204\n"
"v_mfma_f32_16x16x32_bf16 v[196:199], a[132:135], a[52:55], v[196:199]\n"
"v_exp_f32 v205, v205\n"
"v_mfma_f32_16x16x32_bf16 v[196:199], a[136:139], a[56:59], v[196:199]\n"
"v_exp_f32 v206, v206\n"
"v_mfma_f32_16x16x32_bf16 v[196:199], a[140:143], a[60:63], v[196:199]\n"
"v_exp_f32 v207, v207\n"
"v_mfma_f32_16x16x32_bf16 v[32:35], a[144:147], v[208:211], v[32:35]\n"
"v_mfma_f32_16x16x32_bf16 v[36:39], a[148:151], v[208:211], v[36:39]\n"
"v_cvt_pk_bf16_f32 v212, v200, v201\n"
"v_cvt_pk_bf16_f32 v213, v202, v203\n"
"v_mfma_f32_16x16x32_bf16 v[40:43], a[152:155], v[208:211], v[40:43]\n"
"v_cvt_pk_bf16_f32 v214, v204, v205\n"
"v_cvt_pk_bf16_f32 v215, v206, v207\n"
"v_mfma_f32_16x16x32_bf16 v[44:47], a[156:159], v[208:211], v[44:47]\n"
"v_mfma_f32_16x16x32_bf16 v[48:51], a[160:163], v[208:211], v[48:51]\n"
"v_mfma_f32_16x16x32_bf16 v[52:55], a[164:167], v[208:211], v[52:55]\n"
"v_mfma_f32_16x16x32_bf16 v[56:59], a[168:171], v[208:211], v[56:59]\n"
"v_mfma_f32_16x16x32_bf16 v[60:63], a[172:175], v[208:211], v[60:63]\n"
"v_mfma_f32_16x16x32_bf16 a[212:215], a[236:239], v[208:211], a[212:215]\n"
"v_mfma_f32_16x16x32_bf16 v[200:203], a[112:115], a[64:67], v[232:235]\n"
"v_exp_f32 v192, v192\n"
"v_mfma_f32_16x16x32_bf16 v[200:203], a[116:119], a[68:71], v[200:203]\n"
"v_exp_f32 v193, v193\n"
"v_mfma_f32_16x16x32_bf16 v[200:203], a[120:123], a[72:75], v[200:203]\n"
"v_exp_f32 v194, v194\n"
"v_mfma_f32_16x16x32_bf16 v[200:203], a[124:127], a[76:79], v[200:203]\n"
"v_exp_f32 v195, v195\n"
"v_mfma_f32_16x16x32_bf16 v[204:207], a[128:131], a[64:67], v[232:235]\n"
"v_exp_f32 v196, v196\n"
"v_mfma_f32_16x16x32_bf16 v[204:207], a[132:135], a[68:71], v[204:207]\n"
"v_exp_f32 v197, v197\n"
"v_mfma_f32_16x16x32_bf16 v[204:207], a[136:139], a[72:75], v[204:207]\n"
"v_exp_f32 v198, v198\n"
"v_mfma_f32_16x16x32_bf16 v[204:207], a[140:143], a[76:79], v[204:207]\n"
"v_exp_f32 v199, v199\n"
"v_mfma_f32_16x16x32_bf16 v[64:67], a[144:147], v[212:215], v[64:67]\n"
"v_mfma_f32_16x16x32_bf16 v[68:71], a[148:151], v[212:215], v[68:71]\n"
"v_cvt_pk_bf16_f32 v208, v192, v193\n"
"v_cvt_pk_bf16_f32 v209, v194, v195\n"
"v_mfma_f32_16x16x32_bf16 v[72:75], a[152:155], v[212:215], v[72:75]\n"
"v_cvt_pk_bf16_f32 v210, v196, v197\n"
"v_cvt_pk_bf16_f32 v211, v198, v199\n"
"v_mfma_f32_16x16x32_bf16 v[76:79], a[156:159], v[212:215], v[76:79]\n"
"s_lshl_b32 s33, s28, 3\n"
"v_mfma_f32_16x16x32_bf16 v[80:83], a[160:163], v[212:215], v[80:83]\n"
"s_sub_i32 s32, s30, s33\n"
"v_mfma_f32_16x16x32_bf16 v[84:87], a[164:167], v[212:215], v[84:87]\n"
"s_max_i32 s30, s32, -400\n"
"v_mfma_f32_16x16x32_bf16 v[88:91], a[168:171], v[212:215], v[88:91]\n"
"s_add_i32 s30, s30, s33\n"
"v_mfma_f32_16x16x32_bf16 v[92:95], a[172:175], v[212:215], v[92:95]\n"
"s_add_i32 s30, s30, s66\n"
"v_mfma_f32_16x16x32_bf16 a[216:219], a[236:239], v[212:215], a[216:219]\n"
"v_mfma_f32_16x16x32_bf16 v[192:195], a[112:115], a[80:83], v[236:239]\n"
"v_exp_f32 v200, v200\n"
"v_mfma_f32_16x16x32_bf16 v[192:195], a[116:119], a[84:87], v[192:195]\n"
"v_exp_f32 v201, v201\n"
"v_mfma_f32_16x16x32_bf16 v[192:195], a[120:123], a[88:91], v[192:195]\n"
"v_exp_f32 v202, v202\n"
"v_mfma_f32_16x16x32_bf16 v[192:195], a[124:127], a[92:95], v[192:195]\n"
"v_exp_f32 v203, v203\n"
"v_mfma_f32_16x16x32_bf16 v[196:199], a[128:131], a[80:83], v[236:239]\n"
"v_exp_f32 v204, v204\n"
"v_mfma_f32_16x16x32_bf16 v[196:199], a[132:135], a[84:87], v[196:199]\n"
"v_exp_f32 v205, v205\n"
"v_mfma_f32_16x16x32_bf16 v[196:199], a[136:139], a[88:91], v[196:199]\n"
"v_exp_f32 v206, v206\n"
"v_mfma_f32_16x16x32_bf16 v[196:199], a[140:143], a[92:95], v[196:199]\n"
"v_exp_f32 v207, v207\n"
"v_mfma_f32_16x16x32_bf16 v[96:99], a[144:147], v[208:211], v[96:99]\n"
"v_mfma_f32_16x16x32_bf16 v[100:103], a[148:151], v[208:211], v[100:103]\n"
"v_cvt_pk_bf16_f32 v212, v200, v201\n"
"v_cvt_pk_bf16_f32 v213, v202, v203\n"
"v_mfma_f32_16x16x32_bf16 v[104:107], a[152:155], v[208:211], v[104:107]\n"
"v_cvt_pk_bf16_f32 v214, v204, v205\n"
"v_cvt_pk_bf16_f32 v215, v206, v207\n"
"v_mfma_f32_16x16x32_bf16 v[108:111], a[156:159], v[208:211], v[108:111]\n"
"s_sub_i32 s66, s66, 15\n"
"v_mfma_f32_16x16x32_bf16 v[112:115], a[160:163], v[208:211], v[112:115]\n"
"s_max_i32 s32, s32, 0\n"
"v_mfma_f32_16x16x32_bf16 v[116:119], a[164:167], v[208:211], v[116:119]\n"
"s_mul_i32 s32, s32, 13\n"
"v_mfma_f32_16x16x32_bf16 v[120:123], a[168:171], v[208:211], v[120:123]\n"
"s_lshr_b32 s32, s32, 8\n"
"v_mfma_f32_16x16x32_bf16 v[124:127], a[172:175], v[208:211], v[124:127]\n"
"s_min_u32 s31, s32, 24\n"
"v_mfma_f32_16x16x32_bf16 a[220:223], a[236:239], v[208:211], a[220:223]\n"
"1:\n"
"s_sub_u32 s31, s31, 1\n"
"s_cbranch_scc1 2f\n"
"s_nop 7\n"
"s_branch 1b\n"
"2:\n"
"s_waitcnt vmcnt(16)\n"
"s_barrier\n"
"v_mfma_f32_16x16x32_bf16 v[200:203], a[112:115], a[96:99], v[240:243]\n"
"v_exp_f32 v192, v192\n"
"v_mfma_f32_16x16x32_bf16 v[200:203], a[116:119], a[100:103], v[200:203]\n"
"ds_read_b128 a[112:115], v244\n"
"ds_read_b128 a[116:119], v244 offset:64\n"
"v_mfma_f32_16x16x32_bf16 v[200:203], a[120:123], a[104:107], v[200:203]\n"
"v_exp_f32 v193, v193\n"
"v_mfma_f32_16x16x32_bf16 v[200:203], a[124:127], a[108:111], v[200:203]\n"
"ds_read_b128 a[120:123], v244 offset:128\n"
"ds_read_b128 a[124:127], v244 offset:192\n"
"v_mfma_f32_16x16x32_bf16 v[204:207], a[128:131], a[96:99], v[240:243]\n"
"v_exp_f32 v194, v194\n"
"v_mfma_f32_16x16x32_bf16 v[204:207], a[132:135], a[100:103], v[204:207]\n"
"ds_read_b128 a[128:131], v244 offset:4352\n"
"ds_read_b128 a[132:135], v244 offset:4416\n"
"v_mfma_f32_16x16x32_bf16 v[204:207], a[136:139], a[104:107], v[204:207]\n"
"v_exp_f32 v195, v195\n"
"v_mfma_f32_16x16x32_bf16 v[204:207], a[140:143], a[108:111], v[204:207]\n"
"ds_read_b128 a[136:139], v244 offset:4480\n"
"ds_read_b128 a[140:143], v244 offset:4544\n"
"v_mfma_f32_16x16x32_bf16 v[128:131], a[144:147], v[212:215], v[128:131]\n"
"v_exp_f32 v196, v196\n"
"v_mfma_f32_16x16x32_bf16 v[132:135], a[148:151], v[212:215], v[132:135]\n"
"v_exp_f32 v197, v197\n"
"v_mfma_f32_16x16x32_bf16 v[136:139], a[152:155], v[212:215], v[136:139]\n"
"v_exp_f32 v198, v198\n"
"v_mfma_f32_16x16x32_bf16 v[140:143], a[156:159], v[212:215], v[140:143]\n"
"v_exp_f32 v199, v199\n"
"v_mfma_f32_16x16x32_bf16 v[144:147], a[160:163], v[212:215], v[144:147]\n"
"v_cvt_pk_bf16_f32 v208, v192, v193\n"
"v_cvt_pk_bf16_f32 v209, v194, v195\n"
"v_mfma_f32_16x16x32_bf16 v[148:151], a[164:167], v[212:215], v[148:151]\n"
"v_cvt_pk_bf16_f32 v210, v196, v197\n"
"v_cvt_pk_bf16_f32 v211, v198, v199\n"
"v_mfma_f32_16x16x32_bf16 v[152:155], a[168:171], v[212:215], v[152:155]\n"
"v_mfma_f32_16x16x32_bf16 v[156:159], a[172:175], v[212:215], v[156:159]\n"
"v_mfma_f32_16x16x32_bf16 a[224:227], a[236:239], v[212:215], a[224:227]\n"
"s_waitcnt lgkmcnt(7)\n"
"v_mfma_f32_16x16x32_bf16 v[192:195], a[112:115], a[0:3], v[216:219]\n"
"s_waitcnt lgkmcnt(6)\n"
"v_exp_f32 v200, v200\n"
"v_mfma_f32_16x16x32_bf16 v[192:195], a[116:119], a[4:7], v[192:195]\n"
"s_waitcnt lgkmcnt(5)\n"
"v_exp_f32 v201, v201\n"
"v_mfma_f32_16x16x32_bf16 v[192:195], a[120:123], a[8:11], v[192:195]\n"
"s_waitcnt lgkmcnt(4)\n"
"v_exp_f32 v202, v202\n"
"v_mfma_f32_16x16x32_bf16 v[192:195], a[124:127], a[12:15], v[192:195]\n"
"s_waitcnt lgkmcnt(3)\n"
"v_exp_f32 v203, v203\n"
"v_mfma_f32_16x16x32_bf16 v[196:199], a[128:131], a[0:3], v[216:219]\n"
"s_waitcnt lgkmcnt(2)\n"
"v_exp_f32 v204, v204\n"
"v_mfma_f32_16x16x32_bf16 v[196:199], a[132:135], a[4:7], v[196:199]\n"
"s_waitcnt lgkmcnt(1)\n"
"v_exp_f32 v205, v205\n"
"v_mfma_f32_16x16x32_bf16 v[196:199], a[136:139], a[8:11], v[196:199]\n"
"s_waitcnt lgkmcnt(0)\n"
"v_exp_f32 v206, v206\n"
"v_mfma_f32_16x16x32_bf16 v[196:199], a[140:143], a[12:15], v[196:199]\n"
"v_exp_f32 v207, v207\n"
"v_mfma_f32_16x16x32_bf16 v[160:163], a[144:147], v[208:211], v[160:163]\n"
"v_mfma_f32_16x16x32_bf16 v[164:167], a[148:151], v[208:211], v[164:167]\n"
"s_add_u32 m0, s50, 111616\n"
"v_cvt_pk_bf16_f32 v212, v200, v201\n"
"v_mfma_f32_16x16x32_bf16 v[168:171], a[152:155], v[208:211], v[168:171]\n"
"buffer_load_dwordx4 v250, s[36:39], s53 offen nt lds\n"
"v_mfma_f32_16x16x32_bf16 v[172:175], a[156:159], v[208:211], v[172:175]\n"
"s_add_u32 m0, s50, 112416\n"
"v_cvt_pk_bf16_f32 v213, v202, v203\n"
"v_mfma_f32_16x16x32_bf16 v[176:179], a[160:163], v[208:211], v[176:179]\n"
"buffer_load_dwordx4 v250, s[36:39], s53 offen offset:256 nt lds\n"
"v_mfma_f32_16x16x32_bf16 v[180:183], a[164:167], v[208:211], v[180:183]\n"
"s_add_u32 m0, s50, 113216\n"
"v_cvt_pk_bf16_f32 v214, v204, v205\n"
"v_mfma_f32_16x16x32_bf16 v[184:187], a[168:171], v[208:211], v[184:187]\n"
"buffer_load_dwordx4 v250, s[36:39], s53 offen offset:512 nt lds\n"
"v_mfma_f32_16x16x32_bf16 v[188:191], a[172:175], v[208:211], v[188:191]\n"
"s_add_u32 m0, s50, 114016\n"
"v_cvt_pk_bf16_f32 v215, v206, v207\n"
"v_mfma_f32_16x16x32_bf16 a[228:231], a[236:239], v[208:211], a[228:231]\n"
"s_sub_u32 s55, s55, 1\n"
"buffer_load_dwordx4 v250, s[36:39], s53 offen offset:768 nt lds\n"
"s_cbranch_scc0 L1_loop_h1\n"
"s_nop 1\n"
"v_mfma_f32_16x16x32_bf16 a[176:179], a[144:147], v[212:215], a[176:179]\n"
"v_mfma_f32_16x16x32_bf16 a[180:183], a[148:151], v[212:215], a[180:183]\n"
"v_mfma_f32_16x16x32_bf16 a[184:187], a[152:155], v[212:215], a[184:187]\n"
"v_mfma_f32_16x16x32_bf16 a[188:191], a[156:159], v[212:215], a[188:191]\n"
"v_mfma_f32_16x16x32_bf16 a[192:195], a[160:163], v[212:215], a[192:195]\n"
"v_mfma_f32_16x16x32_bf16 a[196:199], a[164:167], v[212:215], a[196:199]\n"
"v_mfma_f32_16x16x32_bf16 a[200:203], a[168:171], v[212:215], a[200:203]\n"
"v_mfma_f32_16x16x32_bf16 a[204:207], a[172:175], v[212:215], a[204:207]\n"
"v_mfma_f32_16x16x32_bf16 a[232:235], a[236:239], v[212:215], a[232:235]\n"
"s_waitcnt lgkmcnt(0)\n"
"s_and_b32 s12, s24, 7\n"
"s_lshr_b32 s13, s24, 3\n"
"s_mul_i32 s48, s21, 34816\n"
"s_lshl_b32 s14, s21, 17\n"
"s_lshl_b32 s15, s12, 8\n"
"s_add_u32 s14, s14, s15\n"
"s_lshl_b32 s15, s13, 22\n"
"s_add_u32 s14, s14, s15\n"
"s_add_u32 s32, s16, s14\n"
"s_addc_u32 s33, s17, 0\n"
"s_and_b32 s33, s33, 65535\n"
"s_sub_u32 s34, 8388608, s14\n"
"s_mov_b32 s35, 131072\n"
"s_lshl_b32 s49, s12, 19\n"
"v_lshrrev_b32 v208, 4, v251\n"
"v_lshrrev_b32 v209, 4, v208\n"
"v_and_b32 v210, 15, v208\n"
"v_and_b32 v197, 1, v209\n"
"s_lshl_b32 s15, s13, 7\n"
"v_xor_b32 v198, v210, v197\n"
"v_lshlrev_b32 v198, 4, v198\n"
"v_xor_b32 v198, s15, v198\n"
"v_lshl_add_u32 v228, v209, 13, v198\n"
"v_add_u32 v229, 2048, v228\n"
"v_add_u32 v230, 4096, v228\n"
"v_add_u32 v231, 6144, v228\n"
"v_add_u32 v232, 32768, v228\n"
"v_add_u32 v233, 34816, v228\n"
"v_add_u32 v234, 36864, v228\n"
"v_add_u32 v235, 38912, v228\n"
"s_mul_i32 s25, s21, 4352\n"
"s_add_u32 s25, s25, 139264\n"
"v_add_u32 v252, s25, v251\n"
"s_xor_b32 s26, s21, 1\n"
"s_mul_i32 s26, s26, 4352\n"
"s_add_u32 s26, s26, 139264\n"
"v_add_u32 v253, s26, v251\n"
"v_lshrrev_b32 v204, 2, v251\n"
"v_add_u32 v205, s26, v204\n"
"v_add_u32 v204, s25, v204\n"
"v_mov_b32 v200, 2139095039\n"
"v_mov_b32 v201, 0\n"
"s_nop 3\n"
"ds_write_b128 v252, a[176:179]\n"
"ds_write_b128 v252, a[180:183] offset:1024\n"
"ds_write_b128 v252, a[184:187] offset:2048\n"
"ds_write_b128 v252, a[188:191] offset:3072\n"
"ds_write_b32 v204, a232 offset:4096\n"
"s_waitcnt vmcnt(0)\n"
"v_accvgpr_read_b32 v206, a208\n"
"v_min_f32 v200, v200, v206\n"
"v_max_f32 v201, v201, v206\n"
"v_rcp_f32 v202, v206\n"
"s_nop 0\n"
"v_pk_mul_f32 v[0:1], v[0:1], v[202:203] op_sel_hi:[1,0]\n"
"v_pk_mul_f32 v[2:3], v[2:3], v[202:203] op_sel_hi:[1,0]\n"
"v_pk_mul_f32 v[4:5], v[4:5], v[202:203] op_sel_hi:[1,0]\n"
"v_pk_mul_f32 v[6:7], v[6:7], v[202:203] op_sel_hi:[1,0]\n"
"v_cvt_pk_bf16_f32 v0, v0, v1\n"
"v_cvt_pk_bf16_f32 v1, v2, v3\n"
"v_cvt_pk_bf16_f32 v2, v4, v5\n"
"v_cvt_pk_bf16_f32 v3, v6, v7\n"
"s_mov_b32 s25, 6144\n"
"buffer_store_dwordx4 v[0:3], v251, s[40:43], s25 offen sc1\n"
"v_mfma_f32_16x16x32_bf16 v[192:195], v[216:219], v[0:3], 0\n"
"v_pk_mul_f32 v[8:9], v[8:9], v[202:203] op_sel_hi:[1,0]\n"
"v_pk_mul_f32 v[10:11], v[10:11], v[202:203] op_sel_hi:[1,0]\n"
"v_pk_mul_f32 v[12:13], v[12:13], v[202:203] op_sel_hi:[1,0]\n"
"v_pk_mul_f32 v[14:15], v[14:15], v[202:203] op_sel_hi:[1,0]\n"
"v_cvt_pk_bf16_f32 v8, v8, v9\n"
"v_cvt_pk_bf16_f32 v9, v10, v11\n"
"v_cvt_pk_bf16_f32 v10, v12, v13\n"
"v_cvt_pk_bf16_f32 v11, v14, v15\n"
"s_mov_b32 s25, 19456\n"
"buffer_store_dwordx4 v[8:11], v251, s[40:43], s25 offen sc1\n"
"v_mfma_f32_16x16x32_bf16 v[192:195], v[216:219], v[8:11], v[192:195]\n"
"v_pk_mul_f32 v[16:17], v[16:17], v[202:203] op_sel_hi:[1,0]\n"
"v_pk_mul_f32 v[18:19], v[18:19], v[202:203] op_sel_hi:[1,0]\n"
"v_pk_mul_f32 v[20:21], v[20:21], v[202:203] op_sel_hi:[1,0]\n"
"v_pk_mul_f32 v[22:23], v[22:23], v[202:203] op_sel_hi:[1,0]\n"
"v_cvt_pk_bf16_f32 v16, v16, v17\n"
"v_cvt_pk_bf16_f32 v17, v18, v19\n"
"v_cvt_pk_bf16_f32 v18, v20, v21\n"
"v_cvt_pk_bf16_f32 v19, v22, v23\n"
"s_mov_b32 s25, 32768\n"
"buffer_store_dwordx4 v[16:19], v251, s[40:43], s25 offen sc1\n"
"v_mfma_f32_16x16x32_bf16 v[192:195], v[216:219], v[16:19], v[192:195]\n"
"v_pk_mul_f32 v[24:25], v[24:25], v[202:203] op_sel_hi:[1,0]\n"
"v_pk_mul_f32 v[26:27], v[26:27], v[202:203] op_sel_hi:[1,0]\n"
"v_pk_mul_f32 v[28:29], v[28:29], v[202:203] op_sel_hi:[1,0]\n"
"v_pk_mul_f32 v[30:31], v[30:31], v[202:203] op_sel_hi:[1,0]\n"
"v_cvt_pk_bf16_f32 v24, v24, v25\n"
"v_cvt_pk_bf16_f32 v25, v26, v27\n"
"v_cvt_pk_bf16_f32 v26, v28, v29\n"
"v_cvt_pk_bf16_f32 v27, v30, v31\n"
"s_mov_b32 s25, 46080\n"
"buffer_store_dwordx4 v[24:27], v251, s[40:43], s25 offen sc1\n"
"v_mfma_f32_16x16x32_bf16 v[192:195], v[216:219], v[24:27], v[192:195]\n"
"v_accvgpr_read_b32 v206, a212\n"
"v_min_f32 v200, v200, v206\n"
"v_max_f32 v201, v201, v206\n"
"v_rcp_f32 v202, v206\n"
"s_nop 0\n"
"v_pk_mul_f32 v[32:33], v[32:33], v[202:203] op_sel_hi:[1,0]\n"
"v_pk_mul_f32 v[34:35], v[34:35], v[202:203] op_sel_hi:[1,0]\n"
"v_pk_mul_f32 v[36:37], v[36:37], v[202:203] op_sel_hi:[1,0]\n"
"v_pk_mul_f32 v[38:39], v[38:39], v[202:203] op_sel_hi:[1,0]\n"
"v_cvt_pk_bf16_f32 v32, v32, v33\n"
"v_cvt_pk_bf16_f32 v33, v34, v35\n"
"v_cvt_pk_bf16_f32 v34, v36, v37\n"
"v_cvt_pk_bf16_f32 v35, v38, v39\n"
"s_mov_b32 s25, 7168\n"
"buffer_store_dwordx4 v[32:35], v251, s[40:43], s25 offen sc1\n"
"v_mfma_f32_16x16x32_bf16 v[192:195], v[216:219], v[32:35], v[192:195]\n"
"v_pk_mul_f32 v[40:41], v[40:41], v[202:203] op_sel_hi:[1,0]\n"
"v_pk_mul_f32 v[42:43], v[42:43], v[202:203] op_sel_hi:[1,0]\n"
"v_pk_mul_f32 v[44:45], v[44:45], v[202:203] op_sel_hi:[1,0]\n"
"v_pk_mul_f32 v[46:47], v[46:47], v[202:203] op_sel_hi:[1,0]\n"
"v_cvt_pk_bf16_f32 v40, v40, v41\n"
"v_cvt_pk_bf16_f32 v41, v42, v43\n"
"v_cvt_pk_bf16_f32 v42, v44, v45\n"
"v_cvt_pk_bf16_f32 v43, v46, v47\n"
"s_mov_b32 s25, 20480\n"
"buffer_store_dwordx4 v[40:43], v251, s[40:43], s25 offen sc1\n"
"v_mfma_f32_16x16x32_bf16 v[192:195], v[216:219], v[40:43], v[192:195]\n"
"v_pk_mul_f32 v[48:49], v[48:49], v[202:203] op_sel_hi:[1,0]\n"
"v_pk_mul_f32 v[50:51], v[50:51], v[202:203] op_sel_hi:[1,0]\n"
"v_pk_mul_f32 v[52:53], v[52:53], v[202:203] op_sel_hi:[1,0]\n"
"v_pk_mul_f32 v[54:55], v[54:55], v[202:203] op_sel_hi:[1,0]\n"
"v_cvt_pk_bf16_f32 v48, v48, v49\n"
"v_cvt_pk_bf16_f32 v49, v50, v51\n"
"v_cvt_pk_bf16_f32 v50, v52, v53\n"
"v_cvt_pk_bf16_f32 v51, v54, v55\n"
"s_mov_b32 s25, 33792\n"
"buffer_store_dwordx4 v[48:51], v251, s[40:43], s25 offen sc1\n"
"v_mfma_f32_16x16x32_bf16 v[192:195], v[216:219], v[48:51], v[192:195]\n"
"v_pk_mul_f32 v[56:57], v[56:57], v[202:203] op_sel_hi:[1,0]\n"
"v_pk_mul_f32 v[58:59], v[58:59], v[202:203] op_sel_hi:[1,0]\n"
"v_pk_mul_f32 v[60:61], v[60:61], v[202:203] op_sel_hi:[1,0]\n"
"v_pk_mul_f32 v[62:63], v[62:63], v[202:203] op_sel_hi:[1,0]\n"
"v_cvt_pk_bf16_f32 v56, v56, v57\n"
"v_cvt_pk_bf16_f32 v57, v58, v59\n"
"v_cvt_pk_bf16_f32 v58, v60, v61\n"
"v_cvt_pk_bf16_f32 v59, v62, v63\n"
"s_mov_b32 s25, 47104\n"
"buffer_store_dwordx4 v[56:59], v251, s[40:43], s25 offen sc1\n"
"v_mfma_f32_16x16x32_bf16 v[192:195], v[216:219], v[56:59], v[192:195]\n"
"v_lshrrev_b32 v211, 2, v210\n"
"v_and_b32 v196, 3, v208\n"
"v_xor_b32 v198, v196, v197\n"
"v_lshlrev_b32 v198, 4, v198\n"
"v_lshl_add_u32 v198, v209, 8, v198\n"
"v_mul_u32_u24 v199, 1088, v211\n"
"v_add3_u32 v198, v198, v199, s48\n"
"v_and_b32 v199, 1, v196\n"
"v_lshl_add_u32 v224, v199, 3, v198\n"
"v_xor_b32 v199, 1, v199\n"
"v_lshl_add_u32 v225, v199, 3, v198\n"
"v_add_u32 v226, 4294932480, v224\n"
"v_add_u32 v227, 4294932480, v225\n"
"s_mul_i32 s14, s22, 3072\n"
"s_sub_u32 s15, 3072, s14\n"
"s_add_u32 s14, s14, 32768\n"
"s_add_u32 s15, s15, 32768\n"
"v_add_u32 v236, s14, v251\n"
"v_add_u32 v237, s15, v251\n"
"v_add_u32 v238, 38912, v251\n"
"v_add_u32 v239, 4294940672, v236\n"
"v_add_u32 v240, 4294940672, v237\n"
"s_lshl_b32 s14, s23, 1\n"
"s_add_u32 s14, s14, s13\n"
"s_lshl_b32 s15, s14, 4\n"
"s_add_u32 s15, s15, s22\n"
"s_mul_i32 s15, s15, 53248\n"
"s_add_u32 s28, s18, s15\n"
"s_addc_u32 s29, s19, 0\n"
"s_and_b32 s29, s29, 65535\n"
"s_mov_b32 s30, 1650688\n"
"s_mov_b32 s31, 131072\n"
"s_lshl_b32 s14, s14, 11\n"
"s_lshl_b32 s15, s21, 5\n"
"s_add_u32 s14, s14, s15\n"
"v_and_b32 v198, 7, v208\n"
"v_lshlrev_b32 v198, 8, v198\n"
"v_add_u32 v241, s14, v198\n"
"s_cmp_eq_u32 s12, 0\n"
"s_cselect_b32 s59, 1, 0\n"
"s_mov_b32 s66, 0\n"
"s_add_u32 s70, s48, 17408\n"
"s_mov_b32 s72, 17408\n"
"s_waitcnt lgkmcnt(0)\n"
"s_barrier\n"
"ds_read_b32 v207, v205 offset:4096\n"
"ds_read_b128 v[196:199], v253\n"
"ds_read_b128 v[220:223], v253 offset:1024\n"
"s_add_u32 m0, s48, 0\n"
"v_accvgpr_read_b32 v206, a232\n"
"buffer_load_dwordx4 v228, s[32:35], s49 offen lds\n"
"v_accvgpr_read_b32 v208, a192\n"
"v_accvgpr_read_b32 v209, a193\n"
"v_accvgpr_read_b32 v210, a194\n"
"s_add_u32 m0, s48, 1088\n"
"v_accvgpr_read_b32 v211, a195\n"
"buffer_load_dwordx4 v229, s[32:35], s49 offen lds\n"
"v_accvgpr_read_b32 v212, a196\n"
"v_accvgpr_read_b32 v213, a197\n"
"v_accvgpr_read_b32 v214, a198\n"
"s_add_u32 m0, s48, 2176\n"
"v_accvgpr_read_b32 v215, a199\n"
"buffer_load_dwordx4 v230, s[32:35], s49 offen lds\n"
"s_add_u32 m0, s48, 3264\n"
"s_nop 0\n"
"buffer_load_dwordx4 v231, s[32:35], s49 offen lds\n"
"s_add_u32 m0, s48, 4352\n"
"s_nop 0\n"
"buffer_load_dwordx4 v232, s[32:35], s49 offen lds\n"
"s_add_u32 m0, s48, 5440\n"
"s_nop 0\n"
"buffer_load_dwordx4 v233, s[32:35], s49 offen lds\n"
"s_add_u32 m0, s48, 6528\n"
"s_nop 0\n"
"buffer_load_dwordx4 v234, s[32:35], s49 offen lds\n"
"s_waitcnt lgkmcnt(2)\n"
"v_add_f32 v206, v206, v207\n"
"s_waitcnt lgkmcnt(1)\n"
"v_pk_add_f32 v[208:209], v[208:209], v[196:197]\n"
"v_pk_add_f32 v[210:211], v[210:211], v[198:199]\n"
"s_add_u32 m0, s48, 7616\n"
"s_waitcnt lgkmcnt(0)\n"
"v_pk_add_f32 v[212:213], v[212:213], v[220:221]\n"
"buffer_load_dwordx4 v235, s[32:35], s49 offen lds\n"
"s_add_u32 s49, s49, 65536\n"
"v_pk_add_f32 v[214:215], v[214:215], v[222:223]\n"
"ds_read_b128 v[196:199], v253 offset:2048\n"
"ds_read_b128 v[220:223], v253 offset:3072\n"
"s_add_u32 m0, s48, 8704\n"
"v_min_f32 v200, v200, v206\n"
"buffer_load_dwordx4 v228, s[32:35], s49 offen lds\n"
"v_max_f32 v201, v201, v206\n"
"v_rcp_f32 v202, v206\n"
"s_nop 0\n"
"s_add_u32 m0, s48, 9792\n"
"v_pk_mul_f32 v[208:209], v[208:209], v[202:203] op_sel_hi:[1,0]\n"
"buffer_load_dwordx4 v229, s[32:35], s49 offen lds\n"
"v_pk_mul_f32 v[210:211], v[210:211], v[202:203] op_sel_hi:[1,0]\n"
"v_pk_mul_f32 v[212:213], v[212:213], v[202:203] op_sel_hi:[1,0]\n"
"v_pk_mul_f32 v[214:215], v[214:215], v[202:203] op_sel_hi:[1,0]\n"
"s_add_u32 m0, s48, 10880\n"
"v_cvt_pk_bf16_f32 v208, v208, v209\n"
"buffer_load_dwordx4 v230, s[32:35], s49 offen lds\n"
"v_cvt_pk_bf16_f32 v209, v210, v211\n"
"v_cvt_pk_bf16_f32 v210, v212, v213\n"
"v_cvt_pk_bf16_f32 v211, v214, v215\n"
"s_add_u32 m0, s48, 11968\n"
"s_mov_b32 s25, 38912\n"
"buffer_load_dwordx4 v231, s[32:35], s49 offen lds\n"
"buffer_store_dwordx4 v[208:211], v251, s[40:43], s25 offen sc1\n"
"v_mfma_f32_16x16x32_bf16 v[192:195], v[216:219], v[208:211], v[192:195]\n"
"s_waitcnt lgkmcnt(0)\n"
"s_waitcnt lgkmcnt(1)\n"
"v_accvgpr_write_b32 a136, v208\n"
"s_add_u32 m0, s48, 13056\n"
"v_accvgpr_write_b32 a137, v209\n"
"buffer_load_dwordx4 v232, s[32:35], s49 offen lds\n"
"v_accvgpr_write_b32 a138, v210\n"
"v_accvgpr_write_b32 a139, v211\n"
"v_accvgpr_read_b32 v208, a200\n"
"s_add_u32 m0, s48, 14144\n"
"v_accvgpr_read_b32 v209, a201\n"
"buffer_load_dwordx4 v233, s[32:35], s49 offen lds\n"
"v_accvgpr_read_b32 v210, a202\n"
"v_accvgpr_read_b32 v211, a203\n"
"v_accvgpr_read_b32 v212, a204\n"
"s_add_u32 m0, s48, 15232\n"
"v_accvgpr_read_b32 v213, a205\n"
"buffer_load_dwordx4 v234, s[32:35], s49 offen lds\n"
"v_accvgpr_read_b32 v214, a206\n"
"v_accvgpr_read_b32 v215, a207\n"
"v_pk_add_f32 v[208:209], v[208:209], v[196:197]\n"
"s_add_u32 m0, s48, 16320\n"
"v_pk_add_f32 v[210:211], v[210:211], v[198:199]\n"
"buffer_load_dwordx4 v235, s[32:35], s49 offen lds\n"
"s_lshl_b32 s49, s59, 19\n"
"v_pk_add_f32 v[212:213], v[212:213], v[220:221]\n"
"v_pk_add_f32 v[214:215], v[214:215], v[222:223]\n"
"v_pk_mul_f32 v[208:209], v[208:209], v[202:203] op_sel_hi:[1,0]\n"
"v_pk_mul_f32 v[210:211], v[210:211], v[202:203] op_sel_hi:[1,0]\n"
"v_pk_mul_f32 v[212:213], v[212:213], v[202:203] op_sel_hi:[1,0]\n"
"v_pk_mul_f32 v[214:215], v[214:215], v[202:203] op_sel_hi:[1,0]\n"
"v_cvt_pk_bf16_f32 v208, v208, v209\n"
"v_cvt_pk_bf16_f32 v209, v210, v211\n"
"v_cvt_pk_bf16_f32 v210, v212, v213\n"
"v_cvt_pk_bf16_f32 v211, v214, v215\n"
"s_mov_b32 s25, 52224\n"
"buffer_store_dwordx4 v[208:211], v251, s[40:43], s25 offen sc1\n"
"v_mfma_f32_16x16x32_bf16 v[192:195], v[216:219], v[208:211], v[192:195]\n"
"v_accvgpr_write_b32 a164, v208\n"
"v_accvgpr_write_b32 a165, v209\n"
"v_accvgpr_write_b32 a166, v210\n"
"v_accvgpr_write_b32 a167, v211\n"
"s_cmp_eq_u32 s22, 0\n"
"s_cbranch_scc1 L1_ko0_h1\n"
"v_accvgpr_write_b32 a180, v0\n"
"v_accvgpr_write_b32 a181, v1\n"
"v_accvgpr_write_b32 a182, v2\n"
"v_accvgpr_write_b32 a183, v3\n"
"v_accvgpr_write_b32 a208, v8\n"
"v_accvgpr_write_b32 a209, v9\n"
"v_accvgpr_write_b32 a210, v10\n"
"v_accvgpr_write_b32 a211, v11\n"
"v_accvgpr_write_b32 a124, v16\n"
"v_accvgpr_write_b32 a125, v17\n"
"v_accvgpr_write_b32 a126, v18\n"
"v_accvgpr_write_b32 a127, v19\n"
"v_accvgpr_write_b32 a152, v24\n"
"v_accvgpr_write_b32 a153, v25\n"
"v_accvgpr_write_b32 a154, v26\n"
"v_accvgpr_write_b32 a155, v27\n"
"s_branch L1_ko0_h1e\n"
"L1_ko0_h1:\n"
"v_accvgpr_write_b32 a168, v0\n"
"v_accvgpr_write_b32 a169, v1\n"
"v_accvgpr_write_b32 a170, v2\n"
"v_accvgpr_write_b32 a171, v3\n"
"v_accvgpr_write_b32 a196, v8\n"
"v_accvgpr_write_b32 a197, v9\n"
"v_accvgpr_write_b32 a198, v10\n"
"v_accvgpr_write_b32 a199, v11\n"
"v_accvgpr_write_b32 a112, v16\n"
"v_accvgpr_write_b32 a113, v17\n"
"v_accvgpr_write_b32 a114, v18\n"
"v_accvgpr_write_b32 a115, v19\n"
"v_accvgpr_write_b32 a140, v24\n"
"v_accvgpr_write_b32 a141, v25\n"
"v_accvgpr_write_b32 a142, v26\n"
"v_accvgpr_write_b32 a143, v27\n"
"L1_ko0_h1e:\n"
"s_cmp_eq_u32 s22, 0\n"
"s_cbranch_scc1 L1_ko1_h1\n"
"v_accvgpr_write_b32 a184, v32\n"
"v_accvgpr_write_b32 a185, v33\n"
"v_accvgpr_write_b32 a186, v34\n"
"v_accvgpr_write_b32 a187, v35\n"
"v_accvgpr_write_b32 a212, v40\n"
"v_accvgpr_write_b32 a213, v41\n"
"v_accvgpr_write_b32 a214, v42\n"
"v_accvgpr_write_b32 a215, v43\n"
"v_accvgpr_write_b32 a128, v48\n"
"v_accvgpr_write_b32 a129, v49\n"
"v_accvgpr_write_b32 a130, v50\n"
"v_accvgpr_write_b32 a131, v51\n"
"v_accvgpr_write_b32 a156, v56\n"
"v_accvgpr_write_b32 a157, v57\n"
"v_accvgpr_write_b32 a158, v58\n"
"v_accvgpr_write_b32 a159, v59\n"
"s_branch L1_ko1_h1e\n"
"L1_ko1_h1:\n"
"v_accvgpr_write_b32 a172, v32\n"
"v_accvgpr_write_b32 a173, v33\n"
"v_accvgpr_write_b32 a174, v34\n"
"v_accvgpr_write_b32 a175, v35\n"
"v_accvgpr_write_b32 a200, v40\n"
"v_accvgpr_write_b32 a201, v41\n"
"v_accvgpr_write_b32 a202, v42\n"
"v_accvgpr_write_b32 a203, v43\n"
"v_accvgpr_write_b32 a116, v48\n"
"v_accvgpr_write_b32 a117, v49\n"
"v_accvgpr_write_b32 a118, v50\n"
"v_accvgpr_write_b32 a119, v51\n"
"v_accvgpr_write_b32 a144, v56\n"
"v_accvgpr_write_b32 a145, v57\n"
"v_accvgpr_write_b32 a146, v58\n"
"v_accvgpr_write_b32 a147, v59\n"
"L1_ko1_h1e:\n"
"v_accvgpr_read_b32 v206, a216\n"
"v_min_f32 v200, v200, v206\n"
"v_max_f32 v201, v201, v206\n"
"v_rcp_f32 v202, v206\n"
"s_nop 0\n"
"v_pk_mul_f32 v[64:65], v[64:65], v[202:203] op_sel_hi:[1,0]\n"
"v_pk_mul_f32 v[66:67], v[66:67], v[202:203] op_sel_hi:[1,0]\n"
"v_pk_mul_f32 v[68:69], v[68:69], v[202:203] op_sel_hi:[1,0]\n"
"v_pk_mul_f32 v[70:71], v[70:71], v[202:203] op_sel_hi:[1,0]\n"
"v_cvt_pk_bf16_f32 v64, v64, v65\n"
"v_cvt_pk_bf16_f32 v65, v66, v67\n"
"v_cvt_pk_bf16_f32 v66, v68, v69\n"
"v_cvt_pk_bf16_f32 v67, v70, v71\n"
"s_mov_b32 s25, 8192\n"
"buffer_store_dwordx4 v[64:67], v251, s[40:43], s25 offen sc1\n"
"v_mfma_f32_16x16x32_bf16 v[192:195], v[216:219], v[64:67], v[192:195]\n"
"v_pk_mul_f32 v[72:73], v[72:73], v[202:203] op_sel_hi:[1,0]\n"
"v_pk_mul_f32 v[74:75], v[74:75], v[202:203] op_sel_hi:[1,0]\n"
"v_pk_mul_f32 v[76:77], v[76:77], v[202:203] op_sel_hi:[1,0]\n"
"v_pk_mul_f32 v[78:79], v[78:79], v[202:203] op_sel_hi:[1,0]\n"
"v_cvt_pk_bf16_f32 v72, v72, v73\n"
"v_cvt_pk_bf16_f32 v73, v74, v75\n"
"v_cvt_pk_bf16_f32 v74, v76, v77\n"
"v_cvt_pk_bf16_f32 v75, v78, v79\n"
"s_mov_b32 s25, 21504\n"
"buffer_store_dwordx4 v[72:75], v251, s[40:43], s25 offen sc1\n"
"v_mfma_f32_16x16x32_bf16 v[192:195], v[216:219], v[72:75], v[192:195]\n"
"v_pk_mul_f32 v[80:81], v[80:81], v[202:203] op_sel_hi:[1,0]\n"
"v_pk_mul_f32 v[82:83], v[82:83], v[202:203] op_sel_hi:[1,0]\n"
"v_pk_mul_f32 v[84:85], v[84:85], v[202:203] op_sel_hi:[1,0]\n"
"v_pk_mul_f32 v[86:87], v[86:87], v[202:203] op_sel_hi:[1,0]\n"
"v_cvt_pk_bf16_f32 v80, v80, v81\n"
"v_cvt_pk_bf16_f32 v81, v82, v83\n"
"v_cvt_pk_bf16_f32 v82, v84, v85\n"
"v_cvt_pk_bf16_f32 v83, v86, v87\n"
"s_mov_b32 s25, 34816\n"
"buffer_store_dwordx4 v[80:83], v251, s[40:43], s25 offen sc1\n"
"v_mfma_f32_16x16x32_bf16 v[192:195], v[216:219], v[80:83], v[192:195]\n"
"v_pk_mul_f32 v[88:89], v[88:89], v[202:203] op_sel_hi:[1,0]\n"
"v_pk_mul_f32 v[90:91], v[90:91], v[202:203] op_sel_hi:[1,0]\n"
"v_pk_mul_f32 v[92:93], v[92:93], v[202:203] op_sel_hi:[1,0]\n"
"v_pk_mul_f32 v[94:95], v[94:95], v[202:203] op_sel_hi:[1,0]\n"
"v_cvt_pk_bf16_f32 v88, v88, v89\n"
"v_cvt_pk_bf16_f32 v89, v90, v91\n"
"v_cvt_pk_bf16_f32 v90, v92, v93\n"
"v_cvt_pk_bf16_f32 v91, v94, v95\n"
"s_mov_b32 s25, 48128\n"
"buffer_store_dwordx4 v[88:91], v251, s[40:43], s25 offen sc1\n"
"v_mfma_f32_16x16x32_bf16 v[192:195], v[216:219], v[88:91], v[192:195]\n"
"s_cmp_eq_u32 s22, 0\n"
"s_cbranch_scc1 L1_ko2_h1\n"
"v_accvgpr_write_b32 a188, v64\n"
"v_accvgpr_write_b32 a189, v65\n"
"v_accvgpr_write_b32 a190, v66\n"
"v_accvgpr_write_b32 a191, v67\n"
"v_accvgpr_write_b32 a216, v72\n"
"v_accvgpr_write_b32 a217, v73\n"
"v_accvgpr_write_b32 a218, v74\n"
"v_accvgpr_write_b32 a219, v75\n"
"v_accvgpr_write_b32 a132, v80\n"
"v_accvgpr_write_b32 a133, v81\n"
"v_accvgpr_write_b32 a134, v82\n"
"v_accvgpr_write_b32 a135, v83\n"
"v_accvgpr_write_b32 a160, v88\n"
"v_accvgpr_write_b32 a161, v89\n"
"v_accvgpr_write_b32 a162, v90\n"
"v_accvgpr_write_b32 a163, v91\n"
"s_branch L1_ko2_h1e\n"
"L1_ko2_h1:\n"
"v_accvgpr_write_b32 a176, v64\n"
"v_accvgpr_write_b32 a177, v65\n"
"v_accvgpr_write_b32 a178, v66\n"
"v_accvgpr_write_b32 a179, v67\n"
"v_accvgpr_write_b32 a204, v72\n"
"v_accvgpr_write_b32 a205, v73\n"
"v_accvgpr_write_b32 a206, v74\n"
"v_accvgpr_write_b32 a207, v75\n"
"v_accvgpr_write_b32 a120, v80\n"
"v_accvgpr_write_b32 a121, v81\n"
"v_accvgpr_write_b32 a122, v82\n"
"v_accvgpr_write_b32 a123, v83\n"
"v_accvgpr_write_b32 a148, v88\n"
"v_accvgpr_write_b32 a149, v89\n"
"v_accvgpr_write_b32 a150, v90\n"
"v_accvgpr_write_b32 a151, v91\n"
"L1_ko2_h1e:\n"
"v_accvgpr_read_b32 v206, a220\n"
"v_min_f32 v200, v200, v206\n"
"v_max_f32 v201, v201, v206\n"
"v_rcp_f32 v202, v206\n"
"s_nop 0\n"
"v_pk_mul_f32 v[96:97], v[96:97], v[202:203] op_sel_hi:[1,0]\n"
"v_pk_mul_f32 v[98:99], v[98:99], v[202:203] op_sel_hi:[1,0]\n"
"v_pk_mul_f32 v[100:101], v[100:101], v[202:203] op_sel_hi:[1,0]\n"
"v_pk_mul_f32 v[102:103], v[102:103], v[202:203] op_sel_hi:[1,0]\n"
"v_cvt_pk_bf16_f32 v96, v96, v97\n"
"v_cvt_pk_bf16_f32 v97, v98, v99\n"
"v_cvt_pk_bf16_f32 v98, v100, v101\n"
"v_cvt_pk_bf16_f32 v99, v102, v103\n"
"s_mov_b32 s25, 9216\n"
"buffer_store_dwordx4 v[96:99], v251, s[40:43], s25 offen sc1\n"
"v_mfma_f32_16x16x32_bf16 v[192:195], v[216:219], v[96:99], v[192:195]\n"
"v_pk_mul_f32 v[104:105], v[104:105], v[202:203] op_sel_hi:[1,0]\n"
"v_pk_mul_f32 v[106:107], v[106:107], v[202:203] op_sel_hi:[1,0]\n"
"v_pk_mul_f32 v[108:109], v[108:109], v[202:203] op_sel_hi:[1,0]\n"
"v_pk_mul_f32 v[110:111], v[110:111], v[202:203] op_sel_hi:[1,0]\n"
"v_cvt_pk_bf16_f32 v104, v104, v105\n"
"v_cvt_pk_bf16_f32 v105, v106, v107\n"
"v_cvt_pk_bf16_f32 v106, v108, v109\n"
"v_cvt_pk_bf16_f32 v107, v110, v111\n"
"s_mov_b32 s25, 22528\n"
"buffer_store_dwordx4 v[104:107], v251, s[40:43], s25 offen sc1\n"
"v_mfma_f32_16x16x32_bf16 v[192:195], v[216:219], v[104:107], v[192:195]\n"
"v_pk_mul_f32 v[112:113], v[112:113], v[202:203] op_sel_hi:[1,0]\n"
"v_pk_mul_f32 v[114:115], v[114:115], v[202:203] op_sel_hi:[1,0]\n"
"v_pk_mul_f32 v[116:117], v[116:117], v[202:203] op_sel_hi:[1,0]\n"
"v_pk_mul_f32 v[118:119], v[118:119], v[202:203] op_sel_hi:[1,0]\n"
"v_cvt_pk_bf16_f32 v112, v112, v113\n"
"v_cvt_pk_bf16_f32 v113, v114, v115\n"
"v_cvt_pk_bf16_f32 v114, v116, v117\n"
"v_cvt_pk_bf16_f32 v115, v118, v119\n"
"s_mov_b32 s25, 35840\n"
"buffer_store_dwordx4 v[112:115], v251, s[40:43], s25 offen sc1\n"
"v_mfma_f32_16x16x32_bf16 v[192:195], v[216:219], v[112:115], v[192:195]\n"
"v_pk_mul_f32 v[120:121], v[120:121], v[202:203] op_sel_hi:[1,0]\n"
"v_pk_mul_f32 v[122:123], v[122:123], v[202:203] op_sel_hi:[1,0]\n"
"v_pk_mul_f32 v[124:125], v[124:125], v[202:203] op_sel_hi:[1,0]\n"
"v_pk_mul_f32 v[126:127], v[126:127], v[202:203] op_sel_hi:[1,0]\n"
"v_cvt_pk_bf16_f32 v120, v120, v121\n"
"v_cvt_pk_bf16_f32 v121, v122, v123\n"
"v_cvt_pk_bf16_f32 v122, v124, v125\n"
"v_cvt_pk_bf16_f32 v123, v126, v127\n"
"s_mov_b32 s25, 49152\n"
"buffer_store_dwordx4 v[120:123], v251, s[40:43], s25 offen sc1\n"
"v_mfma_f32_16x16x32_bf16 v[192:195], v[216:219], v[120:123], v[192:195]\n"
"s_cmp_eq_u32 s22, 0\n"
"s_cbranch_scc1 L1_ko3_h1\n"
"v_accvgpr_write_b32 a168, v96\n"
"v_accvgpr_write_b32 a169, v97\n"
"v_accvgpr_write_b32 a170, v98\n"
"v_accvgpr_write_b32 a171, v99\n"
"v_accvgpr_write_b32 a196, v104\n"
"v_accvgpr_write_b32 a197, v105\n"
"v_accvgpr_write_b32 a198, v106\n"
"v_accvgpr_write_b32 a199, v107\n"
"v_accvgpr_write_b32 a112, v112\n"
"v_accvgpr_write_b32 a113, v113\n"
"v_accvgpr_write_b32 a114, v114\n"
"v_accvgpr_write_b32 a115, v115\n"
"v_accvgpr_write_b32 a140, v120\n"
"v_accvgpr_write_b32 a141, v121\n"
"v_accvgpr_write_b32 a142, v122\n"
"v_accvgpr_write_b32 a143, v123\n"
"s_branch L1_ko3_h1e\n"
"L1_ko3_h1:\n"
"v_accvgpr_write_b32 a180, v96\n"
"v_accvgpr_write_b32 a181, v97\n"
"v_accvgpr_write_b32 a182, v98\n"
"v_accvgpr_write_b32 a183, v99\n"
"v_accvgpr_write_b32 a208, v104\n"
"v_accvgpr_write_b32 a209, v105\n"
"v_accvgpr_write_b32 a210, v106\n"
"v_accvgpr_write_b32 a211, v107\n"
"v_accvgpr_write_b32 a124, v112\n"
"v_accvgpr_write_b32 a125, v113\n"
"v_accvgpr_write_b32 a126, v114\n"
"v_accvgpr_write_b32 a127, v115\n"
"v_accvgpr_write_b32 a152, v120\n"
"v_accvgpr_write_b32 a153, v121\n"
"v_accvgpr_write_b32 a154, v122\n"
"v_accvgpr_write_b32 a155, v123\n"
"L1_ko3_h1e:\n"
"v_accvgpr_read_b32 v206, a224\n"
"v_min_f32 v200, v200, v206\n"
"v_max_f32 v201, v201, v206\n"
"v_rcp_f32 v202, v206\n"
"s_nop 0\n"
"v_pk_mul_f32 v[128:129], v[128:129], v[202:203] op_sel_hi:[1,0]\n"
"v_pk_mul_f32 v[130:131], v[130:131], v[202:203] op_sel_hi:[1,0]\n"
"v_pk_mul_f32 v[132:133], v[132:133], v[202:203] op_sel_hi:[1,0]\n"
"v_pk_mul_f32 v[134:135], v[134:135], v[202:203] op_sel_hi:[1,0]\n"
"v_cvt_pk_bf16_f32 v128, v128, v129\n"
"v_cvt_pk_bf16_f32 v129, v130, v131\n"
"v_cvt_pk_bf16_f32 v130, v132, v133\n"
"v_cvt_pk_bf16_f32 v131, v134, v135\n"
"s_mov_b32 s25, 10240\n"
"buffer_store_dwordx4 v[128:131], v251, s[40:43], s25 offen sc1\n"
"v_mfma_f32_16x16x32_bf16 v[192:195], v[216:219], v[128:131], v[192:195]\n"
"v_pk_mul_f32 v[136:137], v[136:137], v[202:203] op_sel_hi:[1,0]\n"
"v_pk_mul_f32 v[138:139], v[138:139], v[202:203] op_sel_hi:[1,0]\n"
"v_pk_mul_f32 v[140:141], v[140:141], v[202:203] op_sel_hi:[1,0]\n"
"v_pk_mul_f32 v[142:143], v[142:143], v[202:203] op_sel_hi:[1,0]\n"
"v_cvt_pk_bf16_f32 v136, v136, v137\n"
"v_cvt_pk_bf16_f32 v137, v138, v139\n"
"v_cvt_pk_bf16_f32 v138, v140, v141\n"
"v_cvt_pk_bf16_f32 v139, v142, v143\n"
"s_mov_b32 s25, 23552\n"
"buffer_store_dwordx4 v[136:139], v251, s[40:43], s25 offen sc1\n"
"v_mfma_f32_16x16x32_bf16 v[192:195], v[216:219], v[136:139], v[192:195]\n"
"v_pk_mul_f32 v[144:145], v[144:145], v[202:203] op_sel_hi:[1,0]\n"
"v_pk_mul_f32 v[146:147], v[146:147], v[202:203] op_sel_hi:[1,0]\n"
"v_pk_mul_f32 v[148:149], v[148:149], v[202:203] op_sel_hi:[1,0]\n"
"v_pk_mul_f32 v[150:151], v[150:151], v[202:203] op_sel_hi:[1,0]\n"
"v_cvt_pk_bf16_f32 v144, v144, v145\n"
"v_cvt_pk_bf16_f32 v145, v146, v147\n"
"v_cvt_pk_bf16_f32 v146, v148, v149\n"
"v_cvt_pk_bf16_f32 v147, v150, v151\n"
"s_mov_b32 s25, 36864\n"
"buffer_store_dwordx4 v[144:147], v251, s[40:43], s25 offen sc1\n"
"v_mfma_f32_16x16x32_bf16 v[192:195], v[216:219], v[144:147], v[192:195]\n"
"v_pk_mul_f32 v[152:153], v[152:153], v[202:203] op_sel_hi:[1,0]\n"
"v_pk_mul_f32 v[154:155], v[154:155], v[202:203] op_sel_hi:[1,0]\n"
"v_pk_mul_f32 v[156:157], v[156:157], v[202:203] op_sel_hi:[1,0]\n"
"v_pk_mul_f32 v[158:159], v[158:159], v[202:203] op_sel_hi:[1,0]\n"
"v_cvt_pk_bf16_f32 v152, v152, v153\n"
"v_cvt_pk_bf16_f32 v153, v154, v155\n"
"v_cvt_pk_bf16_f32 v154, v156, v157\n"
"v_cvt_pk_bf16_f32 v155, v158, v159\n"
"s_mov_b32 s25, 50176\n"
"buffer_store_dwordx4 v[152:155], v251, s[40:43], s25 offen sc1\n"
"v_mfma_f32_16x16x32_bf16 v[192:195], v[216:219], v[152:155], v[192:195]\n"
"s_cmp_eq_u32 s22, 0\n"
"s_cbranch_scc1 L1_ko4_h1\n"
"v_accvgpr_write_b32 a172, v128\n"
"v_accvgpr_write_b32 a173, v129\n"
"v_accvgpr_write_b32 a174, v130\n"
"v_accvgpr_write_b32 a175, v131\n"
"v_accvgpr_write_b32 a200, v136\n"
"v_accvgpr_write_b32 a201, v137\n"
"v_accvgpr_write_b32 a202, v138\n"
"v_accvgpr_write_b32 a203, v139\n"
"v_accvgpr_write_b32 a116, v144\n"
"v_accvgpr_write_b32 a117, v145\n"
"v_accvgpr_write_b32 a118, v146\n"
"v_accvgpr_write_b32 a119, v147\n"
"v_accvgpr_write_b32 a144, v152\n"
"v_accvgpr_write_b32 a145, v153\n"
"v_accvgpr_write_b32 a146, v154\n"
"v_accvgpr_write_b32 a147, v155\n"
"s_branch L1_ko4_h1e\n"
"L1_ko4_h1:\n"
"v_accvgpr_write_b32 a184, v128\n"
"v_accvgpr_write_b32 a185, v129\n"
"v_accvgpr_write_b32 a186, v130\n"
"v_accvgpr_write_b32 a187, v131\n"
"v_accvgpr_write_b32 a212, v136\n"
"v_accvgpr_write_b32 a213, v137\n"
"v_accvgpr_write_b32 a214, v138\n"
"v_accvgpr_write_b32 a215, v139\n"
"v_accvgpr_write_b32 a128, v144\n"
"v_accvgpr_write_b32 a129, v145\n"
"v_accvgpr_write_b32 a130, v146\n"
"v_accvgpr_write_b32 a131, v147\n"
"v_accvgpr_write_b32 a156, v152\n"
"v_accvgpr_write_b32 a157, v153\n"
"v_accvgpr_write_b32 a158, v154\n"
"v_accvgpr_write_b32 a159, v155\n"
"L1_ko4_h1e:\n"
"v_accvgpr_read_b32 v206, a228\n"
"v_min_f32 v200, v200, v206\n"
"v_max_f32 v201, v201, v206\n"
"v_rcp_f32 v202, v206\n"
"s_nop 0\n"
"v_pk_mul_f32 v[160:161], v[160:161], v[202:203] op_sel_hi:[1,0]\n"
"v_pk_mul_f32 v[162:163], v[162:163], v[202:203] op_sel_hi:[1,0]\n"
"v_pk_mul_f32 v[164:165], v[164:165], v[202:203] op_sel_hi:[1,0]\n"
"v_pk_mul_f32 v[166:167], v[166:167], v[202:203] op_sel_hi:[1,0]\n"
"v_cvt_pk_bf16_f32 v160, v160, v161\n"
"v_cvt_pk_bf16_f32 v161, v162, v163\n"
"v_cvt_pk_bf16_f32 v162, v164, v165\n"
"v_cvt_pk_bf16_f32 v163, v166, v167\n"
"s_mov_b32 s25, 11264\n"
"buffer_store_dwordx4 v[160:163], v251, s[40:43], s25 offen sc1\n"
"v_mfma_f32_16x16x32_bf16 v[192:195], v[216:219], v[160:163], v[192:195]\n"
"v_pk_mul_f32 v[168:169], v[168:169], v[202:203] op_sel_hi:[1,0]\n"
"v_pk_mul_f32 v[170:171], v[170:171], v[202:203] op_sel_hi:[1,0]\n"
"v_pk_mul_f32 v[172:173], v[172:173], v[202:203] op_sel_hi:[1,0]\n"
"v_pk_mul_f32 v[174:175], v[174:175], v[202:203] op_sel_hi:[1,0]\n"
"v_cvt_pk_bf16_f32 v168, v168, v169\n"
"v_cvt_pk_bf16_f32 v169, v170, v171\n"
"v_cvt_pk_bf16_f32 v170, v172, v173\n"
"v_cvt_pk_bf16_f32 v171, v174, v175\n"
"s_mov_b32 s25, 24576\n"
"buffer_store_dwordx4 v[168:171], v251, s[40:43], s25 offen sc1\n"
"v_mfma_f32_16x16x32_bf16 v[192:195], v[216:219], v[168:171], v[192:195]\n"
"v_pk_mul_f32 v[176:177], v[176:177], v[202:203] op_sel_hi:[1,0]\n"
"v_pk_mul_f32 v[178:179], v[178:179], v[202:203] op_sel_hi:[1,0]\n"
"v_pk_mul_f32 v[180:181], v[180:181], v[202:203] op_sel_hi:[1,0]\n"
"v_pk_mul_f32 v[182:183], v[182:183], v[202:203] op_sel_hi:[1,0]\n"
"v_cvt_pk_bf16_f32 v176, v176, v177\n"
"v_cvt_pk_bf16_f32 v177, v178, v179\n"
"v_cvt_pk_bf16_f32 v178, v180, v181\n"
"v_cvt_pk_bf16_f32 v179, v182, v183\n"
"s_mov_b32 s25, 37888\n"
"buffer_store_dwordx4 v[176:179], v251, s[40:43], s25 offen sc1\n"
"v_mfma_f32_16x16x32_bf16 v[192:195], v[216:219], v[176:179], v[192:195]\n"
"v_pk_mul_f32 v[184:185], v[184:185], v[202:203] op_sel_hi:[1,0]\n"
"v_pk_mul_f32 v[186:187], v[186:187], v[202:203] op_sel_hi:[1,0]\n"
"v_pk_mul_f32 v[188:189], v[188:189], v[202:203] op_sel_hi:[1,0]\n"
"v_pk_mul_f32 v[190:191], v[190:191], v[202:203] op_sel_hi:[1,0]\n"
"v_cvt_pk_bf16_f32 v184, v184, v185\n"
"v_cvt_pk_bf16_f32 v185, v186, v187\n"
"v_cvt_pk_bf16_f32 v186, v188, v189\n"
"v_cvt_pk_bf16_f32 v187, v190, v191\n"
"s_mov_b32 s25, 51200\n"
"buffer_store_dwordx4 v[184:187], v251, s[40:43], s25 offen sc1\n"
"v_mfma_f32_16x16x32_bf16 v[192:195], v[216:219], v[184:187], v[192:195]\n"
"s_cmp_eq_u32 s22, 0\n"
"s_cbranch_scc1 L1_ko5_h1\n"
"v_accvgpr_write_b32 a176, v160\n"
"v_accvgpr_write_b32 a177, v161\n"
"v_accvgpr_write_b32 a178, v162\n"
"v_accvgpr_write_b32 a179, v163\n"
"v_accvgpr_write_b32 a204, v168\n"
"v_accvgpr_write_b32 a205, v169\n"
"v_accvgpr_write_b32 a206, v170\n"
"v_accvgpr_write_b32 a207, v171\n"
"v_accvgpr_write_b32 a120, v176\n"
"v_accvgpr_write_b32 a121, v177\n"
"v_accvgpr_write_b32 a122, v178\n"
"v_accvgpr_write_b32 a123, v179\n"
"v_accvgpr_write_b32 a148, v184\n"
"v_accvgpr_write_b32 a149, v185\n"
"v_accvgpr_write_b32 a150, v186\n"
"v_accvgpr_write_b32 a151, v187\n"
"s_branch L1_ko5_h1e\n"
"L1_ko5_h1:\n"
"v_accvgpr_write_b32 a188, v160\n"
"v_accvgpr_write_b32 a189, v161\n"
"v_accvgpr_write_b32 a190, v162\n"
"v_accvgpr_write_b32 a191, v163\n"
"v_accvgpr_write_b32 a216, v168\n"
"v_accvgpr_write_b32 a217, v169\n"
"v_accvgpr_write_b32 a218, v170\n"
"v_accvgpr_write_b32 a219, v171\n"
"v_accvgpr_write_b32 a132, v176\n"
"v_accvgpr_write_b32 a133, v177\n"
"v_accvgpr_write_b32 a134, v178\n"
"v_accvgpr_write_b32 a135, v179\n"
"v_accvgpr_write_b32 a160, v184\n"
"v_accvgpr_write_b32 a161, v185\n"
"v_accvgpr_write_b32 a162, v186\n"
"v_accvgpr_write_b32 a163, v187\n"
"L1_ko5_h1e:\n"
"v_mov_b32 v204, 163832\n"
"s_cmp_lg_u32 s58, 0\n"
"s_cbranch_scc1 L1_sync_h1\n"
"v_cmp_gt_f32 vcc, 813694976, v200\n"
"s_nop 0\n"
"s_mov_b64 s[64:65], vcc\n"
"v_cmp_lt_f32 vcc, 1568669696, v201\n"
"s_nop 0\n"
"s_or_b64 s[64:65], s[64:65], vcc\n"
"s_nop 3\n"
"v_cmp_u_f32 vcc, v192, v192\n"
"s_nop 0\n"
"s_or_b64 s[64:65], s[64:65], vcc\n"
"v_mov_b32 v204, 163832\n"
"v_mov_b32 v205, 1\n"
"s_cbranch_scc0 L1_sync_h1\n"
"ds_write_b32 v204, v205\n"
"L1_sync_h1:\n"
"s_waitcnt lgkmcnt(0)\n"
"s_waitcnt vmcnt(17)\n"
"s_barrier\n"
"ds_read_b32 v205, v204\n"
"ds_read_b64_tr_b16 a[224:225], v224 offset:128\n"
"ds_read_b64_tr_b16 a[226:227], v224 offset:4480\n"
"ds_read_b64_tr_b16 a[228:229], v224 offset:8832\n"
"ds_read_b64_tr_b16 a[230:231], v224 offset:13184\n"
"ds_read_b64_tr_b16 a[232:233], v225 offset:128\n"
"ds_read_b64_tr_b16 a[234:235], v225 offset:4480\n"
"ds_read_b64_tr_b16 a[236:237], v225 offset:8832\n"
"ds_read_b64_tr_b16 a[238:239], v225 offset:13184\n"
"ds_read_b64_tr_b16 a[240:241], v224 offset:192\n"
"ds_read_b64_tr_b16 a[242:243], v224 offset:4544\n"
"ds_read_b64_tr_b16 a[244:245], v224 offset:8896\n"
"ds_read_b64_tr_b16 a[246:247], v224 offset:13248\n"
"ds_read_b64_tr_b16 a[248:249], v225 offset:192\n"
"ds_read_b64_tr_b16 a[250:251], v225 offset:4544\n"
"ds_read_b64_tr_b16 a[252:253], v225 offset:8896\n"
"ds_read_b64_tr_b16 a[254:255], v225 offset:13248\n"
"s_waitcnt lgkmcnt(15)\n"
"v_readfirstlane_b32 s25, v205\n"
"s_nop 0\n"
"s_andn2_b32 s25, s25, s58\n"
"s_cbranch_scc0 L2_go\n"
"v_mov_b32 v205, 1065369472\n"
"s_waitcnt lgkmcnt(0)\n"
"v_accvgpr_write_b32 a236, v205\n"
"v_accvgpr_write_b32 a237, v205\n"
"v_accvgpr_write_b32 a238, v205\n"
"v_accvgpr_write_b32 a239, v205\n"
"s_mov_b32 s58, 1\n"
"s_waitcnt vmcnt(0) lgkmcnt(0)\n"
"s_barrier\n"
"v_lshrrev_b32 v80, 4, v251\n"
"v_and_b32 v81, 15, v80\n"
"v_lshrrev_b32 v82, 4, v80\n"
"v_lshlrev_b32 v82, 4, v82\n"
"v_lshl_add_u32 v81, v81, 8, v82\n"
"v_xor_b32 v82, 16, v80\n"
"v_lshlrev_b32 v82, 2, v82\n"
"v_xor_b32 v83, 32, v80\n"
"v_lshlrev_b32 v83, 2, v83\n"
"s_mov_b32 s26, 0\n"
"v_mov_b32 v64, 4286578688\n"
"v_mov_b32 v65, 4286578688\n"
"v_mov_b32 v66, 4286578688\n"
"v_mov_b32 v67, 4286578688\n"
"v_mov_b32 v68, 4286578688\n"
"v_mov_b32 v69, 4286578688\n"
"v_mov_b32 v70, 4286578688\n"
"L1_fb_h1:\n"
"buffer_load_dwordx4 a[112:115], v81, s[44:47], s26 offen\n"
"buffer_load_dwordx4 a[116:119], v81, s[44:47], s26 offen offset:64\n"
"buffer_load_dwordx4 a[120:123], v81, s[44:47], s26 offen offset:128\n"
"buffer_load_dwordx4 a[124:127], v81, s[44:47], s26 offen offset:192\n"
"s_waitcnt vmcnt(0)\n"
"v_mfma_f32_16x16x32_bf16 v[0:3], a[112:115], a[0:3], 0\n"
"v_mfma_f32_16x16x32_bf16 v[0:3], a[116:119], a[4:7], v[0:3]\n"
"v_mfma_f32_16x16x32_bf16 v[0:3], a[120:123], a[8:11], v[0:3]\n"
"v_mfma_f32_16x16x32_bf16 v[0:3], a[124:127], a[12:15], v[0:3]\n"
"v_mfma_f32_16x16x32_bf16 v[4:7], a[112:115], a[16:19], 0\n"
"v_mfma_f32_16x16x32_bf16 v[4:7], a[116:119], a[20:23], v[4:7]\n"
"v_mfma_f32_16x16x32_bf16 v[4:7], a[120:123], a[24:27], v[4:7]\n"
"v_mfma_f32_16x16x32_bf16 v[4:7], a[124:127], a[28:31], v[4:7]\n"
"v_mfma_f32_16x16x32_bf16 v[8:11], a[112:115], a[32:35], 0\n"
"v_mfma_f32_16x16x32_bf16 v[8:11], a[116:119], a[36:39], v[8:11]\n"
"v_mfma_f32_16x16x32_bf16 v[8:11], a[120:123], a[40:43], v[8:11]\n"
"v_mfma_f32_16x16x32_bf16 v[8:11], a[124:127], a[44:47], v[8:11]\n"
"v_mfma_f32_16x16x32_bf16 v[12:15], a[112:115], a[48:51], 0\n"
"v_mfma_f32_16x16x32_bf16 v[12:15], a[116:119], a[52:55], v[12:15]\n"
"v_mfma_f32_16x16x32_bf16 v[12:15], a[120:123], a[56:59], v[12:15]\n"
"v_mfma_f32_16x16x32_bf16 v[12:15], a[124:127], a[60:63], v[12:15]\n"
"v_mfma_f32_16x16x32_bf16 v[16:19], a[112:115], a[64:67], 0\n"
"v_mfma_f32_16x16x32_bf16 v[16:19], a[116:119], a[68:71], v[16:19]\n"
"v_mfma_f32_16x16x32_bf16 v[16:19], a[120:123], a[72:75], v[16:19]\n"
"v_mfma_f32_16x16x32_bf16 v[16:19], a[124:127], a[76:79], v[16:19]\n"
"v_mfma_f32_16x16x32_bf16 v[20:23], a[112:115], a[80:83], 0\n"
"v_mfma_f32_16x16x32_bf16 v[20:23], a[116:119], a[84:87], v[20:23]\n"
"v_mfma_f32_16x16x32_bf16 v[20:23], a[120:123], a[88:91], v[20:23]\n"
"v_mfma_f32_16x16x32_bf16 v[20:23], a[124:127], a[92:95], v[20:23]\n"
"v_mfma_f32_16x16x32_bf16 v[24:27], a[112:115], a[96:99], 0\n"
"v_mfma_f32_16x16x32_bf16 v[24:27], a[116:119], a[100:103], v[24:27]\n"
"v_mfma_f32_16x16x32_bf16 v[24:27], a[120:123], a[104:107], v[24:27]\n"
"v_mfma_f32_16x16x32_bf16 v[24:27], a[124:127], a[108:111], v[24:27]\n"
"s_nop 7\n"
"v_max3_f32 v64, v64, v0, v1\n"
"v_max3_f32 v64, v64, v2, v3\n"
"v_max3_f32 v65, v65, v4, v5\n"
"v_max3_f32 v65, v65, v6, v7\n"
"v_max3_f32 v66, v66, v8, v9\n"
"v_max3_f32 v66, v66, v10, v11\n"
"v_max3_f32 v67, v67, v12, v13\n"
"v_max3_f32 v67, v67, v14, v15\n"
"v_max3_f32 v68, v68, v16, v17\n"
"v_max3_f32 v68, v68, v18, v19\n"
"v_max3_f32 v69, v69, v20, v21\n"
"v_max3_f32 v69, v69, v22, v23\n"
"v_max3_f32 v70, v70, v24, v25\n"
"v_max3_f32 v70, v70, v26, v27\n"
"s_add_u32 s26, s26, 4096\n"
"s_cmp_lt_u32 s26, 262144\n"
"s_cbranch_scc1 L1_fb_h1\n"
"ds_bpermute_b32 v72, v82, v64\n"
"ds_bpermute_b32 v73, v82, v65\n"
"ds_bpermute_b32 v74, v82, v66\n"
"ds_bpermute_b32 v75, v82, v67\n"
"ds_bpermute_b32 v76, v82, v68\n"
"ds_bpermute_b32 v77, v82, v69\n"
"ds_bpermute_b32 v78, v82, v70\n"
"s_waitcnt lgkmcnt(0)\n"
"v_max_f32 v64, v64, v72\n"
"v_max_f32 v65, v65, v73\n"
"v_max_f32 v66, v66, v74\n"
"v_max_f32 v67, v67, v75\n"
"v_max_f32 v68, v68, v76\n"
"v_max_f32 v69, v69, v77\n"
"v_max_f32 v70, v70, v78\n"
"ds_bpermute_b32 v72, v83, v64\n"
"ds_bpermute_b32 v73, v83, v65\n"
"ds_bpermute_b32 v74, v83, v66\n"
"ds_bpermute_b32 v75, v83, v67\n"
"ds_bpermute_b32 v76, v83, v68\n"
"ds_bpermute_b32 v77, v83, v69\n"
"ds_bpermute_b32 v78, v83, v70\n"
"s_waitcnt lgkmcnt(0)\n"
"v_max_f32 v64, v64, v72\n"
"v_max_f32 v65, v65, v73\n"
"v_max_f32 v66, v66, v74\n"
"v_max_f32 v67, v67, v75\n"
"v_max_f32 v68, v68, v76\n"
"v_max_f32 v69, v69, v77\n"
"v_max_f32 v70, v70, v78\n"
"v_xor_b32 v216, 2147483648, v64\n"
"v_xor_b32 v217, 2147483648, v64\n"
"v_xor_b32 v218, 2147483648, v64\n"
"v_xor_b32 v219, 2147483648, v64\n"
"v_xor_b32 v220, 2147483648, v65\n"
"v_xor_b32 v221, 2147483648, v65\n"
"v_xor_b32 v222, 2147483648, v65\n"
"v_xor_b32 v223, 2147483648, v65\n"
"v_xor_b32 v224, 2147483648, v66\n"
"v_xor_b32 v225, 2147483648, v66\n"
"v_xor_b32 v226, 2147483648, v66\n"
"v_xor_b32 v227, 2147483648, v66\n"
"v_xor_b32 v228, 2147483648, v67\n"
"v_xor_b32 v229, 2147483648, v67\n"
"v_xor_b32 v230, 2147483648, v67\n"
"v_xor_b32 v231, 2147483648, v67\n"
"v_xor_b32 v232, 2147483648, v68\n"
"v_xor_b32 v233, 2147483648, v68\n"
"v_xor_b32 v234, 2147483648, v68\n"
"v_xor_b32 v235, 2147483648, v68\n"
"v_xor_b32 v236, 2147483648, v69\n"
"v_xor_b32 v237, 2147483648, v69\n"
"v_xor_b32 v238, 2147483648, v69\n"
"v_xor_b32 v239, 2147483648, v69\n"
"v_xor_b32 v240, 2147483648, v70\n"
"v_xor_b32 v241, 2147483648, v70\n"
"v_xor_b32 v242, 2147483648, v70\n"
"v_xor_b32 v243, 2147483648, v70\n"
"s_mov_b32 s53, s67\n"
"s_add_u32 s54, s67, 4096\n"
"s_mov_b32 s38, -1\n"
"s_add_u32 m0, s50, 8704\n"
"s_nop 0\n"
"buffer_load_dwordx4 v250, s[36:39], s53 offen nt lds\n"
"s_add_u32 m0, s50, 9504\n"
"s_nop 0\n"
"buffer_load_dwordx4 v250, s[36:39], s53 offen offset:256 nt lds\n"
"s_add_u32 m0, s50, 10304\n"
"s_nop 0\n"
"buffer_load_dwordx4 v250, s[36:39], s53 offen offset:512 nt lds\n"
"s_add_u32 m0, s50, 11104\n"
"s_nop 0\n"
"buffer_load_dwordx4 v250, s[36:39], s53 offen offset:768 nt lds\n"
"s_add_u32 m0, s50, 12928\n"
"s_nop 0\n"
"buffer_load_dwordx4 v250, s[36:39], s54 offen nt lds\n"
"s_add_u32 m0, s50, 13728\n"
"s_nop 0\n"
"buffer_load_dwordx4 v250, s[36:39], s54 offen offset:256 nt lds\n"
"s_add_u32 m0, s50, 14528\n"
"s_nop 0\n"
"buffer_load_dwordx4 v250, s[36:39], s54 offen offset:512 nt lds\n"
"s_add_u32 m0, s50, 15328\n"
"s_nop 0\n"
"buffer_load_dwordx4 v250, s[36:39], s54 offen offset:768 nt lds\n"
"s_add_u32 s53, s53, 8192\n"
"s_and_b32 s53, s53, 258047\n"
"s_lshl_b32 s38, s38, 1\n"
"s_add_u32 s54, s53, 4096\n"
"s_add_u32 m0, s50, 43008\n"
"s_nop 0\n"
"buffer_load_dwordx4 v250, s[36:39], s53 offen nt lds\n"
"s_add_u32 m0, s50, 43808\n"
"s_nop 0\n"
"buffer_load_dwordx4 v250, s[36:39], s53 offen offset:256 nt lds\n"
"s_add_u32 m0, s50, 44608\n"
"s_nop 0\n"
"buffer_load_dwordx4 v250, s[36:39], s53 offen offset:512 nt lds\n"
"s_add_u32 m0, s50, 45408\n"
"s_nop 0\n"
"buffer_load_dwordx4 v250, s[36:39], s53 offen offset:768 nt lds\n"
"s_add_u32 m0, s50, 47232\n"
"s_nop 0\n"
"buffer_load_dwordx4 v250, s[36:39], s54 offen nt lds\n"
"s_add_u32 m0, s50, 48032\n"
"s_nop 0\n"
"buffer_load_dwordx4 v250, s[36:39], s54 offen offset:256 nt lds\n"
"s_add_u32 m0, s50, 48832\n"
"s_nop 0\n"
"buffer_load_dwordx4 v250, s[36:39], s54 offen offset:512 nt lds\n"
"s_add_u32 m0, s50, 49632\n"
"s_nop 0\n"
"buffer_load_dwordx4 v250, s[36:39], s54 offen offset:768 nt lds\n"
"s_add_u32 s53, s53, 8192\n"
"s_and_b32 s53, s53, 258047\n"
"s_lshl_b32 s38, s38, 1\n"
"s_add_u32 s54, s53, 4096\n"
"s_add_u32 m0, s50, 77312\n"
"s_nop 0\n"
"buffer_load_dwordx4 v250, s[36:39], s53 offen nt lds\n"
"s_add_u32 m0, s50, 78112\n"
"s_nop 0\n"
"buffer_load_dwordx4 v250, s[36:39], s53 offen offset:256 nt lds\n"
"s_add_u32 m0, s50, 78912\n"
"s_nop 0\n"
"buffer_load_dwordx4 v250, s[36:39], s53 offen offset:512 nt lds\n"
"s_add_u32 m0, s50, 79712\n"
"s_nop 0\n"
"buffer_load_dwordx4 v250, s[36:39], s53 offen offset:768 nt lds\n"
"s_add_u32 m0, s50, 81536\n"
"s_nop 0\n"
"buffer_load_dwordx4 v250, s[36:39], s54 offen nt lds\n"
"s_add_u32 m0, s50, 82336\n"
"s_nop 0\n"
"buffer_load_dwordx4 v250, s[36:39], s54 offen offset:256 nt lds\n"
"s_add_u32 m0, s50, 83136\n"
"s_nop 0\n"
"buffer_load_dwordx4 v250, s[36:39], s54 offen offset:512 nt lds\n"
"s_add_u32 m0, s50, 83936\n"
"s_nop 0\n"
"buffer_load_dwordx4 v250, s[36:39], s54 offen offset:768 nt lds\n"
"s_add_u32 s53, s53, 8192\n"
"s_and_b32 s53, s53, 258047\n"
"s_lshl_b32 s38, s38, 1\n"
"s_add_u32 s54, s53, 4096\n"
"s_add_u32 m0, s50, 111616\n"
"s_nop 0\n"
"buffer_load_dwordx4 v250, s[36:39], s53 offen nt lds\n"
"s_add_u32 m0, s50, 112416\n"
"s_nop 0\n"
"buffer_load_dwordx4 v250, s[36:39], s53 offen offset:256 nt lds\n"
"s_add_u32 m0, s50, 113216\n"
"s_nop 0\n"
"buffer_load_dwordx4 v250, s[36:39], s53 offen offset:512 nt lds\n"
"s_add_u32 m0, s50, 114016\n"
"s_nop 0\n"
"buffer_load_dwordx4 v250, s[36:39], s53 offen offset:768 nt lds\n"
"s_add_u32 m0, s50, 115840\n"
"s_nop 0\n"
"buffer_load_dwordx4 v250, s[36:39], s54 offen nt lds\n"
"s_add_u32 m0, s50, 116640\n"
"s_nop 0\n"
"buffer_load_dwordx4 v250, s[36:39], s54 offen offset:256 nt lds\n"
"s_add_u32 m0, s50, 117440\n"
"s_nop 0\n"
"buffer_load_dwordx4 v250, s[36:39], s54 offen offset:512 nt lds\n"
"s_add_u32 m0, s50, 118240\n"
"s_nop 0\n"
"buffer_load_dwordx4 v250, s[36:39], s54 offen offset:768 nt lds\n"
"s_add_u32 s53, s53, 8192\n"
"s_and_b32 s53, s53, 258047\n"
"s_lshl_b32 s38, s38, 1\n"
"s_add_u32 s54, s53, 4096\n"
"s_waitcnt vmcnt(0)\n"
"s_barrier\n"
"s_branch L1_attn_h1\n"
"L2_go:\n"
"v_mov_b32 v246, s74\n"
"v_mov_b32 v247, s75\n"
"s_waitcnt lgkmcnt(14)\n"
"v_mfma_f32_16x16x32_bf16 v[16:19], a[224:227], a[112:115], 0\n"
"s_waitcnt lgkmcnt(12)\n"
"ds_read_b64_tr_b16 a[0:1], v224\n"
"v_mfma_f32_16x16x32_bf16 v[16:19], a[228:231], a[140:143], v[16:19]\n"
"s_waitcnt lgkmcnt(11)\n"
"ds_read_b64_tr_b16 a[2:3], v224 offset:4352\n"
"s_add_u32 m0, s70, 0\n"
"buffer_load_dwordx2 v[242:243], v241, s[76:79], 0 offen sc1\n"
"v_mfma_f32_16x16x32_bf16 v[20:23], a[232:235], a[112:115], 0\n"
"s_waitcnt lgkmcnt(10)\n"
"buffer_load_dwordx4 v228, s[32:35], s49 offen lds\n"
"ds_read_b64_tr_b16 a[4:5], v224 offset:8704\n"
"v_mfma_f32_16x16x32_bf16 v[20:23], a[236:239], a[140:143], v[20:23]\n"
"s_waitcnt lgkmcnt(9)\n"
"ds_read_b64_tr_b16 a[6:7], v224 offset:13056\n"
"v_mfma_f32_16x16x32_bf16 v[24:27], a[240:243], a[112:115], 0\n"
"s_waitcnt lgkmcnt(8)\n"
"ds_read_b64_tr_b16 a[8:9], v225\n"
"s_add_u32 m0, s70, 1088\n"
"v_mfma_f32_16x16x32_bf16 v[24:27], a[244:247], a[140:143], v[24:27]\n"
"s_waitcnt lgkmcnt(7)\n"
"buffer_load_dwordx4 v229, s[32:35], s49 offen lds\n"
"ds_read_b64_tr_b16 a[10:11], v225 offset:4352\n"
"v_mfma_f32_16x16x32_bf16 v[28:31], a[248:251], a[112:115], 0\n"
"s_waitcnt lgkmcnt(6)\n"
"ds_read_b64_tr_b16 a[12:13], v225 offset:8704\n"
"v_mfma_f32_16x16x32_bf16 v[28:31], a[252:255], a[140:143], v[28:31]\n"
"ds_read_b64_tr_b16 a[14:15], v225 offset:13056\n"
"s_add_u32 m0, s70, 2176\n"
"v_mfma_f32_16x16x32_bf16 v[48:51], a[224:227], a[116:119], 0\n"
"buffer_load_dwordx4 v230, s[32:35], s49 offen lds\n"
"ds_read_b64_tr_b16 a[16:17], v224 offset:64\n"
"v_mfma_f32_16x16x32_bf16 v[48:51], a[228:231], a[144:147], v[48:51]\n"
"ds_read_b64_tr_b16 a[18:19], v224 offset:4416\n"
"v_mfma_f32_16x16x32_bf16 v[52:55], a[232:235], a[116:119], 0\n"
"ds_read_b64_tr_b16 a[20:21], v224 offset:8768\n"
"s_add_u32 m0, s70, 3264\n"
"v_mfma_f32_16x16x32_bf16 v[52:55], a[236:239], a[144:147], v[52:55]\n"
"buffer_load_dwordx4 v231, s[32:35], s49 offen lds\n"
"ds_read_b64_tr_b16 a[22:23], v224 offset:13120\n"
"v_mfma_f32_16x16x32_bf16 v[56:59], a[240:243], a[116:119], 0\n"
"ds_read_b64_tr_b16 a[24:25], v225 offset:64\n"
"v_mfma_f32_16x16x32_bf16 v[56:59], a[244:247], a[144:147], v[56:59]\n"
"ds_read_b64_tr_b16 a[26:27], v225 offset:4416\n"
"s_add_u32 m0, s70, 4352\n"
"v_mfma_f32_16x16x32_bf16 v[60:63], a[248:251], a[116:119], 0\n"
"buffer_load_dwordx4 v232, s[32:35], s49 offen lds\n"
"ds_read_b64_tr_b16 a[28:29], v225 offset:8768\n"
"v_mfma_f32_16x16x32_bf16 v[60:63], a[252:255], a[144:147], v[60:63]\n"
"ds_read_b64_tr_b16 a[30:31], v225 offset:13120\n"
"v_mfma_f32_16x16x32_bf16 v[80:83], a[224:227], a[120:123], 0\n"
"s_add_u32 m0, s70, 5440\n"
"v_mfma_f32_16x16x32_bf16 v[80:83], a[228:231], a[148:151], v[80:83]\n"
"buffer_load_dwordx4 v233, s[32:35], s49 offen lds\n"
"v_mfma_f32_16x16x32_bf16 v[84:87], a[232:235], a[120:123], 0\n"
"v_mfma_f32_16x16x32_bf16 v[84:87], a[236:239], a[148:151], v[84:87]\n"
"s_add_u32 m0, s70, 6528\n"
"v_mfma_f32_16x16x32_bf16 v[88:91], a[240:243], a[120:123], 0\n"
"buffer_load_dwordx4 v234, s[32:35], s49 offen lds\n"
"v_mfma_f32_16x16x32_bf16 v[88:91], a[244:247], a[148:151], v[88:91]\n"
"v_mfma_f32_16x16x32_bf16 v[92:95], a[248:251], a[120:123], 0\n"
"s_add_u32 m0, s70, 7616\n"
"v_mfma_f32_16x16x32_bf16 v[92:95], a[252:255], a[148:151], v[92:95]\n"
"buffer_load_dwordx4 v235, s[32:35], s49 offen lds\n"
"s_add_u32 s49, s49, 65536\n"
"v_mfma_f32_16x16x32_bf16 v[112:115], a[224:227], a[124:127], 0\n"
"v_mfma_f32_16x16x32_bf16 v[112:115], a[228:231], a[152:155], v[112:115]\n"
"s_add_u32 m0, s70, 8704\n"
"v_mfma_f32_16x16x32_bf16 v[116:119], a[232:235], a[124:127], 0\n"
"buffer_load_dwordx4 v228, s[32:35], s49 offen lds\n"
"v_mfma_f32_16x16x32_bf16 v[116:119], a[236:239], a[152:155], v[116:119]\n"
"v_mfma_f32_16x16x32_bf16 v[120:123], a[240:243], a[124:127], 0\n"
"s_add_u32 m0, s70, 9792\n"
"v_mfma_f32_16x16x32_bf16 v[120:123], a[244:247], a[152:155], v[120:123]\n"
"buffer_load_dwordx4 v229, s[32:35], s49 offen lds\n"
"v_mfma_f32_16x16x32_bf16 v[124:127], a[248:251], a[124:127], 0\n"
"v_mfma_f32_16x16x32_bf16 v[124:127], a[252:255], a[152:155], v[124:127]\n"
"s_add_u32 m0, s70, 10880\n"
"v_mfma_f32_16x16x32_bf16 v[144:147], a[224:227], a[128:131], 0\n"
"buffer_load_dwordx4 v230, s[32:35], s49 offen lds\n"
"v_mfma_f32_16x16x32_bf16 v[144:147], a[228:231], a[156:159], v[144:147]\n"
"v_mfma_f32_16x16x32_bf16 v[148:151], a[232:235], a[128:131], 0\n"
"s_add_u32 m0, s70, 11968\n"
"v_mfma_f32_16x16x32_bf16 v[148:151], a[236:239], a[156:159], v[148:151]\n"
"buffer_load_dwordx4 v231, s[32:35], s49 offen lds\n"
"v_mfma_f32_16x16x32_bf16 v[152:155], a[240:243], a[128:131], 0\n"
"v_mfma_f32_16x16x32_bf16 v[152:155], a[244:247], a[156:159], v[152:155]\n"
"s_add_u32 m0, s70, 13056\n"
"v_mfma_f32_16x16x32_bf16 v[156:159], a[248:251], a[128:131], 0\n"
"buffer_load_dwordx4 v232, s[32:35], s49 offen lds\n"
"v_mfma_f32_16x16x32_bf16 v[156:159], a[252:255], a[156:159], v[156:159]\n"
"v_mfma_f32_16x16x32_bf16 v[176:179], a[224:227], a[132:135], 0\n"
"s_add_u32 m0, s70, 14144\n"
"v_mfma_f32_16x16x32_bf16 v[176:179], a[228:231], a[160:163], v[176:179]\n"
"buffer_load_dwordx4 v233, s[32:35], s49 offen lds\n"
"v_mfma_f32_16x16x32_bf16 v[180:183], a[232:235], a[132:135], 0\n"
"v_mfma_f32_16x16x32_bf16 v[180:183], a[236:239], a[160:163], v[180:183]\n"
"s_add_u32 m0, s70, 15232\n"
"v_mfma_f32_16x16x32_bf16 v[184:187], a[240:243], a[132:135], 0\n"
"buffer_load_dwordx4 v234, s[32:35], s49 offen lds\n"
"v_mfma_f32_16x16x32_bf16 v[184:187], a[244:247], a[160:163], v[184:187]\n"
"v_mfma_f32_16x16x32_bf16 v[188:191], a[248:251], a[132:135], 0\n"
"s_add_u32 m0, s70, 16320\n"
"v_mfma_f32_16x16x32_bf16 v[188:191], a[252:255], a[160:163], v[188:191]\n"
"buffer_load_dwordx4 v235, s[32:35], s49 offen lds\n"
"v_mfma_f32_16x16x32_bf16 v[208:211], a[224:227], a[136:139], 0\n"
"v_mfma_f32_16x16x32_bf16 v[208:211], a[228:231], a[164:167], v[208:211]\n"
"v_mfma_f32_16x16x32_bf16 v[212:215], a[232:235], a[136:139], 0\n"
"v_mfma_f32_16x16x32_bf16 v[212:215], a[236:239], a[164:167], v[212:215]\n"
"v_mfma_f32_16x16x32_bf16 v[216:219], a[240:243], a[136:139], 0\n"
"v_mfma_f32_16x16x32_bf16 v[216:219], a[244:247], a[164:167], v[216:219]\n"
"v_mfma_f32_16x16x32_bf16 v[220:223], a[248:251], a[136:139], 0\n"
"v_mfma_f32_16x16x32_bf16 v[220:223], a[252:255], a[164:167], v[220:223]\n"
"s_waitcnt lgkmcnt(14)\n"
"v_mfma_f32_16x16x32_bf16 v[0:3], a[0:3], a[112:115], 0\n"
"s_waitcnt lgkmcnt(12)\n"
"s_mul_i32 s51, s59, 106496\n"
"v_mfma_f32_16x16x32_bf16 v[0:3], a[4:7], a[140:143], v[0:3]\n"
"s_waitcnt lgkmcnt(10)\n"
"s_add_u32 s57, s51, 13312\n"
"v_mfma_f32_16x16x32_bf16 v[4:7], a[8:11], a[112:115], 0\n"
"s_waitcnt lgkmcnt(8)\n"
"v_mfma_f32_16x16x32_bf16 v[4:7], a[12:15], a[140:143], v[4:7]\n"
"s_waitcnt lgkmcnt(6)\n"
"v_mfma_f32_16x16x32_bf16 v[8:11], a[16:19], a[112:115], 0\n"
"s_waitcnt lgkmcnt(4)\n"
"v_mfma_f32_16x16x32_bf16 v[8:11], a[20:23], a[140:143], v[8:11]\n"
"s_waitcnt lgkmcnt(2)\n"
"v_mfma_f32_16x16x32_bf16 v[12:15], a[24:27], a[112:115], 0\n"
"s_waitcnt lgkmcnt(0)\n"
"v_mfma_f32_16x16x32_bf16 v[12:15], a[28:31], a[140:143], v[12:15]\n"
"v_mfma_f32_16x16x32_bf16 v[32:35], a[0:3], a[116:119], 0\n"
"v_mfma_f32_16x16x32_bf16 v[32:35], a[4:7], a[144:147], v[32:35]\n"
"v_mfma_f32_16x16x32_bf16 v[36:39], a[8:11], a[116:119], 0\n"
"v_mfma_f32_16x16x32_bf16 v[36:39], a[12:15], a[144:147], v[36:39]\n"
"v_mfma_f32_16x16x32_bf16 v[40:43], a[16:19], a[116:119], 0\n"
"s_lshl_b32 s14, s23, 4\n"
"s_add_u32 s14, s14, s24\n"
"s_lshl_b32 s14, s14, 3\n"
"v_mfma_f32_16x16x32_bf16 v[40:43], a[20:23], a[144:147], v[40:43]\n"
"s_add_u32 s14, s14, s21\n"
"s_lshl_b32 s14, s14, 5\n"
"v_mfma_f32_16x16x32_bf16 v[44:47], a[24:27], a[116:119], 0\n"
"s_waitcnt vmcnt(17)\n"
"s_mov_b64 exec, 1\n"
"buffer_store_dwordx2 v[246:247], off, s[76:79], s14 sc1\n"
"s_mov_b64 exec, -1\n"
"s_waitcnt vmcnt(16)\n"
"v_cmp_eq_u64 vcc, s[74:75], v[242:243]\n"
"s_nop 0\n"
"s_or_b32 s66, s66, vcc_lo\n"
"s_bitcmp1_b32 s66, s59\n"
"s_cbranch_scc0 L2_c0_s\n"
"L2_c0_r:\n"
"v_mfma_f32_16x16x32_bf16 v[44:47], a[28:31], a[144:147], v[44:47]\n"
"buffer_load_dwordx4 a[112:115], v236, s[28:31], s51 offen sc1\n"
"v_mfma_f32_16x16x32_bf16 v[64:67], a[0:3], a[120:123], 0\n"
"v_mfma_f32_16x16x32_bf16 v[64:67], a[4:7], a[148:151], v[64:67]\n"
"v_mfma_f32_16x16x32_bf16 v[68:71], a[8:11], a[120:123], 0\n"
"buffer_load_dwordx4 a[140:143], v236, s[28:31], s57 offen sc1\n"
"v_mfma_f32_16x16x32_bf16 v[68:71], a[12:15], a[148:151], v[68:71]\n"
"v_mfma_f32_16x16x32_bf16 v[72:75], a[16:19], a[120:123], 0\n"
"v_mfma_f32_16x16x32_bf16 v[72:75], a[20:23], a[148:151], v[72:75]\n"
"buffer_load_dwordx4 a[116:119], v236, s[28:31], s51 offen offset:1024 sc1\n"
"v_mfma_f32_16x16x32_bf16 v[76:79], a[24:27], a[120:123], 0\n"
"v_mfma_f32_16x16x32_bf16 v[76:79], a[28:31], a[148:151], v[76:79]\n"
"v_mfma_f32_16x16x32_bf16 v[96:99], a[0:3], a[124:127], 0\n"
"buffer_load_dwordx4 a[144:147], v236, s[28:31], s57 offen offset:1024 sc1\n"
"v_mfma_f32_16x16x32_bf16 v[96:99], a[4:7], a[152:155], v[96:99]\n"
"v_mfma_f32_16x16x32_bf16 v[100:103], a[8:11], a[124:127], 0\n"
"v_mfma_f32_16x16x32_bf16 v[100:103], a[12:15], a[152:155], v[100:103]\n"
"buffer_load_dwordx4 a[120:123], v236, s[28:31], s51 offen offset:2048 sc1\n"
"v_mfma_f32_16x16x32_bf16 v[104:107], a[16:19], a[124:127], 0\n"
"v_mfma_f32_16x16x32_bf16 v[104:107], a[20:23], a[152:155], v[104:107]\n"
"v_mfma_f32_16x16x32_bf16 v[108:111], a[24:27], a[124:127], 0\n"
"buffer_load_dwordx4 a[148:151], v236, s[28:31], s57 offen offset:2048 sc1\n"
"v_mfma_f32_16x16x32_bf16 v[108:111], a[28:31], a[152:155], v[108:111]\n"
"ds_read_b64_tr_b16 a[224:225], v226 offset:128\n"
"v_mfma_f32_16x16x32_bf16 v[128:131], a[0:3], a[128:131], 0\n"
"ds_read_b64_tr_b16 a[226:227], v226 offset:4480\n"
"v_mfma_f32_16x16x32_bf16 v[128:131], a[4:7], a[156:159], v[128:131]\n"
"ds_read_b64_tr_b16 a[228:229], v226 offset:8832\n"
"buffer_load_dwordx4 a[124:127], v237, s[28:31], s51 offen sc1\n"
"v_mfma_f32_16x16x32_bf16 v[132:135], a[8:11], a[128:131], 0\n"
"ds_read_b64_tr_b16 a[230:231], v226 offset:13184\n"
"v_mfma_f32_16x16x32_bf16 v[132:135], a[12:15], a[156:159], v[132:135]\n"
"ds_read_b64_tr_b16 a[232:233], v227 offset:128\n"
"v_mfma_f32_16x16x32_bf16 v[136:139], a[16:19], a[128:131], 0\n"
"ds_read_b64_tr_b16 a[234:235], v227 offset:4480\n"
"buffer_load_dwordx4 a[152:155], v237, s[28:31], s57 offen sc1\n"
"v_mfma_f32_16x16x32_bf16 v[136:139], a[20:23], a[156:159], v[136:139]\n"
"ds_read_b64_tr_b16 a[236:237], v227 offset:8832\n"
"v_mfma_f32_16x16x32_bf16 v[140:143], a[24:27], a[128:131], 0\n"
"ds_read_b64_tr_b16 a[238:239], v227 offset:13184\n"
"v_mfma_f32_16x16x32_bf16 v[140:143], a[28:31], a[156:159], v[140:143]\n"
"ds_read_b64_tr_b16 a[240:241], v226 offset:192\n"
"buffer_load_dwordx4 a[128:131], v237, s[28:31], s51 offen offset:1024 sc1\n"
"v_mfma_f32_16x16x32_bf16 v[160:163], a[0:3], a[132:135], 0\n"
"ds_read_b64_tr_b16 a[242:243], v226 offset:4544\n"
"v_mfma_f32_16x16x32_bf16 v[160:163], a[4:7], a[160:163], v[160:163]\n"
"ds_read_b64_tr_b16 a[244:245], v226 offset:8896\n"
"v_mfma_f32_16x16x32_bf16 v[164:167], a[8:11], a[132:135], 0\n"
"ds_read_b64_tr_b16 a[246:247], v226 offset:13248\n"
"buffer_load_dwordx4 a[156:159], v237, s[28:31], s57 offen offset:1024 sc1\n"
"v_mfma_f32_16x16x32_bf16 v[164:167], a[12:15], a[160:163], v[164:167]\n"
"ds_read_b64_tr_b16 a[248:249], v227 offset:192\n"
"v_mfma_f32_16x16x32_bf16 v[168:171], a[16:19], a[132:135], 0\n"
"ds_read_b64_tr_b16 a[250:251], v227 offset:4544\n"
"v_mfma_f32_16x16x32_bf16 v[168:171], a[20:23], a[160:163], v[168:171]\n"
"ds_read_b64_tr_b16 a[252:253], v227 offset:8896\n"
"v_mfma_f32_16x16x32_bf16 v[172:175], a[24:27], a[132:135], 0\n"
"ds_read_b64_tr_b16 a[254:255], v227 offset:13248\n"
"v_mfma_f32_16x16x32_bf16 v[172:175], a[28:31], a[160:163], v[172:175]\n"
"buffer_load_dwordx4 a[132:135], v237, s[28:31], s51 offen offset:2048 sc1\n"
"v_mfma_f32_16x16x32_bf16 v[192:195], a[0:3], a[136:139], 0\n"
"buffer_load_dwordx4 a[160:163], v237, s[28:31], s57 offen offset:2048 sc1\n"
"v_mfma_f32_16x16x32_bf16 v[192:195], a[4:7], a[164:167], v[192:195]\n"
"v_mfma_f32_16x16x32_bf16 v[196:199], a[8:11], a[136:139], 0\n"
"v_mfma_f32_16x16x32_bf16 v[196:199], a[12:15], a[164:167], v[196:199]\n"
"v_mfma_f32_16x16x32_bf16 v[200:203], a[16:19], a[136:139], 0\n"
"v_mfma_f32_16x16x32_bf16 v[200:203], a[20:23], a[164:167], v[200:203]\n"
"v_mfma_f32_16x16x32_bf16 v[204:207], a[24:27], a[136:139], 0\n"
"s_waitcnt lgkmcnt(0)\n"
"v_mfma_f32_16x16x32_bf16 v[204:207], a[28:31], a[164:167], v[204:207]\n"
"buffer_load_dwordx4 a[136:139], v238, s[28:31], s51 offen sc1\n"
"v_mfma_f32_16x16x32_bf16 v[16:19], a[224:227], a[168:171], v[16:19]\n"
"ds_read_b64_tr_b16 a[0:1], v226\n"
"buffer_load_dwordx4 a[164:167], v238, s[28:31], s57 offen sc1\n"
"v_mfma_f32_16x16x32_bf16 v[16:19], a[228:231], a[196:199], v[16:19]\n"
"ds_read_b64_tr_b16 a[2:3], v226 offset:4352\n"
"v_mfma_f32_16x16x32_bf16 v[20:23], a[232:235], a[168:171], v[20:23]\n"
"ds_read_b64_tr_b16 a[4:5], v226 offset:8704\n"
"v_mfma_f32_16x16x32_bf16 v[20:23], a[236:239], a[196:199], v[20:23]\n"
"ds_read_b64_tr_b16 a[6:7], v226 offset:13056\n"
"v_mfma_f32_16x16x32_bf16 v[24:27], a[240:243], a[168:171], v[24:27]\n"
"ds_read_b64_tr_b16 a[8:9], v227\n"
"v_mfma_f32_16x16x32_bf16 v[24:27], a[244:247], a[196:199], v[24:27]\n"
"ds_read_b64_tr_b16 a[10:11], v227 offset:4352\n"
"v_mfma_f32_16x16x32_bf16 v[28:31], a[248:251], a[168:171], v[28:31]\n"
"ds_read_b64_tr_b16 a[12:13], v227 offset:8704\n"
"v_mfma_f32_16x16x32_bf16 v[28:31], a[252:255], a[196:199], v[28:31]\n"
"ds_read_b64_tr_b16 a[14:15], v227 offset:13056\n"
"v_mfma_f32_16x16x32_bf16 v[48:51], a[224:227], a[172:175], v[48:51]\n"
"ds_read_b64_tr_b16 a[16:17], v226 offset:64\n"
"v_mfma_f32_16x16x32_bf16 v[48:51], a[228:231], a[200:203], v[48:51]\n"
"ds_read_b64_tr_b16 a[18:19], v226 offset:4416\n"
"v_mfma_f32_16x16x32_bf16 v[52:55], a[232:235], a[172:175], v[52:55]\n"
"ds_read_b64_tr_b16 a[20:21], v226 offset:8768\n"
"v_mfma_f32_16x16x32_bf16 v[52:55], a[236:239], a[200:203], v[52:55]\n"
"ds_read_b64_tr_b16 a[22:23], v226 offset:13120\n"
"v_mfma_f32_16x16x32_bf16 v[56:59], a[240:243], a[172:175], v[56:59]\n"
"ds_read_b64_tr_b16 a[24:25], v227 offset:64\n"
"v_mfma_f32_16x16x32_bf16 v[56:59], a[244:247], a[200:203], v[56:59]\n"
"ds_read_b64_tr_b16 a[26:27], v227 offset:4416\n"
"v_mfma_f32_16x16x32_bf16 v[60:63], a[248:251], a[172:175], v[60:63]\n"
"ds_read_b64_tr_b16 a[28:29], v227 offset:8768\n"
"v_mfma_f32_16x16x32_bf16 v[60:63], a[252:255], a[200:203], v[60:63]\n"
"ds_read_b64_tr_b16 a[30:31], v227 offset:13120\n"
"v_mfma_f32_16x16x32_bf16 v[80:83], a[224:227], a[176:179], v[80:83]\n"
"v_add_u32 v244, s48, v251\n"
"s_xor_b32 s40, s21, 2\n"
"v_mfma_f32_16x16x32_bf16 v[80:83], a[228:231], a[204:207], v[80:83]\n"
"s_mul_i32 s40, s40, 34816\n"
"v_mfma_f32_16x16x32_bf16 v[84:87], a[232:235], a[176:179], v[84:87]\n"
"s_add_u32 s40, s40, 4096\n"
"v_add_u32 v245, s40, v251\n"
"v_mfma_f32_16x16x32_bf16 v[84:87], a[236:239], a[204:207], v[84:87]\n"
"s_lshl_b32 s40, s21, 10\n"
"v_mfma_f32_16x16x32_bf16 v[88:91], a[240:243], a[176:179], v[88:91]\n"
"v_add_u32 v253, s40, v251\n"
"v_mfma_f32_16x16x32_bf16 v[88:91], a[244:247], a[204:207], v[88:91]\n"
"v_add_u32 v254, 69632, v253\n"
"s_and_b32 s40, s21, 1\n"
"v_mfma_f32_16x16x32_bf16 v[92:95], a[248:251], a[176:179], v[92:95]\n"
"s_lshl_b32 s40, s40, 1\n"
"v_mfma_f32_16x16x32_bf16 v[92:95], a[252:255], a[204:207], v[92:95]\n"
"s_add_u32 s40, s40, s22\n"
"v_mfma_f32_16x16x32_bf16 v[112:115], a[224:227], a[180:183], v[112:115]\n"
"s_mul_i32 s42, s40, 98304\n"
"v_lshrrev_b32 v248, 8, v251\n"
"v_mfma_f32_16x16x32_bf16 v[112:115], a[228:231], a[208:211], v[112:115]\n"
"v_lshlrev_b32 v248, 4, v248\n"
"v_mfma_f32_16x16x32_bf16 v[116:119], a[232:235], a[180:183], v[116:119]\n"
"v_bfe_u32 v249, v251, 4, 4\n"
"v_mfma_f32_16x16x32_bf16 v[116:119], a[236:239], a[208:211], v[116:119]\n"
"v_lshl_add_u32 v250, v249, 11, v248\n"
"v_add_u32 v250, s42, v250\n"
"v_mfma_f32_16x16x32_bf16 v[120:123], a[240:243], a[180:183], v[120:123]\n"
"v_add_u32 v224, s72, v224\n"
"v_mfma_f32_16x16x32_bf16 v[120:123], a[244:247], a[208:211], v[120:123]\n"
"v_add_u32 v225, s72, v225\n"
"v_mfma_f32_16x16x32_bf16 v[124:127], a[248:251], a[180:183], v[124:127]\n"
"v_add_u32 v226, s72, v226\n"
"v_mfma_f32_16x16x32_bf16 v[124:127], a[252:255], a[208:211], v[124:127]\n"
"v_add_u32 v227, s72, v227\n"
"v_mfma_f32_16x16x32_bf16 v[144:147], a[224:227], a[184:187], v[144:147]\n"
"s_sub_u32 s70, s70, s72\n"
"v_mfma_f32_16x16x32_bf16 v[144:147], a[228:231], a[212:215], v[144:147]\n"
"s_sub_u32 s72, 0, s72\n"
"v_mfma_f32_16x16x32_bf16 v[148:151], a[232:235], a[184:187], v[148:151]\n"
"s_lshl_b32 s40, s22, 6\n"
"v_mfma_f32_16x16x32_bf16 v[148:151], a[236:239], a[212:215], v[148:151]\n"
"s_add_u32 s40, s40, 393216\n"
"v_mfma_f32_16x16x32_bf16 v[152:155], a[240:243], a[184:187], v[152:155]\n"
"s_sub_u32 s42, s40, s42\n"
"s_mul_i32 s40, s23, 425984\n"
"v_mfma_f32_16x16x32_bf16 v[152:155], a[244:247], a[212:215], v[152:155]\n"
"s_lshl_b32 s41, s12, 1\n"
"v_mfma_f32_16x16x32_bf16 v[156:159], a[248:251], a[184:187], v[156:159]\n"
"s_add_u32 s41, s41, s13\n"
"s_lshl_b32 s41, s41, 7\n"
"buffer_load_dwordx2 v[242:243], v241, s[76:79], 0 offen sc1\n"
"v_mfma_f32_16x16x32_bf16 v[156:159], a[252:255], a[212:215], v[156:159]\n"
"s_add_u32 s40, s40, s41\n"
"v_mfma_f32_16x16x32_bf16 v[176:179], a[224:227], a[188:191], v[176:179]\n"
"s_add_u32 s8, s68, s40\n"
"s_addc_u32 s9, s69, 0\n"
"v_mfma_f32_16x16x32_bf16 v[176:179], a[228:231], a[216:219], v[176:179]\n"
"s_and_b32 s9, s9, 65535\n"
"v_mfma_f32_16x16x32_bf16 v[180:183], a[232:235], a[188:191], v[180:183]\n"
"s_sub_u32 s10, 425984, s41\n"
"v_mfma_f32_16x16x32_bf16 v[180:183], a[236:239], a[216:219], v[180:183]\n"
"s_mov_b32 s11, 131072\n"
"v_mfma_f32_16x16x32_bf16 v[184:187], a[240:243], a[188:191], v[184:187]\n"
"s_lshl_b32 s40, s23, 3\n"
"s_add_u32 s40, s40, s12\n"
"v_mfma_f32_16x16x32_bf16 v[184:187], a[244:247], a[216:219], v[184:187]\n"
"s_lshl_b32 s40, s40, 2\n"
"v_mfma_f32_16x16x32_bf16 v[188:191], a[248:251], a[188:191], v[188:191]\n"
"s_add_u32 s40, s40, s21\n"
"v_mfma_f32_16x16x32_bf16 v[188:191], a[252:255], a[216:219], v[188:191]\n"
"s_waitcnt lgkmcnt(14)\n"
"s_mul_i32 s40, s40, 26624\n"
"s_add_u32 s40, s40, 41943040\n"
"v_mfma_f32_16x16x32_bf16 v[0:3], a[0:3], a[168:171], v[0:3]\n"
"s_waitcnt lgkmcnt(12)\n"
"s_add_u32 s36, s18, s40\n"
"s_addc_u32 s37, s19, 0\n"
"v_mfma_f32_16x16x32_bf16 v[0:3], a[4:7], a[196:199], v[0:3]\n"
"s_waitcnt lgkmcnt(10)\n"
"v_mfma_f32_16x16x32_bf16 v[4:7], a[8:11], a[168:171], v[4:7]\n"
"s_waitcnt lgkmcnt(8)\n"
"s_and_b32 s37, s37, 65535\n"
"s_mov_b32 s38, 26624\n"
"v_mfma_f32_16x16x32_bf16 v[4:7], a[12:15], a[196:199], v[4:7]\n"
"s_waitcnt lgkmcnt(6)\n"
"s_mov_b32 s39, 131072\n"
"v_mfma_f32_16x16x32_bf16 v[8:11], a[16:19], a[168:171], v[8:11]\n"
"s_waitcnt lgkmcnt(4)\n"
"s_mul_i32 s45, s13, 13312\n"
"v_mfma_f32_16x16x32_bf16 v[8:11], a[20:23], a[196:199], v[8:11]\n"
"s_waitcnt lgkmcnt(2)\n"
"s_sub_u32 s46, 13312, s45\n"
"s_lshl_b32 s40, s23, 4\n"
"v_mfma_f32_16x16x32_bf16 v[12:15], a[24:27], a[168:171], v[12:15]\n"
"s_waitcnt lgkmcnt(0)\n"
"s_add_u32 s40, s40, s24\n"
"v_mfma_f32_16x16x32_bf16 v[12:15], a[28:31], a[196:199], v[12:15]\n"
"s_lshl_b32 s40, s40, 8\n"
"s_lshl_b32 s41, s21, 5\n"
"buffer_load_dwordx4 a[168:171], v239, s[28:31], s51 offen sc1\n"
"v_mfma_f32_16x16x32_bf16 v[32:35], a[0:3], a[172:175], v[32:35]\n"
"s_add_u32 s40, s40, s41\n"
"v_mfma_f32_16x16x32_bf16 v[32:35], a[4:7], a[200:203], v[32:35]\n"
"s_add_u32 s73, s40, 128\n"
"v_mfma_f32_16x16x32_bf16 v[36:39], a[8:11], a[172:175], v[36:39]\n"
"s_xor_b32 s44, s73, 2048\n"
"s_cmp_eq_u32 s12, 7\n"
"s_cselect_b32 s47, 6, 7\n"
"v_mfma_f32_16x16x32_bf16 v[36:39], a[12:15], a[200:203], v[36:39]\n"
"s_add_u32 s40, s47, 8\n"
"buffer_load_dwordx4 a[196:199], v239, s[28:31], s57 offen sc1\n"
"v_mfma_f32_16x16x32_bf16 v[40:43], a[16:19], a[172:175], v[40:43]\n"
"s_lshl_b32 s41, s13, 3\n"
"v_mfma_f32_16x16x32_bf16 v[40:43], a[20:23], a[200:203], v[40:43]\n"
"s_sub_u32 s40, s40, s41\n"
"s_mul_i32 s61, s40, 106496\n"
"v_mfma_f32_16x16x32_bf16 v[44:47], a[24:27], a[172:175], v[44:47]\n"
"s_lshl_b32 s60, s40, 19\n"
"v_mfma_f32_16x16x32_bf16 v[44:47], a[28:31], a[200:203], v[44:47]\n"
"s_and_b32 s40, s21, 1\n"
"buffer_load_dwordx4 a[172:175], v239, s[28:31], s51 offen offset:1024 sc1\n"
"v_mfma_f32_16x16x32_bf16 v[64:67], a[0:3], a[176:179], v[64:67]\n"
"s_lshl_b32 s40, s40, 1\n"
"s_sub_u32 s40, 1, s40\n"
"v_mfma_f32_16x16x32_bf16 v[64:67], a[4:7], a[204:207], v[64:67]\n"
"s_mul_i32 s62, s40, 26624\n"
"v_mfma_f32_16x16x32_bf16 v[68:71], a[8:11], a[176:179], v[68:71]\n"
"s_mul_i32 s62, s62, s13\n"
"v_mfma_f32_16x16x32_bf16 v[68:71], a[12:15], a[204:207], v[68:71]\n"
"s_lshl_b32 s40, s40, 17\n"
"s_sub_u32 s41, 1, s13\n"
"buffer_load_dwordx4 a[200:203], v239, s[28:31], s57 offen offset:1024 sc1\n"
"v_mfma_f32_16x16x32_bf16 v[72:75], a[16:19], a[176:179], v[72:75]\n"
"s_mul_i32 s40, s40, s41\n"
"v_mfma_f32_16x16x32_bf16 v[72:75], a[20:23], a[204:207], v[72:75]\n"
"s_add_u32 s60, s60, s40\n"
"v_mfma_f32_16x16x32_bf16 v[76:79], a[24:27], a[176:179], v[76:79]\n"
"s_waitcnt vmcnt(19)\n"
"s_barrier\n"
"v_mfma_f32_16x16x32_bf16 v[76:79], a[28:31], a[204:207], v[76:79]\n"
"buffer_load_dwordx4 a[176:179], v239, s[28:31], s51 offen offset:2048 sc1\n"
"ds_read_b64_tr_b16 a[224:225], v224 offset:128\n"
"v_mfma_f32_16x16x32_bf16 v[96:99], a[0:3], a[180:183], v[96:99]\n"
"ds_read_b64_tr_b16 a[226:227], v224 offset:4480\n"
"v_mfma_f32_16x16x32_bf16 v[96:99], a[4:7], a[208:211], v[96:99]\n"
"ds_read_b64_tr_b16 a[228:229], v224 offset:8832\n"
"v_mfma_f32_16x16x32_bf16 v[100:103], a[8:11], a[180:183], v[100:103]\n"
"ds_read_b64_tr_b16 a[230:231], v224 offset:13184\n"
"v_mfma_f32_16x16x32_bf16 v[100:103], a[12:15], a[208:211], v[100:103]\n"
"buffer_load_dwordx4 a[204:207], v239, s[28:31], s57 offen offset:2048 sc1\n"
"ds_read_b64_tr_b16 a[232:233], v225 offset:128\n"
"v_mfma_f32_16x16x32_bf16 v[104:107], a[16:19], a[180:183], v[104:107]\n"
"ds_read_b64_tr_b16 a[234:235], v225 offset:4480\n"
"v_mfma_f32_16x16x32_bf16 v[104:107], a[20:23], a[208:211], v[104:107]\n"
"ds_read_b64_tr_b16 a[236:237], v225 offset:8832\n"
"v_mfma_f32_16x16x32_bf16 v[108:111], a[24:27], a[180:183], v[108:111]\n"
"ds_read_b64_tr_b16 a[238:239], v225 offset:13184\n"
"v_mfma_f32_16x16x32_bf16 v[108:111], a[28:31], a[208:211], v[108:111]\n"
"buffer_load_dwordx4 a[180:183], v240, s[28:31], s51 offen sc1\n"
"ds_read_b64_tr_b16 a[240:241], v224 offset:192\n"
"v_mfma_f32_16x16x32_bf16 v[128:131], a[0:3], a[184:187], v[128:131]\n"
"ds_read_b64_tr_b16 a[242:243], v224 offset:4544\n"
"v_mfma_f32_16x16x32_bf16 v[128:131], a[4:7], a[212:215], v[128:131]\n"
"ds_read_b64_tr_b16 a[244:245], v224 offset:8896\n"
"v_mfma_f32_16x16x32_bf16 v[132:135], a[8:11], a[184:187], v[132:135]\n"
"ds_read_b64_tr_b16 a[246:247], v224 offset:13248\n"
"v_mfma_f32_16x16x32_bf16 v[132:135], a[12:15], a[212:215], v[132:135]\n"
"buffer_load_dwordx4 a[208:211], v240, s[28:31], s57 offen sc1\n"
"ds_read_b64_tr_b16 a[248:249], v225 offset:192\n"
"v_mfma_f32_16x16x32_bf16 v[136:139], a[16:19], a[184:187], v[136:139]\n"
"ds_read_b64_tr_b16 a[250:251], v225 offset:4544\n"
"v_mfma_f32_16x16x32_bf16 v[136:139], a[20:23], a[212:215], v[136:139]\n"
"ds_read_b64_tr_b16 a[252:253], v225 offset:8896\n"
"v_mfma_f32_16x16x32_bf16 v[140:143], a[24:27], a[184:187], v[140:143]\n"
"ds_read_b64_tr_b16 a[254:255], v225 offset:13248\n"
"v_mfma_f32_16x16x32_bf16 v[140:143], a[28:31], a[212:215], v[140:143]\n"
"buffer_load_dwordx4 a[184:187], v240, s[28:31], s51 offen offset:1024 sc1\n"
"v_mfma_f32_16x16x32_bf16 v[160:163], a[0:3], a[188:191], v[160:163]\n"
"s_add_u32 s59, s59, 1\n"
"v_mfma_f32_16x16x32_bf16 v[160:163], a[4:7], a[216:219], v[160:163]\n"
"s_cmp_eq_u32 s59, s12\n"
"v_mfma_f32_16x16x32_bf16 v[164:167], a[8:11], a[188:191], v[164:167]\n"
"s_addc_u32 s59, s59, 0\n"
"v_mfma_f32_16x16x32_bf16 v[164:167], a[12:15], a[216:219], v[164:167]\n"
"buffer_load_dwordx4 a[212:215], v240, s[28:31], s57 offen offset:1024 sc1\n"
"s_lshl_b32 s49, s59, 19\n"
"v_mfma_f32_16x16x32_bf16 v[168:171], a[16:19], a[188:191], v[168:171]\n"
"v_mfma_f32_16x16x32_bf16 v[168:171], a[20:23], a[216:219], v[168:171]\n"
"v_mfma_f32_16x16x32_bf16 v[172:175], a[24:27], a[188:191], v[172:175]\n"
"s_waitcnt lgkmcnt(0)\n"
"v_mfma_f32_16x16x32_bf16 v[172:175], a[28:31], a[216:219], v[172:175]\n"
"buffer_load_dwordx4 a[188:191], v240, s[28:31], s51 offen offset:2048 sc1\n"
"buffer_load_dwordx4 a[216:219], v240, s[28:31], s57 offen offset:2048 sc1\n"
"s_sub_u32 s52, 4, s13\n"
".p2align 6\n"
"L2_loop:\n"
"s_waitcnt vmcnt(26)\n"
"v_mfma_f32_16x16x32_bf16 v[16:19], a[224:227], a[112:115], v[16:19]\n"
"s_waitcnt vmcnt(25)\n"
"ds_read_b64_tr_b16 a[0:1], v224\n"
"s_add_u32 m0, s70, 0\n"
"v_mfma_f32_16x16x32_bf16 v[16:19], a[228:231], a[140:143], v[16:19]\n"
"buffer_load_dwordx4 v228, s[32:35], s49 offen lds\n"
"ds_read_b64_tr_b16 a[2:3], v224 offset:4352\n"
"v_mfma_f32_16x16x32_bf16 v[20:23], a[232:235], a[112:115], v[20:23]\n"
"ds_read_b64_tr_b16 a[4:5], v224 offset:8704\n"
"v_mfma_f32_16x16x32_bf16 v[20:23], a[236:239], a[140:143], v[20:23]\n"
"ds_read_b64_tr_b16 a[6:7], v224 offset:13056\n"
"v_mfma_f32_16x16x32_bf16 v[24:27], a[240:243], a[112:115], v[24:27]\n"
"ds_read_b64_tr_b16 a[8:9], v225\n"
"s_add_u32 m0, s70, 1088\n"
"v_mfma_f32_16x16x32_bf16 v[24:27], a[244:247], a[140:143], v[24:27]\n"
"buffer_load_dwordx4 v229, s[32:35], s49 offen lds\n"
"ds_read_b64_tr_b16 a[10:11], v225 offset:4352\n"
"v_mfma_f32_16x16x32_bf16 v[28:31], a[248:251], a[112:115], v[28:31]\n"
"ds_read_b64_tr_b16 a[12:13], v225 offset:8704\n"
"v_mfma_f32_16x16x32_bf16 v[28:31], a[252:255], a[140:143], v[28:31]\n"
"s_waitcnt vmcnt(26)\n"
"ds_read_b64_tr_b16 a[14:15], v225 offset:13056\n"
"v_mfma_f32_16x16x32_bf16 v[48:51], a[224:227], a[116:119], v[48:51]\n"
"s_waitcnt vmcnt(25)\n"
"ds_read_b64_tr_b16 a[16:17], v224 offset:64\n"
"v_mfma_f32_16x16x32_bf16 v[48:51], a[228:231], a[144:147], v[48:51]\n"
"ds_read_b64_tr_b16 a[18:19], v224 offset:4416\n"
"s_add_u32 m0, s70, 2176\n"
"v_mfma_f32_16x16x32_bf16 v[52:55], a[232:235], a[116:119], v[52:55]\n"
"buffer_load_dwordx4 v230, s[32:35], s49 offen lds\n"
"ds_read_b64_tr_b16 a[20:21], v224 offset:8768\n"
"v_mfma_f32_16x16x32_bf16 v[52:55], a[236:239], a[144:147], v[52:55]\n"
"ds_read_b64_tr_b16 a[22:23], v224 offset:13120\n"
"v_mfma_f32_16x16x32_bf16 v[56:59], a[240:243], a[116:119], v[56:59]\n"
"ds_read_b64_tr_b16 a[24:25], v225 offset:64\n"
"v_mfma_f32_16x16x32_bf16 v[56:59], a[244:247], a[144:147], v[56:59]\n"
"ds_read_b64_tr_b16 a[26:27], v225 offset:4416\n"
"v_mfma_f32_16x16x32_bf16 v[60:63], a[248:251], a[116:119], v[60:63]\n"
"ds_read_b64_tr_b16 a[28:29], v225 offset:8768\n"
"s_add_u32 m0, s70, 3264\n"
"v_mfma_f32_16x16x32_bf16 v[60:63], a[252:255], a[144:147], v[60:63]\n"
"s_waitcnt vmcnt(25)\n"
"buffer_load_dwordx4 v231, s[32:35], s49 offen lds\n"
"ds_read_b64_tr_b16 a[30:31], v225 offset:13120\n"
"v_mfma_f32_16x16x32_bf16 v[80:83], a[224:227], a[120:123], v[80:83]\n"
"s_waitcnt vmcnt(25)\n"
"v_mfma_f32_16x16x32_bf16 v[80:83], a[228:231], a[148:151], v[80:83]\n"
"v_mfma_f32_16x16x32_bf16 v[84:87], a[232:235], a[120:123], v[84:87]\n"
"v_mfma_f32_16x16x32_bf16 v[84:87], a[236:239], a[148:151], v[84:87]\n"
"s_add_u32 m0, s70, 4352\n"
"v_mfma_f32_16x16x32_bf16 v[88:91], a[240:243], a[120:123], v[88:91]\n"
"buffer_load_dwordx4 v232, s[32:35], s49 offen lds\n"
"v_mfma_f32_16x16x32_bf16 v[88:91], a[244:247], a[148:151], v[88:91]\n"
"v_mfma_f32_16x16x32_bf16 v[92:95], a[248:251], a[120:123], v[92:95]\n"
"v_mfma_f32_16x16x32_bf16 v[92:95], a[252:255], a[148:151], v[92:95]\n"
"s_waitcnt vmcnt(25)\n"
"v_mfma_f32_16x16x32_bf16 v[112:115], a[224:227], a[124:127], v[112:115]\n"
"s_waitcnt vmcnt(24)\n"
"s_add_u32 m0, s70, 5440\n"
"v_mfma_f32_16x16x32_bf16 v[112:115], a[228:231], a[152:155], v[112:115]\n"
"buffer_load_dwordx4 v233, s[32:35], s49 offen lds\n"
"v_mfma_f32_16x16x32_bf16 v[116:119], a[232:235], a[124:127], v[116:119]\n"
"v_mfma_f32_16x16x32_bf16 v[116:119], a[236:239], a[152:155], v[116:119]\n"
"v_mfma_f32_16x16x32_bf16 v[120:123], a[240:243], a[124:127], v[120:123]\n"
"s_add_u32 m0, s70, 6528\n"
"v_mfma_f32_16x16x32_bf16 v[120:123], a[244:247], a[152:155], v[120:123]\n"
"buffer_load_dwordx4 v234, s[32:35], s49 offen lds\n"
"v_mfma_f32_16x16x32_bf16 v[124:127], a[248:251], a[124:127], v[124:127]\n"
"v_mfma_f32_16x16x32_bf16 v[124:127], a[252:255], a[152:155], v[124:127]\n"
"s_waitcnt vmcnt(25)\n"
"v_mfma_f32_16x16x32_bf16 v[144:147], a[224:227], a[128:131], v[144:147]\n"
"s_waitcnt vmcnt(24)\n"
"v_mfma_f32_16x16x32_bf16 v[144:147], a[228:231], a[156:159], v[144:147]\n"
"s_add_u32 m0, s70, 7616\n"
"v_mfma_f32_16x16x32_bf16 v[148:151], a[232:235], a[128:131], v[148:151]\n"
"buffer_load_dwordx4 v235, s[32:35], s49 offen lds\n"
"s_add_u32 s49, s49, 65536\n"
"v_mfma_f32_16x16x32_bf16 v[148:151], a[236:239], a[156:159], v[148:151]\n"
"v_mfma_f32_16x16x32_bf16 v[152:155], a[240:243], a[128:131], v[152:155]\n"
"v_mfma_f32_16x16x32_bf16 v[152:155], a[244:247], a[156:159], v[152:155]\n"
"v_mfma_f32_16x16x32_bf16 v[156:159], a[248:251], a[128:131], v[156:159]\n"
"s_add_u32 m0, s70, 8704\n"
"v_mfma_f32_16x16x32_bf16 v[156:159], a[252:255], a[156:159], v[156:159]\n"
"s_waitcnt vmcnt(24)\n"
"buffer_load_dwordx4 v228, s[32:35], s49 offen lds\n"
"v_mfma_f32_16x16x32_bf16 v[176:179], a[224:227], a[132:135], v[176:179]\n"
"s_waitcnt vmcnt(24)\n"
"v_mfma_f32_16x16x32_bf16 v[176:179], a[228:231], a[160:163], v[176:179]\n"
"v_mfma_f32_16x16x32_bf16 v[180:183], a[232:235], a[132:135], v[180:183]\n"
"v_mfma_f32_16x16x32_bf16 v[180:183], a[236:239], a[160:163], v[180:183]\n"
"s_add_u32 m0, s70, 9792\n"
"v_mfma_f32_16x16x32_bf16 v[184:187], a[240:243], a[132:135], v[184:187]\n"
"buffer_load_dwordx4 v229, s[32:35], s49 offen lds\n"
"v_mfma_f32_16x16x32_bf16 v[184:187], a[244:247], a[160:163], v[184:187]\n"
"v_mfma_f32_16x16x32_bf16 v[188:191], a[248:251], a[132:135], v[188:191]\n"
"v_mfma_f32_16x16x32_bf16 v[188:191], a[252:255], a[160:163], v[188:191]\n"
"s_waitcnt vmcnt(24)\n"
"v_mfma_f32_16x16x32_bf16 v[208:211], a[224:227], a[136:139], v[208:211]\n"
"s_waitcnt vmcnt(23)\n"
"s_add_u32 m0, s70, 10880\n"
"v_mfma_f32_16x16x32_bf16 v[208:211], a[228:231], a[164:167], v[208:211]\n"
"buffer_load_dwordx4 v230, s[32:35], s49 offen lds\n"
"v_mfma_f32_16x16x32_bf16 v[212:215], a[232:235], a[136:139], v[212:215]\n"
"v_mfma_f32_16x16x32_bf16 v[212:215], a[236:239], a[164:167], v[212:215]\n"
"v_mfma_f32_16x16x32_bf16 v[216:219], a[240:243], a[136:139], v[216:219]\n"
"s_add_u32 m0, s70, 11968\n"
"v_mfma_f32_16x16x32_bf16 v[216:219], a[244:247], a[164:167], v[216:219]\n"
"buffer_load_dwordx4 v231, s[32:35], s49 offen lds\n"
"v_mfma_f32_16x16x32_bf16 v[220:223], a[248:251], a[136:139], v[220:223]\n"
"v_mfma_f32_16x16x32_bf16 v[220:223], a[252:255], a[164:167], v[220:223]\n"
"s_waitcnt lgkmcnt(14)\n"
"v_mfma_f32_16x16x32_bf16 v[0:3], a[0:3], a[112:115], v[0:3]\n"
"s_waitcnt lgkmcnt(12)\n"
"s_mul_i32 s51, s59, 106496\n"
"v_mfma_f32_16x16x32_bf16 v[0:3], a[4:7], a[140:143], v[0:3]\n"
"s_waitcnt lgkmcnt(10)\n"
"s_add_u32 m0, s70, 13056\n"
"s_add_u32 s57, s51, 13312\n"
"v_mfma_f32_16x16x32_bf16 v[4:7], a[8:11], a[112:115], v[4:7]\n"
"s_waitcnt lgkmcnt(8)\n"
"buffer_load_dwordx4 v232, s[32:35], s49 offen lds\n"
"v_mfma_f32_16x16x32_bf16 v[4:7], a[12:15], a[140:143], v[4:7]\n"
"s_waitcnt lgkmcnt(6)\n"
"v_mfma_f32_16x16x32_bf16 v[8:11], a[16:19], a[112:115], v[8:11]\n"
"s_waitcnt lgkmcnt(4)\n"
"v_mfma_f32_16x16x32_bf16 v[8:11], a[20:23], a[140:143], v[8:11]\n"
"s_waitcnt lgkmcnt(2)\n"
"v_mfma_f32_16x16x32_bf16 v[12:15], a[24:27], a[112:115], v[12:15]\n"
"s_waitcnt lgkmcnt(0)\n"
"s_add_u32 m0, s70, 14144\n"
"v_mfma_f32_16x16x32_bf16 v[12:15], a[28:31], a[140:143], v[12:15]\n"
"buffer_load_dwordx4 v233, s[32:35], s49 offen lds\n"
"v_mfma_f32_16x16x32_bf16 v[32:35], a[0:3], a[116:119], v[32:35]\n"
"v_mfma_f32_16x16x32_bf16 v[32:35], a[4:7], a[144:147], v[32:35]\n"
"v_mfma_f32_16x16x32_bf16 v[36:39], a[8:11], a[116:119], v[36:39]\n"
"v_mfma_f32_16x16x32_bf16 v[36:39], a[12:15], a[144:147], v[36:39]\n"
"s_add_u32 m0, s70, 15232\n"
"v_mfma_f32_16x16x32_bf16 v[40:43], a[16:19], a[116:119], v[40:43]\n"
"buffer_load_dwordx4 v234, s[32:35], s49 offen lds\n"
"v_mfma_f32_16x16x32_bf16 v[40:43], a[20:23], a[144:147], v[40:43]\n"
"v_mfma_f32_16x16x32_bf16 v[44:47], a[24:27], a[116:119], v[44:47]\n"
"v_mfma_f32_16x16x32_bf16 v[44:47], a[28:31], a[144:147], v[44:47]\n"
"v_mfma_f32_16x16x32_bf16 v[64:67], a[0:3], a[120:123], v[64:67]\n"
"s_add_u32 m0, s70, 16320\n"
"v_mfma_f32_16x16x32_bf16 v[64:67], a[4:7], a[148:151], v[64:67]\n"
"buffer_load_dwordx4 v235, s[32:35], s49 offen lds\n"
"v_mfma_f32_16x16x32_bf16 v[68:71], a[8:11], a[120:123], v[68:71]\n"
"v_mfma_f32_16x16x32_bf16 v[68:71], a[12:15], a[148:151], v[68:71]\n"
"v_mfma_f32_16x16x32_bf16 v[72:75], a[16:19], a[120:123], v[72:75]\n"
"s_waitcnt vmcnt(28)\n"
"v_cmp_eq_u64 vcc, s[74:75], v[242:243]\n"
"s_nop 0\n"
"s_or_b32 s66, s66, vcc_lo\n"
"s_bitcmp1_b32 s66, s59\n"
"s_cbranch_scc0 L2_c1_s\n"
"L2_c1_r:\n"
"v_mfma_f32_16x16x32_bf16 v[72:75], a[20:23], a[148:151], v[72:75]\n"
"buffer_load_dwordx4 a[112:115], v236, s[28:31], s51 offen sc1\n"
"v_mfma_f32_16x16x32_bf16 v[76:79], a[24:27], a[120:123], v[76:79]\n"
"v_mfma_f32_16x16x32_bf16 v[76:79], a[28:31], a[148:151], v[76:79]\n"
"v_mfma_f32_16x16x32_bf16 v[96:99], a[0:3], a[124:127], v[96:99]\n"
"v_mfma_f32_16x16x32_bf16 v[96:99], a[4:7], a[152:155], v[96:99]\n"
"buffer_load_dwordx4 a[140:143], v236, s[28:31], s57 offen sc1\n"
"v_mfma_f32_16x16x32_bf16 v[100:103], a[8:11], a[124:127], v[100:103]\n"
"v_mfma_f32_16x16x32_bf16 v[100:103], a[12:15], a[152:155], v[100:103]\n"
"v_mfma_f32_16x16x32_bf16 v[104:107], a[16:19], a[124:127], v[104:107]\n"
"v_mfma_f32_16x16x32_bf16 v[104:107], a[20:23], a[152:155], v[104:107]\n"
"v_mfma_f32_16x16x32_bf16 v[108:111], a[24:27], a[124:127], v[108:111]\n"
"buffer_load_dwordx4 a[116:119], v236, s[28:31], s51 offen offset:1024 sc1\n"
"v_mfma_f32_16x16x32_bf16 v[108:111], a[28:31], a[152:155], v[108:111]\n"
"ds_read_b64_tr_b16 a[224:225], v226 offset:128\n"
"v_mfma_f32_16x16x32_bf16 v[128:131], a[0:3], a[128:131], v[128:131]\n"
"ds_read_b64_tr_b16 a[226:227], v226 offset:4480\n"
"v_mfma_f32_16x16x32_bf16 v[128:131], a[4:7], a[156:159], v[128:131]\n"
"ds_read_b64_tr_b16 a[228:229], v226 offset:8832\n"
"v_mfma_f32_16x16x32_bf16 v[132:135], a[8:11], a[128:131], v[132:135]\n"
"ds_read_b64_tr_b16 a[230:231], v226 offset:13184\n"
"v_mfma_f32_16x16x32_bf16 v[132:135], a[12:15], a[156:159], v[132:135]\n"
"ds_read_b64_tr_b16 a[232:233], v227 offset:128\n"
"buffer_load_dwordx4 a[144:147], v236, s[28:31], s57 offen offset:1024 sc1\n"
"v_mfma_f32_16x16x32_bf16 v[136:139], a[16:19], a[128:131], v[136:139]\n"
"ds_read_b64_tr_b16 a[234:235], v227 offset:4480\n"
"v_mfma_f32_16x16x32_bf16 v[136:139], a[20:23], a[156:159], v[136:139]\n"
"ds_read_b64_tr_b16 a[236:237], v227 offset:8832\n"
"v_mfma_f32_16x16x32_bf16 v[140:143], a[24:27], a[128:131], v[140:143]\n"
"ds_read_b64_tr_b16 a[238:239], v227 offset:13184\n"
"v_mfma_f32_16x16x32_bf16 v[140:143], a[28:31], a[156:159], v[140:143]\n"
"ds_read_b64_tr_b16 a[240:241], v226 offset:192\n"
"v_mfma_f32_16x16x32_bf16 v[160:163], a[0:3], a[132:135], v[160:163]\n"
"ds_read_b64_tr_b16 a[242:243], v226 offset:4544\n"
"buffer_load_dwordx4 a[120:123], v236, s[28:31], s51 offen offset:2048 sc1\n"
"v_mfma_f32_16x16x32_bf16 v[160:163], a[4:7], a[160:163], v[160:163]\n"
"ds_read_b64_tr_b16 a[244:245], v226 offset:8896\n"
"v_mfma_f32_16x16x32_bf16 v[164:167], a[8:11], a[132:135], v[164:167]\n"
"ds_read_b64_tr_b16 a[246:247], v226 offset:13248\n"
"v_mfma_f32_16x16x32_bf16 v[164:167], a[12:15], a[160:163], v[164:167]\n"
"ds_read_b64_tr_b16 a[248:249], v227 offset:192\n"
"v_mfma_f32_16x16x32_bf16 v[168:171], a[16:19], a[132:135], v[168:171]\n"
"ds_read_b64_tr_b16 a[250:251], v227 offset:4544\n"
"v_mfma_f32_16x16x32_bf16 v[168:171], a[20:23], a[160:163], v[168:171]\n"
"ds_read_b64_tr_b16 a[252:253], v227 offset:8896\n"
"buffer_load_dwordx4 a[148:151], v236, s[28:31], s57 offen offset:2048 sc1\n"
"v_mfma_f32_16x16x32_bf16 v[172:175], a[24:27], a[132:135], v[172:175]\n"
"ds_read_b64_tr_b16 a[254:255], v227 offset:13248\n"
"v_mfma_f32_16x16x32_bf16 v[172:175], a[28:31], a[160:163], v[172:175]\n"
"v_mfma_f32_16x16x32_bf16 v[192:195], a[0:3], a[136:139], v[192:195]\n"
"v_mfma_f32_16x16x32_bf16 v[192:195], a[4:7], a[164:167], v[192:195]\n"
"buffer_load_dwordx4 a[124:127], v237, s[28:31], s51 offen sc1\n"
"v_mfma_f32_16x16x32_bf16 v[196:199], a[8:11], a[136:139], v[196:199]\n"
"v_mfma_f32_16x16x32_bf16 v[196:199], a[12:15], a[164:167], v[196:199]\n"
"v_mfma_f32_16x16x32_bf16 v[200:203], a[16:19], a[136:139], v[200:203]\n"
"v_mfma_f32_16x16x32_bf16 v[200:203], a[20:23], a[164:167], v[200:203]\n"
"v_mfma_f32_16x16x32_bf16 v[204:207], a[24:27], a[136:139], v[204:207]\n"
"s_waitcnt lgkmcnt(0)\n"
"buffer_load_dwordx4 a[152:155], v237, s[28:31], s57 offen sc1\n"
"v_mfma_f32_16x16x32_bf16 v[204:207], a[28:31], a[164:167], v[204:207]\n"
"s_waitcnt vmcnt(35)\n"
"v_mfma_f32_16x16x32_bf16 v[16:19], a[224:227], a[168:171], v[16:19]\n"
"s_waitcnt vmcnt(34)\n"
"ds_read_b64_tr_b16 a[0:1], v226\n"
"v_mfma_f32_16x16x32_bf16 v[16:19], a[228:231], a[196:199], v[16:19]\n"
"ds_read_b64_tr_b16 a[2:3], v226 offset:4352\n"
"v_mfma_f32_16x16x32_bf16 v[20:23], a[232:235], a[168:171], v[20:23]\n"
"ds_read_b64_tr_b16 a[4:5], v226 offset:8704\n"
"v_mfma_f32_16x16x32_bf16 v[20:23], a[236:239], a[196:199], v[20:23]\n"
"ds_read_b64_tr_b16 a[6:7], v226 offset:13056\n"
"buffer_load_dwordx4 a[128:131], v237, s[28:31], s51 offen offset:1024 sc1\n"
"v_mfma_f32_16x16x32_bf16 v[24:27], a[240:243], a[168:171], v[24:27]\n"
"ds_read_b64_tr_b16 a[8:9], v227\n"
"v_mfma_f32_16x16x32_bf16 v[24:27], a[244:247], a[196:199], v[24:27]\n"
"ds_read_b64_tr_b16 a[10:11], v227 offset:4352\n"
"v_mfma_f32_16x16x32_bf16 v[28:31], a[248:251], a[168:171], v[28:31]\n"
"ds_read_b64_tr_b16 a[12:13], v227 offset:8704\n"
"v_mfma_f32_16x16x32_bf16 v[28:31], a[252:255], a[196:199], v[28:31]\n"
"s_waitcnt vmcnt(34)\n"
"ds_read_b64_tr_b16 a[14:15], v227 offset:13056\n"
"v_mfma_f32_16x16x32_bf16 v[48:51], a[224:227], a[172:175], v[48:51]\n"
"s_waitcnt vmcnt(33)\n"
"ds_read_b64_tr_b16 a[16:17], v226 offset:64\n"
"buffer_load_dwordx4 a[156:159], v237, s[28:31], s57 offen offset:1024 sc1\n"
"v_mfma_f32_16x16x32_bf16 v[48:51], a[228:231], a[200:203], v[48:51]\n"
"ds_read_b64_tr_b16 a[18:19], v226 offset:4416\n"
"v_mfma_f32_16x16x32_bf16 v[52:55], a[232:235], a[172:175], v[52:55]\n"
"ds_read_b64_tr_b16 a[20:21], v226 offset:8768\n"
"v_mfma_f32_16x16x32_bf16 v[52:55], a[236:239], a[200:203], v[52:55]\n"
"ds_read_b64_tr_b16 a[22:23], v226 offset:13120\n"
"v_mfma_f32_16x16x32_bf16 v[56:59], a[240:243], a[172:175], v[56:59]\n"
"ds_read_b64_tr_b16 a[24:25], v227 offset:64\n"
"v_mfma_f32_16x16x32_bf16 v[56:59], a[244:247], a[200:203], v[56:59]\n"
"ds_read_b64_tr_b16 a[26:27], v227 offset:4416\n"
"buffer_load_dwordx4 a[132:135], v237, s[28:31], s51 offen offset:2048 sc1\n"
"v_mfma_f32_16x16x32_bf16 v[60:63], a[248:251], a[172:175], v[60:63]\n"
"ds_read_b64_tr_b16 a[28:29], v227 offset:8768\n"
"v_mfma_f32_16x16x32_bf16 v[60:63], a[252:255], a[200:203], v[60:63]\n"
"s_waitcnt vmcnt(34)\n"
"ds_read_b64_tr_b16 a[30:31], v227 offset:13120\n"
"v_mfma_f32_16x16x32_bf16 v[80:83], a[224:227], a[176:179], v[80:83]\n"
"s_waitcnt vmcnt(33)\n"
"v_mfma_f32_16x16x32_bf16 v[80:83], a[228:231], a[204:207], v[80:83]\n"
"buffer_load_dwordx4 a[160:163], v237, s[28:31], s57 offen offset:2048 sc1\n"
"v_mfma_f32_16x16x32_bf16 v[84:87], a[232:235], a[176:179], v[84:87]\n"
"v_mfma_f32_16x16x32_bf16 v[84:87], a[236:239], a[204:207], v[84:87]\n"
"v_mfma_f32_16x16x32_bf16 v[88:91], a[240:243], a[176:179], v[88:91]\n"
"v_mfma_f32_16x16x32_bf16 v[88:91], a[244:247], a[204:207], v[88:91]\n"
"v_mfma_f32_16x16x32_bf16 v[92:95], a[248:251], a[176:179], v[92:95]\n"
"buffer_load_dwordx4 a[136:139], v238, s[28:31], s51 offen sc1\n"
"v_mfma_f32_16x16x32_bf16 v[92:95], a[252:255], a[204:207], v[92:95]\n"
"s_waitcnt vmcnt(34)\n"
"v_mfma_f32_16x16x32_bf16 v[112:115], a[224:227], a[180:183], v[112:115]\n"
"s_waitcnt vmcnt(33)\n"
"v_mfma_f32_16x16x32_bf16 v[112:115], a[228:231], a[208:211], v[112:115]\n"
"v_mfma_f32_16x16x32_bf16 v[116:119], a[232:235], a[180:183], v[116:119]\n"
"v_mfma_f32_16x16x32_bf16 v[116:119], a[236:239], a[208:211], v[116:119]\n"
"buffer_load_dwordx4 a[164:167], v238, s[28:31], s57 offen sc1\n"
"v_mfma_f32_16x16x32_bf16 v[120:123], a[240:243], a[180:183], v[120:123]\n"
"v_add_u32 v224, s72, v224\n"
"v_mfma_f32_16x16x32_bf16 v[120:123], a[244:247], a[208:211], v[120:123]\n"
"v_add_u32 v225, s72, v225\n"
"v_mfma_f32_16x16x32_bf16 v[124:127], a[248:251], a[180:183], v[124:127]\n"
"v_add_u32 v226, s72, v226\n"
"v_mfma_f32_16x16x32_bf16 v[124:127], a[252:255], a[208:211], v[124:127]\n"
"s_waitcnt vmcnt(33)\n"
"v_add_u32 v227, s72, v227\n"
"v_mfma_f32_16x16x32_bf16 v[144:147], a[224:227], a[184:187], v[144:147]\n"
"s_waitcnt vmcnt(32)\n"
"s_sub_u32 s70, s70, s72\n"
"v_mfma_f32_16x16x32_bf16 v[144:147], a[228:231], a[212:215], v[144:147]\n"
"s_sub_u32 s72, 0, s72\n"
"v_mfma_f32_16x16x32_bf16 v[148:151], a[232:235], a[184:187], v[148:151]\n"
"v_mfma_f32_16x16x32_bf16 v[148:151], a[236:239], a[212:215], v[148:151]\n"
"v_mfma_f32_16x16x32_bf16 v[152:155], a[240:243], a[184:187], v[152:155]\n"
"v_mfma_f32_16x16x32_bf16 v[152:155], a[244:247], a[212:215], v[152:155]\n"
"v_mfma_f32_16x16x32_bf16 v[156:159], a[248:251], a[184:187], v[156:159]\n"
"buffer_load_dwordx2 v[242:243], v241, s[76:79], 0 offen sc1\n"
"v_mfma_f32_16x16x32_bf16 v[156:159], a[252:255], a[212:215], v[156:159]\n"
"s_waitcnt vmcnt(32)\n"
"v_mfma_f32_16x16x32_bf16 v[176:179], a[224:227], a[188:191], v[176:179]\n"
"s_waitcnt vmcnt(31)\n"
"v_mfma_f32_16x16x32_bf16 v[176:179], a[228:231], a[216:219], v[176:179]\n"
"v_mfma_f32_16x16x32_bf16 v[180:183], a[232:235], a[188:191], v[180:183]\n"
"v_mfma_f32_16x16x32_bf16 v[180:183], a[236:239], a[216:219], v[180:183]\n"
"v_mfma_f32_16x16x32_bf16 v[184:187], a[240:243], a[188:191], v[184:187]\n"
"v_mfma_f32_16x16x32_bf16 v[184:187], a[244:247], a[216:219], v[184:187]\n"
"v_mfma_f32_16x16x32_bf16 v[188:191], a[248:251], a[188:191], v[188:191]\n"
"v_mfma_f32_16x16x32_bf16 v[188:191], a[252:255], a[216:219], v[188:191]\n"
"s_waitcnt lgkmcnt(14)\n"
"v_mfma_f32_16x16x32_bf16 v[0:3], a[0:3], a[168:171], v[0:3]\n"
"s_waitcnt lgkmcnt(12)\n"
"v_mfma_f32_16x16x32_bf16 v[0:3], a[4:7], a[196:199], v[0:3]\n"
"s_waitcnt lgkmcnt(10)\n"
"v_mfma_f32_16x16x32_bf16 v[4:7], a[8:11], a[168:171], v[4:7]\n"
"s_waitcnt lgkmcnt(8)\n"
"v_mfma_f32_16x16x32_bf16 v[4:7], a[12:15], a[196:199], v[4:7]\n"
"s_waitcnt lgkmcnt(6)\n"
"v_mfma_f32_16x16x32_bf16 v[8:11], a[16:19], a[168:171], v[8:11]\n"
"s_waitcnt lgkmcnt(4)\n"
"v_mfma_f32_16x16x32_bf16 v[8:11], a[20:23], a[196:199], v[8:11]\n"
"s_waitcnt lgkmcnt(2)\n"
"v_mfma_f32_16x16x32_bf16 v[12:15], a[24:27], a[168:171], v[12:15]\n"
"s_waitcnt lgkmcnt(0)\n"
"v_mfma_f32_16x16x32_bf16 v[12:15], a[28:31], a[196:199], v[12:15]\n"
"buffer_load_dwordx4 a[168:171], v239, s[28:31], s51 offen sc1\n"
"v_mfma_f32_16x16x32_bf16 v[32:35], a[0:3], a[172:175], v[32:35]\n"
"v_mfma_f32_16x16x32_bf16 v[32:35], a[4:7], a[200:203], v[32:35]\n"
"v_mfma_f32_16x16x32_bf16 v[36:39], a[8:11], a[172:175], v[36:39]\n"
"v_mfma_f32_16x16x32_bf16 v[36:39], a[12:15], a[200:203], v[36:39]\n"
"buffer_load_dwordx4 a[196:199], v239, s[28:31], s57 offen sc1\n"
"v_mfma_f32_16x16x32_bf16 v[40:43], a[16:19], a[172:175], v[40:43]\n"
"v_mfma_f32_16x16x32_bf16 v[40:43], a[20:23], a[200:203], v[40:43]\n"
"v_mfma_f32_16x16x32_bf16 v[44:47], a[24:27], a[172:175], v[44:47]\n"
"v_mfma_f32_16x16x32_bf16 v[44:47], a[28:31], a[200:203], v[44:47]\n"
"buffer_load_dwordx4 a[172:175], v239, s[28:31], s51 offen offset:1024 sc1\n"
"v_mfma_f32_16x16x32_bf16 v[64:67], a[0:3], a[176:179], v[64:67]\n"
"v_mfma_f32_16x16x32_bf16 v[64:67], a[4:7], a[204:207], v[64:67]\n"
"v_mfma_f32_16x16x32_bf16 v[68:71], a[8:11], a[176:179], v[68:71]\n"
"v_mfma_f32_16x16x32_bf16 v[68:71], a[12:15], a[204:207], v[68:71]\n"
"buffer_load_dwordx4 a[200:203], v239, s[28:31], s57 offen offset:1024 sc1\n"
"v_mfma_f32_16x16x32_bf16 v[72:75], a[16:19], a[176:179], v[72:75]\n"
"v_mfma_f32_16x16x32_bf16 v[72:75], a[20:23], a[204:207], v[72:75]\n"
"v_mfma_f32_16x16x32_bf16 v[76:79], a[24:27], a[176:179], v[76:79]\n"
"s_waitcnt vmcnt(19)\n"
"s_barrier\n"
"v_mfma_f32_16x16x32_bf16 v[76:79], a[28:31], a[204:207], v[76:79]\n"
"buffer_load_dwordx4 a[176:179], v239, s[28:31], s51 offen offset:2048 sc1\n"
"ds_read_b64_tr_b16 a[224:225], v224 offset:128\n"
"v_mfma_f32_16x16x32_bf16 v[96:99], a[0:3], a[180:183], v[96:99]\n"
"ds_read_b64_tr_b16 a[226:227], v224 offset:4480\n"
"v_mfma_f32_16x16x32_bf16 v[96:99], a[4:7], a[208:211], v[96:99]\n"
"ds_read_b64_tr_b16 a[228:229], v224 offset:8832\n"
"v_mfma_f32_16x16x32_bf16 v[100:103], a[8:11], a[180:183], v[100:103]\n"
"ds_read_b64_tr_b16 a[230:231], v224 offset:13184\n"
"v_mfma_f32_16x16x32_bf16 v[100:103], a[12:15], a[208:211], v[100:103]\n"
"buffer_load_dwordx4 a[204:207], v239, s[28:31], s57 offen offset:2048 sc1\n"
"ds_read_b64_tr_b16 a[232:233], v225 offset:128\n"
"v_mfma_f32_16x16x32_bf16 v[104:107], a[16:19], a[180:183], v[104:107]\n"
"ds_read_b64_tr_b16 a[234:235], v225 offset:4480\n"
"v_mfma_f32_16x16x32_bf16 v[104:107], a[20:23], a[208:211], v[104:107]\n"
"ds_read_b64_tr_b16 a[236:237], v225 offset:8832\n"
"v_mfma_f32_16x16x32_bf16 v[108:111], a[24:27], a[180:183], v[108:111]\n"
"ds_read_b64_tr_b16 a[238:239], v225 offset:13184\n"
"v_mfma_f32_16x16x32_bf16 v[108:111], a[28:31], a[208:211], v[108:111]\n"
"buffer_load_dwordx4 a[180:183], v240, s[28:31], s51 offen sc1\n"
"ds_read_b64_tr_b16 a[240:241], v224 offset:192\n"
"v_mfma_f32_16x16x32_bf16 v[128:131], a[0:3], a[184:187], v[128:131]\n"
"ds_read_b64_tr_b16 a[242:243], v224 offset:4544\n"
"v_mfma_f32_16x16x32_bf16 v[128:131], a[4:7], a[212:215], v[128:131]\n"
"ds_read_b64_tr_b16 a[244:245], v224 offset:8896\n"
"v_mfma_f32_16x16x32_bf16 v[132:135], a[8:11], a[184:187], v[132:135]\n"
"ds_read_b64_tr_b16 a[246:247], v224 offset:13248\n"
"v_mfma_f32_16x16x32_bf16 v[132:135], a[12:15], a[212:215], v[132:135]\n"
"buffer_load_dwordx4 a[208:211], v240, s[28:31], s57 offen sc1\n"
"ds_read_b64_tr_b16 a[248:249], v225 offset:192\n"
"v_mfma_f32_16x16x32_bf16 v[136:139], a[16:19], a[184:187], v[136:139]\n"
"ds_read_b64_tr_b16 a[250:251], v225 offset:4544\n"
"v_mfma_f32_16x16x32_bf16 v[136:139], a[20:23], a[212:215], v[136:139]\n"
"ds_read_b64_tr_b16 a[252:253], v225 offset:8896\n"
"v_mfma_f32_16x16x32_bf16 v[140:143], a[24:27], a[184:187], v[140:143]\n"
"ds_read_b64_tr_b16 a[254:255], v225 offset:13248\n"
"v_mfma_f32_16x16x32_bf16 v[140:143], a[28:31], a[212:215], v[140:143]\n"
"buffer_load_dwordx4 a[184:187], v240, s[28:31], s51 offen offset:1024 sc1\n"
"v_mfma_f32_16x16x32_bf16 v[160:163], a[0:3], a[188:191], v[160:163]\n"
"s_add_u32 s59, s59, 1\n"
"v_mfma_f32_16x16x32_bf16 v[160:163], a[4:7], a[216:219], v[160:163]\n"
"s_cmp_eq_u32 s59, s12\n"
"v_mfma_f32_16x16x32_bf16 v[164:167], a[8:11], a[188:191], v[164:167]\n"
"s_addc_u32 s59, s59, 0\n"
"v_mfma_f32_16x16x32_bf16 v[164:167], a[12:15], a[216:219], v[164:167]\n"
"buffer_load_dwordx4 a[212:215], v240, s[28:31], s57 offen offset:1024 sc1\n"
"s_lshl_b32 s49, s59, 19\n"
"v_mfma_f32_16x16x32_bf16 v[168:171], a[16:19], a[188:191], v[168:171]\n"
"v_mfma_f32_16x16x32_bf16 v[168:171], a[20:23], a[216:219], v[168:171]\n"
"v_mfma_f32_16x16x32_bf16 v[172:175], a[24:27], a[188:191], v[172:175]\n"
"s_waitcnt lgkmcnt(0)\n"
"v_mfma_f32_16x16x32_bf16 v[172:175], a[28:31], a[216:219], v[172:175]\n"
"s_sub_u32 s52, s52, 1\n"
"buffer_load_dwordx4 a[188:191], v240, s[28:31], s51 offen offset:2048 sc1\n"
"buffer_load_dwordx4 a[216:219], v240, s[28:31], s57 offen offset:2048 sc1\n"
"s_cbranch_scc0 L2_loop\n"
"s_waitcnt vmcnt(26)\n"
"v_mfma_f32_16x16x32_bf16 v[16:19], a[224:227], a[112:115], v[16:19]\n"
"s_waitcnt vmcnt(25)\n"
"ds_read_b64_tr_b16 a[0:1], v224\n"
"s_add_u32 m0, s70, 0\n"
"v_mfma_f32_16x16x32_bf16 v[16:19], a[228:231], a[140:143], v[16:19]\n"
"buffer_load_dwordx4 v228, s[32:35], s49 offen lds\n"
"ds_read_b64_tr_b16 a[2:3], v224 offset:4352\n"
"v_mfma_f32_16x16x32_bf16 v[20:23], a[232:235], a[112:115], v[20:23]\n"
"ds_read_b64_tr_b16 a[4:5], v224 offset:8704\n"
"v_mfma_f32_16x16x32_bf16 v[20:23], a[236:239], a[140:143], v[20:23]\n"
"ds_read_b64_tr_b16 a[6:7], v224 offset:13056\n"
"v_mfma_f32_16x16x32_bf16 v[24:27], a[240:243], a[112:115], v[24:27]\n"
"ds_read_b64_tr_b16 a[8:9], v225\n"
"s_add_u32 m0, s70, 1088\n"
"v_mfma_f32_16x16x32_bf16 v[24:27], a[244:247], a[140:143], v[24:27]\n"
"buffer_load_dwordx4 v229, s[32:35], s49 offen lds\n"
"ds_read_b64_tr_b16 a[10:11], v225 offset:4352\n"
"v_mfma_f32_16x16x32_bf16 v[28:31], a[248:251], a[112:115], v[28:31]\n"
"ds_read_b64_tr_b16 a[12:13], v225 offset:8704\n"
"v_mfma_f32_16x16x32_bf16 v[28:31], a[252:255], a[140:143], v[28:31]\n"
"s_waitcnt vmcnt(26)\n"
"ds_read_b64_tr_b16 a[14:15], v225 offset:13056\n"
"v_mfma_f32_16x16x32_bf16 v[48:51], a[224:227], a[116:119], v[48:51]\n"
"s_waitcnt vmcnt(25)\n"
"ds_read_b64_tr_b16 a[16:17], v224 offset:64\n"
"s_add_u32 m0, s70, 2176\n"
"v_mfma_f32_16x16x32_bf16 v[48:51], a[228:231], a[144:147], v[48:51]\n"
"buffer_load_dwordx4 v230, s[32:35], s49 offen lds\n"
"ds_read_b64_tr_b16 a[18:19], v224 offset:4416\n"
"v_mfma_f32_16x16x32_bf16 v[52:55], a[232:235], a[116:119], v[52:55]\n"
"ds_read_b64_tr_b16 a[20:21], v224 offset:8768\n"
"v_mfma_f32_16x16x32_bf16 v[52:55], a[236:239], a[144:147], v[52:55]\n"
"ds_read_b64_tr_b16 a[22:23], v224 offset:13120\n"
"v_mfma_f32_16x16x32_bf16 v[56:59], a[240:243], a[116:119], v[56:59]\n"
"ds_read_b64_tr_b16 a[24:25], v225 offset:64\n"
"s_add_u32 m0, s70, 3264\n"
"v_mfma_f32_16x16x32_bf16 v[56:59], a[244:247], a[144:147], v[56:59]\n"
"buffer_load_dwordx4 v231, s[32:35], s49 offen lds\n"
"ds_read_b64_tr_b16 a[26:27], v225 offset:4416\n"
"v_mfma_f32_16x16x32_bf16 v[60:63], a[248:251], a[116:119], v[60:63]\n"
"ds_read_b64_tr_b16 a[28:29], v225 offset:8768\n"
"v_mfma_f32_16x16x32_bf16 v[60:63], a[252:255], a[144:147], v[60:63]\n"
"s_waitcnt vmcnt(26)\n"
"ds_read_b64_tr_b16 a[30:31], v225 offset:13120\n"
"v_mfma_f32_16x16x32_bf16 v[80:83], a[224:227], a[120:123], v[80:83]\n"
"s_waitcnt vmcnt(25)\n"
"s_add_u32 m0, s70, 4352\n"
"v_mfma_f32_16x16x32_bf16 v[80:83], a[228:231], a[148:151], v[80:83]\n"
"buffer_load_dwordx4 v232, s[32:35], s49 offen lds\n"
"v_mfma_f32_16x16x32_bf16 v[84:87], a[232:235], a[120:123], v[84:87]\n"
"v_mfma_f32_16x16x32_bf16 v[84:87], a[236:239], a[148:151], v[84:87]\n"
"v_mfma_f32_16x16x32_bf16 v[88:91], a[240:243], a[120:123], v[88:91]\n"
"s_add_u32 m0, s70, 5440\n"
"v_mfma_f32_16x16x32_bf16 v[88:91], a[244:247], a[148:151], v[88:91]\n"
"buffer_load_dwordx4 v233, s[32:35], s49 offen lds\n"
"v_mfma_f32_16x16x32_bf16 v[92:95], a[248:251], a[120:123], v[92:95]\n"
"v_mfma_f32_16x16x32_bf16 v[92:95], a[252:255], a[148:151], v[92:95]\n"
"s_waitcnt vmcnt(26)\n"
"v_mfma_f32_16x16x32_bf16 v[112:115], a[224:227], a[124:127], v[112:115]\n"
"s_waitcnt vmcnt(25)\n"
"s_add_u32 m0, s70, 6528\n"
"v_mfma_f32_16x16x32_bf16 v[112:115], a[228:231], a[152:155], v[112:115]\n"
"buffer_load_dwordx4 v234, s[32:35], s49 offen lds\n"
"v_mfma_f32_16x16x32_bf16 v[116:119], a[232:235], a[124:127], v[116:119]\n"
"v_mfma_f32_16x16x32_bf16 v[116:119], a[236:239], a[152:155], v[116:119]\n"
"v_mfma_f32_16x16x32_bf16 v[120:123], a[240:243], a[124:127], v[120:123]\n"
"s_add_u32 m0, s70, 7616\n"
"v_mfma_f32_16x16x32_bf16 v[120:123], a[244:247], a[152:155], v[120:123]\n"
"buffer_load_dwordx4 v235, s[32:35], s49 offen lds\n"
"s_add_u32 s49, s49, 65536\n"
"v_mfma_f32_16x16x32_bf16 v[124:127], a[248:251], a[124:127], v[124:127]\n"
"v_mfma_f32_16x16x32_bf16 v[124:127], a[252:255], a[152:155], v[124:127]\n"
"s_waitcnt vmcnt(26)\n"
"v_mfma_f32_16x16x32_bf16 v[144:147], a[224:227], a[128:131], v[144:147]\n"
"s_waitcnt vmcnt(25)\n"
"s_add_u32 m0, s70, 8704\n"
"v_mfma_f32_16x16x32_bf16 v[144:147], a[228:231], a[156:159], v[144:147]\n"
"buffer_load_dwordx4 v228, s[32:35], s49 offen lds\n"
"v_mfma_f32_16x16x32_bf16 v[148:151], a[232:235], a[128:131], v[148:151]\n"
"v_mfma_f32_16x16x32_bf16 v[148:151], a[236:239], a[156:159], v[148:151]\n"
"v_mfma_f32_16x16x32_bf16 v[152:155], a[240:243], a[128:131], v[152:155]\n"
"s_add_u32 m0, s70, 9792\n"
"v_mfma_f32_16x16x32_bf16 v[152:155], a[244:247], a[156:159], v[152:155]\n"
"buffer_load_dwordx4 v229, s[32:35], s49 offen lds\n"
"v_mfma_f32_16x16x32_bf16 v[156:159], a[248:251], a[128:131], v[156:159]\n"
"v_mfma_f32_16x16x32_bf16 v[156:159], a[252:255], a[156:159], v[156:159]\n"
"s_waitcnt vmcnt(26)\n"
"v_mfma_f32_16x16x32_bf16 v[176:179], a[224:227], a[132:135], v[176:179]\n"
"s_waitcnt vmcnt(25)\n"
"s_add_u32 m0, s70, 10880\n"
"v_mfma_f32_16x16x32_bf16 v[176:179], a[228:231], a[160:163], v[176:179]\n"
"buffer_load_dwordx4 v230, s[32:35], s49 offen lds\n"
"v_mfma_f32_16x16x32_bf16 v[180:183], a[232:235], a[132:135], v[180:183]\n"
"v_mfma_f32_16x16x32_bf16 v[180:183], a[236:239], a[160:163], v[180:183]\n"
"v_mfma_f32_16x16x32_bf16 v[184:187], a[240:243], a[132:135], v[184:187]\n"
"s_add_u32 m0, s70, 11968\n"
"v_mfma_f32_16x16x32_bf16 v[184:187], a[244:247], a[160:163], v[184:187]\n"
"buffer_load_dwordx4 v231, s[32:35], s49 offen lds\n"
"v_mfma_f32_16x16x32_bf16 v[188:191], a[248:251], a[132:135], v[188:191]\n"
"v_mfma_f32_16x16x32_bf16 v[188:191], a[252:255], a[160:163], v[188:191]\n"
"s_waitcnt vmcnt(26)\n"
"v_mfma_f32_16x16x32_bf16 v[208:211], a[224:227], a[136:139], v[208:211]\n"
"s_waitcnt vmcnt(25)\n"
"s_add_u32 m0, s70, 13056\n"
"v_mfma_f32_16x16x32_bf16 v[208:211], a[228:231], a[164:167], v[208:211]\n"
"buffer_load_dwordx4 v232, s[32:35], s49 offen lds\n"
"v_mfma_f32_16x16x32_bf16 v[212:215], a[232:235], a[136:139], v[212:215]\n"
"v_mfma_f32_16x16x32_bf16 v[212:215], a[236:239], a[164:167], v[212:215]\n"
"v_mfma_f32_16x16x32_bf16 v[216:219], a[240:243], a[136:139], v[216:219]\n"
"s_add_u32 m0, s70, 14144\n"
"v_mfma_f32_16x16x32_bf16 v[216:219], a[244:247], a[164:167], v[216:219]\n"
"buffer_load_dwordx4 v233, s[32:35], s49 offen lds\n"
"v_mfma_f32_16x16x32_bf16 v[220:223], a[248:251], a[136:139], v[220:223]\n"
"v_mfma_f32_16x16x32_bf16 v[220:223], a[252:255], a[164:167], v[220:223]\n"
"s_waitcnt lgkmcnt(14)\n"
"v_mfma_f32_16x16x32_bf16 v[0:3], a[0:3], a[112:115], v[0:3]\n"
"s_waitcnt lgkmcnt(12)\n"
"s_add_u32 m0, s70, 15232\n"
"s_mul_i32 s51, s59, 106496\n"
"v_mfma_f32_16x16x32_bf16 v[0:3], a[4:7], a[140:143], v[0:3]\n"
"s_waitcnt lgkmcnt(10)\n"
"buffer_load_dwordx4 v234, s[32:35], s49 offen lds\n"
"s_add_u32 s57, s51, 13312\n"
"v_mfma_f32_16x16x32_bf16 v[4:7], a[8:11], a[112:115], v[4:7]\n"
"s_waitcnt lgkmcnt(8)\n"
"v_mfma_f32_16x16x32_bf16 v[4:7], a[12:15], a[140:143], v[4:7]\n"
"s_waitcnt lgkmcnt(6)\n"
"v_mfma_f32_16x16x32_bf16 v[8:11], a[16:19], a[112:115], v[8:11]\n"
"s_add_u32 m0, s70, 16320\n"
"s_waitcnt vmcnt(27)\n"
"v_cmp_eq_u64 vcc, s[74:75], v[242:243]\n"
"s_nop 0\n"
"s_or_b32 s66, s66, vcc_lo\n"
"s_bitcmp1_b32 s66, s59\n"
"s_cbranch_scc0 L2_c2_s\n"
"L2_c2_r:\n"
"s_lshl_b32 s14, s13, 11\n"
"s_sub_u32 s14, 2048, s14\n"
"s_mov_b32 s59, s47\n"
"v_add_u32 v241, s14, v241\n"
"s_mov_b32 s66, 0\n"
"buffer_load_dwordx2 v[242:243], v241, s[76:79], 0 offen sc1\n"
"s_waitcnt lgkmcnt(4)\n"
"v_mfma_f32_16x16x32_bf16 v[8:11], a[20:23], a[140:143], v[8:11]\n"
"s_waitcnt lgkmcnt(2)\n"
"buffer_load_dwordx4 v235, s[32:35], s49 offen lds\n"
"v_mfma_f32_16x16x32_bf16 v[12:15], a[24:27], a[112:115], v[12:15]\n"
"s_waitcnt lgkmcnt(0)\n"
"v_mfma_f32_16x16x32_bf16 v[12:15], a[28:31], a[140:143], v[12:15]\n"
"buffer_load_dwordx4 a[112:115], v236, s[28:31], s51 offen sc1\n"
"v_mfma_f32_16x16x32_bf16 v[32:35], a[0:3], a[116:119], v[32:35]\n"
"v_mfma_f32_16x16x32_bf16 v[32:35], a[4:7], a[144:147], v[32:35]\n"
"v_mfma_f32_16x16x32_bf16 v[36:39], a[8:11], a[116:119], v[36:39]\n"
"v_mfma_f32_16x16x32_bf16 v[36:39], a[12:15], a[144:147], v[36:39]\n"
"buffer_load_dwordx4 a[140:143], v236, s[28:31], s57 offen sc1\n"
"v_mfma_f32_16x16x32_bf16 v[40:43], a[16:19], a[116:119], v[40:43]\n"
"v_mfma_f32_16x16x32_bf16 v[40:43], a[20:23], a[144:147], v[40:43]\n"
"v_mfma_f32_16x16x32_bf16 v[44:47], a[24:27], a[116:119], v[44:47]\n"
"v_mfma_f32_16x16x32_bf16 v[44:47], a[28:31], a[144:147], v[44:47]\n"
"buffer_load_dwordx4 a[44:47], v240, s[28:31], s51 offen sc1\n"
"v_mfma_f32_16x16x32_bf16 v[64:67], a[0:3], a[120:123], v[64:67]\n"
"v_mfma_f32_16x16x32_bf16 v[64:67], a[4:7], a[148:151], v[64:67]\n"
"v_mfma_f32_16x16x32_bf16 v[68:71], a[8:11], a[120:123], v[68:71]\n"
"v_mfma_f32_16x16x32_bf16 v[68:71], a[12:15], a[148:151], v[68:71]\n"
"buffer_load_dwordx4 a[116:119], v236, s[28:31], s51 offen offset:1024 sc1\n"
"v_mfma_f32_16x16x32_bf16 v[72:75], a[16:19], a[120:123], v[72:75]\n"
"v_mfma_f32_16x16x32_bf16 v[72:75], a[20:23], a[148:151], v[72:75]\n"
"v_mfma_f32_16x16x32_bf16 v[76:79], a[24:27], a[120:123], v[76:79]\n"
"v_mfma_f32_16x16x32_bf16 v[76:79], a[28:31], a[148:151], v[76:79]\n"
"buffer_load_dwordx4 a[144:147], v236, s[28:31], s57 offen offset:1024 sc1\n"
"v_mfma_f32_16x16x32_bf16 v[96:99], a[0:3], a[124:127], v[96:99]\n"
"v_mfma_f32_16x16x32_bf16 v[96:99], a[4:7], a[152:155], v[96:99]\n"
"v_mfma_f32_16x16x32_bf16 v[100:103], a[8:11], a[124:127], v[100:103]\n"
"v_mfma_f32_16x16x32_bf16 v[100:103], a[12:15], a[152:155], v[100:103]\n"
"buffer_load_dwordx4 a[68:71], v240, s[28:31], s57 offen sc1\n"
"v_mfma_f32_16x16x32_bf16 v[104:107], a[16:19], a[124:127], v[104:107]\n"
"v_mfma_f32_16x16x32_bf16 v[104:107], a[20:23], a[152:155], v[104:107]\n"
"v_mfma_f32_16x16x32_bf16 v[108:111], a[24:27], a[124:127], v[108:111]\n"
"v_mfma_f32_16x16x32_bf16 v[108:111], a[28:31], a[152:155], v[108:111]\n"
"ds_read_b64_tr_b16 a[224:225], v226 offset:128\n"
"buffer_load_dwordx4 a[120:123], v236, s[28:31], s51 offen offset:2048 sc1\n"
"v_mfma_f32_16x16x32_bf16 v[128:131], a[0:3], a[128:131], v[128:131]\n"
"ds_read_b64_tr_b16 a[226:227], v226 offset:4480\n"
"v_mfma_f32_16x16x32_bf16 v[128:131], a[4:7], a[156:159], v[128:131]\n"
"ds_read_b64_tr_b16 a[228:229], v226 offset:8832\n"
"v_mfma_f32_16x16x32_bf16 v[132:135], a[8:11], a[128:131], v[132:135]\n"
"ds_read_b64_tr_b16 a[230:231], v226 offset:13184\n"
"v_mfma_f32_16x16x32_bf16 v[132:135], a[12:15], a[156:159], v[132:135]\n"
"ds_read_b64_tr_b16 a[232:233], v227 offset:128\n"
"buffer_load_dwordx4 a[148:151], v236, s[28:31], s57 offen offset:2048 sc1\n"
"v_mfma_f32_16x16x32_bf16 v[136:139], a[16:19], a[128:131], v[136:139]\n"
"ds_read_b64_tr_b16 a[234:235], v227 offset:4480\n"
"v_mfma_f32_16x16x32_bf16 v[136:139], a[20:23], a[156:159], v[136:139]\n"
"ds_read_b64_tr_b16 a[236:237], v227 offset:8832\n"
"v_mfma_f32_16x16x32_bf16 v[140:143], a[24:27], a[128:131], v[140:143]\n"
"ds_read_b64_tr_b16 a[238:239], v227 offset:13184\n"
"v_mfma_f32_16x16x32_bf16 v[140:143], a[28:31], a[156:159], v[140:143]\n"
"ds_read_b64_tr_b16 a[240:241], v226 offset:192\n"
"buffer_load_dwordx4 a[48:51], v240, s[28:31], s51 offen offset:1024 sc1\n"
"v_mfma_f32_16x16x32_bf16 v[160:163], a[0:3], a[132:135], v[160:163]\n"
"ds_read_b64_tr_b16 a[242:243], v226 offset:4544\n"
"v_mfma_f32_16x16x32_bf16 v[160:163], a[4:7], a[160:163], v[160:163]\n"
"ds_read_b64_tr_b16 a[244:245], v226 offset:8896\n"
"v_mfma_f32_16x16x32_bf16 v[164:167], a[8:11], a[132:135], v[164:167]\n"
"ds_read_b64_tr_b16 a[246:247], v226 offset:13248\n"
"v_mfma_f32_16x16x32_bf16 v[164:167], a[12:15], a[160:163], v[164:167]\n"
"ds_read_b64_tr_b16 a[248:249], v227 offset:192\n"
"buffer_load_dwordx4 a[124:127], v237, s[28:31], s51 offen sc1\n"
"v_mfma_f32_16x16x32_bf16 v[168:171], a[16:19], a[132:135], v[168:171]\n"
"ds_read_b64_tr_b16 a[250:251], v227 offset:4544\n"
"v_mfma_f32_16x16x32_bf16 v[168:171], a[20:23], a[160:163], v[168:171]\n"
"ds_read_b64_tr_b16 a[252:253], v227 offset:8896\n"
"v_mfma_f32_16x16x32_bf16 v[172:175], a[24:27], a[132:135], v[172:175]\n"
"ds_read_b64_tr_b16 a[254:255], v227 offset:13248\n"
"v_mfma_f32_16x16x32_bf16 v[172:175], a[28:31], a[160:163], v[172:175]\n"
"buffer_load_dwordx4 a[152:155], v237, s[28:31], s57 offen sc1\n"
"v_mfma_f32_16x16x32_bf16 v[192:195], a[0:3], a[136:139], v[192:195]\n"
"v_mfma_f32_16x16x32_bf16 v[192:195], a[4:7], a[164:167], v[192:195]\n"
"v_mfma_f32_16x16x32_bf16 v[196:199], a[8:11], a[136:139], v[196:199]\n"
"v_mfma_f32_16x16x32_bf16 v[196:199], a[12:15], a[164:167], v[196:199]\n"
"buffer_load_dwordx4 a[72:75], v240, s[28:31], s57 offen offset:1024 sc1\n"
"v_mfma_f32_16x16x32_bf16 v[200:203], a[16:19], a[136:139], v[200:203]\n"
"v_mfma_f32_16x16x32_bf16 v[200:203], a[20:23], a[164:167], v[200:203]\n"
"v_mfma_f32_16x16x32_bf16 v[204:207], a[24:27], a[136:139], v[204:207]\n"
"s_waitcnt lgkmcnt(0)\n"
"v_mfma_f32_16x16x32_bf16 v[204:207], a[28:31], a[164:167], v[204:207]\n"
"s_waitcnt vmcnt(40)\n"
"buffer_load_dwordx4 a[128:131], v237, s[28:31], s51 offen offset:1024 sc1\n"
"v_mfma_f32_16x16x32_bf16 v[16:19], a[224:227], a[168:171], v[16:19]\n"
"s_waitcnt vmcnt(40)\n"
"ds_read_b64_tr_b16 a[0:1], v226\n"
"v_mfma_f32_16x16x32_bf16 v[16:19], a[228:231], a[196:199], v[16:19]\n"
"ds_read_b64_tr_b16 a[2:3], v226 offset:4352\n"
"v_mfma_f32_16x16x32_bf16 v[20:23], a[232:235], a[168:171], v[20:23]\n"
"ds_read_b64_tr_b16 a[4:5], v226 offset:8704\n"
"v_mfma_f32_16x16x32_bf16 v[20:23], a[236:239], a[196:199], v[20:23]\n"
"ds_read_b64_tr_b16 a[6:7], v226 offset:13056\n"
"buffer_load_dwordx4 a[156:159], v237, s[28:31], s57 offen offset:1024 sc1\n"
"v_mfma_f32_16x16x32_bf16 v[24:27], a[240:243], a[168:171], v[24:27]\n"
"ds_read_b64_tr_b16 a[8:9], v227\n"
"v_mfma_f32_16x16x32_bf16 v[24:27], a[244:247], a[196:199], v[24:27]\n"
"ds_read_b64_tr_b16 a[10:11], v227 offset:4352\n"
"v_mfma_f32_16x16x32_bf16 v[28:31], a[248:251], a[168:171], v[28:31]\n"
"ds_read_b64_tr_b16 a[12:13], v227 offset:8704\n"
"v_mfma_f32_16x16x32_bf16 v[28:31], a[252:255], a[196:199], v[28:31]\n"
"s_waitcnt vmcnt(40)\n"
"ds_read_b64_tr_b16 a[14:15], v227 offset:13056\n"
"buffer_load_dwordx4 a[52:55], v240, s[28:31], s51 offen offset:2048 sc1\n"
"v_mfma_f32_16x16x32_bf16 v[48:51], a[224:227], a[172:175], v[48:51]\n"
"s_waitcnt vmcnt(40)\n"
"ds_read_b64_tr_b16 a[16:17], v226 offset:64\n"
"v_mfma_f32_16x16x32_bf16 v[48:51], a[228:231], a[200:203], v[48:51]\n"
"ds_read_b64_tr_b16 a[18:19], v226 offset:4416\n"
"v_mfma_f32_16x16x32_bf16 v[52:55], a[232:235], a[172:175], v[52:55]\n"
"ds_read_b64_tr_b16 a[20:21], v226 offset:8768\n"
"v_mfma_f32_16x16x32_bf16 v[52:55], a[236:239], a[200:203], v[52:55]\n"
"ds_read_b64_tr_b16 a[22:23], v226 offset:13120\n"
"buffer_load_dwordx4 a[132:135], v237, s[28:31], s51 offen offset:2048 sc1\n"
"v_mfma_f32_16x16x32_bf16 v[56:59], a[240:243], a[172:175], v[56:59]\n"
"ds_read_b64_tr_b16 a[24:25], v227 offset:64\n"
"v_mfma_f32_16x16x32_bf16 v[56:59], a[244:247], a[200:203], v[56:59]\n"
"ds_read_b64_tr_b16 a[26:27], v227 offset:4416\n"
"v_mfma_f32_16x16x32_bf16 v[60:63], a[248:251], a[172:175], v[60:63]\n"
"ds_read_b64_tr_b16 a[28:29], v227 offset:8768\n"
"v_mfma_f32_16x16x32_bf16 v[60:63], a[252:255], a[200:203], v[60:63]\n"
"s_waitcnt vmcnt(40)\n"
"ds_read_b64_tr_b16 a[30:31], v227 offset:13120\n"
"buffer_load_dwordx4 a[160:163], v237, s[28:31], s57 offen offset:2048 sc1\n"
"v_mfma_f32_16x16x32_bf16 v[80:83], a[224:227], a[176:179], v[80:83]\n"
"s_waitcnt vmcnt(40)\n"
"v_add_u32 v224, s72, v224\n"
"v_mfma_f32_16x16x32_bf16 v[80:83], a[228:231], a[204:207], v[80:83]\n"
"v_add_u32 v225, s72, v225\n"
"v_mfma_f32_16x16x32_bf16 v[84:87], a[232:235], a[176:179], v[84:87]\n"
"v_add_u32 v226, s72, v226\n"
"v_mfma_f32_16x16x32_bf16 v[84:87], a[236:239], a[204:207], v[84:87]\n"
"v_add_u32 v227, s72, v227\n"
"buffer_load_dwordx4 a[76:79], v240, s[28:31], s57 offen offset:2048 sc1\n"
"v_mfma_f32_16x16x32_bf16 v[88:91], a[240:243], a[176:179], v[88:91]\n"
"s_sub_u32 s70, s70, s72\n"
"v_mfma_f32_16x16x32_bf16 v[88:91], a[244:247], a[204:207], v[88:91]\n"
"s_mov_b32 s49, s60\n"
"s_add_u32 s63, s61, 13312\n"
"v_mfma_f32_16x16x32_bf16 v[92:95], a[248:251], a[176:179], v[92:95]\n"
"v_mfma_f32_16x16x32_bf16 v[92:95], a[252:255], a[204:207], v[92:95]\n"
"s_waitcnt vmcnt(40)\n"
"buffer_load_dwordx4 a[136:139], v238, s[28:31], s51 offen sc1\n"
"v_mfma_f32_16x16x32_bf16 v[112:115], a[224:227], a[180:183], v[112:115]\n"
"s_waitcnt vmcnt(40)\n"
"v_mfma_f32_16x16x32_bf16 v[112:115], a[228:231], a[208:211], v[112:115]\n"
"v_mfma_f32_16x16x32_bf16 v[116:119], a[232:235], a[180:183], v[116:119]\n"
"v_mfma_f32_16x16x32_bf16 v[116:119], a[236:239], a[208:211], v[116:119]\n"
"buffer_load_dwordx4 a[164:167], v238, s[28:31], s57 offen sc1\n"
"v_mfma_f32_16x16x32_bf16 v[120:123], a[240:243], a[180:183], v[120:123]\n"
"v_mfma_f32_16x16x32_bf16 v[120:123], a[244:247], a[208:211], v[120:123]\n"
"v_mfma_f32_16x16x32_bf16 v[124:127], a[248:251], a[180:183], v[124:127]\n"
"s_waitcnt lgkmcnt(0)\n"
"s_waitcnt vmcnt(20)\n"
"s_barrier\n"
"v_mfma_f32_16x16x32_bf16 v[124:127], a[252:255], a[208:211], v[124:127]\n"
"buffer_load_dwordx4 a[32:35], v239, s[28:31], s51 offen sc1\n"
"v_mfma_f32_16x16x32_bf16 v[144:147], a[224:227], a[184:187], v[144:147]\n"
"v_mfma_f32_16x16x32_bf16 v[144:147], a[228:231], a[212:215], v[144:147]\n"
"v_mfma_f32_16x16x32_bf16 v[148:151], a[232:235], a[184:187], v[148:151]\n"
"v_mfma_f32_16x16x32_bf16 v[148:151], a[236:239], a[212:215], v[148:151]\n"
"buffer_load_dwordx4 a[56:59], v239, s[28:31], s57 offen sc1\n"
"v_mfma_f32_16x16x32_bf16 v[152:155], a[240:243], a[184:187], v[152:155]\n"
"v_mfma_f32_16x16x32_bf16 v[152:155], a[244:247], a[212:215], v[152:155]\n"
"v_mfma_f32_16x16x32_bf16 v[156:159], a[248:251], a[184:187], v[156:159]\n"
"v_cmp_eq_u64 vcc, s[74:75], v[242:243]\n"
"s_nop 0\n"
"s_or_b32 s66, s66, vcc_lo\n"
"s_bitcmp1_b32 s66, s59\n"
"s_cbranch_scc0 L2_c3_s\n"
"L2_c3_r:\n"
"v_subrev_u32 v242, s62, v239\n"
"s_sub_u32 s14, 1, s13\n"
"v_subrev_u32 v243, s62, v240\n"
"s_lshl_b32 s14, s14, 31\n"
"v_or_b32 v238, s14, v238\n"
"v_mfma_f32_16x16x32_bf16 v[156:159], a[252:255], a[212:215], v[156:159]\n"
"buffer_load_dwordx4 a[36:39], v239, s[28:31], s51 offen offset:1024 sc1\n"
"v_mfma_f32_16x16x32_bf16 v[176:179], a[224:227], a[188:191], v[176:179]\n"
"v_mfma_f32_16x16x32_bf16 v[176:179], a[228:231], a[216:219], v[176:179]\n"
"v_mfma_f32_16x16x32_bf16 v[180:183], a[232:235], a[188:191], v[180:183]\n"
"v_mfma_f32_16x16x32_bf16 v[180:183], a[236:239], a[216:219], v[180:183]\n"
"buffer_load_dwordx4 a[60:63], v239, s[28:31], s57 offen offset:1024 sc1\n"
"v_mfma_f32_16x16x32_bf16 v[184:187], a[240:243], a[188:191], v[184:187]\n"
"v_mfma_f32_16x16x32_bf16 v[184:187], a[244:247], a[216:219], v[184:187]\n"
"v_mfma_f32_16x16x32_bf16 v[188:191], a[248:251], a[188:191], v[188:191]\n"
"v_mfma_f32_16x16x32_bf16 v[188:191], a[252:255], a[216:219], v[188:191]\n"
"buffer_load_dwordx4 a[40:43], v239, s[28:31], s51 offen offset:2048 sc1\n"
"v_mfma_f32_16x16x32_bf16 v[96:99], a[0:3], a[180:183], v[96:99]\n"
"v_mfma_f32_16x16x32_bf16 v[96:99], a[4:7], a[208:211], v[96:99]\n"
"v_mfma_f32_16x16x32_bf16 v[100:103], a[8:11], a[180:183], v[100:103]\n"
"v_mfma_f32_16x16x32_bf16 v[100:103], a[12:15], a[208:211], v[100:103]\n"
"buffer_load_dwordx4 a[64:67], v239, s[28:31], s57 offen offset:2048 sc1\n"
"v_mfma_f32_16x16x32_bf16 v[104:107], a[16:19], a[180:183], v[104:107]\n"
"v_mfma_f32_16x16x32_bf16 v[104:107], a[20:23], a[208:211], v[104:107]\n"
"v_mfma_f32_16x16x32_bf16 v[108:111], a[24:27], a[180:183], v[108:111]\n"
"s_add_u32 m0, s70, 0\n"
"v_mfma_f32_16x16x32_bf16 v[108:111], a[28:31], a[208:211], v[108:111]\n"
"buffer_load_dwordx4 v228, s[32:35], s49 offen lds\n"
"v_mfma_f32_16x16x32_bf16 v[128:131], a[0:3], a[184:187], v[128:131]\n"
"v_mfma_f32_16x16x32_bf16 v[128:131], a[4:7], a[212:215], v[128:131]\n"
"v_mfma_f32_16x16x32_bf16 v[132:135], a[8:11], a[184:187], v[132:135]\n"
"s_add_u32 m0, s70, 1088\n"
"v_mfma_f32_16x16x32_bf16 v[132:135], a[12:15], a[212:215], v[132:135]\n"
"buffer_load_dwordx4 v229, s[32:35], s49 offen lds\n"
"v_mfma_f32_16x16x32_bf16 v[136:139], a[16:19], a[184:187], v[136:139]\n"
"v_mfma_f32_16x16x32_bf16 v[136:139], a[20:23], a[212:215], v[136:139]\n"
"v_mfma_f32_16x16x32_bf16 v[140:143], a[24:27], a[184:187], v[140:143]\n"
"s_add_u32 m0, s70, 2176\n"
"v_mfma_f32_16x16x32_bf16 v[140:143], a[28:31], a[212:215], v[140:143]\n"
"buffer_load_dwordx4 v230, s[32:35], s49 offen lds\n"
"v_mfma_f32_16x16x32_bf16 v[160:163], a[0:3], a[188:191], v[160:163]\n"
"v_mfma_f32_16x16x32_bf16 v[160:163], a[4:7], a[216:219], v[160:163]\n"
"v_mfma_f32_16x16x32_bf16 v[164:167], a[8:11], a[188:191], v[164:167]\n"
"s_add_u32 m0, s70, 3264\n"
"v_mfma_f32_16x16x32_bf16 v[164:167], a[12:15], a[216:219], v[164:167]\n"
"buffer_load_dwordx4 v231, s[32:35], s49 offen lds\n"
"v_mfma_f32_16x16x32_bf16 v[168:171], a[16:19], a[188:191], v[168:171]\n"
"v_mfma_f32_16x16x32_bf16 v[168:171], a[20:23], a[216:219], v[168:171]\n"
"v_mfma_f32_16x16x32_bf16 v[172:175], a[24:27], a[188:191], v[172:175]\n"
"s_add_u32 m0, s70, 4352\n"
"v_mfma_f32_16x16x32_bf16 v[172:175], a[28:31], a[216:219], v[172:175]\n"
"buffer_load_dwordx4 v232, s[32:35], s49 offen lds\n"
"ds_read_b64_tr_b16 a[224:225], v224 offset:128\n"
"v_mfma_f32_16x16x32_bf16 v[0:3], a[0:3], a[168:171], v[0:3]\n"
"ds_read_b64_tr_b16 a[226:227], v224 offset:4480\n"
"v_mfma_f32_16x16x32_bf16 v[0:3], a[4:7], a[196:199], v[0:3]\n"
"ds_read_b64_tr_b16 a[228:229], v224 offset:8832\n"
"v_mfma_f32_16x16x32_bf16 v[4:7], a[8:11], a[168:171], v[4:7]\n"
"s_add_u32 m0, s70, 5440\n"
"ds_read_b64_tr_b16 a[230:231], v224 offset:13184\n"
"v_mfma_f32_16x16x32_bf16 v[4:7], a[12:15], a[196:199], v[4:7]\n"
"buffer_load_dwordx4 v233, s[32:35], s49 offen lds\n"
"ds_read_b64_tr_b16 a[232:233], v225 offset:128\n"
"v_mfma_f32_16x16x32_bf16 v[8:11], a[16:19], a[168:171], v[8:11]\n"
"ds_read_b64_tr_b16 a[234:235], v225 offset:4480\n"
"v_mfma_f32_16x16x32_bf16 v[8:11], a[20:23], a[196:199], v[8:11]\n"
"ds_read_b64_tr_b16 a[236:237], v225 offset:8832\n"
"v_mfma_f32_16x16x32_bf16 v[12:15], a[24:27], a[168:171], v[12:15]\n"
"s_add_u32 m0, s70, 6528\n"
"ds_read_b64_tr_b16 a[238:239], v225 offset:13184\n"
"v_mfma_f32_16x16x32_bf16 v[12:15], a[28:31], a[196:199], v[12:15]\n"
"buffer_load_dwordx4 v234, s[32:35], s49 offen lds\n"
"ds_read_b64_tr_b16 a[240:241], v224 offset:192\n"
"v_mfma_f32_16x16x32_bf16 v[32:35], a[0:3], a[172:175], v[32:35]\n"
"ds_read_b64_tr_b16 a[242:243], v224 offset:4544\n"
"v_mfma_f32_16x16x32_bf16 v[32:35], a[4:7], a[200:203], v[32:35]\n"
"ds_read_b64_tr_b16 a[244:245], v224 offset:8896\n"
"v_mfma_f32_16x16x32_bf16 v[36:39], a[8:11], a[172:175], v[36:39]\n"
"s_add_u32 m0, s70, 7616\n"
"ds_read_b64_tr_b16 a[246:247], v224 offset:13248\n"
"v_mfma_f32_16x16x32_bf16 v[36:39], a[12:15], a[200:203], v[36:39]\n"
"buffer_load_dwordx4 v235, s[32:35], s49 offen lds\n"
"s_add_u32 s49, s49, 65536\n"
"ds_read_b64_tr_b16 a[248:249], v225 offset:192\n"
"v_mfma_f32_16x16x32_bf16 v[40:43], a[16:19], a[172:175], v[40:43]\n"
"ds_read_b64_tr_b16 a[250:251], v225 offset:4544\n"
"v_mfma_f32_16x16x32_bf16 v[40:43], a[20:23], a[200:203], v[40:43]\n"
"ds_read_b64_tr_b16 a[252:253], v225 offset:8896\n"
"v_mfma_f32_16x16x32_bf16 v[44:47], a[24:27], a[172:175], v[44:47]\n"
"s_add_u32 m0, s70, 8704\n"
"ds_read_b64_tr_b16 a[254:255], v225 offset:13248\n"
"v_mfma_f32_16x16x32_bf16 v[44:47], a[28:31], a[200:203], v[44:47]\n"
"buffer_load_dwordx4 v228, s[32:35], s49 offen lds\n"
"v_mfma_f32_16x16x32_bf16 v[64:67], a[0:3], a[176:179], v[64:67]\n"
"v_mfma_f32_16x16x32_bf16 v[64:67], a[4:7], a[204:207], v[64:67]\n"
"v_mfma_f32_16x16x32_bf16 v[68:71], a[8:11], a[176:179], v[68:71]\n"
"s_add_u32 m0, s70, 9792\n"
"v_mfma_f32_16x16x32_bf16 v[68:71], a[12:15], a[204:207], v[68:71]\n"
"buffer_load_dwordx4 v229, s[32:35], s49 offen lds\n"
"v_mfma_f32_16x16x32_bf16 v[72:75], a[16:19], a[176:179], v[72:75]\n"
"v_mfma_f32_16x16x32_bf16 v[72:75], a[20:23], a[204:207], v[72:75]\n"
"v_mfma_f32_16x16x32_bf16 v[76:79], a[24:27], a[176:179], v[76:79]\n"
"s_add_u32 m0, s70, 10880\n"
"s_waitcnt lgkmcnt(0)\n"
"v_mfma_f32_16x16x32_bf16 v[76:79], a[28:31], a[204:207], v[76:79]\n"
"buffer_load_dwordx4 v230, s[32:35], s49 offen lds\n"
"s_waitcnt vmcnt(36)\n"
"v_mfma_f32_16x16x32_bf16 v[16:19], a[224:227], a[112:115], v[16:19]\n"
"s_waitcnt vmcnt(35)\n"
"ds_read_b64_tr_b16 a[0:1], v226 offset:128\n"
"v_mfma_f32_16x16x32_bf16 v[16:19], a[228:231], a[140:143], v[16:19]\n"
"ds_read_b64_tr_b16 a[2:3], v226 offset:4480\n"
"v_mfma_f32_16x16x32_bf16 v[20:23], a[232:235], a[112:115], v[20:23]\n"
"s_add_u32 m0, s70, 11968\n"
"ds_read_b64_tr_b16 a[4:5], v226 offset:8832\n"
"v_mfma_f32_16x16x32_bf16 v[20:23], a[236:239], a[140:143], v[20:23]\n"
"buffer_load_dwordx4 v231, s[32:35], s49 offen lds\n"
"ds_read_b64_tr_b16 a[6:7], v226 offset:13184\n"
"v_mfma_f32_16x16x32_bf16 v[24:27], a[240:243], a[112:115], v[24:27]\n"
"ds_read_b64_tr_b16 a[8:9], v227 offset:128\n"
"v_mfma_f32_16x16x32_bf16 v[24:27], a[244:247], a[140:143], v[24:27]\n"
"ds_read_b64_tr_b16 a[10:11], v227 offset:4480\n"
"v_mfma_f32_16x16x32_bf16 v[28:31], a[248:251], a[112:115], v[28:31]\n"
"s_add_u32 m0, s70, 13056\n"
"ds_read_b64_tr_b16 a[12:13], v227 offset:8832\n"
"v_mfma_f32_16x16x32_bf16 v[28:31], a[252:255], a[140:143], v[28:31]\n"
"s_waitcnt vmcnt(34)\n"
"buffer_load_dwordx4 v232, s[32:35], s49 offen lds\n"
"ds_read_b64_tr_b16 a[14:15], v227 offset:13184\n"
"v_mfma_f32_16x16x32_bf16 v[48:51], a[224:227], a[116:119], v[48:51]\n"
"s_waitcnt vmcnt(34)\n"
"ds_read_b64_tr_b16 a[16:17], v226 offset:192\n"
"v_mfma_f32_16x16x32_bf16 v[48:51], a[228:231], a[144:147], v[48:51]\n"
"ds_read_b64_tr_b16 a[18:19], v226 offset:4544\n"
"v_mfma_f32_16x16x32_bf16 v[52:55], a[232:235], a[116:119], v[52:55]\n"
"s_add_u32 m0, s70, 14144\n"
"ds_read_b64_tr_b16 a[20:21], v226 offset:8896\n"
"v_mfma_f32_16x16x32_bf16 v[52:55], a[236:239], a[144:147], v[52:55]\n"
"buffer_load_dwordx4 v233, s[32:35], s49 offen lds\n"
"ds_read_b64_tr_b16 a[22:23], v226 offset:13248\n"
"v_mfma_f32_16x16x32_bf16 v[56:59], a[240:243], a[116:119], v[56:59]\n"
"ds_read_b64_tr_b16 a[24:25], v227 offset:192\n"
"v_mfma_f32_16x16x32_bf16 v[56:59], a[244:247], a[144:147], v[56:59]\n"
"ds_read_b64_tr_b16 a[26:27], v227 offset:4544\n"
"v_mfma_f32_16x16x32_bf16 v[60:63], a[248:251], a[116:119], v[60:63]\n"
"s_add_u32 m0, s70, 15232\n"
"ds_read_b64_tr_b16 a[28:29], v227 offset:8896\n"
"v_mfma_f32_16x16x32_bf16 v[60:63], a[252:255], a[144:147], v[60:63]\n"
"s_waitcnt vmcnt(33)\n"
"buffer_load_dwordx4 v234, s[32:35], s49 offen lds\n"
"ds_read_b64_tr_b16 a[30:31], v227 offset:13248\n"
"v_mfma_f32_16x16x32_bf16 v[80:83], a[224:227], a[120:123], v[80:83]\n"
"s_waitcnt vmcnt(33)\n"
"v_mfma_f32_16x16x32_bf16 v[80:83], a[228:231], a[148:151], v[80:83]\n"
"v_mfma_f32_16x16x32_bf16 v[84:87], a[232:235], a[120:123], v[84:87]\n"
"s_add_u32 m0, s70, 16320\n"
"v_mfma_f32_16x16x32_bf16 v[84:87], a[236:239], a[148:151], v[84:87]\n"
"buffer_load_dwordx4 v235, s[32:35], s49 offen lds\n"
"v_mfma_f32_16x16x32_bf16 v[88:91], a[240:243], a[120:123], v[88:91]\n"
"v_mfma_f32_16x16x32_bf16 v[88:91], a[244:247], a[148:151], v[88:91]\n"
"v_mfma_f32_16x16x32_bf16 v[92:95], a[248:251], a[120:123], v[92:95]\n"
"v_mfma_f32_16x16x32_bf16 v[92:95], a[252:255], a[148:151], v[92:95]\n"
"s_waitcnt vmcnt(32)\n"
"buffer_load_dwordx4 a[192:195], v238, s[28:31], s61 offen sc1\n"
"v_mfma_f32_16x16x32_bf16 v[112:115], a[224:227], a[124:127], v[112:115]\n"
"s_waitcnt vmcnt(32)\n"
"v_mfma_f32_16x16x32_bf16 v[112:115], a[228:231], a[152:155], v[112:115]\n"
"v_mfma_f32_16x16x32_bf16 v[116:119], a[232:235], a[124:127], v[116:119]\n"
"v_mfma_f32_16x16x32_bf16 v[116:119], a[236:239], a[152:155], v[116:119]\n"
"buffer_load_dwordx4 a[220:223], v238, s[28:31], s63 offen sc1\n"
"v_mfma_f32_16x16x32_bf16 v[120:123], a[240:243], a[124:127], v[120:123]\n"
"v_mfma_f32_16x16x32_bf16 v[120:123], a[244:247], a[152:155], v[120:123]\n"
"v_mfma_f32_16x16x32_bf16 v[124:127], a[248:251], a[124:127], v[124:127]\n"
"v_mfma_f32_16x16x32_bf16 v[124:127], a[252:255], a[152:155], v[124:127]\n"
"s_waitcnt vmcnt(31)\n"
"buffer_load_dwordx4 a[180:183], v243, s[28:31], s61 offen sc1\n"
"v_mfma_f32_16x16x32_bf16 v[144:147], a[224:227], a[128:131], v[144:147]\n"
"s_waitcnt vmcnt(31)\n"
"ds_read_b64_tr_b16 a[80:81], v224\n"
"v_mfma_f32_16x16x32_bf16 v[144:147], a[228:231], a[156:159], v[144:147]\n"
"ds_read_b64_tr_b16 a[82:83], v224 offset:4352\n"
"v_mfma_f32_16x16x32_bf16 v[148:151], a[232:235], a[128:131], v[148:151]\n"
"ds_read_b64_tr_b16 a[84:85], v224 offset:8704\n"
"v_mfma_f32_16x16x32_bf16 v[148:151], a[236:239], a[156:159], v[148:151]\n"
"buffer_load_dwordx4 a[208:211], v243, s[28:31], s63 offen sc1\n"
"ds_read_b64_tr_b16 a[86:87], v224 offset:13056\n"
"v_mfma_f32_16x16x32_bf16 v[152:155], a[240:243], a[128:131], v[152:155]\n"
"ds_read_b64_tr_b16 a[88:89], v225\n"
"v_mfma_f32_16x16x32_bf16 v[152:155], a[244:247], a[156:159], v[152:155]\n"
"ds_read_b64_tr_b16 a[90:91], v225 offset:4352\n"
"v_mfma_f32_16x16x32_bf16 v[156:159], a[248:251], a[128:131], v[156:159]\n"
"ds_read_b64_tr_b16 a[92:93], v225 offset:8704\n"
"v_mfma_f32_16x16x32_bf16 v[156:159], a[252:255], a[156:159], v[156:159]\n"
"s_waitcnt vmcnt(30)\n"
"buffer_load_dwordx4 a[184:187], v243, s[28:31], s61 offen offset:1024 sc1\n"
"ds_read_b64_tr_b16 a[94:95], v225 offset:13056\n"
"v_mfma_f32_16x16x32_bf16 v[176:179], a[224:227], a[132:135], v[176:179]\n"
"s_waitcnt vmcnt(30)\n"
"ds_read_b64_tr_b16 a[96:97], v224 offset:64\n"
"v_mfma_f32_16x16x32_bf16 v[176:179], a[228:231], a[160:163], v[176:179]\n"
"ds_read_b64_tr_b16 a[98:99], v224 offset:4416\n"
"v_mfma_f32_16x16x32_bf16 v[180:183], a[232:235], a[132:135], v[180:183]\n"
"ds_read_b64_tr_b16 a[100:101], v224 offset:8768\n"
"v_mfma_f32_16x16x32_bf16 v[180:183], a[236:239], a[160:163], v[180:183]\n"
"buffer_load_dwordx4 a[212:215], v243, s[28:31], s63 offen offset:1024 sc1\n"
"ds_read_b64_tr_b16 a[102:103], v224 offset:13120\n"
"v_mfma_f32_16x16x32_bf16 v[184:187], a[240:243], a[132:135], v[184:187]\n"
"ds_read_b64_tr_b16 a[104:105], v225 offset:64\n"
"v_mfma_f32_16x16x32_bf16 v[184:187], a[244:247], a[160:163], v[184:187]\n"
"ds_read_b64_tr_b16 a[106:107], v225 offset:4416\n"
"v_mfma_f32_16x16x32_bf16 v[188:191], a[248:251], a[132:135], v[188:191]\n"
"ds_read_b64_tr_b16 a[108:109], v225 offset:8768\n"
"v_mfma_f32_16x16x32_bf16 v[188:191], a[252:255], a[160:163], v[188:191]\n"
"s_waitcnt vmcnt(29)\n"
"buffer_load_dwordx4 a[188:191], v243, s[28:31], s61 offen offset:2048 sc1\n"
"ds_read_b64_tr_b16 a[110:111], v225 offset:13120\n"
"v_mfma_f32_16x16x32_bf16 v[208:211], a[224:227], a[136:139], v[208:211]\n"
"s_waitcnt vmcnt(29)\n"
"v_mfma_f32_16x16x32_bf16 v[208:211], a[228:231], a[164:167], v[208:211]\n"
"v_mfma_f32_16x16x32_bf16 v[212:215], a[232:235], a[136:139], v[212:215]\n"
"v_mfma_f32_16x16x32_bf16 v[212:215], a[236:239], a[164:167], v[212:215]\n"
"buffer_load_dwordx4 a[216:219], v243, s[28:31], s63 offen offset:2048 sc1\n"
"v_mfma_f32_16x16x32_bf16 v[216:219], a[240:243], a[136:139], v[216:219]\n"
"v_mfma_f32_16x16x32_bf16 v[216:219], a[244:247], a[164:167], v[216:219]\n"
"v_mfma_f32_16x16x32_bf16 v[220:223], a[248:251], a[136:139], v[220:223]\n"
"s_waitcnt lgkmcnt(0)\n"
"v_mfma_f32_16x16x32_bf16 v[220:223], a[252:255], a[164:167], v[220:223]\n"
"buffer_load_dwordx4 a[168:171], v242, s[28:31], s61 offen sc1\n"
"v_mfma_f32_16x16x32_bf16 v[112:115], a[0:3], a[44:47], v[112:115]\n"
"v_mfma_f32_16x16x32_bf16 v[112:115], a[4:7], a[68:71], v[112:115]\n"
"v_mfma_f32_16x16x32_bf16 v[116:119], a[8:11], a[44:47], v[116:119]\n"
"v_mfma_f32_16x16x32_bf16 v[116:119], a[12:15], a[68:71], v[116:119]\n"
"buffer_load_dwordx4 a[196:199], v242, s[28:31], s63 offen sc1\n"
"v_mfma_f32_16x16x32_bf16 v[120:123], a[16:19], a[44:47], v[120:123]\n"
"v_subrev_u32 v229, s72, v224\n"
"v_mfma_f32_16x16x32_bf16 v[120:123], a[20:23], a[68:71], v[120:123]\n"
"v_subrev_u32 v230, s72, v225\n"
"v_mfma_f32_16x16x32_bf16 v[124:127], a[24:27], a[44:47], v[124:127]\n"
"s_and_b32 s14, s21, 1\n"
"v_mfma_f32_16x16x32_bf16 v[124:127], a[28:31], a[68:71], v[124:127]\n"
"buffer_load_dwordx4 a[172:175], v242, s[28:31], s61 offen offset:1024 sc1\n"
"v_bfe_u32 v228, v251, 8, 1\n"
"v_mfma_f32_16x16x32_bf16 v[144:147], a[0:3], a[48:51], v[144:147]\n"
"v_xor_b32 v228, s14, v228\n"
"v_mfma_f32_16x16x32_bf16 v[144:147], a[4:7], a[72:75], v[144:147]\n"
"v_lshl_add_u32 v228, v228, 3, v250\n"
"v_mfma_f32_16x16x32_bf16 v[148:151], a[8:11], a[48:51], v[148:151]\n"
"s_sub_u32 s14, s70, s48\n"
"v_mfma_f32_16x16x32_bf16 v[148:151], a[12:15], a[72:75], v[148:151]\n"
"buffer_load_dwordx4 a[200:203], v242, s[28:31], s63 offen offset:1024 sc1\n"
"v_add_u32 v244, s14, v244\n"
"v_mfma_f32_16x16x32_bf16 v[152:155], a[16:19], a[48:51], v[152:155]\n"
"v_add_u32 v245, s14, v245\n"
"v_mfma_f32_16x16x32_bf16 v[152:155], a[20:23], a[72:75], v[152:155]\n"
"v_add_u32 v253, s14, v253\n"
"v_mfma_f32_16x16x32_bf16 v[156:159], a[24:27], a[48:51], v[156:159]\n"
"v_add_u32 v254, s14, v254\n"
"v_mfma_f32_16x16x32_bf16 v[156:159], a[28:31], a[72:75], v[156:159]\n"
"buffer_load_dwordx4 a[176:179], v242, s[28:31], s61 offen offset:2048 sc1\n"
"v_mfma_f32_16x16x32_bf16 v[176:179], a[0:3], a[52:55], v[176:179]\n"
"v_mfma_f32_16x16x32_bf16 v[176:179], a[4:7], a[76:79], v[176:179]\n"
"v_mfma_f32_16x16x32_bf16 v[180:183], a[8:11], a[52:55], v[180:183]\n"
"v_mfma_f32_16x16x32_bf16 v[180:183], a[12:15], a[76:79], v[180:183]\n"
"buffer_load_dwordx4 a[204:207], v242, s[28:31], s63 offen offset:2048 sc1\n"
"v_mfma_f32_16x16x32_bf16 v[184:187], a[16:19], a[52:55], v[184:187]\n"
"v_mfma_f32_16x16x32_bf16 v[184:187], a[20:23], a[76:79], v[184:187]\n"
"v_mfma_f32_16x16x32_bf16 v[188:191], a[24:27], a[52:55], v[188:191]\n"
"v_mfma_f32_16x16x32_bf16 v[188:191], a[28:31], a[76:79], v[188:191]\n"
"s_waitcnt vmcnt(35)\n"
"v_mfma_f32_16x16x32_bf16 v[16:19], a[0:3], a[32:35], v[16:19]\n"
"s_waitcnt vmcnt(34)\n"
"v_mfma_f32_16x16x32_bf16 v[16:19], a[4:7], a[56:59], v[16:19]\n"
"v_mfma_f32_16x16x32_bf16 v[20:23], a[8:11], a[32:35], v[20:23]\n"
"v_mfma_f32_16x16x32_bf16 v[20:23], a[12:15], a[56:59], v[20:23]\n"
"v_mfma_f32_16x16x32_bf16 v[24:27], a[16:19], a[32:35], v[24:27]\n"
"s_waitcnt vmcnt(14)\n"
"ds_read_b64_tr_b16 a[224:225], v229 offset:128\n"
"v_mfma_f32_16x16x32_bf16 v[24:27], a[20:23], a[56:59], v[24:27]\n"
"ds_read_b64_tr_b16 a[226:227], v229 offset:4480\n"
"v_mfma_f32_16x16x32_bf16 v[28:31], a[24:27], a[32:35], v[28:31]\n"
"ds_read_b64_tr_b16 a[228:229], v229 offset:8832\n"
"v_mfma_f32_16x16x32_bf16 v[28:31], a[28:31], a[56:59], v[28:31]\n"
"ds_read_b64_tr_b16 a[230:231], v229 offset:13184\n"
"v_mfma_f32_16x16x32_bf16 v[48:51], a[0:3], a[36:39], v[48:51]\n"
"ds_read_b64_tr_b16 a[232:233], v230 offset:128\n"
"v_mfma_f32_16x16x32_bf16 v[48:51], a[4:7], a[60:63], v[48:51]\n"
"ds_read_b64_tr_b16 a[234:235], v230 offset:4480\n"
"v_mfma_f32_16x16x32_bf16 v[52:55], a[8:11], a[36:39], v[52:55]\n"
"ds_read_b64_tr_b16 a[236:237], v230 offset:8832\n"
"v_mfma_f32_16x16x32_bf16 v[52:55], a[12:15], a[60:63], v[52:55]\n"
"ds_read_b64_tr_b16 a[238:239], v230 offset:13184\n"
"v_mfma_f32_16x16x32_bf16 v[56:59], a[16:19], a[36:39], v[56:59]\n"
"ds_read_b64_tr_b16 a[240:241], v229 offset:192\n"
"v_mfma_f32_16x16x32_bf16 v[56:59], a[20:23], a[60:63], v[56:59]\n"
"ds_read_b64_tr_b16 a[242:243], v229 offset:4544\n"
"v_mfma_f32_16x16x32_bf16 v[60:63], a[24:27], a[36:39], v[60:63]\n"
"ds_read_b64_tr_b16 a[244:245], v229 offset:8896\n"
"v_mfma_f32_16x16x32_bf16 v[60:63], a[28:31], a[60:63], v[60:63]\n"
"ds_read_b64_tr_b16 a[246:247], v229 offset:13248\n"
"v_mfma_f32_16x16x32_bf16 v[80:83], a[0:3], a[40:43], v[80:83]\n"
"ds_read_b64_tr_b16 a[248:249], v230 offset:192\n"
"v_mfma_f32_16x16x32_bf16 v[80:83], a[4:7], a[64:67], v[80:83]\n"
"ds_read_b64_tr_b16 a[250:251], v230 offset:4544\n"
"v_mfma_f32_16x16x32_bf16 v[84:87], a[8:11], a[40:43], v[84:87]\n"
"ds_read_b64_tr_b16 a[252:253], v230 offset:8896\n"
"v_mfma_f32_16x16x32_bf16 v[84:87], a[12:15], a[64:67], v[84:87]\n"
"ds_read_b64_tr_b16 a[254:255], v230 offset:13248\n"
"v_mfma_f32_16x16x32_bf16 v[88:91], a[16:19], a[40:43], v[88:91]\n"
"v_mfma_f32_16x16x32_bf16 v[88:91], a[20:23], a[64:67], v[88:91]\n"
"v_mfma_f32_16x16x32_bf16 v[92:95], a[24:27], a[40:43], v[92:95]\n"
"v_mfma_f32_16x16x32_bf16 v[92:95], a[28:31], a[64:67], v[92:95]\n"
"s_waitcnt vmcnt(13)\n"
"s_waitcnt lgkmcnt(14)\n"
"v_mfma_f32_16x16x32_bf16 v[208:211], a[224:227], a[192:195], v[208:211]\n"
"s_waitcnt vmcnt(12)\n"
"s_waitcnt lgkmcnt(12)\n"
"v_mfma_f32_16x16x32_bf16 v[208:211], a[228:231], a[220:223], v[208:211]\n"
"s_waitcnt lgkmcnt(10)\n"
"ds_read_b64_tr_b16 a[0:1], v229\n"
"v_mfma_f32_16x16x32_bf16 v[212:215], a[232:235], a[192:195], v[212:215]\n"
"s_waitcnt lgkmcnt(9)\n"
"ds_read_b64_tr_b16 a[2:3], v229 offset:4352\n"
"v_mfma_f32_16x16x32_bf16 v[212:215], a[236:239], a[220:223], v[212:215]\n"
"s_waitcnt lgkmcnt(8)\n"
"ds_read_b64_tr_b16 a[4:5], v229 offset:8704\n"
"v_mfma_f32_16x16x32_bf16 v[216:219], a[240:243], a[192:195], v[216:219]\n"
"s_waitcnt lgkmcnt(7)\n"
"ds_read_b64_tr_b16 a[6:7], v229 offset:13056\n"
"v_mfma_f32_16x16x32_bf16 v[216:219], a[244:247], a[220:223], v[216:219]\n"
"s_waitcnt lgkmcnt(6)\n"
"ds_read_b64_tr_b16 a[8:9], v230\n"
"v_mfma_f32_16x16x32_bf16 v[220:223], a[248:251], a[192:195], v[220:223]\n"
"s_waitcnt lgkmcnt(5)\n"
"ds_read_b64_tr_b16 a[10:11], v230 offset:4352\n"
"v_mfma_f32_16x16x32_bf16 v[220:223], a[252:255], a[220:223], v[220:223]\n"
"s_waitcnt vmcnt(11)\n"
"ds_read_b64_tr_b16 a[12:13], v230 offset:8704\n"
"v_mfma_f32_16x16x32_bf16 v[112:115], a[224:227], a[180:183], v[112:115]\n"
"s_waitcnt vmcnt(10)\n"
"ds_read_b64_tr_b16 a[14:15], v230 offset:13056\n"
"v_mfma_f32_16x16x32_bf16 v[112:115], a[228:231], a[208:211], v[112:115]\n"
"ds_read_b64_tr_b16 a[16:17], v229 offset:64\n"
"v_mfma_f32_16x16x32_bf16 v[116:119], a[232:235], a[180:183], v[116:119]\n"
"ds_read_b64_tr_b16 a[18:19], v229 offset:4416\n"
"v_mfma_f32_16x16x32_bf16 v[116:119], a[236:239], a[208:211], v[116:119]\n"
"ds_read_b64_tr_b16 a[20:21], v229 offset:8768\n"
"v_mfma_f32_16x16x32_bf16 v[120:123], a[240:243], a[180:183], v[120:123]\n"
"ds_read_b64_tr_b16 a[22:23], v229 offset:13120\n"
"v_mfma_f32_16x16x32_bf16 v[120:123], a[244:247], a[208:211], v[120:123]\n"
"ds_read_b64_tr_b16 a[24:25], v230 offset:64\n"
"v_mfma_f32_16x16x32_bf16 v[124:127], a[248:251], a[180:183], v[124:127]\n"
"ds_read_b64_tr_b16 a[26:27], v230 offset:4416\n"
"v_mfma_f32_16x16x32_bf16 v[124:127], a[252:255], a[208:211], v[124:127]\n"
"s_waitcnt vmcnt(9)\n"
"ds_read_b64_tr_b16 a[28:29], v230 offset:8768\n"
"v_mfma_f32_16x16x32_bf16 v[144:147], a[224:227], a[184:187], v[144:147]\n"
"s_waitcnt vmcnt(8)\n"
"ds_read_b64_tr_b16 a[30:31], v230 offset:13120\n"
"v_mfma_f32_16x16x32_bf16 v[144:147], a[228:231], a[212:215], v[144:147]\n"
"ds_write_b128 v244, v[208:211]\n"
"v_mfma_f32_16x16x32_bf16 v[148:151], a[232:235], a[184:187], v[148:151]\n"
"v_mfma_f32_16x16x32_bf16 v[148:151], a[236:239], a[212:215], v[148:151]\n"
"v_mfma_f32_16x16x32_bf16 v[152:155], a[240:243], a[184:187], v[152:155]\n"
"ds_write_b128 v244, v[212:215] offset:1024\n"
"v_mfma_f32_16x16x32_bf16 v[152:155], a[244:247], a[212:215], v[152:155]\n"
"v_mfma_f32_16x16x32_bf16 v[156:159], a[248:251], a[184:187], v[156:159]\n"
"v_mfma_f32_16x16x32_bf16 v[156:159], a[252:255], a[212:215], v[156:159]\n"
"s_waitcnt vmcnt(7)\n"
"ds_write_b128 v244, v[216:219] offset:2048\n"
"v_mfma_f32_16x16x32_bf16 v[176:179], a[224:227], a[188:191], v[176:179]\n"
"s_waitcnt vmcnt(6)\n"
"v_mfma_f32_16x16x32_bf16 v[176:179], a[228:231], a[216:219], v[176:179]\n"
"v_mfma_f32_16x16x32_bf16 v[180:183], a[232:235], a[188:191], v[180:183]\n"
"ds_write_b128 v244, v[220:223] offset:3072\n"
"v_mfma_f32_16x16x32_bf16 v[180:183], a[236:239], a[216:219], v[180:183]\n"
"v_mfma_f32_16x16x32_bf16 v[184:187], a[240:243], a[188:191], v[184:187]\n"
"v_mfma_f32_16x16x32_bf16 v[184:187], a[244:247], a[216:219], v[184:187]\n"
"ds_write_b128 v244, v[112:115] offset:4096\n"
"v_mfma_f32_16x16x32_bf16 v[188:191], a[248:251], a[188:191], v[188:191]\n"
"v_mfma_f32_16x16x32_bf16 v[188:191], a[252:255], a[216:219], v[188:191]\n"
"s_waitcnt vmcnt(5)\n"
"v_mfma_f32_16x16x32_bf16 v[16:19], a[224:227], a[168:171], v[16:19]\n"
"s_waitcnt vmcnt(4)\n"
"ds_write_b128 v244, v[116:119] offset:5120\n"
"v_mfma_f32_16x16x32_bf16 v[16:19], a[228:231], a[196:199], v[16:19]\n"
"v_mfma_f32_16x16x32_bf16 v[20:23], a[232:235], a[168:171], v[20:23]\n"
"v_mfma_f32_16x16x32_bf16 v[20:23], a[236:239], a[196:199], v[20:23]\n"
"ds_write_b128 v244, v[120:123] offset:6144\n"
"v_mfma_f32_16x16x32_bf16 v[24:27], a[240:243], a[168:171], v[24:27]\n"
"v_mfma_f32_16x16x32_bf16 v[24:27], a[244:247], a[196:199], v[24:27]\n"
"v_mfma_f32_16x16x32_bf16 v[28:31], a[248:251], a[168:171], v[28:31]\n"
"ds_write_b128 v244, v[124:127] offset:7168\n"
"v_mfma_f32_16x16x32_bf16 v[28:31], a[252:255], a[196:199], v[28:31]\n"
"s_waitcnt vmcnt(3)\n"
"v_mfma_f32_16x16x32_bf16 v[48:51], a[224:227], a[172:175], v[48:51]\n"
"s_waitcnt vmcnt(2)\n"
"v_mfma_f32_16x16x32_bf16 v[48:51], a[228:231], a[200:203], v[48:51]\n"
"ds_write_b128 v244, v[144:147] offset:8192\n"
"v_mfma_f32_16x16x32_bf16 v[52:55], a[232:235], a[172:175], v[52:55]\n"
"v_mfma_f32_16x16x32_bf16 v[52:55], a[236:239], a[200:203], v[52:55]\n"
"v_mfma_f32_16x16x32_bf16 v[56:59], a[240:243], a[172:175], v[56:59]\n"
"ds_write_b128 v244, v[148:151] offset:9216\n"
"v_mfma_f32_16x16x32_bf16 v[56:59], a[244:247], a[200:203], v[56:59]\n"
"v_mfma_f32_16x16x32_bf16 v[60:63], a[248:251], a[172:175], v[60:63]\n"
"v_mfma_f32_16x16x32_bf16 v[60:63], a[252:255], a[200:203], v[60:63]\n"
"s_waitcnt vmcnt(1)\n"
"ds_write_b128 v244, v[152:155] offset:10240\n"
"v_mfma_f32_16x16x32_bf16 v[80:83], a[224:227], a[176:179], v[80:83]\n"
"s_waitcnt vmcnt(0)\n"
"v_mfma_f32_16x16x32_bf16 v[80:83], a[228:231], a[204:207], v[80:83]\n"
"v_mfma_f32_16x16x32_bf16 v[84:87], a[232:235], a[176:179], v[84:87]\n"
"ds_write_b128 v244, v[156:159] offset:11264\n"
"v_mfma_f32_16x16x32_bf16 v[84:87], a[236:239], a[204:207], v[84:87]\n"
"v_mfma_f32_16x16x32_bf16 v[88:91], a[240:243], a[176:179], v[88:91]\n"
"v_mfma_f32_16x16x32_bf16 v[88:91], a[244:247], a[204:207], v[88:91]\n"
"ds_write_b128 v244, v[176:179] offset:12288\n"
"v_mfma_f32_16x16x32_bf16 v[92:95], a[248:251], a[176:179], v[92:95]\n"
"v_mfma_f32_16x16x32_bf16 v[92:95], a[252:255], a[204:207], v[92:95]\n"
"s_waitcnt lgkmcnt(15)\n"
"v_mfma_f32_16x16x32_bf16 v[0:3], a[0:3], a[168:171], v[0:3]\n"
"s_waitcnt lgkmcnt(15)\n"
"ds_write_b128 v244, v[180:183] offset:13312\n"
"v_mfma_f32_16x16x32_bf16 v[0:3], a[4:7], a[196:199], v[0:3]\n"
"s_waitcnt lgkmcnt(15)\n"
"v_mfma_f32_16x16x32_bf16 v[4:7], a[8:11], a[168:171], v[4:7]\n"
"s_waitcnt lgkmcnt(15)\n"
"v_mfma_f32_16x16x32_bf16 v[4:7], a[12:15], a[196:199], v[4:7]\n"
"s_waitcnt lgkmcnt(15)\n"
"ds_write_b128 v244, v[184:187] offset:14336\n"
"v_mfma_f32_16x16x32_bf16 v[8:11], a[16:19], a[168:171], v[8:11]\n"
"s_waitcnt lgkmcnt(15)\n"
"v_mfma_f32_16x16x32_bf16 v[8:11], a[20:23], a[196:199], v[8:11]\n"
"s_waitcnt lgkmcnt(15)\n"
"v_mfma_f32_16x16x32_bf16 v[12:15], a[24:27], a[168:171], v[12:15]\n"
"s_waitcnt lgkmcnt(15)\n"
"ds_write_b128 v244, v[188:191] offset:15360\n"
"v_mfma_f32_16x16x32_bf16 v[12:15], a[28:31], a[196:199], v[12:15]\n"
"v_mfma_f32_16x16x32_bf16 v[32:35], a[0:3], a[172:175], v[32:35]\n"
"s_waitcnt lgkmcnt(0)\n"
"s_barrier\n"
"v_mfma_f32_16x16x32_bf16 v[32:35], a[4:7], a[200:203], v[32:35]\n"
"ds_read_b128 v[208:211], v253\n"
"v_mfma_f32_16x16x32_bf16 v[36:39], a[8:11], a[172:175], v[36:39]\n"
"ds_read_b128 v[212:215], v253 offset:34816\n"
"v_mfma_f32_16x16x32_bf16 v[36:39], a[12:15], a[200:203], v[36:39]\n"
"ds_read_b128 v[216:219], v254\n"
"v_mfma_f32_16x16x32_bf16 v[40:43], a[16:19], a[172:175], v[40:43]\n"
"ds_read_b128 v[220:223], v254 offset:34816\n"
"v_mfma_f32_16x16x32_bf16 v[40:43], a[20:23], a[200:203], v[40:43]\n"
"ds_read_b128 v[112:115], v245\n"
"v_mfma_f32_16x16x32_bf16 v[44:47], a[24:27], a[172:175], v[44:47]\n"
"ds_read_b128 v[116:119], v245 offset:1024\n"
"v_mfma_f32_16x16x32_bf16 v[44:47], a[28:31], a[200:203], v[44:47]\n"
"ds_read_b128 v[120:123], v245 offset:2048\n"
"v_mfma_f32_16x16x32_bf16 v[64:67], a[0:3], a[176:179], v[64:67]\n"
"s_waitcnt lgkmcnt(5)\n"
"ds_read_b128 v[124:127], v245 offset:3072\n"
"v_pk_add_f32 v[208:209], v[208:209], v[212:213]\n"
"v_mfma_f32_16x16x32_bf16 v[64:67], a[4:7], a[204:207], v[64:67]\n"
"ds_read_b128 v[144:147], v245 offset:4096\n"
"v_pk_add_f32 v[210:211], v[210:211], v[214:215]\n"
"v_mfma_f32_16x16x32_bf16 v[68:71], a[8:11], a[176:179], v[68:71]\n"
"s_waitcnt lgkmcnt(5)\n"
"ds_read_b128 v[148:151], v245 offset:5120\n"
"v_pk_add_f32 v[216:217], v[216:217], v[220:221]\n"
"v_mfma_f32_16x16x32_bf16 v[68:71], a[12:15], a[204:207], v[68:71]\n"
"ds_read_b128 v[152:155], v245 offset:6144\n"
"v_pk_add_f32 v[218:219], v[218:219], v[222:223]\n"
"v_mfma_f32_16x16x32_bf16 v[72:75], a[16:19], a[176:179], v[72:75]\n"
"ds_read_b128 v[156:159], v245 offset:7168\n"
"s_add_u32 s15, s45, 12288\n"
"v_mfma_f32_16x16x32_bf16 v[72:75], a[20:23], a[204:207], v[72:75]\n"
"ds_read_b128 v[176:179], v245 offset:8192\n"
"v_pk_add_f32 v[208:209], v[208:209], v[216:217]\n"
"v_mfma_f32_16x16x32_bf16 v[76:79], a[24:27], a[176:179], v[76:79]\n"
"ds_read_b128 v[180:183], v245 offset:9216\n"
"v_mfma_f32_16x16x32_bf16 v[76:79], a[28:31], a[204:207], v[76:79]\n"
"ds_read_b128 v[184:187], v245 offset:10240\n"
"v_pk_add_f32 v[210:211], v[210:211], v[218:219]\n"
"v_mfma_f32_16x16x32_bf16 v[96:99], a[0:3], a[180:183], v[96:99]\n"
"ds_read_b64_tr_b16 a[224:225], v226\n"
"ds_read_b128 v[188:191], v245 offset:11264\n"
"buffer_store_dwordx4 v[208:211], v251, s[36:39], s15 offen sc1\n"
"v_mfma_f32_16x16x32_bf16 v[96:99], a[4:7], a[208:211], v[96:99]\n"
"ds_read_b64_tr_b16 a[226:227], v226 offset:4352\n"
"s_add_u32 s15, s45, 0\n"
"v_mfma_f32_16x16x32_bf16 v[100:103], a[8:11], a[180:183], v[100:103]\n"
"s_waitcnt lgkmcnt(13)\n"
"ds_read_b64_tr_b16 a[228:229], v226 offset:8704\n"
"v_pk_add_f32 v[16:17], v[16:17], v[112:113]\n"
"v_mfma_f32_16x16x32_bf16 v[100:103], a[12:15], a[208:211], v[100:103]\n"
"ds_read_b64_tr_b16 a[230:231], v226 offset:13056\n"
"v_pk_add_f32 v[18:19], v[18:19], v[114:115]\n"
"v_mfma_f32_16x16x32_bf16 v[104:107], a[16:19], a[180:183], v[104:107]\n"
"ds_read_b64_tr_b16 a[232:233], v227\n"
"v_add_u32 v244, s72, v244\n"
"v_mfma_f32_16x16x32_bf16 v[104:107], a[20:23], a[208:211], v[104:107]\n"
"ds_read_b64_tr_b16 a[234:235], v227 offset:4352\n"
"v_add_u32 v245, s72, v245\n"
"buffer_store_dwordx4 v[16:19], v251, s[36:39], s15 offen sc1\n"
"v_mfma_f32_16x16x32_bf16 v[108:111], a[24:27], a[180:183], v[108:111]\n"
"s_waitcnt lgkmcnt(15)\n"
"ds_read_b64_tr_b16 a[236:237], v227 offset:8704\n"
"v_add_u32 v253, s72, v253\n"
"v_pk_add_f32 v[20:21], v[20:21], v[116:117]\n"
"v_mfma_f32_16x16x32_bf16 v[108:111], a[28:31], a[208:211], v[108:111]\n"
"ds_read_b64_tr_b16 a[238:239], v227 offset:13056\n"
"v_add_u32 v254, s72, v254\n"
"v_pk_add_f32 v[22:23], v[22:23], v[118:119]\n"
"v_mfma_f32_16x16x32_bf16 v[128:131], a[0:3], a[184:187], v[128:131]\n"
"ds_read_b64_tr_b16 a[240:241], v226 offset:64\n"
"buffer_store_dwordx4 v[20:23], v251, s[36:39], s15 offen offset:1024 sc1\n"
"v_mfma_f32_16x16x32_bf16 v[128:131], a[4:7], a[212:215], v[128:131]\n"
"s_waitcnt lgkmcnt(15)\n"
"ds_read_b64_tr_b16 a[242:243], v226 offset:4416\n"
"v_pk_add_f32 v[24:25], v[24:25], v[120:121]\n"
"v_mfma_f32_16x16x32_bf16 v[132:135], a[8:11], a[184:187], v[132:135]\n"
"ds_read_b64_tr_b16 a[244:245], v226 offset:8768\n"
"v_mfma_f32_16x16x32_bf16 v[132:135], a[12:15], a[212:215], v[132:135]\n"
"ds_read_b64_tr_b16 a[246:247], v226 offset:13120\n"
"v_pk_add_f32 v[26:27], v[26:27], v[122:123]\n"
"v_mfma_f32_16x16x32_bf16 v[136:139], a[16:19], a[184:187], v[136:139]\n"
"ds_read_b64_tr_b16 a[248:249], v227 offset:64\n"
"buffer_store_dwordx4 v[24:27], v251, s[36:39], s15 offen offset:2048 sc1\n"
"v_mfma_f32_16x16x32_bf16 v[136:139], a[20:23], a[212:215], v[136:139]\n"
"s_waitcnt lgkmcnt(15)\n"
"ds_read_b64_tr_b16 a[250:251], v227 offset:4416\n"
"v_pk_add_f32 v[28:29], v[28:29], v[124:125]\n"
"v_mfma_f32_16x16x32_bf16 v[140:143], a[24:27], a[184:187], v[140:143]\n"
"ds_read_b64_tr_b16 a[252:253], v227 offset:8768\n"
"v_pk_add_f32 v[30:31], v[30:31], v[126:127]\n"
"v_mfma_f32_16x16x32_bf16 v[140:143], a[28:31], a[212:215], v[140:143]\n"
"ds_read_b64_tr_b16 a[254:255], v227 offset:13120\n"
"buffer_store_dwordx4 v[28:31], v251, s[36:39], s15 offen offset:3072 sc1\n"
"v_mfma_f32_16x16x32_bf16 v[160:163], a[0:3], a[188:191], v[160:163]\n"
"v_mfma_f32_16x16x32_bf16 v[160:163], a[4:7], a[216:219], v[160:163]\n"
"s_add_u32 s15, s45, 4096\n"
"v_mfma_f32_16x16x32_bf16 v[164:167], a[8:11], a[188:191], v[164:167]\n"
"s_waitcnt lgkmcnt(15)\n"
"v_pk_add_f32 v[48:49], v[48:49], v[144:145]\n"
"v_mfma_f32_16x16x32_bf16 v[164:167], a[12:15], a[216:219], v[164:167]\n"
"v_pk_add_f32 v[50:51], v[50:51], v[146:147]\n"
"v_mfma_f32_16x16x32_bf16 v[168:171], a[16:19], a[188:191], v[168:171]\n"
"buffer_store_dwordx4 v[48:51], v251, s[36:39], s15 offen sc1\n"
"v_mfma_f32_16x16x32_bf16 v[168:171], a[20:23], a[216:219], v[168:171]\n"
"s_waitcnt lgkmcnt(15)\n"
"v_pk_add_f32 v[52:53], v[52:53], v[148:149]\n"
"v_mfma_f32_16x16x32_bf16 v[172:175], a[24:27], a[188:191], v[172:175]\n"
"v_mfma_f32_16x16x32_bf16 v[172:175], a[28:31], a[216:219], v[172:175]\n"
"v_pk_add_f32 v[54:55], v[54:55], v[150:151]\n"
"v_mfma_f32_16x16x32_bf16 v[192:195], a[0:3], a[192:195], v[192:195]\n"
"buffer_store_dwordx4 v[52:55], v251, s[36:39], s15 offen offset:1024 sc1\n"
"v_mfma_f32_16x16x32_bf16 v[192:195], a[4:7], a[220:223], v[192:195]\n"
"s_waitcnt lgkmcnt(15)\n"
"v_pk_add_f32 v[56:57], v[56:57], v[152:153]\n"
"v_mfma_f32_16x16x32_bf16 v[196:199], a[8:11], a[192:195], v[196:199]\n"
"v_pk_add_f32 v[58:59], v[58:59], v[154:155]\n"
"v_mfma_f32_16x16x32_bf16 v[196:199], a[12:15], a[220:223], v[196:199]\n"
"buffer_store_dwordx4 v[56:59], v251, s[36:39], s15 offen offset:2048 sc1\n"
"v_mfma_f32_16x16x32_bf16 v[200:203], a[16:19], a[192:195], v[200:203]\n"
"v_mfma_f32_16x16x32_bf16 v[200:203], a[20:23], a[220:223], v[200:203]\n"
"s_waitcnt lgkmcnt(15)\n"
"v_pk_add_f32 v[60:61], v[60:61], v[156:157]\n"
"v_mfma_f32_16x16x32_bf16 v[204:207], a[24:27], a[192:195], v[204:207]\n"
"v_pk_add_f32 v[62:63], v[62:63], v[158:159]\n"
"v_mfma_f32_16x16x32_bf16 v[204:207], a[28:31], a[220:223], v[204:207]\n"
"buffer_store_dwordx4 v[60:63], v251, s[36:39], s15 offen offset:3072 sc1\n"
"v_mfma_f32_16x16x32_bf16 v[192:195], a[80:83], a[136:139], v[192:195]\n"
"s_add_u32 s15, s45, 8192\n"
"v_mfma_f32_16x16x32_bf16 v[192:195], a[84:87], a[164:167], v[192:195]\n"
"s_waitcnt lgkmcnt(15)\n"
"v_pk_add_f32 v[80:81], v[80:81], v[176:177]\n"
"v_mfma_f32_16x16x32_bf16 v[196:199], a[88:91], a[136:139], v[196:199]\n"
"v_mfma_f32_16x16x32_bf16 v[196:199], a[92:95], a[164:167], v[196:199]\n"
"v_pk_add_f32 v[82:83], v[82:83], v[178:179]\n"
"v_mfma_f32_16x16x32_bf16 v[200:203], a[96:99], a[136:139], v[200:203]\n"
"buffer_store_dwordx4 v[80:83], v251, s[36:39], s15 offen sc1\n"
"v_mfma_f32_16x16x32_bf16 v[200:203], a[100:103], a[164:167], v[200:203]\n"
"s_waitcnt lgkmcnt(15)\n"
"v_pk_add_f32 v[84:85], v[84:85], v[180:181]\n"
"v_mfma_f32_16x16x32_bf16 v[204:207], a[104:107], a[136:139], v[204:207]\n"
"v_pk_add_f32 v[86:87], v[86:87], v[182:183]\n"
"v_mfma_f32_16x16x32_bf16 v[204:207], a[108:111], a[164:167], v[204:207]\n"
"buffer_store_dwordx4 v[84:87], v251, s[36:39], s15 offen offset:1024 sc1\n"
"v_mfma_f32_16x16x32_bf16 v[96:99], a[80:83], a[124:127], v[96:99]\n"
"v_mfma_f32_16x16x32_bf16 v[96:99], a[84:87], a[152:155], v[96:99]\n"
"s_waitcnt lgkmcnt(15)\n"
"v_pk_add_f32 v[88:89], v[88:89], v[184:185]\n"
"v_mfma_f32_16x16x32_bf16 v[100:103], a[88:91], a[124:127], v[100:103]\n"
"buffer_load_dwordx2 v[242:243], off, s[76:79], s44 sc1\n"
"v_pk_add_f32 v[90:91], v[90:91], v[186:187]\n"
"v_mfma_f32_16x16x32_bf16 v[100:103], a[92:95], a[152:155], v[100:103]\n"
"buffer_store_dwordx4 v[88:91], v251, s[36:39], s15 offen offset:2048 sc1\n"
"v_mfma_f32_16x16x32_bf16 v[104:107], a[96:99], a[124:127], v[104:107]\n"
"s_waitcnt lgkmcnt(15)\n"
"v_pk_add_f32 v[92:93], v[92:93], v[188:189]\n"
"v_mfma_f32_16x16x32_bf16 v[104:107], a[100:103], a[152:155], v[104:107]\n"
"v_pk_add_f32 v[94:95], v[94:95], v[190:191]\n"
"v_mfma_f32_16x16x32_bf16 v[108:111], a[104:107], a[124:127], v[108:111]\n"
"v_mfma_f32_16x16x32_bf16 v[108:111], a[108:111], a[152:155], v[108:111]\n"
"buffer_store_dwordx4 v[92:95], v251, s[36:39], s15 offen offset:3072 sc1\n"
"v_mfma_f32_16x16x32_bf16 v[128:131], a[80:83], a[128:131], v[128:131]\n"
"v_mfma_f32_16x16x32_bf16 v[128:131], a[84:87], a[156:159], v[128:131]\n"
"v_mfma_f32_16x16x32_bf16 v[132:135], a[88:91], a[128:131], v[132:135]\n"
"s_waitcnt lgkmcnt(0)\n"
"s_barrier\n"
"v_mfma_f32_16x16x32_bf16 v[132:135], a[92:95], a[156:159], v[132:135]\n"
"v_mfma_f32_16x16x32_bf16 v[136:139], a[96:99], a[128:131], v[136:139]\n"
"ds_write_b128 v244, v[192:195]\n"
"v_mfma_f32_16x16x32_bf16 v[136:139], a[100:103], a[156:159], v[136:139]\n"
"v_mfma_f32_16x16x32_bf16 v[140:143], a[104:107], a[128:131], v[140:143]\n"
"v_mfma_f32_16x16x32_bf16 v[140:143], a[108:111], a[156:159], v[140:143]\n"
"v_mfma_f32_16x16x32_bf16 v[160:163], a[80:83], a[132:135], v[160:163]\n"
"ds_write_b128 v244, v[196:199] offset:1024\n"
"v_mfma_f32_16x16x32_bf16 v[160:163], a[84:87], a[160:163], v[160:163]\n"
"v_mfma_f32_16x16x32_bf16 v[164:167], a[88:91], a[132:135], v[164:167]\n"
"v_mfma_f32_16x16x32_bf16 v[164:167], a[92:95], a[160:163], v[164:167]\n"
"v_mfma_f32_16x16x32_bf16 v[168:171], a[96:99], a[132:135], v[168:171]\n"
"ds_write_b128 v244, v[200:203] offset:2048\n"
"v_mfma_f32_16x16x32_bf16 v[168:171], a[100:103], a[160:163], v[168:171]\n"
"v_mfma_f32_16x16x32_bf16 v[172:175], a[104:107], a[132:135], v[172:175]\n"
"v_mfma_f32_16x16x32_bf16 v[172:175], a[108:111], a[160:163], v[172:175]\n"
"v_mfma_f32_16x16x32_bf16 v[96:99], a[224:227], a[44:47], v[96:99]\n"
"ds_write_b128 v244, v[204:207] offset:3072\n"
"v_mfma_f32_16x16x32_bf16 v[96:99], a[228:231], a[68:71], v[96:99]\n"
"v_mfma_f32_16x16x32_bf16 v[100:103], a[232:235], a[44:47], v[100:103]\n"
"v_mfma_f32_16x16x32_bf16 v[100:103], a[236:239], a[68:71], v[100:103]\n"
"v_mfma_f32_16x16x32_bf16 v[104:107], a[240:243], a[44:47], v[104:107]\n"
"v_mfma_f32_16x16x32_bf16 v[104:107], a[244:247], a[68:71], v[104:107]\n"
"v_mfma_f32_16x16x32_bf16 v[108:111], a[248:251], a[44:47], v[108:111]\n"
"ds_write_b128 v244, v[96:99] offset:4096\n"
"v_mfma_f32_16x16x32_bf16 v[108:111], a[252:255], a[68:71], v[108:111]\n"
"v_mfma_f32_16x16x32_bf16 v[128:131], a[224:227], a[48:51], v[128:131]\n"
"v_mfma_f32_16x16x32_bf16 v[128:131], a[228:231], a[72:75], v[128:131]\n"
"v_mfma_f32_16x16x32_bf16 v[132:135], a[232:235], a[48:51], v[132:135]\n"
"ds_write_b128 v244, v[100:103] offset:5120\n"
"v_mfma_f32_16x16x32_bf16 v[132:135], a[236:239], a[72:75], v[132:135]\n"
"v_mfma_f32_16x16x32_bf16 v[136:139], a[240:243], a[48:51], v[136:139]\n"
"v_mfma_f32_16x16x32_bf16 v[136:139], a[244:247], a[72:75], v[136:139]\n"
"v_mfma_f32_16x16x32_bf16 v[140:143], a[248:251], a[48:51], v[140:143]\n"
"ds_write_b128 v244, v[104:107] offset:6144\n"
"v_mfma_f32_16x16x32_bf16 v[140:143], a[252:255], a[72:75], v[140:143]\n"
"v_mfma_f32_16x16x32_bf16 v[160:163], a[224:227], a[52:55], v[160:163]\n"
"v_mfma_f32_16x16x32_bf16 v[160:163], a[228:231], a[76:79], v[160:163]\n"
"v_mfma_f32_16x16x32_bf16 v[164:167], a[232:235], a[52:55], v[164:167]\n"
"ds_write_b128 v244, v[108:111] offset:7168\n"
"v_mfma_f32_16x16x32_bf16 v[164:167], a[236:239], a[76:79], v[164:167]\n"
"v_mfma_f32_16x16x32_bf16 v[168:171], a[240:243], a[52:55], v[168:171]\n"
"v_mfma_f32_16x16x32_bf16 v[168:171], a[244:247], a[76:79], v[168:171]\n"
"v_mfma_f32_16x16x32_bf16 v[172:175], a[248:251], a[52:55], v[172:175]\n"
"ds_write_b128 v244, v[128:131] offset:8192\n"
"v_mfma_f32_16x16x32_bf16 v[172:175], a[252:255], a[76:79], v[172:175]\n"
"v_mfma_f32_16x16x32_bf16 v[0:3], a[80:83], a[112:115], v[0:3]\n"
"v_mfma_f32_16x16x32_bf16 v[0:3], a[84:87], a[140:143], v[0:3]\n"
"v_mfma_f32_16x16x32_bf16 v[4:7], a[88:91], a[112:115], v[4:7]\n"
"ds_write_b128 v244, v[132:135] offset:9216\n"
"v_mfma_f32_16x16x32_bf16 v[4:7], a[92:95], a[140:143], v[4:7]\n"
"v_mfma_f32_16x16x32_bf16 v[8:11], a[96:99], a[112:115], v[8:11]\n"
"v_mfma_f32_16x16x32_bf16 v[8:11], a[100:103], a[140:143], v[8:11]\n"
"v_mfma_f32_16x16x32_bf16 v[12:15], a[104:107], a[112:115], v[12:15]\n"
"ds_write_b128 v244, v[136:139] offset:10240\n"
"v_mfma_f32_16x16x32_bf16 v[12:15], a[108:111], a[140:143], v[12:15]\n"
"v_mfma_f32_16x16x32_bf16 v[0:3], a[224:227], a[32:35], v[0:3]\n"
"v_mfma_f32_16x16x32_bf16 v[0:3], a[228:231], a[56:59], v[0:3]\n"
"v_mfma_f32_16x16x32_bf16 v[4:7], a[232:235], a[32:35], v[4:7]\n"
"ds_write_b128 v244, v[140:143] offset:11264\n"
"v_mfma_f32_16x16x32_bf16 v[4:7], a[236:239], a[56:59], v[4:7]\n"
"v_mfma_f32_16x16x32_bf16 v[8:11], a[240:243], a[32:35], v[8:11]\n"
"v_mfma_f32_16x16x32_bf16 v[8:11], a[244:247], a[56:59], v[8:11]\n"
"s_waitcnt vmcnt(0)\n"
"s_mov_b64 exec, 1\n"
"buffer_store_dwordx2 v[246:247], off, s[76:79], s73 sc1\n"
"s_mov_b64 exec, -1\n"
"buffer_load_dwordx2 v[248:249], off, s[76:79], s44 sc1\n"
"v_mfma_f32_16x16x32_bf16 v[12:15], a[248:251], a[32:35], v[12:15]\n"
"ds_write_b128 v244, v[160:163] offset:12288\n"
"v_mfma_f32_16x16x32_bf16 v[12:15], a[252:255], a[56:59], v[12:15]\n"
"v_mfma_f32_16x16x32_bf16 v[32:35], a[80:83], a[116:119], v[32:35]\n"
"v_mfma_f32_16x16x32_bf16 v[32:35], a[84:87], a[144:147], v[32:35]\n"
"v_mfma_f32_16x16x32_bf16 v[36:39], a[88:91], a[116:119], v[36:39]\n"
"ds_write_b128 v244, v[164:167] offset:13312\n"
"v_mfma_f32_16x16x32_bf16 v[36:39], a[92:95], a[144:147], v[36:39]\n"
"v_mfma_f32_16x16x32_bf16 v[40:43], a[96:99], a[116:119], v[40:43]\n"
"v_mfma_f32_16x16x32_bf16 v[40:43], a[100:103], a[144:147], v[40:43]\n"
"v_mfma_f32_16x16x32_bf16 v[44:47], a[104:107], a[116:119], v[44:47]\n"
"ds_write_b128 v244, v[168:171] offset:14336\n"
"v_mfma_f32_16x16x32_bf16 v[44:47], a[108:111], a[144:147], v[44:47]\n"
"v_mfma_f32_16x16x32_bf16 v[32:35], a[224:227], a[36:39], v[32:35]\n"
"v_mfma_f32_16x16x32_bf16 v[32:35], a[228:231], a[60:63], v[32:35]\n"
"v_mfma_f32_16x16x32_bf16 v[36:39], a[232:235], a[36:39], v[36:39]\n"
"ds_write_b128 v244, v[172:175] offset:15360\n"
"v_mfma_f32_16x16x32_bf16 v[36:39], a[236:239], a[60:63], v[36:39]\n"
"v_mfma_f32_16x16x32_bf16 v[40:43], a[240:243], a[36:39], v[40:43]\n"
"v_mfma_f32_16x16x32_bf16 v[40:43], a[244:247], a[60:63], v[40:43]\n"
"v_mfma_f32_16x16x32_bf16 v[44:47], a[248:251], a[36:39], v[44:47]\n"
"v_mfma_f32_16x16x32_bf16 v[44:47], a[252:255], a[60:63], v[44:47]\n"
"v_mfma_f32_16x16x32_bf16 v[64:67], a[80:83], a[120:123], v[64:67]\n"
"v_cmp_eq_u64 vcc, s[74:75], v[242:243]\n"
"s_nop 0\n"
"s_and_b32 s71, vcc_lo, 1\n"
"s_cselect_b32 s38, 26624, 0\n"
"v_mfma_f32_16x16x32_bf16 v[64:67], a[84:87], a[148:151], v[64:67]\n"
"s_add_u32 s15, s46, 0\n"
"v_mfma_f32_16x16x32_bf16 v[68:71], a[88:91], a[120:123], v[68:71]\n"
"buffer_load_dwordx4 v[112:115], v251, s[36:39], s15 offen sc1\n"
"v_mfma_f32_16x16x32_bf16 v[68:71], a[92:95], a[148:151], v[68:71]\n"
"buffer_load_dwordx4 v[116:119], v251, s[36:39], s15 offen offset:1024 sc1\n"
"v_mfma_f32_16x16x32_bf16 v[72:75], a[96:99], a[120:123], v[72:75]\n"
"buffer_load_dwordx4 v[120:123], v251, s[36:39], s15 offen offset:2048 sc1\n"
"v_mfma_f32_16x16x32_bf16 v[72:75], a[100:103], a[148:151], v[72:75]\n"
"buffer_load_dwordx4 v[124:127], v251, s[36:39], s15 offen offset:3072 sc1\n"
"s_add_u32 s15, s46, 4096\n"
"v_mfma_f32_16x16x32_bf16 v[76:79], a[104:107], a[120:123], v[76:79]\n"
"buffer_load_dwordx4 v[144:147], v251, s[36:39], s15 offen sc1\n"
"v_mfma_f32_16x16x32_bf16 v[76:79], a[108:111], a[148:151], v[76:79]\n"
"buffer_load_dwordx4 v[148:151], v251, s[36:39], s15 offen offset:1024 sc1\n"
"v_mfma_f32_16x16x32_bf16 v[64:67], a[224:227], a[40:43], v[64:67]\n"
"buffer_load_dwordx4 v[152:155], v251, s[36:39], s15 offen offset:2048 sc1\n"
"v_mfma_f32_16x16x32_bf16 v[64:67], a[228:231], a[64:67], v[64:67]\n"
"buffer_load_dwordx4 v[156:159], v251, s[36:39], s15 offen offset:3072 sc1\n"
"s_add_u32 s15, s46, 8192\n"
"v_mfma_f32_16x16x32_bf16 v[68:71], a[232:235], a[40:43], v[68:71]\n"
"buffer_load_dwordx4 v[176:179], v251, s[36:39], s15 offen sc1\n"
"v_mfma_f32_16x16x32_bf16 v[68:71], a[236:239], a[64:67], v[68:71]\n"
"buffer_load_dwordx4 v[180:183], v251, s[36:39], s15 offen offset:1024 sc1\n"
"v_mfma_f32_16x16x32_bf16 v[72:75], a[240:243], a[40:43], v[72:75]\n"
"buffer_load_dwordx4 v[184:187], v251, s[36:39], s15 offen offset:2048 sc1\n"
"v_mfma_f32_16x16x32_bf16 v[72:75], a[244:247], a[64:67], v[72:75]\n"
"buffer_load_dwordx4 v[188:191], v251, s[36:39], s15 offen offset:3072 sc1\n"
"s_add_u32 s15, s46, 12288\n"
"v_mfma_f32_16x16x32_bf16 v[76:79], a[248:251], a[40:43], v[76:79]\n"
"s_waitcnt lgkmcnt(0)\n"
"s_barrier\n"
"buffer_load_dwordx4 v[212:215], v251, s[36:39], s15 offen sc1\n"
"v_mfma_f32_16x16x32_bf16 v[76:79], a[252:255], a[64:67], v[76:79]\n"
"ds_read_b128 v[192:195], v253\n"
"ds_read_b128 v[196:199], v253 offset:34816\n"
"ds_read_b128 v[200:203], v254\n"
"ds_read_b128 v[204:207], v254 offset:34816\n"
"ds_read_b128 v[96:99], v245\n"
"ds_read_b128 v[100:103], v245 offset:1024\n"
"ds_read_b128 v[104:107], v245 offset:2048\n"
"ds_read_b128 v[108:111], v245 offset:3072\n"
"ds_read_b128 v[128:131], v245 offset:4096\n"
"ds_read_b128 v[132:135], v245 offset:5120\n"
"ds_read_b128 v[136:139], v245 offset:6144\n"
"ds_read_b128 v[140:143], v245 offset:7168\n"
"ds_read_b128 v[160:163], v245 offset:8192\n"
"ds_read_b128 v[164:167], v245 offset:9216\n"
"ds_read_b128 v[168:171], v245 offset:10240\n"
"ds_read_b128 v[172:175], v245 offset:11264\n"
"s_waitcnt lgkmcnt(14)\n"
"v_pk_add_f32 v[192:193], v[192:193], v[196:197]\n"
"v_pk_add_f32 v[194:195], v[194:195], v[198:199]\n"
"s_waitcnt lgkmcnt(12)\n"
"v_pk_add_f32 v[200:201], v[200:201], v[204:205]\n"
"v_pk_add_f32 v[202:203], v[202:203], v[206:207]\n"
"v_pk_add_f32 v[192:193], v[192:193], v[200:201]\n"
"v_pk_add_f32 v[194:195], v[194:195], v[202:203]\n"
"s_waitcnt lgkmcnt(11)\n"
"v_pk_add_f32 v[0:1], v[0:1], v[96:97]\n"
"v_pk_add_f32 v[2:3], v[2:3], v[98:99]\n"
"s_waitcnt lgkmcnt(10)\n"
"v_pk_add_f32 v[4:5], v[4:5], v[100:101]\n"
"v_pk_add_f32 v[6:7], v[6:7], v[102:103]\n"
"s_waitcnt lgkmcnt(9)\n"
"v_pk_add_f32 v[8:9], v[8:9], v[104:105]\n"
"v_pk_add_f32 v[10:11], v[10:11], v[106:107]\n"
"s_waitcnt lgkmcnt(8)\n"
"v_pk_add_f32 v[12:13], v[12:13], v[108:109]\n"
"v_pk_add_f32 v[14:15], v[14:15], v[110:111]\n"
"s_waitcnt lgkmcnt(7)\n"
"v_pk_add_f32 v[32:33], v[32:33], v[128:129]\n"
"v_pk_add_f32 v[34:35], v[34:35], v[130:131]\n"
"s_waitcnt lgkmcnt(6)\n"
"v_pk_add_f32 v[36:37], v[36:37], v[132:133]\n"
"v_pk_add_f32 v[38:39], v[38:39], v[134:135]\n"
"s_waitcnt lgkmcnt(5)\n"
"v_pk_add_f32 v[40:41], v[40:41], v[136:137]\n"
"v_pk_add_f32 v[42:43], v[42:43], v[138:139]\n"
"s_waitcnt lgkmcnt(4)\n"
"v_pk_add_f32 v[44:45], v[44:45], v[140:141]\n"
"v_pk_add_f32 v[46:47], v[46:47], v[142:143]\n"
"s_waitcnt lgkmcnt(3)\n"
"v_pk_add_f32 v[64:65], v[64:65], v[160:161]\n"
"v_pk_add_f32 v[66:67], v[66:67], v[162:163]\n"
"s_waitcnt lgkmcnt(2)\n"
"v_pk_add_f32 v[68:69], v[68:69], v[164:165]\n"
"v_pk_add_f32 v[70:71], v[70:71], v[166:167]\n"
"s_waitcnt lgkmcnt(1)\n"
"v_pk_add_f32 v[72:73], v[72:73], v[168:169]\n"
"v_pk_add_f32 v[74:75], v[74:75], v[170:171]\n"
"s_waitcnt lgkmcnt(0)\n"
"v_pk_add_f32 v[76:77], v[76:77], v[172:173]\n"
"v_pk_add_f32 v[78:79], v[78:79], v[174:175]\n"
"s_cmp_eq_u32 s71, 0\n"
"s_cbranch_scc1 L2_late_c\n"
"L2_late_r:\n"
"s_waitcnt vmcnt(0)\n"
"v_pk_add_f32 v[0:1], v[0:1], v[112:113]\n"
"v_pk_add_f32 v[2:3], v[2:3], v[114:115]\n"
"v_pk_add_f32 v[4:5], v[4:5], v[116:117]\n"
"v_pk_add_f32 v[6:7], v[6:7], v[118:119]\n"
"v_pk_add_f32 v[8:9], v[8:9], v[120:121]\n"
"v_pk_add_f32 v[10:11], v[10:11], v[122:123]\n"
"v_pk_add_f32 v[12:13], v[12:13], v[124:125]\n"
"v_pk_add_f32 v[14:15], v[14:15], v[126:127]\n"
"v_cvt_pk_bf16_f32 v112, v0, v1\n"
"v_cvt_pk_bf16_f32 v113, v2, v3\n"
"v_cvt_pk_bf16_f32 v114, v4, v5\n"
"v_cvt_pk_bf16_f32 v115, v6, v7\n"
"v_cvt_pk_bf16_f32 v120, v8, v9\n"
"v_cvt_pk_bf16_f32 v121, v10, v11\n"
"v_cvt_pk_bf16_f32 v122, v12, v13\n"
"v_cvt_pk_bf16_f32 v123, v14, v15\n"
"s_mov_b32 exec_lo, 0xffff0000\n"
"s_mov_b32 exec_hi, 0xffff0000\n"
"v_swap_b32 v112, v114\n"
"v_swap_b32 v113, v115\n"
"v_swap_b32 v120, v122\n"
"v_swap_b32 v121, v123\n"
"s_mov_b64 exec, -1\n"
"s_mov_b32 s14, 0\n"
"buffer_store_dwordx4 v[112:115], v250, s[8:11], s14 offen\n"
"buffer_store_dwordx4 v[120:123], v250, s[8:11], s14 offen offset:64\n"
"v_pk_add_f32 v[32:33], v[32:33], v[144:145]\n"
"v_pk_add_f32 v[34:35], v[34:35], v[146:147]\n"
"v_pk_add_f32 v[36:37], v[36:37], v[148:149]\n"
"v_pk_add_f32 v[38:39], v[38:39], v[150:151]\n"
"v_pk_add_f32 v[40:41], v[40:41], v[152:153]\n"
"v_pk_add_f32 v[42:43], v[42:43], v[154:155]\n"
"v_pk_add_f32 v[44:45], v[44:45], v[156:157]\n"
"v_pk_add_f32 v[46:47], v[46:47], v[158:159]\n"
"v_cvt_pk_bf16_f32 v144, v32, v33\n"
"v_cvt_pk_bf16_f32 v145, v34, v35\n"
"v_cvt_pk_bf16_f32 v146, v36, v37\n"
"v_cvt_pk_bf16_f32 v147, v38, v39\n"
"v_cvt_pk_bf16_f32 v152, v40, v41\n"
"v_cvt_pk_bf16_f32 v153, v42, v43\n"
"v_cvt_pk_bf16_f32 v154, v44, v45\n"
"v_cvt_pk_bf16_f32 v155, v46, v47\n"
"s_mov_b32 exec_lo, 0xffff0000\n"
"s_mov_b32 exec_hi, 0xffff0000\n"
"v_swap_b32 v144, v146\n"
"v_swap_b32 v145, v147\n"
"v_swap_b32 v152, v154\n"
"v_swap_b32 v153, v155\n"
"s_mov_b64 exec, -1\n"
"s_mov_b32 s14, 32768\n"
"buffer_store_dwordx4 v[144:147], v250, s[8:11], s14 offen\n"
"buffer_store_dwordx4 v[152:155], v250, s[8:11], s14 offen offset:64\n"
"v_pk_add_f32 v[64:65], v[64:65], v[176:177]\n"
"v_pk_add_f32 v[66:67], v[66:67], v[178:179]\n"
"v_pk_add_f32 v[68:69], v[68:69], v[180:181]\n"
"v_pk_add_f32 v[70:71], v[70:71], v[182:183]\n"
"v_pk_add_f32 v[72:73], v[72:73], v[184:185]\n"
"v_pk_add_f32 v[74:75], v[74:75], v[186:187]\n"
"v_pk_add_f32 v[76:77], v[76:77], v[188:189]\n"
"v_pk_add_f32 v[78:79], v[78:79], v[190:191]\n"
"v_pk_add_f32 v[192:193], v[192:193], v[212:213]\n"
"v_pk_add_f32 v[194:195], v[194:195], v[214:215]\n"
"v_cvt_pk_bf16_f32 v176, v64, v65\n"
"v_cvt_pk_bf16_f32 v177, v66, v67\n"
"v_cvt_pk_bf16_f32 v178, v68, v69\n"
"v_cvt_pk_bf16_f32 v179, v70, v71\n"
"v_cvt_pk_bf16_f32 v184, v72, v73\n"
"v_cvt_pk_bf16_f32 v185, v74, v75\n"
"v_cvt_pk_bf16_f32 v186, v76, v77\n"
"v_cvt_pk_bf16_f32 v187, v78, v79\n"
"v_cvt_pk_bf16_f32 v212, v192, v193\n"
"v_cvt_pk_bf16_f32 v213, v194, v195\n"
"s_mov_b32 exec_lo, 0xffff0000\n"
"s_mov_b32 exec_hi, 0xffff0000\n"
"v_swap_b32 v176, v178\n"
"v_swap_b32 v177, v179\n"
"v_swap_b32 v184, v186\n"
"v_swap_b32 v185, v187\n"
"s_mov_b64 exec, -1\n"
"s_mov_b32 s14, 65536\n"
"buffer_store_dwordx4 v[176:179], v250, s[8:11], s14 offen\n"
"buffer_store_dwordx4 v[184:187], v250, s[8:11], s14 offen offset:64\n"
"buffer_store_dwordx2 v[212:213], v228, s[8:11], s42 offen\n"
"s_or_b32 s14, s24, s21\n"
"s_cbranch_scc1 L2_ne\n"
"s_lshl_b32 s14, s23, 8\n"
"s_add_u32 s14, s14, 65536\n"
"s_mov_b64 exec, 1\n"
"buffer_store_dwordx2 v[246:247], off, s[76:79], s14 sc1\n"
"s_mov_b64 exec, -1\n"
"L2_ne:\n"
"s_endpgm\n"
"L2_late_c:\n"
"s_mov_b32 s56, 1048576\n"
"s_branch L2_late_w\n"
"L2_late_l:\n"
"buffer_load_dwordx2 v[248:249], off, s[76:79], s44 sc1\n"
"L2_late_w:\n"
"s_waitcnt vmcnt(0)\n"
"v_cmp_eq_u64 vcc, s[74:75], v[248:249]\n"
"s_sub_u32 s56, s56, 1\n"
"s_cbranch_scc1 L2_late_x\n"
"s_cmp_eq_u32 vcc_lo, 0\n"
"s_cbranch_scc1 L2_late_l\n"
"L2_late_x:\n"
"s_mov_b32 s38, 26624\n"
"s_add_u32 s15, s46, 0\n"
"buffer_load_dwordx4 v[112:115], v251, s[36:39], s15 offen sc1\n"
"buffer_load_dwordx4 v[116:119], v251, s[36:39], s15 offen offset:1024 sc1\n"
"buffer_load_dwordx4 v[120:123], v251, s[36:39], s15 offen offset:2048 sc1\n"
"buffer_load_dwordx4 v[124:127], v251, s[36:39], s15 offen offset:3072 sc1\n"
"s_add_u32 s15, s46, 4096\n"
"buffer_load_dwordx4 v[144:147], v251, s[36:39], s15 offen sc1\n"
"buffer_load_dwordx4 v[148:151], v251, s[36:39], s15 offen offset:1024 sc1\n"
"buffer_load_dwordx4 v[152:155], v251, s[36:39], s15 offen offset:2048 sc1\n"
"buffer_load_dwordx4 v[156:159], v251, s[36:39], s15 offen offset:3072 sc1\n"
"s_add_u32 s15, s46, 8192\n"
"buffer_load_dwordx4 v[176:179], v251, s[36:39], s15 offen sc1\n"
"buffer_load_dwordx4 v[180:183], v251, s[36:39], s15 offen offset:1024 sc1\n"
"buffer_load_dwordx4 v[184:187], v251, s[36:39], s15 offen offset:2048 sc1\n"
"buffer_load_dwordx4 v[188:191], v251, s[36:39], s15 offen offset:3072 sc1\n"
"s_add_u32 s15, s46, 12288\n"
"buffer_load_dwordx4 v[212:215], v251, s[36:39], s15 offen sc1\n"
"s_branch L2_late_r\n"
"L2_c0_s:\n"
"s_mov_b32 s56, 1048576\n"
"L2_c0_l:\n"
"s_sleep 1\n"
"buffer_load_dwordx2 v[248:249], v241, s[76:79], 0 offen sc1\n"
"s_waitcnt vmcnt(0)\n"
"v_cmp_eq_u64 vcc, s[74:75], v[248:249]\n"
"s_sub_u32 s56, s56, 1\n"
"s_cbranch_scc1 L2_c0_r\n"
"s_or_b32 s66, s66, vcc_lo\n"
"s_bitcmp1_b32 s66, s59\n"
"s_cbranch_scc0 L2_c0_l\n"
"s_branch L2_c0_r\n"
"L2_c1_s:\n"
"s_mov_b32 s56, 1048576\n"
"L2_c1_l:\n"
"s_sleep 1\n"
"buffer_load_dwordx2 v[248:249], v241, s[76:79], 0 offen sc1\n"
"s_waitcnt vmcnt(0)\n"
"v_cmp_eq_u64 vcc, s[74:75], v[248:249]\n"
"s_sub_u32 s56, s56, 1\n"
"s_cbranch_scc1 L2_c1_r\n"
"s_or_b32 s66, s66, vcc_lo\n"
"s_bitcmp1_b32 s66, s59\n"
"s_cbranch_scc0 L2_c1_l\n"
"s_branch L2_c1_r\n"
"L2_c2_s:\n"
"s_mov_b32 s56, 1048576\n"
"L2_c2_l:\n"
"s_sleep 1\n"
"buffer_load_dwordx2 v[248:249], v241, s[76:79], 0 offen sc1\n"
"s_waitcnt vmcnt(0)\n"
"v_cmp_eq_u64 vcc, s[74:75], v[248:249]\n"
"s_sub_u32 s56, s56, 1\n"
"s_cbranch_scc1 L2_c2_r\n"
"s_or_b32 s66, s66, vcc_lo\n"
"s_bitcmp1_b32 s66, s59\n"
"s_cbranch_scc0 L2_c2_l\n"
"s_branch L2_c2_r\n"
"L2_c3_s:\n"
"s_mov_b32 s56, 1048576\n"
"L2_c3_l:\n"
"s_sleep 1\n"
"buffer_load_dwordx2 v[248:249], v241, s[76:79], 0 offen sc1\n"
"s_waitcnt vmcnt(0)\n"
"v_cmp_eq_u64 vcc, s[74:75], v[248:249]\n"
"s_sub_u32 s56, s56, 1\n"
"s_cbranch_scc1 L2_c3_r\n"
"s_or_b32 s66, s66, vcc_lo\n"
"s_bitcmp1_b32 s66, s59\n"
"s_cbranch_scc0 L2_c3_l\n"
"s_branch L2_c3_r\n"
"L_end:\n"
  :: "s"(__builtin_amdgcn_kernarg_segment_ptr()), "{v0}"(threadIdx.x), "s"(blockIdx.x), "s"(ldsa)
  : CLOBBERS);
}

extern "C" void kernel_launch(void* const* d_in, const int* in_sizes, int n_in, void* d_out, int out_size, void* d_ws, size_t ws_size,
                              hipStream_t stream) {
  mha_block<<<256, 256, 0, stream>>>(d_in[0], d_in[1], d_in[2], d_in[3], d_in[4], d_ws, d_out);
}
```
